```python
import torch

def _softmax(x, axis=-1):
    return torch.softmax(x, dim=axis)
def _gelu(x):
    return torch.nn.functional.gelu(x, approximate="tanh")
def _silu(x):
    return torch.nn.functional.silu(x)
def _sigmoid(x):
    return torch.sigmoid(x)
def _layernorm(x, axis=-1, eps=1e-5):
    m = x.mean(dim=axis, keepdim=True)
    v = x.var(dim=axis, keepdim=True, unbiased=False)
    return (x - m) / torch.sqrt(v + eps)
def _scatter_add(tgt, idx, src, axis=0):
    return tgt.clone().index_add_(axis, idx, src)
def _segment_sum(src, seg, n):
    out = torch.zeros((n,) + src.shape[1:], dtype=src.dtype)
    return out.index_add_(0, seg, src)
def _pad(x, pw):
    flat = tuple(v for pair in reversed(pw) for v in pair)
    return torch.nn.functional.pad(x, flat)

kernel_name = 'sequential_preLN__sliding_local_dilated__moe_arch_mod__prompt'

def setup_inputs():

    _w = (lambda a: (lambda t: t.to(torch.bfloat16) if t.is_floating_point() else t)(torch.as_tensor(a)))
    def _A_setup():
        B, S, D = 1, 2048, 640
        return {
            "x": _w((lambda *s: torch.randn(*s, dtype=torch.bfloat16) * 0.05)(B, S, D)),
            "wqkv": _w((lambda *s: torch.randn(*s, dtype=torch.bfloat16) * 0.05)(D, 3 * D)),
            "wo": _w((lambda *s: torch.randn(*s, dtype=torch.bfloat16) * 0.05)(D, D)),
        }
    _A_in = _A_setup()

    def _B_setup():
        B, S, D, F, C = 1, 2048, 640, 1536, 1
        return {
            "x": _w((lambda *s: torch.randn(*s, dtype=torch.bfloat16) * 0.05)(B, S, D)),
            "w_r": _w((lambda *s: torch.randn(*s, dtype=torch.bfloat16) * 0.05)(D)),
            "w_gate": _w((lambda *s: torch.randn(*s, dtype=torch.bfloat16) * 0.05)(D, F)),
            "w_up": _w((lambda *s: torch.randn(*s, dtype=torch.bfloat16) * 0.05)(D, F)),
            "w_down": _w((lambda *s: torch.randn(*s, dtype=torch.bfloat16) * 0.05)(F, D)),
        }
    _B_in = _B_setup()

    return {
        "A_x": _A_in["x"],
        "A_wqkv": _A_in["wqkv"],
        "A_wo": _A_in["wo"],
        "B_x": _B_in["x"],
        "B_w_r": _B_in["w_r"],
        "B_w_gate": _B_in["w_gate"],
        "B_w_up": _B_in["w_up"],
        "B_w_down": _B_in["w_down"],
    }

def reference(A_x, A_wqkv, A_wo, B_x, B_w_r, B_w_gate, B_w_up, B_w_down):
    def _A_ref(x, wqkv, wo):
        B, S, D = x.shape
        H = 32; Dh = D // H
        W, dil = 8, 2
        qkv = (lambda x, *d: x.permute(*d))((x @ wqkv).reshape(B, S, 3, H, Dh), 2, 0, 3, 1, 4)
        q, k, v = qkv[0], qkv[1], qkv[2]
        scale = Dh ** -0.5
        pos = torch.arange(S)
        off = torch.arange(W) * dil
        idx = pos[:, None] - off[None, :]
        mask = idx >= 0
        idx_c = torch.where(mask, idx, 0).reshape(-1)
        kg = k[:, :, idx_c, :].reshape(B, H, S, W, Dh)
        vg = v[:, :, idx_c, :].reshape(B, H, S, W, Dh)
        logits = torch.einsum("bhsd,bhswd->bhsw", q, kg) * scale
        logits = torch.where(mask[None, None, :, :], logits, -1e9)
        attn = _softmax(logits, axis=-1)
        o = torch.einsum("bhsw,bhswd->bhsd", attn, vg)
        return (lambda x, *d: x.permute(*d))(o, 0, 2, 1, 3).reshape(B, S, D) @ wo

    def _B_ref(x, w_r, w_gate, w_up, w_down):
        B, S, D = x.shape
        C = 1
        r = x @ w_r
        idx = torch.argsort(-r, axis=-1)[:, :C]
        idx_e = (lambda x, n, axis: torch.repeat_interleave(x, n, dim=axis))(idx.reshape(B, C, 1), D, axis=2)
        x_sel = (lambda x, i, axis: torch.take_along_dim(x, i, dim=axis))(x, idx_e, axis=1)
        g = _silu(x_sel @ w_gate)
        u = x_sel @ w_up
        y = (g * u) @ w_down
        r_sel = _sigmoid((lambda x, i, axis: torch.take_along_dim(x, i, dim=axis))(r, idx, axis=1))
        y = y * r_sel.reshape(B, C, 1)
        b_off = (torch.arange(B) * S).reshape(B, 1)
        flat_idx = (idx + b_off).reshape(B * C)
        zero = (x * 0.0).reshape(B * S, D)
        upd = _scatter_add(zero, flat_idx, y.reshape(B * C, D), axis=0)
        return x + upd.reshape(B, S, D)

    x = A_x
    h = x + _A_ref(_layernorm(x, axis=-1), A_wqkv, A_wo)
    h2 = h + _B_ref(_layernorm(h, axis=-1), B_w_r, B_w_gate, B_w_up, B_w_down)
    return h2
```

```cpp
#define PCD 224
#include <hip/hip_runtime.h>
#include <hip/hip_bf16.h>
#include <stdint.h>

#define SEQ 2048
#define DM 640
#define FF 1536

typedef __attribute__((ext_vector_type(8))) short bf16x8;
typedef __attribute__((ext_vector_type(4))) short s16x4;
typedef __attribute__((ext_vector_type(4))) float f32x4;
typedef __attribute__((ext_vector_type(2))) float f32x2;
typedef __attribute__((ext_vector_type(2))) __bf16 bf16x2_t;
typedef __attribute__((address_space(3))) s16x4* lds_s16x4_p;
typedef unsigned short u16;
typedef unsigned int u32;
typedef unsigned long long u64;
typedef __attribute__((ext_vector_type(4))) unsigned int u32x4;
typedef __attribute__((ext_vector_type(2))) unsigned int u32x2;

#define LSTAMP(i)
#define FLUSH_STAMPS

#define MAPV 2
#define HK0 15
#define RGAP 6
#define CGAP 6
#define KGAP 10
#define CDEL 16
#define PDEL2 7
#define OBACK 2
#define OSL 10
#define PDELAY 220
#define FDELAY 450
#define KSTART 1050
#define PGAP 5
#define PDEL1 2
#define PSL 200
#define VDELAY 150
#define FNT " nt"
#define CCD 306
#define PTHR 0.91f
#define GADD 58
#define OORD 0, 4, 5, 6, 7, 1, 2, 3
#define CODE_LINES 280

#define LDS_BARRIER() do { __builtin_amdgcn_sched_barrier(0); asm volatile("s_waitcnt lgkmcnt(0)\n\ts_barrier" ::: "memory"); __builtin_amdgcn_sched_barrier(0); } while (0)
__device__ __forceinline__ float bf2f(u16 v) { return __uint_as_float(((u32)v) << 16); }
__device__ __forceinline__ u16 f2bf(float f) { return __builtin_bit_cast(u16, (__bf16)f); }
__device__ __forceinline__ float rbf(float f) { return bf2f(f2bf(f)); }
__device__ __forceinline__ float lo_f(u32 w) { return __uint_as_float(w << 16); }
__device__ __forceinline__ float hi_f(u32 w) { return __uint_as_float(w & 0xFFFF0000u); }
__device__ __forceinline__ u32 cvt2(f32x2 v) { return __builtin_bit_cast(u32, __builtin_convertvector(v, bf16x2_t)); }
__device__ __forceinline__ f32x2 unpk(u32 w) { return (f32x2){lo_f(w), hi_f(w)}; }
#define NORM2(HV, MB, RD) cvt2(unpk(cvt2(unpk(HV) - (f32x2){MB, MB})) * (f32x2){RD, RD})
__device__ __forceinline__ s16x4 tr_read(const char* p) {
  return __builtin_amdgcn_ds_read_tr16_b64_v4i16((lds_s16x4_p)(p));
}
template <int V> struct icst { static constexpr int value = V; };
template <int CTRL>
__device__ __forceinline__ float dppf(float v) {
  return __builtin_bit_cast(float, __builtin_amdgcn_update_dpp(0, __builtin_bit_cast(int, v), CTRL, 0xf, 0xf, true));
}
__device__ __forceinline__ float row8_sum(float v) {
  v += dppf<0xB1>(v);
  v += dppf<0x4E>(v);
  v += dppf<0x141>(v);
  return v;
}
__device__ __forceinline__ float row16_sum(float v) {
  v = row8_sum(v);
  v += dppf<0x140>(v);
  return v;
}
__device__ __forceinline__ float xg_sum(float v) {
  u32 a = __float_as_uint(v);
  auto r = __builtin_amdgcn_permlane32_swap(a, a, false, false);
  v = __uint_as_float(r[0]) + __uint_as_float(r[1]);
  a = __float_as_uint(v);
  auto r2 = __builtin_amdgcn_permlane16_swap(a, a, false, false);
  return __uint_as_float(r2[0]) + __uint_as_float(r2[1]);
}
__device__ __forceinline__ float tr_sum(f32x4 ys) {
  auto r01 = __builtin_amdgcn_permlane32_swap(__float_as_uint(ys[0]), __float_as_uint(ys[1]), false, false);
  auto r23 = __builtin_amdgcn_permlane32_swap(__float_as_uint(ys[2]), __float_as_uint(ys[3]), false, false);
  const u32 u = __float_as_uint(__uint_as_float(r01[0]) + __uint_as_float(r01[1]));
  const u32 vv = __float_as_uint(__uint_as_float(r23[0]) + __uint_as_float(r23[1]));
  auto r2 = __builtin_amdgcn_permlane16_swap(u, vv, false, false);
  return __uint_as_float(r2[0]) + __uint_as_float(r2[1]);
}
__device__ __forceinline__ float wave_sum(float v) { return xg_sum(row16_sum(v)); }
template <int CTRL>
__device__ __forceinline__ u32 dppu(u32 v) { return (u32)__builtin_amdgcn_update_dpp(0, (int)v, CTRL, 0xf, 0xf, true); }
__device__ __forceinline__ u32 wave_max(u32 v) {
  v = max(v, dppu<0xB1>(v)); v = max(v, dppu<0x4E>(v)); v = max(v, dppu<0x141>(v)); v = max(v, dppu<0x140>(v));
  return max(max((u32)__builtin_amdgcn_readlane(v, 0), (u32)__builtin_amdgcn_readlane(v, 16)),
             max((u32)__builtin_amdgcn_readlane(v, 32), (u32)__builtin_amdgcn_readlane(v, 48)));
}
__device__ __forceinline__ u32 mono(float f) {
  const u32 b = __float_as_uint(f);
  return b ^ ((u32)((int)b >> 31) | 0x80000000u);
}
__device__ __forceinline__ float dot2b(u32 a, u32 b, float c) {
  return __builtin_amdgcn_fdot2_f32_bf16(__builtin_bit_cast(bf16x2_t, a), __builtin_bit_cast(bf16x2_t, b), c, false);
}
template <int OFF>
__device__ __forceinline__ void dma_m0(u32 voff, const char* sbase, u32 dst) {
  asm volatile("s_mov_b32 m0, %2\n\ts_nop 0\n\tglobal_load_lds_dwordx4 %0, %1 offset:%3"
               :: "v"(voff), "s"(sbase), "s"(dst), "n"(OFF) : "memory");
}
template <int OFF>
__device__ __forceinline__ void dma_nx(u32 voff, const char* sbase) {
  asm volatile("global_load_lds_dwordx4 %0, %1 offset:%2" :: "v"(voff), "s"(sbase), "n"(OFF) : "memory");
}
__device__ __forceinline__ u32 lds_ld32(u32 addr) {
  u32 v;
  asm volatile("ds_read_b32 %0, %1\n\ts_waitcnt lgkmcnt(0)" : "=v"(v) : "v"(addr) : "memory");
  return v;
}
__device__ __forceinline__ u32x4 lds_ld128(u32 addr) {
  u32x4 v;
  asm volatile("ds_read_b128 %0, %1\n\ts_waitcnt lgkmcnt(0)" : "=v"(v) : "v"(addr) : "memory");
  return v;
}
__device__ __forceinline__ bf16x8 mk8(u32 a, u32 b, u32 c, u32 d) {
  return __builtin_bit_cast(bf16x8, (u32x4){a, b, c, d});
}
__device__ __forceinline__ bf16x8 tr_frag(const char* p, int off2) {
  s16x4 b0 = tr_read(p);
  s16x4 b1 = tr_read(p + off2);
  bf16x8 r;
  r[0] = b0[0]; r[1] = b0[1]; r[2] = b0[2]; r[3] = b0[3];
  r[4] = b1[0]; r[5] = b1[1]; r[6] = b1[2]; r[7] = b1[3];
  return r;
}
#define SPIN_CAP 400000
#define MFMA(A, B, C) __builtin_amdgcn_mfma_f32_16x16x32_bf16(A, B, C, 0, 0, 0)

#define HSLOT 16384
#define XRING 98304
#define XSLOT 12288
#define LDS_TOTAL 163840
#define EXO 102400
#define HIM 148480
#define PST_OFF (EXO + 40960)
#define TAB_OFF 161280
#define ONES_OFF 162048
#define CTL_OFF 162176
#define WR_OFF 162304
#define REC_OFF 162816
#define HN_OFF 124928
#define HSL 1344
#define TCH_OFF 162560
#define ACT_OFF 135168
#define KEY_OFF (ACT_OFF + 5120)
#define POST_OFF (KEY_OFF + 256)
#define DOWN_OFF 81920
#define WS_EPOCH 0
#define WS_HFLAG 4096
#define WS_VP 40960
#define WS_LN1 16384
#define WS_FILL 131072
#define WS_O WS_FILL
#define WS_STAT (WS_O + 2621440)
#define WS_CAND (WS_STAT + 425984)
#define WS_PART (WS_CAND + 133120)
#define WS_BXF (WS_PART + 122880)
#define WS_PC (WS_BXF + 26624)
#define WS_PART2 (WS_PC + 66560)
#define WS_PK (4u << 20)
#define SENT 0xFFFFFFFFu

#define GST4(VO, SB, O, V) asm volatile("global_store_dwordx4 %0, %1, %2 offset:%3 sc1\n\ts_nop 1" ::"v"(VO), "v"(V), "s"(SB), "n"(O) : "memory")
#define GST2(VO, SB, O, V) asm volatile("global_store_dwordx2 %0, %1, %2 offset:%3 sc1\n\ts_nop 1" ::"v"(VO), "v"(V), "s"(SB), "n"(O) : "memory")
#define GLD2(D, VO, SB, O) asm volatile("global_load_dwordx2 %0, %1, %2 offset:%3 sc1" : "=v"(D) : "v"(VO), "s"(SB), "n"(O) : "memory")

__device__ __forceinline__ void touch_line(const void* p) {
  asm volatile("s_mov_b32 m0, %1\n\ts_nop 0\n\tglobal_load_lds_dword %0, off" ::"v"(p), "s"(TCH_OFF) : "memory");
}
__device__ __forceinline__ u32 w_off(int i, int lane, int hg) {
  int cc = (lane & 31) ^ (((2 * i + (lane >> 5)) & 7) << 1);
  cc = cc > 29 ? 29 : cc;
  return (lane >> 5) * 3840 + ((cc >= 10 ? 1120 : 0) + (cc >= 20 ? 1120 : 0)) + hg * 160 + cc * 16;
}
#define W_ROW(I) (((((2 * (I)) & 0x13) | (((2 * (I)) & 4) << 1) | (((2 * (I)) & 8) >> 1))) * 3840)
__device__ __forceinline__ u32 x_off(int i, int lane, int r0) {
  const int xr = i * 8 + (lane >> 3);
  int grow = r0 - 16 + xr;
  grow = grow < 0 ? 0 : (grow > SEQ - 1 ? SEQ - 1 : grow);
  return grow * 1280 + (((lane & 7) ^ ((xr >> 1) & 7)) * 16);
}

#define ATT_ASM \
  "ds_read_b32 v28, %[aq] offset:64\n\t" \
  "ds_read_b32 v30, %[aka]\n\t" \
  "ds_read_b32 v31, %[akb]\n\t" \
  "v_mov_b64 v[38:39], 0\n\t" \
  "v_mov_b64 v[42:43], 0\n\t" \
  "s_lshl_b32 s41, %[kh], 5\n\t" \
  "s_lshl_b64 s[46:47], 0xffff, s41\n\t" \
  "s_lshl_b64 s[48:49], s[46:47], 16\n\t" \
  "s_mov_b32 s50, 0xaaaaaaaa\n\t" \
  "s_mov_b32 s51, 0xaaaaaaaa\n\t" \
  "s_waitcnt lgkmcnt(0)\n\t" \
  "s_barrier\n\t" \
  "ds_read_b64 v[36:37], %[eh] offset:512\n\t" \
  "ds_read_b64 v[44:45], %[eh] offset:15360\n\t" \
  "ds_read_b64 v[46:47], %[e4] offset:27648\n\t" \
  "ds_read_b64 v[48:49], %[eh] offset:15872\n\t" \
  "ds_read_b64 v[50:51], %[e4] offset:28160\n\t" \
  "s_mov_b64 exec, s[46:47]\n\t" \
  "ds_read_b64 v[38:39], %[e4] offset:12800\n\t" \
  "s_mov_b64 exec, s[48:49]\n\t" \
  "ds_read_b64 v[42:43], %[e4] offset:12800\n\t" \
  "s_mov_b64 exec, -1\n\t" \
  "ds_read_b64 v[40:41], %[eh] offset:3584\n\t" \
  "ds_read_b64 v[52:53], %[eh] offset:18432\n\t" \
  "ds_read_b64 v[54:55], %[e4] offset:27648\n\t" \
  "ds_read_b64 v[56:57], %[eh] offset:18944\n\t" \
  "ds_read_b64 v[58:59], %[e4] offset:28160\n\t" \
  "v_mul_f32 v28, 0x3ea52b5b, v28\n\t" \
  "v_pk_mul_f32 v[32:33], v[28:29], v[30:31] op_sel_hi:[0,1]\n\t" \
  "s_waitcnt lgkmcnt(6)\n\t" \
  "v_mfma_f32_16x16x32_bf16 v[44:47], v[44:47], v[36:39], 0\n\t" \
  "ds_read2st64_b64 v[60:63], %[eh] offset0:60 offset1:61\n\t" \
  "v_mfma_f32_16x16x32_bf16 v[48:51], v[48:51], v[36:39], 0\n\t" \
  "ds_read2st64_b64 v[64:67], %[eh] offset0:66 offset1:67\n\t" \
  "s_waitcnt lgkmcnt(2)\n\t" \
  "v_mfma_f32_16x16x32_bf16 v[52:55], v[52:55], v[40:43], 0\n\t" \
  "ds_read2st64_b64 v[68:71], %[e4] offset0:84 offset1:85\n\t" \
  "v_mfma_f32_16x16x32_bf16 v[56:59], v[56:59], v[40:43], 0\n\t" \
  "s_mov_b32 exec_lo, 0xfff0ffff\n\t" \
  "s_mov_b32 exec_hi, 0xf000ff00\n\t" \
  "s_nop 0\n\t" \
  "v_mov_b64 v[44:45], v[48:49]\n\t" \
  "s_mov_b32 exec_lo, 0xffc0fffc\n\t" \
  "s_mov_b32 exec_hi, 0xc000fc00\n\t" \
  "v_mov_b64 v[46:47], v[50:51]\n\t" \
  "s_nop 0\n\t" \
  "v_mov_b64 v[54:55], v[58:59]\n\t" \
  "s_mov_b32 exec_lo, 0xfff0ffff\n\t" \
  "s_mov_b32 exec_hi, 0xf000ff00\n\t" \
  "v_mov_b64 v[52:53], v[56:57]\n\t" \
  "s_mov_b64 exec, -1\n\t" \
  "v_cndmask_b32 v44, v44, v45, s[50:51]\n\t" \
  "v_cndmask_b32 v46, v46, v47, s[50:51]\n\t" \
  "v_cndmask_b32 v52, v52, v53, s[50:51]\n\t" \
  "v_cndmask_b32 v54, v54, v55, s[50:51]\n\t" \
  "v_fma_f32 v72, v44, v32, %[b0]\n\t" \
  "v_fma_f32 v73, v46, v33, %[b1]\n\t" \
  "v_fma_f32 v74, v52, v32, %[b0]\n\t" \
  "v_fma_f32 v75, v54, v33, %[b1]\n\t" \
  "v_max_f32 v34, v72, v73\n\t" \
  "v_max_f32 v35, v74, v75\n\t" \
  "ds_read_b128 v[76:79], %[arec]\n\t" \
  "ds_read_b128 v[80:83], %[arec] offset:16\n\t" \
  "v_permlane32_swap_b32 v34, v35\n\t" \
  "v_max_f32 v34, v34, v35\n\t" \
  "v_mov_b32 v35, v34\n\t" \
  "ds_read_b128 v[84:87], %[arec] offset:32\n\t" \
  "s_nop 0\n\t" \
  "v_permlane16_swap_b32 v34, v35\n\t" \
  "v_max_f32 v34, v34, v35\n\t" \
  "v_mov_b32 v35, v34\n\t" \
  "s_nop 1\n\t" \
  "v_permlane32_swap_b32 v34, v35\n\t" \
  "v_sub_f32 v72, v72, v34\n\t" \
  "v_sub_f32 v73, v73, v34\n\t" \
  "v_sub_f32 v74, v74, v35\n\t" \
  "v_sub_f32 v75, v75, v35\n\t" \
  "v_exp_f32 v72, v72\n\t" \
  "v_exp_f32 v73, v73\n\t" \
  "v_exp_f32 v74, v74\n\t" \
  "v_exp_f32 v75, v75\n\t" \
  "v_add_f32 v34, v72, v73\n\t" \
  "v_add_f32 v35, v74, v75\n\t" \
  "v_pk_mul_f32 v[72:73], v[72:73], v[30:31]\n\t" \
  "v_pk_mul_f32 v[74:75], v[74:75], v[30:31]\n\t" \
  "v_permlane32_swap_b32 v34, v35\n\t" \
  "v_add_f32 v34, v34, v35\n\t" \
  "v_mov_b32 v35, v34\n\t" \
  "s_nop 1\n\t" \
  "v_permlane16_swap_b32 v34, v35\n\t" \
  "v_add_f32 v34, v34, v35\n\t" \
  "v_rcp_f32 v34, v34\n\t" \
  "s_nop 0\n\t" \
  "v_mov_b32 v30, v34\n\t" \
  "s_waitcnt lgkmcnt(0)\n\t" \
  "s_nop 0\n\t" \
  "v_permlane32_swap_b32 v34, v30\n\t" \
  "v_pk_mul_f32 v[72:73], v[34:35], v[72:73] op_sel_hi:[0,1]\n\t" \
  "v_pk_mul_f32 v[74:75], v[30:31], v[74:75] op_sel_hi:[0,1]\n\t" \
  "v_cvt_pk_bf16_f32 v72, v72, v73\n\t" \
  "v_cvt_pk_bf16_f32 v74, v74, v75\n\t" \
  "v_perm_b32 v36, 0, v72, %[s0]\n\t" \
  "v_perm_b32 v37, 0, v72, %[s1]\n\t" \
  "v_perm_b32 v38, 0, v72, %[s2]\n\t" \
  "v_perm_b32 v39, 0, v72, %[s3]\n\t" \
  "v_perm_b32 v40, 0, v74, %[s0]\n\t" \
  "v_perm_b32 v41, 0, v74, %[s1]\n\t" \
  "v_mfma_f32_16x16x32_bf16 v[60:63], v[60:63], v[36:39], 0\n\t" \
  "v_perm_b32 v42, 0, v74, %[s2]\n\t" \
  "v_perm_b32 v43, 0, v74, %[s3]\n\t" \
  "v_mfma_f32_16x16x32_bf16 v[52:55], v[68:71], v[36:39], 0\n\t" \
  "s_nop 0\n\t" \
  "v_mfma_f32_16x16x32_bf16 v[64:67], v[64:67], v[40:43], 0\n\t" \
  "v_mfma_f32_16x16x32_bf16 v[56:59], v[68:71], v[40:43], 0\n\t" \
  "s_nop 1\n\t" \
  "v_cvt_pk_bf16_f32 v24, v60, v61\n\t" \
  "v_cvt_pk_bf16_f32 v25, v62, v63\n\t" \
  "s_nop 0\n\t" \
  "v_cvt_pk_bf16_f32 v52, v52, v53\n\t" \
  "v_cvt_pk_bf16_f32 v53, v54, v55\n\t" \
  "v_cvt_pk_bf16_f32 v26, v64, v65\n\t" \
  "v_cvt_pk_bf16_f32 v27, v66, v67\n\t" \
  "global_store_dwordx4 %[l16], v[24:27], %[os] sc1\n\t" \
  "s_mov_b64 exec, s[48:49]\n\t" \
  "v_cvt_pk_bf16_f32 v52, v56, v57\n\t" \
  "v_cvt_pk_bf16_f32 v53, v58, v59\n\t" \
  "s_or_b64 exec, s[46:47], s[48:49]\n\t" \
  "global_store_dwordx2 %[l16], v[52:53], %[os4] sc1\n\t" \
  "s_mov_b64 exec, -1\n\t" \
  "v_mfma_f32_16x16x32_bf16 v[60:63], v[24:27], v[76:79], 0\n\t" \
  "v_mov_b64 v[54:55], 0\n\t" \
  "v_and_b32 v64, 0x7fff7fff, v24\n\t" \
  "v_and_b32 v65, 0x7fff7fff, v25\n\t" \
  "v_and_b32 v66, 0x7fff7fff, v26\n\t" \
  "v_and_b32 v67, 0x7fff7fff, v27\n\t" \
  "v_mfma_f32_16x16x32_bf16 v[60:63], v[52:55], v[80:83], v[60:63]\n\t" \
  "ds_write2st64_b64 %[eh], v[24:25], v[26:27] offset0:1 offset1:7\n\t" \
  "v_mfma_f32_16x16x32_bf16 v[60:63], v[64:67], v[84:87], v[60:63]\n\t" \
  "s_mov_b32 exec_lo, 0x01010101\n\t" \
  "s_mov_b32 exec_hi, 0x01010101\n\t" \
  "s_nop 5\n\t" \
  "v_cvt_pk_bf16_f32 v60, v60, v61\n\t" \
  "v_cvt_pk_bf16_f32 v61, v62, v63\n\t" \
  "v_and_b32 v60, -2, v60\n\t" \
  "global_store_dwordx2 %[vpp], v[60:61], %[sst] sc1\n\t" \
  "s_mov_b64 exec, -1\n\t" \
  ""
#define OP_ASM \
  CNT0 \
  "s_mov_b32 s43, %[cap]\n\t" \
  STP(0) \
  "s_cmp_lt_u32 %[w], 4\n\t" \
  "s_cbranch_scc1 LH_%=\n\t" \
  "s_setprio 3\n\t" \
  "global_load_dwordx4 v[124:127], %[vd], %[oa1] sc1\n\t" \
  "global_load_dwordx4 v[128:131], %[vk], %[oa1] sc1\n\t" \
  "global_load_dwordx4 v[132:135], %[vd], %[oa2] sc1\n\t" \
  "global_load_dwordx4 v[136:139], %[vk], %[oa2] sc1\n\t" \
  "v_add_u32 %[tk], %[sk], %[e4]\n\t" \
  "v_add_u32 %[ta], %[so0], %[pm]\n\t" \
  "ds_read2st64_b64 v[104:107], %[tk] offset0:1 offset1:7\n\t" \
  "v_xor_b32 %[tk], 512, %[tk]\n\t" \
  "ds_read2st64_b64 v[108:111], %[tk] offset0:1 offset1:7\n\t" \
  "ds_read_b64_tr_b16 v[64:65], %[ta] offset:0\n\t" \
  "ds_read_b64_tr_b16 v[66:67], %[ta] offset:3200\n\t" \
  "ds_read_b64_tr_b16 v[68:69], %[ta] offset:8\n\t" \
  "ds_read_b64_tr_b16 v[70:71], %[ta] offset:3208\n\t" \
  "ds_read_b64_tr_b16 v[72:73], %[ta] offset:16\n\t" \
  "ds_read_b64_tr_b16 v[74:75], %[ta] offset:3216\n\t" \
  "ds_read_b64_tr_b16 v[76:77], %[ta] offset:24\n\t" \
  "ds_read_b64_tr_b16 v[78:79], %[ta] offset:3224\n\t" \
  "ds_read_b64_tr_b16 v[80:81], %[ta] offset:32\n\t" \
  "ds_read_b64_tr_b16 v[82:83], %[ta] offset:3232\n\t" \
  "ds_read_b64 v[112:113], %[ra] offset:0\n\t" \
  "ds_read_b64 v[114:115], %[ra] offset:8\n\t" \
  "ds_read_b64 v[116:117], %[ra] offset:16\n\t" \
  "ds_read_b64 v[118:119], %[ra] offset:24\n\t" \
  "ds_read_b64 v[120:121], %[ra] offset:32\n\t" \
  "global_load_dwordx4 v[140:143], %[vd], %[oa3] sc1\n\t" \
  "v_add_u32 %[ta], %[so1], %[pm]\n\t" \
  "s_waitcnt lgkmcnt(5)\n\t" \
  "v_mfma_f32_16x16x32_bf16 v[44:47], v[64:67], v[108:111], 0\n\t" \
  "ds_read_b64_tr_b16 v[84:85], %[ta] offset:0\n\t" \
  "ds_read_b64_tr_b16 v[86:87], %[ta] offset:3200\n\t" \
  "v_mfma_f32_16x16x32_bf16 v[48:51], v[68:71], v[108:111], 0\n\t" \
  "ds_read_b64_tr_b16 v[88:89], %[ta] offset:8\n\t" \
  "ds_read_b64_tr_b16 v[90:91], %[ta] offset:3208\n\t" \
  "v_mfma_f32_16x16x32_bf16 v[52:55], v[72:75], v[108:111], 0\n\t" \
  "ds_read_b64_tr_b16 v[92:93], %[ta] offset:16\n\t" \
  "ds_read_b64_tr_b16 v[94:95], %[ta] offset:3216\n\t" \
  "v_mfma_f32_16x16x32_bf16 v[56:59], v[76:79], v[108:111], 0\n\t" \
  "ds_read_b64_tr_b16 v[96:97], %[ta] offset:24\n\t" \
  "ds_read_b64_tr_b16 v[98:99], %[ta] offset:3224\n\t" \
  "v_mfma_f32_16x16x32_bf16 v[60:63], v[80:83], v[108:111], 0\n\t" \
  "ds_read_b64_tr_b16 v[100:101], %[ta] offset:32\n\t" \
  "ds_read_b64_tr_b16 v[102:103], %[ta] offset:3232\n\t" \
  "s_waitcnt lgkmcnt(10)\n\t" \
  "v_lshlrev_b32 v24, 16, v112\n\t" \
  "v_and_b32 v25, 0xffff0000, v112\n\t" \
  "v_lshlrev_b32 v26, 16, v113\n\t" \
  "v_and_b32 v27, 0xffff0000, v113\n\t" \
  "v_lshlrev_b32 v28, 16, v114\n\t" \
  "v_and_b32 v29, 0xffff0000, v114\n\t" \
  "v_lshlrev_b32 v30, 16, v115\n\t" \
  "v_and_b32 v31, 0xffff0000, v115\n\t" \
  "v_mfma_f32_16x16x32_bf16 v[24:27], v[64:67], v[104:107], v[24:27]\n\t" \
  "v_lshlrev_b32 v32, 16, v116\n\t" \
  "v_and_b32 v33, 0xffff0000, v116\n\t" \
  "v_lshlrev_b32 v34, 16, v117\n\t" \
  "v_and_b32 v35, 0xffff0000, v117\n\t" \
  "v_mfma_f32_16x16x32_bf16 v[28:31], v[68:71], v[104:107], v[28:31]\n\t" \
  "v_lshlrev_b32 v36, 16, v118\n\t" \
  "v_and_b32 v37, 0xffff0000, v118\n\t" \
  "v_lshlrev_b32 v38, 16, v119\n\t" \
  "v_and_b32 v39, 0xffff0000, v119\n\t" \
  "v_mfma_f32_16x16x32_bf16 v[32:35], v[72:75], v[104:107], v[32:35]\n\t" \
  "v_lshlrev_b32 v40, 16, v120\n\t" \
  "v_and_b32 v41, 0xffff0000, v120\n\t" \
  "v_lshlrev_b32 v42, 16, v121\n\t" \
  "v_and_b32 v43, 0xffff0000, v121\n\t" \
  "v_mfma_f32_16x16x32_bf16 v[36:39], v[76:79], v[104:107], v[36:39]\n\t" \
  "s_nop 0\n\t" \
  "v_mfma_f32_16x16x32_bf16 v[40:43], v[80:83], v[104:107], v[40:43]\n\t" \
  "v_add_u32 %[ta], %[so2], %[pm]\n\t" \
  "ds_read_b64_tr_b16 v[104:105], %[ta] offset:0\n\t" \
  "ds_read_b64_tr_b16 v[106:107], %[ta] offset:3200\n\t" \
  "ds_read_b64_tr_b16 v[108:109], %[ta] offset:8\n\t" \
  "ds_read_b64_tr_b16 v[110:111], %[ta] offset:3208\n\t" \
  "ds_read_b64_tr_b16 v[112:113], %[ta] offset:16\n\t" \
  "ds_read_b64_tr_b16 v[114:115], %[ta] offset:3216\n\t" \
  "ds_read_b64_tr_b16 v[116:117], %[ta] offset:24\n\t" \
  "ds_read_b64_tr_b16 v[118:119], %[ta] offset:3224\n\t" \
  "ds_read_b64_tr_b16 v[120:121], %[ta] offset:32\n\t" \
  "ds_read_b64_tr_b16 v[122:123], %[ta] offset:3232\n\t" \
  STP(1) \
  "global_load_dwordx4 v[144:147], %[vk], %[oa3] sc1\n\t" \
  "global_load_dwordx4 v[148:151], %[vd], %[oa4] sc1\n\t" \
  "s_waitcnt vmcnt(5)\n\t" \
  "v_max_u32 %[tk], v124, v128\n\t" \
  "v_cmp_eq_u32 vcc, -1, %[tk]\n\t" \
  "s_cbranch_vccnz LSs1_%=\n\t" \
  "LQs1_%=:\n\t" \
  STP(2) \
  "s_waitcnt lgkmcnt(10)\n\t" \
  "v_mfma_f32_16x16x32_bf16 v[44:47], v[84:87], v[124:127], v[44:47]\n\t" \
  "v_add_u32 %[ta], %[so3], %[pm]\n\t" \
  "ds_read_b64_tr_b16 v[64:65], %[ta] offset:0\n\t" \
  "v_mfma_f32_16x16x32_bf16 v[24:27], v[84:87], v[128:131], v[24:27]\n\t" \
  "ds_read_b64_tr_b16 v[66:67], %[ta] offset:3200\n\t" \
  "global_load_dwordx4 v[152:155], %[vk], %[oa4] sc1\n\t" \
  "v_mfma_f32_16x16x32_bf16 v[48:51], v[88:91], v[124:127], v[48:51]\n\t" \
  "ds_read_b64_tr_b16 v[68:69], %[ta] offset:8\n\t" \
  "v_mfma_f32_16x16x32_bf16 v[28:31], v[88:91], v[128:131], v[28:31]\n\t" \
  "ds_read_b64_tr_b16 v[70:71], %[ta] offset:3208\n\t" \
  "global_load_dwordx4 v[156:159], %[vd], %[oa5] sc1\n\t" \
  "v_mfma_f32_16x16x32_bf16 v[52:55], v[92:95], v[124:127], v[52:55]\n\t" \
  "ds_read_b64_tr_b16 v[72:73], %[ta] offset:16\n\t" \
  "v_mfma_f32_16x16x32_bf16 v[32:35], v[92:95], v[128:131], v[32:35]\n\t" \
  "ds_read_b64_tr_b16 v[74:75], %[ta] offset:3216\n\t" \
  "v_mfma_f32_16x16x32_bf16 v[56:59], v[96:99], v[124:127], v[56:59]\n\t" \
  "ds_read_b64_tr_b16 v[76:77], %[ta] offset:24\n\t" \
  "v_mfma_f32_16x16x32_bf16 v[36:39], v[96:99], v[128:131], v[36:39]\n\t" \
  "ds_read_b64_tr_b16 v[78:79], %[ta] offset:3224\n\t" \
  "s_waitcnt vmcnt(5)\n\t" \
  "v_max_u32 %[tk], v132, v136\n\t" \
  "v_cmp_eq_u32 vcc, -1, %[tk]\n\t" \
  "v_mfma_f32_16x16x32_bf16 v[60:63], v[100:103], v[124:127], v[60:63]\n\t" \
  "ds_read_b64_tr_b16 v[80:81], %[ta] offset:32\n\t" \
  "v_mfma_f32_16x16x32_bf16 v[40:43], v[100:103], v[128:131], v[40:43]\n\t" \
  "ds_read_b64_tr_b16 v[82:83], %[ta] offset:3232\n\t" \
  "s_cbranch_vccnz LSs2_%=\n\t" \
  "LQs2_%=:\n\t" \
  "s_waitcnt lgkmcnt(10)\n\t" \
  "v_mfma_f32_16x16x32_bf16 v[44:47], v[104:107], v[132:135], v[44:47]\n\t" \
  "v_add_u32 %[ta], %[so4], %[pf]\n\t" \
  "ds_read_b64_tr_b16 v[84:85], %[ta] offset:0\n\t" \
  "v_mfma_f32_16x16x32_bf16 v[24:27], v[104:107], v[136:139], v[24:27]\n\t" \
  "ds_read_b64_tr_b16 v[86:87], %[ta] offset:12800\n\t" \
  "global_load_dwordx4 v[124:127], %[vk], %[oa5] sc1\n\t" \
  "v_mfma_f32_16x16x32_bf16 v[48:51], v[108:111], v[132:135], v[48:51]\n\t" \
  "ds_read_b64_tr_b16 v[88:89], %[ta] offset:8\n\t" \
  "v_mfma_f32_16x16x32_bf16 v[28:31], v[108:111], v[136:139], v[28:31]\n\t" \
  "ds_read_b64_tr_b16 v[90:91], %[ta] offset:12808\n\t" \
  "global_load_dwordx4 v[128:131], %[vd], %[oa6] sc1\n\t" \
  "v_mfma_f32_16x16x32_bf16 v[52:55], v[112:115], v[132:135], v[52:55]\n\t" \
  "ds_read_b64_tr_b16 v[92:93], %[ta] offset:16\n\t" \
  "v_mfma_f32_16x16x32_bf16 v[32:35], v[112:115], v[136:139], v[32:35]\n\t" \
  "ds_read_b64_tr_b16 v[94:95], %[ta] offset:12816\n\t" \
  "v_mfma_f32_16x16x32_bf16 v[56:59], v[116:119], v[132:135], v[56:59]\n\t" \
  "ds_read_b64_tr_b16 v[96:97], %[ta] offset:24\n\t" \
  "v_mfma_f32_16x16x32_bf16 v[36:39], v[116:119], v[136:139], v[36:39]\n\t" \
  "ds_read_b64_tr_b16 v[98:99], %[ta] offset:12824\n\t" \
  "s_waitcnt vmcnt(5)\n\t" \
  "v_max_u32 %[tk], v140, v144\n\t" \
  "v_cmp_eq_u32 vcc, -1, %[tk]\n\t" \
  "v_mfma_f32_16x16x32_bf16 v[60:63], v[120:123], v[132:135], v[60:63]\n\t" \
  "ds_read_b64_tr_b16 v[100:101], %[ta] offset:32\n\t" \
  "v_mfma_f32_16x16x32_bf16 v[40:43], v[120:123], v[136:139], v[40:43]\n\t" \
  "ds_read_b64_tr_b16 v[102:103], %[ta] offset:12832\n\t" \
  "s_cbranch_vccnz LSs3_%=\n\t" \
  "LQs3_%=:\n\t" \
  "s_waitcnt lgkmcnt(10)\n\t" \
  "v_mfma_f32_16x16x32_bf16 v[44:47], v[64:67], v[140:143], v[44:47]\n\t" \
  "v_add_u32 %[ta], %[so5], %[pm]\n\t" \
  "ds_read_b64_tr_b16 v[104:105], %[ta] offset:0\n\t" \
  "v_mfma_f32_16x16x32_bf16 v[24:27], v[64:67], v[144:147], v[24:27]\n\t" \
  "ds_read_b64_tr_b16 v[106:107], %[ta] offset:3200\n\t" \
  "global_load_dwordx4 v[132:135], %[vk], %[oa6] sc1\n\t" \
  "v_mfma_f32_16x16x32_bf16 v[48:51], v[68:71], v[140:143], v[48:51]\n\t" \
  "ds_read_b64_tr_b16 v[108:109], %[ta] offset:8\n\t" \
  "v_mfma_f32_16x16x32_bf16 v[28:31], v[68:71], v[144:147], v[28:31]\n\t" \
  "ds_read_b64_tr_b16 v[110:111], %[ta] offset:3208\n\t" \
  "global_load_dwordx4 v[136:139], %[vd], %[oa7] sc1\n\t" \
  "v_mfma_f32_16x16x32_bf16 v[52:55], v[72:75], v[140:143], v[52:55]\n\t" \
  "ds_read_b64_tr_b16 v[112:113], %[ta] offset:16\n\t" \
  "v_mfma_f32_16x16x32_bf16 v[32:35], v[72:75], v[144:147], v[32:35]\n\t" \
  "ds_read_b64_tr_b16 v[114:115], %[ta] offset:3216\n\t" \
  "v_mfma_f32_16x16x32_bf16 v[56:59], v[76:79], v[140:143], v[56:59]\n\t" \
  "ds_read_b64_tr_b16 v[116:117], %[ta] offset:24\n\t" \
  "v_mfma_f32_16x16x32_bf16 v[36:39], v[76:79], v[144:147], v[36:39]\n\t" \
  "ds_read_b64_tr_b16 v[118:119], %[ta] offset:3224\n\t" \
  "s_waitcnt vmcnt(5)\n\t" \
  "v_max3_u32 %[tk], v148, v150, v152\n\t" \
  "v_max_u32 %[tk], %[tk], v154\n\t" \
  "v_cmp_eq_u32 vcc, -1, %[tk]\n\t" \
  "v_mfma_f32_16x16x32_bf16 v[60:63], v[80:83], v[140:143], v[60:63]\n\t" \
  "ds_read_b64_tr_b16 v[120:121], %[ta] offset:32\n\t" \
  "v_mfma_f32_16x16x32_bf16 v[40:43], v[80:83], v[144:147], v[40:43]\n\t" \
  "ds_read_b64_tr_b16 v[122:123], %[ta] offset:3232\n\t" \
  "s_cbranch_vccnz LSs4_%=\n\t" \
  "LQs4_%=:\n\t" \
  "s_waitcnt lgkmcnt(10)\n\t" \
  "v_mfma_f32_16x16x32_bf16 v[44:47], v[84:87], v[148:151], v[44:47]\n\t" \
  "v_add_u32 %[ta], %[so6], %[pm]\n\t" \
  "ds_read_b64_tr_b16 v[64:65], %[ta] offset:0\n\t" \
  "v_mfma_f32_16x16x32_bf16 v[24:27], v[84:87], v[152:155], v[24:27]\n\t" \
  "ds_read_b64_tr_b16 v[66:67], %[ta] offset:3200\n\t" \
  "global_load_dwordx4 v[140:143], %[vd], %[oa8] sc1\n\t" \
  "v_mfma_f32_16x16x32_bf16 v[48:51], v[88:91], v[148:151], v[48:51]\n\t" \
  "ds_read_b64_tr_b16 v[68:69], %[ta] offset:8\n\t" \
  "v_mfma_f32_16x16x32_bf16 v[28:31], v[88:91], v[152:155], v[28:31]\n\t" \
  "ds_read_b64_tr_b16 v[70:71], %[ta] offset:3208\n\t" \
  "global_load_dwordx4 v[144:147], %[vd], %[oa9] sc1\n\t" \
  "v_mfma_f32_16x16x32_bf16 v[52:55], v[92:95], v[148:151], v[52:55]\n\t" \
  "ds_read_b64_tr_b16 v[72:73], %[ta] offset:16\n\t" \
  "v_mfma_f32_16x16x32_bf16 v[32:35], v[92:95], v[152:155], v[32:35]\n\t" \
  "ds_read_b64_tr_b16 v[74:75], %[ta] offset:3216\n\t" \
  "v_mfma_f32_16x16x32_bf16 v[56:59], v[96:99], v[148:151], v[56:59]\n\t" \
  "ds_read_b64_tr_b16 v[76:77], %[ta] offset:24\n\t" \
  "v_mfma_f32_16x16x32_bf16 v[36:39], v[96:99], v[152:155], v[36:39]\n\t" \
  "ds_read_b64_tr_b16 v[78:79], %[ta] offset:3224\n\t" \
  "s_waitcnt vmcnt(5)\n\t" \
  "v_max_u32 %[tk], v156, v124\n\t" \
  "v_cmp_eq_u32 vcc, -1, %[tk]\n\t" \
  "v_mfma_f32_16x16x32_bf16 v[60:63], v[100:103], v[148:151], v[60:63]\n\t" \
  "ds_read_b64_tr_b16 v[80:81], %[ta] offset:32\n\t" \
  "v_mfma_f32_16x16x32_bf16 v[40:43], v[100:103], v[152:155], v[40:43]\n\t" \
  "ds_read_b64_tr_b16 v[82:83], %[ta] offset:3232\n\t" \
  "s_cbranch_vccnz LSs5_%=\n\t" \
  "LQs5_%=:\n\t" \
  STP(3) \
  "s_waitcnt lgkmcnt(10)\n\t" \
  "v_mfma_f32_16x16x32_bf16 v[44:47], v[104:107], v[156:159], v[44:47]\n\t" \
  "v_add_u32 %[ta], %[so7], %[pm]\n\t" \
  "ds_read_b64_tr_b16 v[84:85], %[ta] offset:0\n\t" \
  "v_mfma_f32_16x16x32_bf16 v[24:27], v[104:107], v[124:127], v[24:27]\n\t" \
  "ds_read_b64_tr_b16 v[86:87], %[ta] offset:3200\n\t" \
  "global_load_dwordx4 v[148:151], %[vk], %[oa7] sc1\n\t" \
  "v_mfma_f32_16x16x32_bf16 v[48:51], v[108:111], v[156:159], v[48:51]\n\t" \
  "ds_read_b64_tr_b16 v[88:89], %[ta] offset:8\n\t" \
  "v_mfma_f32_16x16x32_bf16 v[28:31], v[108:111], v[124:127], v[28:31]\n\t" \
  "ds_read_b64_tr_b16 v[90:91], %[ta] offset:3208\n\t" \
  "global_load_dwordx4 v[152:155], %[vk], %[oa8] sc1\n\t" \
  "v_mfma_f32_16x16x32_bf16 v[52:55], v[112:115], v[156:159], v[52:55]\n\t" \
  "ds_read_b64_tr_b16 v[92:93], %[ta] offset:16\n\t" \
  "v_mfma_f32_16x16x32_bf16 v[32:35], v[112:115], v[124:127], v[32:35]\n\t" \
  "ds_read_b64_tr_b16 v[94:95], %[ta] offset:3216\n\t" \
  "v_mfma_f32_16x16x32_bf16 v[56:59], v[116:119], v[156:159], v[56:59]\n\t" \
  "ds_read_b64_tr_b16 v[96:97], %[ta] offset:24\n\t" \
  "v_mfma_f32_16x16x32_bf16 v[36:39], v[116:119], v[124:127], v[36:39]\n\t" \
  "ds_read_b64_tr_b16 v[98:99], %[ta] offset:3224\n\t" \
  "s_waitcnt vmcnt(5)\n\t" \
  "v_max_u32 %[tk], v128, v132\n\t" \
  "v_cmp_eq_u32 vcc, -1, %[tk]\n\t" \
  "v_mfma_f32_16x16x32_bf16 v[60:63], v[120:123], v[156:159], v[60:63]\n\t" \
  "ds_read_b64_tr_b16 v[100:101], %[ta] offset:32\n\t" \
  "v_mfma_f32_16x16x32_bf16 v[40:43], v[120:123], v[124:127], v[40:43]\n\t" \
  "ds_read_b64_tr_b16 v[102:103], %[ta] offset:3232\n\t" \
  "s_cbranch_vccnz LSs6_%=\n\t" \
  "LQs6_%=:\n\t" \
  "s_waitcnt lgkmcnt(10)\n\t" \
  "v_mfma_f32_16x16x32_bf16 v[44:47], v[64:67], v[128:131], v[44:47]\n\t" \
  "v_add_u32 %[ta], %[so8], %[pm]\n\t" \
  "ds_read_b64_tr_b16 v[104:105], %[ta] offset:0\n\t" \
  "v_mfma_f32_16x16x32_bf16 v[24:27], v[64:67], v[132:135], v[24:27]\n\t" \
  "ds_read_b64_tr_b16 v[106:107], %[ta] offset:3200\n\t" \
  "global_load_dwordx4 v[156:159], %[vk], %[oa9] sc1\n\t" \
  "v_mfma_f32_16x16x32_bf16 v[48:51], v[68:71], v[128:131], v[48:51]\n\t" \
  "ds_read_b64_tr_b16 v[108:109], %[ta] offset:8\n\t" \
  "v_mfma_f32_16x16x32_bf16 v[28:31], v[68:71], v[132:135], v[28:31]\n\t" \
  "ds_read_b64_tr_b16 v[110:111], %[ta] offset:3208\n\t" \
  "v_mfma_f32_16x16x32_bf16 v[52:55], v[72:75], v[128:131], v[52:55]\n\t" \
  "ds_read_b64_tr_b16 v[112:113], %[ta] offset:16\n\t" \
  "v_mfma_f32_16x16x32_bf16 v[32:35], v[72:75], v[132:135], v[32:35]\n\t" \
  "ds_read_b64_tr_b16 v[114:115], %[ta] offset:3216\n\t" \
  "v_mfma_f32_16x16x32_bf16 v[56:59], v[76:79], v[128:131], v[56:59]\n\t" \
  "ds_read_b64_tr_b16 v[116:117], %[ta] offset:24\n\t" \
  "v_mfma_f32_16x16x32_bf16 v[36:39], v[76:79], v[132:135], v[36:39]\n\t" \
  "ds_read_b64_tr_b16 v[118:119], %[ta] offset:3224\n\t" \
  "s_waitcnt vmcnt(4)\n\t" \
  "v_max_u32 %[tk], v136, v140\n\t" \
  "v_cmp_eq_u32 vcc, -1, %[tk]\n\t" \
  "v_mfma_f32_16x16x32_bf16 v[60:63], v[80:83], v[128:131], v[60:63]\n\t" \
  "ds_read_b64_tr_b16 v[120:121], %[ta] offset:32\n\t" \
  "v_mfma_f32_16x16x32_bf16 v[40:43], v[80:83], v[132:135], v[40:43]\n\t" \
  "ds_read_b64_tr_b16 v[122:123], %[ta] offset:3232\n\t" \
  "s_cbranch_vccnz LSd7_%=\n\t" \
  "LQd7_%=:\n\t" \
  "s_waitcnt lgkmcnt(10)\n\t" \
  "v_mfma_f32_16x16x32_bf16 v[44:47], v[84:87], v[136:139], v[44:47]\n\t" \
  "v_add_u32 %[ta], %[so9], %[pf]\n\t" \
  "ds_read_b64_tr_b16 v[64:65], %[ta] offset:0\n\t" \
  "ds_read_b64_tr_b16 v[66:67], %[ta] offset:12800\n\t" \
  "v_mfma_f32_16x16x32_bf16 v[48:51], v[88:91], v[136:139], v[48:51]\n\t" \
  "ds_read_b64_tr_b16 v[68:69], %[ta] offset:8\n\t" \
  "ds_read_b64_tr_b16 v[70:71], %[ta] offset:12808\n\t" \
  "v_mfma_f32_16x16x32_bf16 v[52:55], v[92:95], v[136:139], v[52:55]\n\t" \
  "ds_read_b64_tr_b16 v[72:73], %[ta] offset:16\n\t" \
  "ds_read_b64_tr_b16 v[74:75], %[ta] offset:12816\n\t" \
  "v_mfma_f32_16x16x32_bf16 v[56:59], v[96:99], v[136:139], v[56:59]\n\t" \
  "ds_read_b64_tr_b16 v[76:77], %[ta] offset:24\n\t" \
  "ds_read_b64_tr_b16 v[78:79], %[ta] offset:12824\n\t" \
  "v_mfma_f32_16x16x32_bf16 v[60:63], v[100:103], v[136:139], v[60:63]\n\t" \
  "ds_read_b64_tr_b16 v[80:81], %[ta] offset:32\n\t" \
  "ds_read_b64_tr_b16 v[82:83], %[ta] offset:12832\n\t" \
  "s_waitcnt lgkmcnt(10)\n\t" \
  "v_mfma_f32_16x16x32_bf16 v[44:47], v[104:107], v[140:143], v[44:47]\n\t" \
  "v_mfma_f32_16x16x32_bf16 v[48:51], v[108:111], v[140:143], v[48:51]\n\t" \
  "v_mfma_f32_16x16x32_bf16 v[52:55], v[112:115], v[140:143], v[52:55]\n\t" \
  "s_waitcnt vmcnt(2)\n\t" \
  "v_max3_u32 %[tk], v144, v146, v148\n\t" \
  "v_cmp_eq_u32 vcc, -1, %[tk]\n\t" \
  "v_mfma_f32_16x16x32_bf16 v[56:59], v[116:119], v[140:143], v[56:59]\n\t" \
  "v_mfma_f32_16x16x32_bf16 v[60:63], v[120:123], v[140:143], v[60:63]\n\t" \
  "s_cbranch_vccnz LSd9_%=\n\t" \
  "LQd9_%=:\n\t" \
  "s_waitcnt lgkmcnt(0)\n\t" \
  "v_mfma_f32_16x16x32_bf16 v[44:47], v[64:67], v[144:147], v[44:47]\n\t" \
  "ds_read2_b64 v[124:127], %[aw] offset0:0 offset1:1\n\t" \
  "ds_read2_b64 v[128:131], %[aw] offset0:2 offset1:3\n\t" \
  "ds_read_b64 v[132:133], %[aw] offset:32\n\t" \
  "v_mov_b64 v[134:135], 0\n\t" \
  "v_mfma_f32_16x16x32_bf16 v[48:51], v[68:71], v[144:147], v[48:51]\n\t" \
  "v_mfma_f32_16x16x32_bf16 v[52:55], v[72:75], v[144:147], v[52:55]\n\t" \
  "v_add_u32 %[ta], %[sd], %[l16]\n\t" \
  "v_mfma_f32_16x16x32_bf16 v[56:59], v[76:79], v[144:147], v[56:59]\n\t" \
  "v_mfma_f32_16x16x32_bf16 v[60:63], v[80:83], v[144:147], v[60:63]\n\t" \
  "v_mov_b32 v137, %[sc]\n\t" \
  "v_mov_b32 v136, 1\n\t" \
  "v_mfma_f32_16x16x32_bf16 v[24:27], v[84:87], v[148:151], v[24:27]\n\t" \
  "ds_write_b128 %[ta], v[44:47] offset:0\n\t" \
  "v_mfma_f32_16x16x32_bf16 v[28:31], v[88:91], v[148:151], v[28:31]\n\t" \
  "ds_write_b128 %[ta], v[48:51] offset:1024\n\t" \
  "v_mfma_f32_16x16x32_bf16 v[32:35], v[92:95], v[148:151], v[32:35]\n\t" \
  "ds_write_b128 %[ta], v[52:55] offset:2048\n\t" \
  "s_waitcnt vmcnt(0)\n\t" \
  "v_max3_u32 %[tk], v152, v156, v158\n\t" \
  "v_cmp_eq_u32 vcc, -1, %[tk]\n\t" \
  "v_mfma_f32_16x16x32_bf16 v[36:39], v[96:99], v[148:151], v[36:39]\n\t" \
  "ds_write_b128 %[ta], v[56:59] offset:3072\n\t" \
  "v_mfma_f32_16x16x32_bf16 v[40:43], v[100:103], v[148:151], v[40:43]\n\t" \
  "ds_write_b128 %[ta], v[60:63] offset:4096\n\t" \
  "s_mov_b64 exec, 1\n\t" \
  "ds_add_u32 v137, v136\n\t" \
  "s_mov_b64 exec, -1\n\t" \
  "s_cbranch_vccnz LSk8_%=\n\t" \
  "LQk8_%=:\n\t" \
  "v_mfma_f32_16x16x32_bf16 v[24:27], v[104:107], v[152:155], v[24:27]\n\t" \
  "v_mov_b64 v[94:95], 0\n\t" \
  "v_mfma_f32_16x16x32_bf16 v[28:31], v[108:111], v[152:155], v[28:31]\n\t" \
  "v_xor_b32 v96, 4, v137\n\t" \
  "v_mfma_f32_16x16x32_bf16 v[32:35], v[112:115], v[152:155], v[32:35]\n\t" \
  "v_add_u32 %[tk], %[sr], %[l16]\n\t" \
  "v_mfma_f32_16x16x32_bf16 v[36:39], v[116:119], v[152:155], v[36:39]\n\t" \
  "v_mfma_f32_16x16x32_bf16 v[40:43], v[120:123], v[152:155], v[40:43]\n\t" \
  "v_mfma_f32_16x16x32_bf16 v[24:27], v[64:67], v[156:159], v[24:27]\n\t" \
  "ds_read_b32 v97, v96\n\t" \
  "ds_read_b128 v[44:47], %[tk] offset:0\n\t" \
  "ds_read_b128 v[48:51], %[tk] offset:1024\n\t" \
  "ds_read_b128 v[52:55], %[tk] offset:2048\n\t" \
  "ds_read_b128 v[56:59], %[tk] offset:3072\n\t" \
  "ds_read_b128 v[60:63], %[tk] offset:4096\n\t" \
  "v_mfma_f32_16x16x32_bf16 v[28:31], v[68:71], v[156:159], v[28:31]\n\t" \
  "v_mfma_f32_16x16x32_bf16 v[32:35], v[72:75], v[156:159], v[32:35]\n\t" \
  "v_mfma_f32_16x16x32_bf16 v[36:39], v[76:79], v[156:159], v[36:39]\n\t" \
  "v_mfma_f32_16x16x32_bf16 v[40:43], v[80:83], v[156:159], v[40:43]\n\t" \
  STP(4) \
  "LD1_%=:\n\t" \
  "s_waitcnt lgkmcnt(0)\n\t" \
  "v_cmp_eq_u32 vcc, 0, v97\n\t" \
  "s_cbranch_vccnz LD2_%=\n\t" \
  "LT_%=:\n\t" \
  "v_mov_b32 v137, %[sa]\n\t" \
  "ds_read_b32 v138, v137 offset:16\n\t" \
  "v_add_f32 v24, v24, v44\n\t" \
  "v_add_f32 v25, v25, v45\n\t" \
  "v_add_f32 v26, v26, v46\n\t" \
  "v_add_f32 v27, v27, v47\n\t" \
  "v_cvt_pk_bf16_f32 v84, v24, v25\n\t" \
  "v_cvt_pk_bf16_f32 v85, v26, v27\n\t" \
  "v_add_f32 v28, v28, v48\n\t" \
  "v_add_f32 v29, v29, v49\n\t" \
  "v_add_f32 v30, v30, v50\n\t" \
  "v_add_f32 v31, v31, v51\n\t" \
  "v_cvt_pk_bf16_f32 v86, v28, v29\n\t" \
  "v_cvt_pk_bf16_f32 v87, v30, v31\n\t" \
  "v_add_f32 v32, v32, v52\n\t" \
  "v_add_f32 v33, v33, v53\n\t" \
  "v_add_f32 v34, v34, v54\n\t" \
  "v_add_f32 v35, v35, v55\n\t" \
  "v_cvt_pk_bf16_f32 v88, v32, v33\n\t" \
  "v_cvt_pk_bf16_f32 v89, v34, v35\n\t" \
  "v_mfma_f32_16x16x32_bf16 v[100:103], v[84:87], v[84:87], 0\n\t" \
  "v_mfma_f32_16x16x32_bf16 v[96:99], v[124:127], v[84:87], 0\n\t" \
  "v_add_f32 v36, v36, v56\n\t" \
  "v_add_f32 v37, v37, v57\n\t" \
  "v_add_f32 v38, v38, v58\n\t" \
  "v_add_f32 v39, v39, v59\n\t" \
  "v_cvt_pk_bf16_f32 v90, v36, v37\n\t" \
  "v_cvt_pk_bf16_f32 v91, v38, v39\n\t" \
  "v_add_f32 v40, v40, v60\n\t" \
  "v_add_f32 v41, v41, v61\n\t" \
  "v_add_f32 v42, v42, v62\n\t" \
  "v_add_f32 v43, v43, v63\n\t" \
  "v_cvt_pk_bf16_f32 v92, v40, v41\n\t" \
  "v_cvt_pk_bf16_f32 v93, v42, v43\n\t" \
  "v_mfma_f32_16x16x32_bf16 v[100:103], v[88:91], v[88:91], v[100:103]\n\t" \
  "v_mfma_f32_16x16x32_bf16 v[96:99], v[128:131], v[88:91], v[96:99]\n\t" \
  "v_mfma_f32_16x16x32_bf16 v[100:103], v[92:95], v[92:95], v[100:103]\n\t" \
  "v_mfma_f32_16x16x32_bf16 v[96:99], v[132:135], v[92:95], v[96:99]\n\t" \
  "s_waitcnt lgkmcnt(0)\n\t" \
  "v_sub_u32 v138, %[ra], v138\n\t" \
  "v_cmp_gt_u32 vcc, 0xa0, v138\n\t" \
  "v_and_b32 %[ta], 0xff, %[l16]\n\t" \
  "v_add_u32 %[tk], %[sp], %[ta]\n\t" \
  "v_mov_b32 v136, 1\n\t" \
  "s_cbranch_vccnz LP_%=\n\t" \
  "LP1_%=:\n\t" \
  "s_mov_b32 exec_lo, 0x00200002\n\t" \
  "s_mov_b32 exec_hi, 0x20000200\n\t" \
  "v_mov_b32 v100, v101\n\t" \
  "s_lshl_b64 exec, exec, 1\n\t" \
  "v_mov_b32 v100, v102\n\t" \
  "s_lshl_b64 exec, exec, 1\n\t" \
  "v_mov_b32 v100, v103\n\t" \
  "s_mov_b32 exec_lo, 0x00f0000f\n\t" \
  "s_mov_b32 exec_hi, 0xf0000f00\n\t" \
  "v_mov_b32 v98, v100\n\t" \
  "global_store_dwordx4 %[ta], v[96:99], %[sst] sc1\n\t" \
  STP(5) \
  "ds_write_b128 %[tk], v[96:99]\n\t" \
  "s_mov_b64 exec, -1\n\t" \
  "ds_write_b64 %[ra], v[84:85] offset:0\n\t" \
  "ds_write_b64 %[ra], v[86:87] offset:8\n\t" \
  "ds_write_b64 %[ra], v[88:89] offset:16\n\t" \
  "ds_write_b64 %[ra], v[90:91] offset:24\n\t" \
  "ds_write_b64 %[ra], v[92:93] offset:32\n\t" \
  "s_mov_b64 exec, 1\n\t" \
  "ds_add_u32 v137, v136\n\t" \
  "s_mov_b64 exec, -1\n\t" \
  "s_setprio 0\n\t" \
  "s_branch LZ_%=\n\t" \
  "LP_%=:\n\t" \
  "s_mov_b64 exec, vcc\n\t" \
  "global_store_dwordx4 v138, v[84:87], %[cp] sc1\n\t" \
  "global_store_dwordx4 v138, v[88:91], %[cp] offset:16 sc1\n\t" \
  "global_store_dwordx2 v138, v[92:93], %[cp] offset:32 sc1\n\t" \
  "ds_write_b32 v137, v136 offset:24\n\t" \
  "s_branch LP1_%=\n\t" \
  "LD2_%=:\n\t" \
  "s_sub_u32 s43, s43, 1\n\t" \
  "s_cbranch_scc1 LT_%=\n\t" \
  "ds_read_b32 v97, v96\n\t" \
  "ds_read_b128 v[44:47], %[tk] offset:0\n\t" \
  "ds_read_b128 v[48:51], %[tk] offset:1024\n\t" \
  "ds_read_b128 v[52:55], %[tk] offset:2048\n\t" \
  "ds_read_b128 v[56:59], %[tk] offset:3072\n\t" \
  "ds_read_b128 v[60:63], %[tk] offset:4096\n\t" \
  "s_branch LD1_%=\n\t" \
  "LH_%=:\n\t" \
  "s_sleep 3\n\t" \
  "global_load_dwordx4 v[140:143], %[vk], %[oa1] sc1\n\t" \
  "global_load_dwordx4 v[144:147], %[vk], %[oa2] sc1\n\t" \
  "global_load_dwordx4 v[148:151], %[vk], %[oa3] sc1\n\t" \
  "global_load_dwordx4 v[152:155], %[vk], %[oa4] sc1\n\t" \
  "global_load_dwordx4 v[156:159], %[vk], %[oa0] sc1\n\t" \
  "s_cmp_eq_u32 %[w], 3\n\t" \
  "s_cbranch_scc0 LG_%=\n\t" \
  "ds_read_b64 v[112:113], %[ra] offset:0\n\t" \
  "ds_read_b64 v[114:115], %[ra] offset:8\n\t" \
  "ds_read_b64 v[116:117], %[ra] offset:16\n\t" \
  "ds_read_b64 v[118:119], %[ra] offset:24\n\t" \
  "ds_read_b64 v[120:121], %[ra] offset:32\n\t" \
  "ds_read2_b64 v[124:127], %[aw] offset0:0 offset1:1\n\t" \
  "ds_read2_b64 v[128:131], %[aw] offset0:2 offset1:3\n\t" \
  "ds_read_b64 v[132:133], %[aw] offset:32\n\t" \
  "v_mov_b64 v[134:135], 0\n\t" \
  "s_waitcnt lgkmcnt(3)\n\t" \
  "v_lshlrev_b32 v24, 16, v112\n\t" \
  "v_and_b32 v25, 0xffff0000, v112\n\t" \
  "v_lshlrev_b32 v26, 16, v113\n\t" \
  "v_and_b32 v27, 0xffff0000, v113\n\t" \
  "v_lshlrev_b32 v28, 16, v114\n\t" \
  "v_and_b32 v29, 0xffff0000, v114\n\t" \
  "v_lshlrev_b32 v30, 16, v115\n\t" \
  "v_and_b32 v31, 0xffff0000, v115\n\t" \
  "v_lshlrev_b32 v32, 16, v116\n\t" \
  "v_and_b32 v33, 0xffff0000, v116\n\t" \
  "v_lshlrev_b32 v34, 16, v117\n\t" \
  "v_and_b32 v35, 0xffff0000, v117\n\t" \
  "v_lshlrev_b32 v36, 16, v118\n\t" \
  "v_and_b32 v37, 0xffff0000, v118\n\t" \
  "v_lshlrev_b32 v38, 16, v119\n\t" \
  "v_and_b32 v39, 0xffff0000, v119\n\t" \
  "v_lshlrev_b32 v40, 16, v120\n\t" \
  "v_and_b32 v41, 0xffff0000, v120\n\t" \
  "v_lshlrev_b32 v42, 16, v121\n\t" \
  "v_and_b32 v43, 0xffff0000, v121\n\t" \
  "s_branch LG2_%=\n\t" \
  "LG_%=:\n\t" \
  "v_mov_b64 v[24:25], 0\n\t" \
  "v_mov_b64 v[26:27], 0\n\t" \
  "v_mov_b64 v[28:29], 0\n\t" \
  "v_mov_b64 v[30:31], 0\n\t" \
  "v_mov_b64 v[32:33], 0\n\t" \
  "v_mov_b64 v[34:35], 0\n\t" \
  "v_mov_b64 v[36:37], 0\n\t" \
  "v_mov_b64 v[38:39], 0\n\t" \
  "v_mov_b64 v[40:41], 0\n\t" \
  "v_mov_b64 v[42:43], 0\n\t" \
  "LG2_%=:\n\t" \
  "v_add_u32 %[ta], %[so1], %[pm]\n\t" \
  "ds_read_b64_tr_b16 v[64:65], %[ta] offset:0\n\t" \
  "ds_read_b64_tr_b16 v[66:67], %[ta] offset:3200\n\t" \
  "ds_read_b64_tr_b16 v[68:69], %[ta] offset:8\n\t" \
  "ds_read_b64_tr_b16 v[70:71], %[ta] offset:3208\n\t" \
  "ds_read_b64_tr_b16 v[72:73], %[ta] offset:16\n\t" \
  "ds_read_b64_tr_b16 v[74:75], %[ta] offset:3216\n\t" \
  "ds_read_b64_tr_b16 v[76:77], %[ta] offset:24\n\t" \
  "ds_read_b64_tr_b16 v[78:79], %[ta] offset:3224\n\t" \
  "ds_read_b64_tr_b16 v[80:81], %[ta] offset:32\n\t" \
  "ds_read_b64_tr_b16 v[82:83], %[ta] offset:3232\n\t" \
  "v_add_u32 %[ta], %[so2], %[pm]\n\t" \
  "ds_read_b64_tr_b16 v[84:85], %[ta] offset:0\n\t" \
  "ds_read_b64_tr_b16 v[86:87], %[ta] offset:3200\n\t" \
  "ds_read_b64_tr_b16 v[88:89], %[ta] offset:8\n\t" \
  "ds_read_b64_tr_b16 v[90:91], %[ta] offset:3208\n\t" \
  "ds_read_b64_tr_b16 v[92:93], %[ta] offset:16\n\t" \
  "ds_read_b64_tr_b16 v[94:95], %[ta] offset:3216\n\t" \
  "ds_read_b64_tr_b16 v[96:97], %[ta] offset:24\n\t" \
  "ds_read_b64_tr_b16 v[98:99], %[ta] offset:3224\n\t" \
  "ds_read_b64_tr_b16 v[100:101], %[ta] offset:32\n\t" \
  "ds_read_b64_tr_b16 v[102:103], %[ta] offset:3232\n\t" \
  STP(1) \
  "s_waitcnt vmcnt(4)\n\t" \
  "v_cmp_eq_u32 vcc, -1, v140\n\t" \
  "s_cbranch_vccnz LSh1_%=\n\t" \
  "LQh1_%=:\n\t" \
  STP(2) \
  "s_waitcnt lgkmcnt(10)\n\t" \
  "v_mfma_f32_16x16x32_bf16 v[24:27], v[64:67], v[140:143], v[24:27]\n\t" \
  "v_add_u32 %[ta], %[so3], %[pm]\n\t" \
  "ds_read_b64_tr_b16 v[104:105], %[ta] offset:0\n\t" \
  "ds_read_b64_tr_b16 v[106:107], %[ta] offset:3200\n\t" \
  "v_mfma_f32_16x16x32_bf16 v[28:31], v[68:71], v[140:143], v[28:31]\n\t" \
  "ds_read_b64_tr_b16 v[108:109], %[ta] offset:8\n\t" \
  "ds_read_b64_tr_b16 v[110:111], %[ta] offset:3208\n\t" \
  "v_mfma_f32_16x16x32_bf16 v[32:35], v[72:75], v[140:143], v[32:35]\n\t" \
  "ds_read_b64_tr_b16 v[112:113], %[ta] offset:16\n\t" \
  "ds_read_b64_tr_b16 v[114:115], %[ta] offset:3216\n\t" \
  "v_mfma_f32_16x16x32_bf16 v[36:39], v[76:79], v[140:143], v[36:39]\n\t" \
  "ds_read_b64_tr_b16 v[116:117], %[ta] offset:24\n\t" \
  "ds_read_b64_tr_b16 v[118:119], %[ta] offset:3224\n\t" \
  "s_waitcnt vmcnt(3)\n\t" \
  "v_cmp_eq_u32 vcc, -1, v144\n\t" \
  "v_mfma_f32_16x16x32_bf16 v[40:43], v[80:83], v[140:143], v[40:43]\n\t" \
  "ds_read_b64_tr_b16 v[120:121], %[ta] offset:32\n\t" \
  "ds_read_b64_tr_b16 v[122:123], %[ta] offset:3232\n\t" \
  "s_cbranch_vccnz LSh2_%=\n\t" \
  "LQh2_%=:\n\t" \
  "s_waitcnt lgkmcnt(10)\n\t" \
  "v_mfma_f32_16x16x32_bf16 v[24:27], v[84:87], v[144:147], v[24:27]\n\t" \
  "v_add_u32 %[ta], %[so4], %[pf]\n\t" \
  "ds_read_b64_tr_b16 v[64:65], %[ta] offset:0\n\t" \
  "ds_read_b64_tr_b16 v[66:67], %[ta] offset:12800\n\t" \
  "v_mfma_f32_16x16x32_bf16 v[28:31], v[88:91], v[144:147], v[28:31]\n\t" \
  "ds_read_b64_tr_b16 v[68:69], %[ta] offset:8\n\t" \
  "ds_read_b64_tr_b16 v[70:71], %[ta] offset:12808\n\t" \
  "v_mfma_f32_16x16x32_bf16 v[32:35], v[92:95], v[144:147], v[32:35]\n\t" \
  "ds_read_b64_tr_b16 v[72:73], %[ta] offset:16\n\t" \
  "ds_read_b64_tr_b16 v[74:75], %[ta] offset:12816\n\t" \
  "v_mfma_f32_16x16x32_bf16 v[36:39], v[96:99], v[144:147], v[36:39]\n\t" \
  "ds_read_b64_tr_b16 v[76:77], %[ta] offset:24\n\t" \
  "ds_read_b64_tr_b16 v[78:79], %[ta] offset:12824\n\t" \
  "s_waitcnt vmcnt(2)\n\t" \
  "v_cmp_eq_u32 vcc, -1, v148\n\t" \
  "v_mfma_f32_16x16x32_bf16 v[40:43], v[100:103], v[144:147], v[40:43]\n\t" \
  "ds_read_b64_tr_b16 v[80:81], %[ta] offset:32\n\t" \
  "ds_read_b64_tr_b16 v[82:83], %[ta] offset:12832\n\t" \
  "s_cbranch_vccnz LSh3_%=\n\t" \
  "LQh3_%=:\n\t" \
  "s_waitcnt lgkmcnt(10)\n\t" \
  "v_mfma_f32_16x16x32_bf16 v[24:27], v[104:107], v[148:151], v[24:27]\n\t" \
  "v_add_u32 %[ta], %[so0], %[pm]\n\t" \
  "ds_read_b64_tr_b16 v[84:85], %[ta] offset:0\n\t" \
  "ds_read_b64_tr_b16 v[86:87], %[ta] offset:3200\n\t" \
  "v_mfma_f32_16x16x32_bf16 v[28:31], v[108:111], v[148:151], v[28:31]\n\t" \
  "ds_read_b64_tr_b16 v[88:89], %[ta] offset:8\n\t" \
  "ds_read_b64_tr_b16 v[90:91], %[ta] offset:3208\n\t" \
  "v_mfma_f32_16x16x32_bf16 v[32:35], v[112:115], v[148:151], v[32:35]\n\t" \
  "ds_read_b64_tr_b16 v[92:93], %[ta] offset:16\n\t" \
  "ds_read_b64_tr_b16 v[94:95], %[ta] offset:3216\n\t" \
  "v_mfma_f32_16x16x32_bf16 v[36:39], v[116:119], v[148:151], v[36:39]\n\t" \
  "ds_read_b64_tr_b16 v[96:97], %[ta] offset:24\n\t" \
  "ds_read_b64_tr_b16 v[98:99], %[ta] offset:3224\n\t" \
  "s_waitcnt vmcnt(1)\n\t" \
  "v_max_u32 %[tk], v152, v154\n\t" \
  "v_cmp_eq_u32 vcc, -1, %[tk]\n\t" \
  "v_mfma_f32_16x16x32_bf16 v[40:43], v[120:123], v[148:151], v[40:43]\n\t" \
  "ds_read_b64_tr_b16 v[100:101], %[ta] offset:32\n\t" \
  "ds_read_b64_tr_b16 v[102:103], %[ta] offset:3232\n\t" \
  "s_cbranch_vccnz LSh4_%=\n\t" \
  "LQh4_%=:\n\t" \
  "s_waitcnt lgkmcnt(10)\n\t" \
  "v_mfma_f32_16x16x32_bf16 v[24:27], v[64:67], v[152:155], v[24:27]\n\t" \
  "v_mfma_f32_16x16x32_bf16 v[28:31], v[68:71], v[152:155], v[28:31]\n\t" \
  "v_mfma_f32_16x16x32_bf16 v[32:35], v[72:75], v[152:155], v[32:35]\n\t" \
  "v_mfma_f32_16x16x32_bf16 v[36:39], v[76:79], v[152:155], v[36:39]\n\t" \
  "s_waitcnt vmcnt(0)\n\t" \
  "v_cmp_eq_u32 vcc, -1, v156\n\t" \
  "v_mfma_f32_16x16x32_bf16 v[40:43], v[80:83], v[152:155], v[40:43]\n\t" \
  "s_cbranch_vccnz LSh0_%=\n\t" \
  "LQh0_%=:\n\t" \
  STP(3) \
  "s_waitcnt lgkmcnt(0)\n\t" \
  "v_mfma_f32_16x16x32_bf16 v[24:27], v[84:87], v[156:159], v[24:27]\n\t" \
  "v_mfma_f32_16x16x32_bf16 v[28:31], v[88:91], v[156:159], v[28:31]\n\t" \
  "v_mfma_f32_16x16x32_bf16 v[32:35], v[92:95], v[156:159], v[32:35]\n\t" \
  "v_mfma_f32_16x16x32_bf16 v[36:39], v[96:99], v[156:159], v[36:39]\n\t" \
  "v_mfma_f32_16x16x32_bf16 v[40:43], v[100:103], v[156:159], v[40:43]\n\t" \
  STP(4) \
  "s_cmp_eq_u32 %[w], 3\n\t" \
  "s_cbranch_scc1 LF_%=\n\t" \
  "s_setprio 3\n\t" \
  "v_add_u32 %[ta], %[sd], %[l16]\n\t" \
  "v_mov_b32 v137, %[sc]\n\t" \
  "v_mov_b32 v136, 1\n\t" \
  "ds_write_b128 %[ta], v[24:27] offset:0\n\t" \
  "ds_write_b128 %[ta], v[28:31] offset:1024\n\t" \
  "ds_write_b128 %[ta], v[32:35] offset:2048\n\t" \
  "ds_write_b128 %[ta], v[36:39] offset:3072\n\t" \
  "ds_write_b128 %[ta], v[40:43] offset:4096\n\t" \
  "s_mov_b64 exec, 1\n\t" \
  "ds_add_u32 v137, v136\n\t" \
  "s_mov_b64 exec, -1\n\t" \
  STP(5) \
  "s_branch LZ_%=\n\t" \
  "LF_%=:\n\t" \
  "s_setprio 3\n\t" \
  "v_mov_b64 v[94:95], 0\n\t" \
  "v_mov_b32 v96, %[sc]\n\t" \
  "v_add_u32 %[ta], %[sr], %[l16]\n\t" \
  "LF1_%=:\n\t" \
  "ds_read_b32 v97, v96\n\t" \
  "s_waitcnt lgkmcnt(0)\n\t" \
  "v_cmp_gt_u32 vcc, 3, v97\n\t" \
  "s_cbranch_vccz LF2_%=\n\t" \
  "s_sleep 1\n\t" \
  "s_sub_u32 s43, s43, 1\n\t" \
  "s_cbranch_scc0 LF1_%=\n\t" \
  "LF2_%=:\n\t" \
  "ds_read_b128 v[64:67], %[ta] offset:0\n\t" \
  "ds_read_b128 v[68:71], %[ta] offset:1024\n\t" \
  "ds_read_b128 v[72:75], %[ta] offset:2048\n\t" \
  "ds_read_b128 v[76:79], %[ta] offset:3072\n\t" \
  "ds_read_b128 v[80:83], %[ta] offset:4096\n\t" \
  "ds_read_b128 v[104:107], %[ta] offset:5120\n\t" \
  "ds_read_b128 v[108:111], %[ta] offset:6144\n\t" \
  "ds_read_b128 v[112:115], %[ta] offset:7168\n\t" \
  "ds_read_b128 v[116:119], %[ta] offset:8192\n\t" \
  "ds_read_b128 v[120:123], %[ta] offset:9216\n\t" \
  "ds_read_b128 v[44:47], %[ta] offset:10240\n\t" \
  "ds_read_b128 v[48:51], %[ta] offset:11264\n\t" \
  "ds_read_b128 v[52:55], %[ta] offset:12288\n\t" \
  "ds_read_b128 v[56:59], %[ta] offset:13312\n\t" \
  "ds_read_b128 v[60:63], %[ta] offset:14336\n\t" \
  "s_waitcnt lgkmcnt(10)\n\t" \
  "v_pk_add_f32 v[24:25], v[24:25], v[64:65]\n\t" \
  "v_pk_add_f32 v[26:27], v[26:27], v[66:67]\n\t" \
  "v_pk_add_f32 v[28:29], v[28:29], v[68:69]\n\t" \
  "v_pk_add_f32 v[30:31], v[30:31], v[70:71]\n\t" \
  "v_pk_add_f32 v[32:33], v[32:33], v[72:73]\n\t" \
  "v_pk_add_f32 v[34:35], v[34:35], v[74:75]\n\t" \
  "v_pk_add_f32 v[36:37], v[36:37], v[76:77]\n\t" \
  "v_pk_add_f32 v[38:39], v[38:39], v[78:79]\n\t" \
  "v_pk_add_f32 v[40:41], v[40:41], v[80:81]\n\t" \
  "v_pk_add_f32 v[42:43], v[42:43], v[82:83]\n\t" \
  "s_waitcnt lgkmcnt(5)\n\t" \
  "v_pk_add_f32 v[24:25], v[24:25], v[104:105]\n\t" \
  "v_pk_add_f32 v[26:27], v[26:27], v[106:107]\n\t" \
  "v_pk_add_f32 v[28:29], v[28:29], v[108:109]\n\t" \
  "v_pk_add_f32 v[30:31], v[30:31], v[110:111]\n\t" \
  "v_pk_add_f32 v[32:33], v[32:33], v[112:113]\n\t" \
  "v_pk_add_f32 v[34:35], v[34:35], v[114:115]\n\t" \
  "v_pk_add_f32 v[36:37], v[36:37], v[116:117]\n\t" \
  "v_pk_add_f32 v[38:39], v[38:39], v[118:119]\n\t" \
  "v_pk_add_f32 v[40:41], v[40:41], v[120:121]\n\t" \
  "v_pk_add_f32 v[42:43], v[42:43], v[122:123]\n\t" \
  "s_waitcnt lgkmcnt(0)\n\t" \
  "s_branch LT_%=\n\t" \
  "LSs1_%=:\n\t" \
  CNT1 \
  "s_sub_u32 s43, s43, 1\n\t" \
  "s_cbranch_scc1 LQs1_%=\n\t" \
  "s_sleep %[oback]\n\t" \
  "global_load_dwordx4 v[124:127], %[vd], %[oa1] sc1\n\t" \
  "global_load_dwordx4 v[128:131], %[vk], %[oa1] sc1\n\t" \
  "s_waitcnt vmcnt(0)\n\t" \
  "v_max_u32 %[tk], v124, v128\n\t" \
  "v_cmp_eq_u32 vcc, -1, %[tk]\n\t" \
  "s_cbranch_vccnz LSs1_%=\n\t" \
  "s_branch LQs1_%=\n\t" \
  "LSs2_%=:\n\t" \
  CNT1 \
  "s_sub_u32 s43, s43, 1\n\t" \
  "s_cbranch_scc1 LQs2_%=\n\t" \
  "s_sleep %[oback]\n\t" \
  "global_load_dwordx4 v[132:135], %[vd], %[oa2] sc1\n\t" \
  "global_load_dwordx4 v[136:139], %[vk], %[oa2] sc1\n\t" \
  "s_waitcnt vmcnt(0)\n\t" \
  "v_max_u32 %[tk], v132, v136\n\t" \
  "v_cmp_eq_u32 vcc, -1, %[tk]\n\t" \
  "s_cbranch_vccnz LSs2_%=\n\t" \
  "s_branch LQs2_%=\n\t" \
  "LSs3_%=:\n\t" \
  CNT1 \
  "s_sub_u32 s43, s43, 1\n\t" \
  "s_cbranch_scc1 LQs3_%=\n\t" \
  "s_sleep %[oback]\n\t" \
  "global_load_dwordx4 v[140:143], %[vd], %[oa3] sc1\n\t" \
  "global_load_dwordx4 v[144:147], %[vk], %[oa3] sc1\n\t" \
  "s_waitcnt vmcnt(0)\n\t" \
  "v_max_u32 %[tk], v140, v144\n\t" \
  "v_cmp_eq_u32 vcc, -1, %[tk]\n\t" \
  "s_cbranch_vccnz LSs3_%=\n\t" \
  "s_branch LQs3_%=\n\t" \
  "LSs4_%=:\n\t" \
  CNT1 \
  "s_sub_u32 s43, s43, 1\n\t" \
  "s_cbranch_scc1 LQs4_%=\n\t" \
  "s_sleep %[oback]\n\t" \
  "global_load_dwordx4 v[148:151], %[vd], %[oa4] sc1\n\t" \
  "global_load_dwordx4 v[152:155], %[vk], %[oa4] sc1\n\t" \
  "s_waitcnt vmcnt(0)\n\t" \
  "v_max3_u32 %[tk], v148, v150, v152\n\t" \
  "v_max_u32 %[tk], %[tk], v154\n\t" \
  "v_cmp_eq_u32 vcc, -1, %[tk]\n\t" \
  "s_cbranch_vccnz LSs4_%=\n\t" \
  "s_branch LQs4_%=\n\t" \
  "LSs5_%=:\n\t" \
  CNT1 \
  "s_sub_u32 s43, s43, 1\n\t" \
  "s_cbranch_scc1 LQs5_%=\n\t" \
  "s_sleep %[oback]\n\t" \
  "global_load_dwordx4 v[156:159], %[vd], %[oa5] sc1\n\t" \
  "global_load_dwordx4 v[124:127], %[vk], %[oa5] sc1\n\t" \
  "s_waitcnt vmcnt(0)\n\t" \
  "v_max_u32 %[tk], v156, v124\n\t" \
  "v_cmp_eq_u32 vcc, -1, %[tk]\n\t" \
  "s_cbranch_vccnz LSs5_%=\n\t" \
  "s_branch LQs5_%=\n\t" \
  "LSs6_%=:\n\t" \
  CNT1 \
  "s_sub_u32 s43, s43, 1\n\t" \
  "s_cbranch_scc1 LQs6_%=\n\t" \
  "s_sleep %[oback]\n\t" \
  "global_load_dwordx4 v[128:131], %[vd], %[oa6] sc1\n\t" \
  "global_load_dwordx4 v[132:135], %[vk], %[oa6] sc1\n\t" \
  "s_waitcnt vmcnt(0)\n\t" \
  "v_max_u32 %[tk], v128, v132\n\t" \
  "v_cmp_eq_u32 vcc, -1, %[tk]\n\t" \
  "s_cbranch_vccnz LSs6_%=\n\t" \
  "s_branch LQs6_%=\n\t" \
  "LSd7_%=:\n\t" \
  CNT1 \
  "s_sub_u32 s43, s43, 1\n\t" \
  "s_cbranch_scc1 LQd7_%=\n\t" \
  "s_sleep %[oback]\n\t" \
  "global_load_dwordx4 v[136:139], %[vd], %[oa7] sc1\n\t" \
  "global_load_dwordx4 v[140:143], %[vd], %[oa8] sc1\n\t" \
  "s_waitcnt vmcnt(0)\n\t" \
  "v_max_u32 %[tk], v136, v140\n\t" \
  "v_cmp_eq_u32 vcc, -1, %[tk]\n\t" \
  "s_cbranch_vccnz LSd7_%=\n\t" \
  "s_branch LQd7_%=\n\t" \
  "LSd9_%=:\n\t" \
  CNT1 \
  "s_sub_u32 s43, s43, 1\n\t" \
  "s_cbranch_scc1 LQd9_%=\n\t" \
  "s_sleep %[oback]\n\t" \
  "global_load_dwordx4 v[144:147], %[vd], %[oa9] sc1\n\t" \
  "global_load_dwordx4 v[148:151], %[vk], %[oa7] sc1\n\t" \
  "s_waitcnt vmcnt(0)\n\t" \
  "v_max3_u32 %[tk], v144, v146, v148\n\t" \
  "v_cmp_eq_u32 vcc, -1, %[tk]\n\t" \
  "s_cbranch_vccnz LSd9_%=\n\t" \
  "s_branch LQd9_%=\n\t" \
  "LSk8_%=:\n\t" \
  CNT1 \
  "s_sub_u32 s43, s43, 1\n\t" \
  "s_cbranch_scc1 LQk8_%=\n\t" \
  "s_sleep %[oback]\n\t" \
  "global_load_dwordx4 v[152:155], %[vk], %[oa8] sc1\n\t" \
  "global_load_dwordx4 v[156:159], %[vk], %[oa9] sc1\n\t" \
  "s_waitcnt vmcnt(0)\n\t" \
  "v_max3_u32 %[tk], v152, v156, v158\n\t" \
  "v_cmp_eq_u32 vcc, -1, %[tk]\n\t" \
  "s_cbranch_vccnz LSk8_%=\n\t" \
  "s_branch LQk8_%=\n\t" \
  "LSh1_%=:\n\t" \
  CNT1 \
  "s_sub_u32 s43, s43, 1\n\t" \
  "s_cbranch_scc1 LQh1_%=\n\t" \
  "s_sleep %[oback]\n\t" \
  "global_load_dwordx4 v[140:143], %[vk], %[oa1] sc1\n\t" \
  "s_waitcnt vmcnt(0)\n\t" \
  "v_cmp_eq_u32 vcc, -1, v140\n\t" \
  "s_cbranch_vccnz LSh1_%=\n\t" \
  "s_branch LQh1_%=\n\t" \
  "LSh2_%=:\n\t" \
  CNT1 \
  "s_sub_u32 s43, s43, 1\n\t" \
  "s_cbranch_scc1 LQh2_%=\n\t" \
  "s_sleep %[oback]\n\t" \
  "global_load_dwordx4 v[144:147], %[vk], %[oa2] sc1\n\t" \
  "s_waitcnt vmcnt(0)\n\t" \
  "v_cmp_eq_u32 vcc, -1, v144\n\t" \
  "s_cbranch_vccnz LSh2_%=\n\t" \
  "s_branch LQh2_%=\n\t" \
  "LSh3_%=:\n\t" \
  CNT1 \
  "s_sub_u32 s43, s43, 1\n\t" \
  "s_cbranch_scc1 LQh3_%=\n\t" \
  "s_sleep %[oback]\n\t" \
  "global_load_dwordx4 v[148:151], %[vk], %[oa3] sc1\n\t" \
  "s_waitcnt vmcnt(0)\n\t" \
  "v_cmp_eq_u32 vcc, -1, v148\n\t" \
  "s_cbranch_vccnz LSh3_%=\n\t" \
  "s_branch LQh3_%=\n\t" \
  "LSh4_%=:\n\t" \
  CNT1 \
  "s_sub_u32 s43, s43, 1\n\t" \
  "s_cbranch_scc1 LQh4_%=\n\t" \
  "s_sleep %[oback]\n\t" \
  "global_load_dwordx4 v[152:155], %[vk], %[oa4] sc1\n\t" \
  "s_waitcnt vmcnt(0)\n\t" \
  "v_max_u32 %[tk], v152, v154\n\t" \
  "v_cmp_eq_u32 vcc, -1, %[tk]\n\t" \
  "s_cbranch_vccnz LSh4_%=\n\t" \
  "s_branch LQh4_%=\n\t" \
  "LSh0_%=:\n\t" \
  CNT1 \
  "s_sub_u32 s43, s43, 1\n\t" \
  "s_cbranch_scc1 LQh0_%=\n\t" \
  "s_sleep %[oback]\n\t" \
  "global_load_dwordx4 v[156:159], %[vk], %[oa0] sc1\n\t" \
  "s_waitcnt vmcnt(0)\n\t" \
  "v_cmp_eq_u32 vcc, -1, v156\n\t" \
  "s_cbranch_vccnz LSh0_%=\n\t" \
  "s_branch LQh0_%=\n\t" \
  "LZ_%=:\n\t" \
  ""
#define COL_ASM \
  "s_mov_b32 s43, %[cap]\n\t" \
  "v_xor_b32 v101, 0x400, %[lq]\n\t" \
  "v_xor_b32 v102, 0x800, %[lq]\n\t" \
  "v_xor_b32 v103, 0xc00, %[lq]\n\t" \
  "v_xor_b32 v104, 0x1000, %[lq]\n\t" \
  "v_xor_b32 v105, 0x1400, %[lq]\n\t" \
  "v_xor_b32 v106, 0x1800, %[lq]\n\t" \
  "v_xor_b32 v107, 0x1c00, %[lq]\n\t" \
  "LC0_%=:\n\t" \
  "ds_read_b32 v96, %[ac]\n\t" \
  "s_waitcnt lgkmcnt(0)\n\t" \
  "v_cmp_gt_u32 vcc, %[nf], v96\n\t" \
  "s_cbranch_vccz LC1_%=\n\t" \
  "s_sleep 1\n\t" \
  "s_sub_u32 s43, s43, 1\n\t" \
  "s_cbranch_scc0 LC0_%=\n\t" \
  "LC1_%=:\n\t" \
  "s_setprio 2\n\t" \
  "s_sleep %[pdel]\n\t" \
  "global_load_dwordx4 v[68:71], v101, %[sp] sc1\n\t" \
  "global_load_dwordx4 v[72:75], v102, %[sp] sc1\n\t" \
  "global_load_dwordx4 v[76:79], v103, %[sp] sc1\n\t" \
  "global_load_dwordx4 v[80:83], v104, %[sp] sc1\n\t" \
  "global_load_dwordx4 v[84:87], v105, %[sp] sc1\n\t" \
  "global_load_dwordx4 v[88:91], v106, %[sp] sc1\n\t" \
  "global_load_dwordx4 v[92:95], v107, %[sp] sc1\n\t" \
  "ds_read_b128 v[64:67], %[ao]\n\t" \
  "s_waitcnt lgkmcnt(0)\n\t" \
  "LC2_%=:\n\t" \
  "s_waitcnt vmcnt(4)\n\t" \
  "v_pk_add_f32 v[108:109], v[64:65], v[68:69]\n\t" \
  "v_add_f32 v110, v66, v70\n\t" \
  "v_pk_add_f32 v[112:113], v[72:73], v[76:77]\n\t" \
  "v_add_f32 v114, v74, v78\n\t" \
  "v_pk_add_f32 v[108:109], v[108:109], v[112:113]\n\t" \
  "v_add_f32 v110, v110, v114\n\t" \
  "s_waitcnt vmcnt(0)\n\t" \
  "v_pk_add_f32 v[116:117], v[80:81], v[84:85]\n\t" \
  "v_add_f32 v118, v82, v86\n\t" \
  "v_pk_add_f32 v[120:121], v[88:89], v[92:93]\n\t" \
  "v_add_f32 v122, v90, v94\n\t" \
  "v_pk_add_f32 v[116:117], v[116:117], v[120:121]\n\t" \
  "v_add_f32 v118, v118, v122\n\t" \
  "v_pk_add_f32 v[108:109], v[108:109], v[116:117]\n\t" \
  "v_add_f32 v110, v110, v118\n\t" \
  STP(0) \
  "v_mul_f32 v112, 0x3acccccd, v108\n\t" \
  "v_add_f32 v111, v108, v109\n\t" \
  "v_mul_f32 v113, v112, v112\n\t" \
  "v_cmp_u_f32 vcc, v111, v110\n\t" \
  "v_fma_f32 v113, v110, %[rdm], -v113\n\t" \
  "s_cbranch_vccnz LC5_%=\n\t" \
  "LC3_%=:\n\t" \
  "v_max_f32 v113, 0, v113\n\t" \
  "v_cvt_pk_bf16_f32 v113, v113, v113\n\t" \
  "v_lshlrev_b32 v113, 16, v113\n\t" \
  "v_add_f32 v113, 0x3727c5ac, v113\n\t" \
  "v_cvt_pk_bf16_f32 v113, v113, v113\n\t" \
  "v_lshlrev_b32 v113, 16, v113\n\t" \
  "v_sqrt_f32 v113, v113\n\t" \
  "v_cvt_pk_bf16_f32 v112, v112, v112\n\t" \
  "v_lshlrev_b32 v112, 16, v112\n\t" \
  "v_cvt_pk_bf16_f32 v113, v113, v113\n\t" \
  "v_lshlrev_b32 v113, 16, v113\n\t" \
  "v_rcp_f32 v113, v113\n\t" \
  "v_fma_f32 v114, -%[swr], v112, v109\n\t" \
  "v_mul_f32 v114, v114, v113\n\t" \
  "v_ashrrev_i32 v115, 31, v114\n\t" \
  "v_or_b32 v115, 0x80000000, v115\n\t" \
  "v_xor_b32 v114, v114, v115\n\t" \
  "v_and_or_b32 v114, v114, %[msk], %[rid]\n\t" \
  "s_nop 1\n\t" \
  "v_max_u32_dpp v114, v114, v114 quad_perm:[1,0,3,2] row_mask:0xf bank_mask:0xf bound_ctrl:1\n\t" \
  "ds_write_b64 %[at], v[112:113]\n\t" \
  "s_nop 0\n\t" \
  "v_max_u32_dpp v114, v114, v114 quad_perm:[2,3,0,1] row_mask:0xf bank_mask:0xf bound_ctrl:1\n\t" \
  "s_nop 1\n\t" \
  "v_max_u32_dpp v114, v114, v114 row_half_mirror row_mask:0xf bank_mask:0xf bound_ctrl:1\n\t" \
  "s_nop 1\n\t" \
  "v_max_u32_dpp v114, v114, v114 row_mirror row_mask:0xf bank_mask:0xf bound_ctrl:1\n\t" \
  "s_nop 0\n\t" \
  "v_readlane_b32 s40, v114, 0\n\t" \
  "v_readlane_b32 s41, v114, 16\n\t" \
  "v_readlane_b32 s42, v114, 32\n\t" \
  "s_max_u32 s40, s40, s41\n\t" \
  "s_max_u32 s40, s40, s42\n\t" \
  "v_mov_b32 v98, s40\n\t" \
  "s_mov_b32 exec_lo, 0x01010101\n\t" \
  "s_mov_b32 exec_hi, 0x0101\n\t" \
  "global_store_dwordx2 %[zr], v[98:99], %[kp] sc1\n\t" \
  "ds_write_b32 %[ak], v98\n\t" \
  "s_mov_b64 exec, -1\n\t" \
  "s_and_b32 s41, s40, 0x7ff\n\t" \
  "s_sub_u32 s41, %[rb], s41\n\t" \
  "s_mul_i32 s42, s41, 160\n\t" \
  "v_add_u32 v115, s42, %[ah]\n\t" \
  "ds_read_b64 v[116:117], v115\n\t" \
  "v_readlane_b32 s44, v112, s41\n\t" \
  "v_readlane_b32 s46, v113, s41\n\t" \
  "s_waitcnt lgkmcnt(0)\n\t" \
  "s_mov_b32 exec_hi, 0\n\t" \
  "v_lshlrev_b32 v118, 16, v116\n\t" \
  "v_and_b32 v119, 0xffff0000, v116\n\t" \
  "v_lshlrev_b32 v120, 16, v117\n\t" \
  "v_and_b32 v121, 0xffff0000, v117\n\t" \
  "v_pk_add_f32 v[118:119], v[118:119], s[44:45] op_sel_hi:[1,0] neg_lo:[0,1] neg_hi:[0,1]\n\t" \
  "v_pk_add_f32 v[120:121], v[120:121], s[44:45] op_sel_hi:[1,0] neg_lo:[0,1] neg_hi:[0,1]\n\t" \
  "v_cvt_pk_bf16_f32 v116, v118, v119\n\t" \
  "v_cvt_pk_bf16_f32 v117, v120, v121\n\t" \
  "v_lshlrev_b32 v118, 16, v116\n\t" \
  "v_and_b32 v119, 0xffff0000, v116\n\t" \
  "v_lshlrev_b32 v120, 16, v117\n\t" \
  "v_and_b32 v121, 0xffff0000, v117\n\t" \
  "v_pk_mul_f32 v[118:119], v[118:119], s[46:47] op_sel_hi:[1,0]\n\t" \
  "v_pk_mul_f32 v[120:121], v[120:121], s[46:47] op_sel_hi:[1,0]\n\t" \
  "v_cvt_pk_bf16_f32 v116, v118, v119\n\t" \
  "v_cvt_pk_bf16_f32 v117, v120, v121\n\t" \
  "s_mov_b32 exec_hi, -1\n\t" \
  "global_store_dwordx2 %[co], v[116:117], %[cp] sc1\n\t" \
  "s_branch LC9_%=\n\t" \
  "LC5_%=:\n\t" \
  "s_sub_u32 s43, s43, 1\n\t" \
  "s_cbranch_scc1 LC3_%=\n\t" \
  "s_mov_b32 s40, 0\n\t" \
  "v_max3_u32 v96, v68, v69, v70\n\t" \
  "v_cmp_eq_u32 vcc, -1, v96\n\t" \
  "s_cbranch_vccz LC61_%=\n\t" \
  "global_load_dwordx4 v[68:71], v101, %[sp] sc1\n\t" \
  "s_mov_b32 s40, 1\n\t" \
  "LC61_%=:\n\t" \
  "v_max3_u32 v96, v72, v73, v74\n\t" \
  "v_cmp_eq_u32 vcc, -1, v96\n\t" \
  "s_cbranch_vccz LC62_%=\n\t" \
  "global_load_dwordx4 v[72:75], v102, %[sp] sc1\n\t" \
  "s_mov_b32 s40, 1\n\t" \
  "LC62_%=:\n\t" \
  "v_max3_u32 v96, v76, v77, v78\n\t" \
  "v_cmp_eq_u32 vcc, -1, v96\n\t" \
  "s_cbranch_vccz LC63_%=\n\t" \
  "global_load_dwordx4 v[76:79], v103, %[sp] sc1\n\t" \
  "s_mov_b32 s40, 1\n\t" \
  "LC63_%=:\n\t" \
  "v_max3_u32 v96, v80, v81, v82\n\t" \
  "v_cmp_eq_u32 vcc, -1, v96\n\t" \
  "s_cbranch_vccz LC64_%=\n\t" \
  "global_load_dwordx4 v[80:83], v104, %[sp] sc1\n\t" \
  "s_mov_b32 s40, 1\n\t" \
  "LC64_%=:\n\t" \
  "v_max3_u32 v96, v84, v85, v86\n\t" \
  "v_cmp_eq_u32 vcc, -1, v96\n\t" \
  "s_cbranch_vccz LC65_%=\n\t" \
  "global_load_dwordx4 v[84:87], v105, %[sp] sc1\n\t" \
  "s_mov_b32 s40, 1\n\t" \
  "LC65_%=:\n\t" \
  "v_max3_u32 v96, v88, v89, v90\n\t" \
  "v_cmp_eq_u32 vcc, -1, v96\n\t" \
  "s_cbranch_vccz LC66_%=\n\t" \
  "global_load_dwordx4 v[88:91], v106, %[sp] sc1\n\t" \
  "s_mov_b32 s40, 1\n\t" \
  "LC66_%=:\n\t" \
  "v_max3_u32 v96, v92, v93, v94\n\t" \
  "v_cmp_eq_u32 vcc, -1, v96\n\t" \
  "s_cbranch_vccz LC67_%=\n\t" \
  "global_load_dwordx4 v[92:95], v107, %[sp] sc1\n\t" \
  "s_mov_b32 s40, 1\n\t" \
  "LC67_%=:\n\t" \
  "s_cmp_eq_u32 s40, 0\n\t" \
  "s_cbranch_scc1 LC3_%=\n\t" \
  "s_waitcnt vmcnt(0)\n\t" \
  "s_branch LC2_%=\n\t" \
  "LC9_%=:\n\t" \
  STP(1) \
  ""
extern "C" __global__ __launch_bounds__(768, 3) void fused_block(const u16* x, const u16* wqkv, const u16* wo, const u16* w_r,
                                                   const u16* w_gate, const u16* w_up, const u16* w_down, u16* out,
                                                   char* ws) {
  extern __shared__ __attribute__((aligned(16))) char smem[];
  const int tid = threadIdx.x;
  const int lane = tid & 63;
  const int w = __builtin_amdgcn_readfirstlane(tid >> 6);
  const int g = lane >> 4, r16 = lane & 15, q4 = (lane & 15) >> 2, p4 = lane & 3;
  const int bid = blockIdx.x;
  const int xcd = bid & 7;
  const int bidx = bid >> 3;
  const u32 l16 = lane * 16;
  u32 tv = 0;
  LSTAMP(0)
  if (w == 11) {
    u64 pc;
    asm volatile("s_getpc_b64 %0" : "=s"(pc));
    if (lane < 10 && bidx * 10 + lane < CODE_LINES) touch_line((const void*)(pc + (bidx * 10 + lane) * 128));
  }

  if (bidx >= 26) {
    const int fb = xcd * 6 + (bidx - 26);
    const u32 tstart = (u32)__builtin_amdgcn_s_memrealtime();
    const bool hurry = ((((u32)(u64)w_gate + fb * 64) >> 7) & 7u) == 3u;
#define WAIT_UNTIL(T)                                                                      \
  _Pragma("nounroll") for (int it = 0; it < 1000; ++it) {                                  \
    if ((int)((u32)__builtin_amdgcn_s_memrealtime() - tstart) >= (T)) break;               \
    __builtin_amdgcn_s_sleep(4);                                                           \
  }
    if (w < 3) {
      int li = (bidx - 26) * 160 + w * 64 + lane;
      if (w < 2 || lane < 32) touch_line((const char*)x + (size_t)(((xcd >> 2) * 13 + 6) * 80 - 16) * 1280 + li * 128);
    }
    u32x2 ep;
    u32 tag0;
    asm volatile("global_load_dwordx2 %0, %1, off sc1" : "=v"(ep) : "v"(ws + WS_EPOCH) : "memory");
    {
      int wi = (((xcd >> 1) & 1) * 6 + (bidx - 26)) * 12 + w;
      wi = wi > 129 ? 129 : wi;
      int row = (xcd >> 2) * 1040 + (xcd & 1) * 520 + wi * 4 + g;
      row = row > SEQ - 1 ? SEQ - 1 : row;
      const u32 vo = row * 1280 + r16 * 16;
      u32x4 xr[5];
#pragma unroll
      for (int i = 0; i < 5; ++i)
        asm volatile("global_load_dwordx4 %0, %1, %2 offset:%3" : "=v"(xr[i]) : "v"(vo), "s"(x), "n"(i * 256) : "memory");
      asm volatile("s_waitcnt vmcnt(0)" ::: "memory");
      asm volatile("" : "+v"(xr[0]), "+v"(xr[1]), "+v"(xr[2]), "+v"(xr[3]), "+v"(xr[4]), "+v"(ep)::"memory");
      tag0 = ep[0] + (ep[0] == SENT - 1u ? 2u : 1u);
      float sm = 0.f;
#pragma unroll
      for (int i = 0; i < 5; ++i)
#pragma unroll
        for (int e = 0; e < 4; ++e) sm = dot2b(xr[i][e], 0x3F803F80u, sm);
      const float mean = row16_sum(sm) * (1.0f / DM);
      float ss = 0.f;
#pragma unroll
      for (int i = 0; i < 5; ++i)
#pragma unroll
        for (int e = 0; e < 4; ++e) {
          const float a = lo_f(xr[i][e]) - mean, b = hi_f(xr[i][e]) - mean;
          ss = __builtin_fmaf(a, a, ss);
          ss = __builtin_fmaf(b, b, ss);
        }
      const float var = row16_sum(ss) * (1.0f / DM);
      const float den = rbf(__builtin_amdgcn_sqrtf(rbf(rbf(var) + 1e-5f)));
      if (r16 == 0) GST2((u32)(row * 8), ws + WS_LN1, 0, ((u32x2){cvt2((f32x2){mean, den}), tag0}));
    }
    u32x4 va[2][5], vb[5];
    const int vrk = (fb >> 4) * 14 + (int)(((((u32)(u64)w_gate + fb * 64) >> 6) - 8u) & 15u);
    const int vq0 = vrk * 4 + w * 2;
    u32x4 ra;
    u32 rb5;
    asm volatile("global_load_dwordx4 %0, %2, %4\n\tglobal_load_dword %1, %3, %4 offset:1024"
                   : "=&v"(ra), "=&v"(rb5) : "v"(l16), "v"((u32)(lane * 4)), "s"(w_r) : "memory");
    if (w >= 2) {
      WAIT_UNTIL(hurry ? 0 : PDELAY + 12 * (w - 2))
      const u32x4 s4 = (u32x4){SENT, SENT, SENT, SENT};
      const char* fp = ws + WS_FILL + (size_t)fb * 73728 + (w - 2) * 7168 + 4096;
      GST4(l16, fp, -4096, s4); GST4(l16, fp, -3072, s4); GST4(l16, fp, -2048, s4); GST4(l16, fp, -1024, s4);
      GST4(l16, fp, 0, s4); GST4(l16, fp, 1024, s4); GST4(l16, fp, 2048, s4);
      if (w < 4) GST4(l16, ws + WS_FILL + (size_t)fb * 73728 + 69632 + w * 1024, 0, s4);
    }
    if (w == 0) {
      const u64 e1 = (((u64)ep[1] << 32) | ep[0]) + (ep[0] == SENT - 1u ? 2ull : 1ull);
      if (lane == 0) *(u32x4*)(smem + CTL_OFF) = (u32x4){(u32)e1, (u32)(e1 >> 32), 0u, 0u};
      if (lane >= 1 && lane < 8) *(u32x4*)(smem + CTL_OFF + lane * 16) = (u32x4){0u, 0u, 0u, 0u};
      if (lane >= 32) *(u32x4*)(smem + KEY_OFF - 512 + lane * 16) = (u32x4){0u, 0u, 0u, 0u};
    }
    if (w >= 6) {
      const u32 o1 = tid >= 636 ? 0x3F803F80u : 0u;
      if (tid < 720) *(u32x4*)(smem + HN_OFF + (tid - 384) * 16) = (u32x4){o1, o1, o1, o1};
    }
    if (w < 2 && !hurry && vrk < 40) {
      WAIT_UNTIL(VDELAY)
      const u32 vo = (vq0 * 4 + g) * 1280 + r16 * 16;
#pragma unroll
      for (int i = 0; i < 5; ++i)
        asm volatile("global_load_dwordx4 %0, %3, %4 offset:%8\n\tglobal_load_dwordx4 %1, %3, %5 offset:%8\n\tglobal_load_dwordx4 %2, %6, %7 offset:%8"
                     : "=&v"(va[0][i]), "=&v"(va[1][i]), "=&v"(vb[i]) : "v"(vo), "s"(wo), "s"((const char*)wo + 5120), "v"((u32)(r16 * 16)), "s"(w_r), "n"(i * 256) : "memory");
      asm volatile("s_waitcnt vmcnt(0)" : "+v"(va[0][0]), "+v"(va[0][1]), "+v"(va[0][2]), "+v"(va[0][3]), "+v"(va[0][4]), "+v"(va[1][0]), "+v"(va[1][1]),
                   "+v"(va[1][2]), "+v"(va[1][3]), "+v"(va[1][4]), "+v"(vb[0]), "+v"(vb[1]), "+v"(vb[2]), "+v"(vb[3]), "+v"(vb[4])::"memory");
      float sb = 0.f;
#pragma unroll
      for (int i = 0; i < 20; ++i) sb = dot2b(vb[i >> 2][i & 3], 0x3F803F80u, sb);
      sb = row16_sum(sb) * (1.0f / DM);
      u32x4 pk2[2];
#pragma unroll
      for (int k = 0; k < 2; ++k) {
        float d = 0.f, sa = 0.f;
#pragma unroll
        for (int i = 0; i < 20; ++i) {
          d = dot2b(va[k][i >> 2][i & 3], vb[i >> 2][i & 3], d);
          sa = dot2b(va[k][i >> 2][i & 3], 0x3F803F80u, sa);
        }
        const u32 vp = __float_as_uint(row16_sum(d - sa * sb));
        pk2[k] = (u32x4){cvt2((f32x2){__uint_as_float(__builtin_amdgcn_readlane(vp, 0)), __uint_as_float(__builtin_amdgcn_readlane(vp, 16))}), tag0,
                          cvt2((f32x2){__uint_as_float(__builtin_amdgcn_readlane(vp, 32)), __uint_as_float(__builtin_amdgcn_readlane(vp, 48))}), tag0};
      }
      if (lane < 8) GST4(l16, ws + WS_VP + vq0 * 64, 0, ((lane & 1) ? pk2[1] : pk2[0]));
      LSTAMP(6)
    } else
      asm volatile("s_waitcnt vmcnt(0)" ::: "memory");
    asm volatile("" : "+v"(ra), "+v"(rb5)::"memory");
    LDS_BARRIER();
    const u32x2 tg = *(const u32x2*)(smem + CTL_OFF);
    const u32 tag = tg[0];
    if (w == 2 && lane == 0) GST2(0u, ws + WS_HFLAG + fb * 128, 0, tg);
    LSTAMP(1)
    if (w >= 3) {
      int li = (bidx - 26) * 534 + (w - 3) * 64 + lane;
      li = li > 3199 ? 3199 : li;
      const int rr = li / 5;
      if (w < 11 || lane < 22) touch_line((const char*)wo + rr * 1280 + ((xcd >> 1) & 1) * 640 + (li - rr * 5) * 128);
    }
    WAIT_UNTIL(hurry ? 0 : FDELAY)
    asm volatile("s_barrier" ::: "memory");
    if (w >= 2) {
      const int ch = (lane & 7) ^ (((lane >> 4) & 1) * 2 + ((w + fb) & 1) * 4);
      const u32 vg = (lane >> 3) * (FF * 2) + fb * 64 + (ch & 3) * 16;
      const u64 gsel = (ch & 4) ? (u64)w_up : (u64)w_gate;
#pragma nounroll
      for (int n0 = w - 2; n0 < 80; n0 += 10) {
        const int n = (fb & 1) ? 79 - n0 : n0;
        const u64 p = gsel + vg + (u32)(n * 8 * FF * 2);
        asm volatile("s_mov_b32 m0, %1\n\ts_nop 0\n\tglobal_load_lds_dwordx4 %0, off" FNT ::"v"(p), "s"(n * 1024) : "memory");
      }
#pragma nounroll
      for (int n = 11 - w; n < 40; n += 10) {
        const u32 row = (u32)(n * 64 + lane) / 80u;
        asm volatile("s_mov_b32 m0, %2\n\ts_nop 0\n\tglobal_load_lds_dwordx4 %0, %1" FNT
                     ::"v"(l16 ^ ((row & 3u) * 32u + (row & 8u) * 16u)), "s"((const char*)w_down + (size_t)fb * 40960 + n * 1024), "s"(DOWN_OFF + n * 1024) : "memory");
      }
      asm volatile("s_waitcnt vmcnt(0)" ::: "memory");
      if (lane == 0) asm volatile("ds_add_u32 %0, %1" ::"v"(CTL_OFF + 48), "v"(1u) : "memory");
    } else {
      WAIT_UNTIL(KSTART)
    }
    if (w >= 4) {
      const int v = w - 4, kq = v & 3, m = v >> 2;
#pragma nounroll
      for (int it = 0; it < SPIN_CAP; ++it) {
        if (__builtin_amdgcn_readfirstlane(lds_ld32(CTL_OFF + 48)) >= 10u) break;
        __builtin_amdgcn_s_sleep(1);
      }
      LSTAMP(2)
      bf16x8 gfr[5], ufr[5], dfr[5];
      {
        const int gx = (160 * kq + 8 * g + q4) * 128 + ((m ^ (q4 >> 1) ^ ((g & 1) * 2)) * 32) + p4 * 8;
        const char* db = smem + DOWN_OFF + (8 * g + q4) * 1280 + p4 * 8;
#pragma unroll
        for (int j = 0; j < 5; ++j) {
          gfr[j] = tr_frag(smem + gx + j * 4096, 512);
          ufr[j] = tr_frag(smem + (gx ^ 64) + j * 4096, 512);
          dfr[j] = tr_frag(db + ((v * 160 + j * 32) ^ (q4 * 32 + (g & 1) * 128)), 5120);
        }
#pragma unroll
        for (int j = 0; j < 5; ++j) asm volatile("" : "+v"(gfr[j]), "+v"(ufr[j]), "+v"(dfr[j]));
      }
      u32 asel[5];
#pragma unroll
      for (int j = 0; j < 5; ++j) {
        asel[j] = r16 == j ? ACT_OFF + 4096 + v * 64 + g * 16 : CTL_OFF + 64;
        asm volatile("" : "+v"(asel[j]));
      }
      u32 ahn = HN_OFF + p4 * HSL + kq * 320 + g * 16, asw = ACT_OFF + p4 * 1024 + kq * 256 + m * 64 + g * 16, asr = ACT_OFF + (lane & 31) * 4;
      u32 aav = ACT_OFF + 4096 + v * 64 + (lane & 31) * 2, vpo = v * 320 + r16 * 64 + g * 16, up3 = lane >= 32 ? 3u : 0u;
      asm volatile("" : "+v"(up3));
      const char* pdst = ws + WS_PART + fb * 2560;
      asm volatile("" : "+v"(ahn), "+v"(asw), "+v"(asr), "+v"(aav), "+v"(vpo));
      asm volatile("" : "+s"(pdst));
      asm volatile("s_mov_b32 s40, %[cap]\n"
                   "1:\n\tds_read_b32 v100, %[ctl]\n\ts_sub_u32 s40, s40, 1\n\ts_cbranch_scc1 2f\n\ts_waitcnt lgkmcnt(0)\n\t"
                   "v_cmp_eq_u32 vcc, 3, v100\n\ts_cbranch_vccz 1b\n"
                   "2:"
                   ::[cap] "n"(SPIN_CAP * 8), [ctl] "v"((u32)(CTL_OFF + 112)) : "memory", "vcc", "scc", "s40", "v100");
      LSTAMP(15)
#pragma nounroll
      for (int pass = 0; pass < 2; ++pass) {
        f32x4 ga = (f32x4){0.f, 0.f, 0.f, 0.f}, ua = ga;
        {
          u32x4 hf[5];
          asm volatile("ds_read_b128 %0, %5\n\tds_read_b128 %1, %5 offset:64\n\tds_read_b128 %2, %5 offset:128\n\t"
                       "ds_read_b128 %3, %5 offset:192\n\tds_read_b128 %4, %5 offset:256\n\ts_waitcnt lgkmcnt(0)"
                       : "=&v"(hf[0]), "=&v"(hf[1]), "=&v"(hf[2]), "=&v"(hf[3]), "=&v"(hf[4]) : "v"(ahn) : "memory");
#pragma unroll
          for (int j = 0; j < 5; ++j) {
            ga = MFMA(gfr[j], __builtin_bit_cast(bf16x8, hf[j]), ga);
            ua = MFMA(ufr[j], __builtin_bit_cast(bf16x8, hf[j]), ua);
          }
        }
        if (r16 < 4) {
          *(f32x4*)(smem + asw) = ga;
          *(f32x4*)(smem + asw + 128) = ua;
        }
        LSTAMP(13)
        LDS_BARRIER();
        LSTAMP(14)
        u32x2 best;
        asm volatile("ds_read_b64 %0, %1\n\ts_waitcnt lgkmcnt(0)" : "=v"(best) : "v"((u32)(CTL_OFF + 120)) : "memory");
        f32x2 s0, s1, s2, s3;
        asm volatile("ds_read2_b32 %0, %4 offset0:0 offset1:32\n\tds_read2_b32 %1, %4 offset0:64 offset1:96\n\t"
                     "ds_read2_b32 %2, %4 offset0:128 offset1:160\n\tds_read2_b32 %3, %4 offset0:192 offset1:224"
                     : "=&v"(s0), "=&v"(s1), "=&v"(s2), "=&v"(s3) : "v"(asr + (((best[1] & 3u) | up3) << 10)) : "memory");
        float rd = __builtin_amdgcn_rcpf(hi_f(best[0])), mb = lo_f(best[0]);
        asm volatile("s_waitcnt lgkmcnt(0)" : "+v"(s0), "+v"(s1), "+v"(s2), "+v"(s3), "+v"(rd), "+v"(mb)::"memory");
        const f32x2 sm = (s0 + s1) + (s2 + s3);
        auto xg = __builtin_amdgcn_permlane32_swap(__float_as_uint(sm[0]), __float_as_uint(sm[0]), false, false);
        auto xu = __builtin_amdgcn_permlane32_swap(__float_as_uint(sm[1]), __float_as_uint(sm[1]), false, false);
        const f32x2 gu = unpk(cvt2((f32x2){(__uint_as_float(xg[0]) - mb * __uint_as_float(xg[1])) * rd,
                                          (__uint_as_float(xu[0]) - mb * __uint_as_float(xu[1])) * rd}));
        const float sg = rbf(gu[0] * __builtin_amdgcn_rcpf(1.0f + __expf(-gu[0])));
        const u32 av = f2bf(sg * gu[1]);
        u32x4 bm[5];
        asm volatile("ds_write_b16 %5, %6\n\tds_read_b128 %0, %7\n\tds_read_b128 %1, %8\n\tds_read_b128 %2, %9\n\t"
                     "ds_read_b128 %3, %10\n\tds_read_b128 %4, %11\n\ts_waitcnt lgkmcnt(0)"
                     : "=&v"(bm[0]), "=&v"(bm[1]), "=&v"(bm[2]), "=&v"(bm[3]), "=&v"(bm[4])
                     : "v"(aav), "v"(av), "v"(asel[0]), "v"(asel[1]), "v"(asel[2]), "v"(asel[3]), "v"(asel[4]) : "memory");
        f32x4 ya = (f32x4){0.f, 0.f, 0.f, 0.f};
#pragma unroll
        for (int j = 0; j < 5; ++j) ya = MFMA(dfr[j], __builtin_bit_cast(bf16x8, bm[j]), ya);
        asm volatile("s_nop 7\n\ts_nop 1" : "+v"(ya));
        if (r16 < 5) GST4(vpo, pdst, 0, ya);
        if (pass) break;
        LSTAMP(16)
        u32 fin = 0;
#pragma nounroll
        for (int it = 0; it < SPIN_CAP * 8; ++it) {
          fin = __builtin_amdgcn_readfirstlane(lds_ld32(CTL_OFF + 36));
          if (fin) break;
          __builtin_amdgcn_s_sleep(4);
        }
        if (fin != 2u) break;
        asm volatile("s_barrier\n\ts_barrier" ::: "memory");
        pdst += WS_PART2 - WS_PART;
      }
      FLUSH_STAMPS
      return;
    }
    if (w == 0) {
      LSTAMP(8)
      asm volatile(
          "s_mov_b32 s41, %[cap]\n\ts_mov_b32 s40, 1\n\ts_mov_b64 s[46:47], 0\n\tv_mov_b32 v5, %[ln]\n\t"
          "s_mov_b32 s54, -1\n\ts_mov_b32 s55, 0x7ffff\n\tv_mov_b32 v12, 0\n\t"
#define PKLD(D) "s_mov_b64 exec, s[54:55]\n\tglobal_atomic_or_x2 " D ", %[vk], %[z], %[kb] sc0 sc1\n\ts_mov_b64 exec, -1\n\t"
          PKLD("v[8:9]") "s_sleep %[gap]\n\t" PKLD("v[10:11]") "s_sleep %[gap]\n\t" PKLD("v[14:15]")
#define MDPP(C) "s_nop 1\n\tv_max_u32_dpp v13, v13, v13 " C " row_mask:0xf bank_mask:0xf bound_ctrl:1\n\t"
#define PSTEP(L, D0, D1, DR)                                                                        \
          L "0:\n\t"                                                                               \
          "s_waitcnt vmcnt(2)\n\t"                                                                 \
          "v_cmp_eq_u32 s[42:43], " D1 ", %[tag]\n\t"                                              \
          "s_and_b64 exec, s[42:43], s[54:55]\n\ts_andn2_b64 s[54:55], s[54:55], s[42:43]\n\t"      \
          "v_and_or_b32 v12, " D0 ", %[msk], %[lid]\n\t"                                           \
          "v_mov_b32 v6, " D0 "\n\ts_mov_b64 exec, -1\n\t"                                         \
          "v_cmp_le_u32 vcc, s40, v12\n\t"                                                         \
          "s_cmp_eq_u64 vcc, s[46:47]\n\ts_cbranch_scc1 " L "1f\n\t"                              \
          "s_swappc_b64 s[50:51], s[48:49]\n"                                                      \
          L "1:\n\t"                                                                               \
          "s_cmp_eq_u64 s[54:55], 0\n\ts_cbranch_scc1 9f\n\t"                                     \
          PKLD(DR)
          "s_getpc_b64 s[48:49]\n\ts_add_u32 s48, s48, 12\n\ts_addc_u32 s49, s49, 0\n\ts_branch 10f\n\t"
          "v_mov_b32 v13, v12\n\t"
          MDPP("quad_perm:[1,0,3,2]") MDPP("quad_perm:[2,3,0,1]") MDPP("row_half_mirror") MDPP("row_mirror")
          "v_bfe_u32 v4, v6, 10, 6\n\tv_readlane_b32 s44, v13, 0\n\tv_readlane_b32 s45, v13, 16\n\tv_readlane_b32 s46, v13, 32\n\tv_readlane_b32 s47, v13, 48\n\t"
          "v_sub_u32 v4, %[rb], v4\n\ts_max_u32 s44, s44, s45\n\ts_max_u32 s46, s46, s47\n\ts_max_u32 s44, s44, s46\n\t"
          "s_and_b32 s45, s44, 0x7fffff\n\ts_bitset1_b32 s45, 23\n\ts_mul_hi_u32 s45, s45, %[shr]\n"
          "5:\n\ts_sub_u32 s40, s44, s45\n\ts_cselect_b32 s40, 1, s40\n\ts_max_u32 s40, s40, 1\n\tv_cmp_le_u32 s[46:47], s40, v12\n\ts_lshr_b32 s45, s45, 1\n\t"
          "s_bcnt1_i32_b64 s52, s[46:47]\n\ts_cmp_gt_u32 s52, 3\n\ts_cbranch_scc1 5b\n\t"
          "v_mbcnt_lo_u32_b32 v13, s46, 0\n\tv_mbcnt_hi_u32_b32 v13, s47, v13\n\tv_sub_u32 v7, %[ln], v13\n\tv_add_u32 v7, s52, v7\n\t"
          "v_cndmask_b32 v13, v7, v13, s[46:47]\n\tv_cndmask_b32 v7, 2, 1, s[46:47]\n\tv_cmp_gt_u32 s[44:45], 3, v13\n\t"
          "v_lshl_add_u32 v13, v13, 4, %[pa]\n\ts_mov_b64 exec, s[44:45]\n\tds_write_b128 v13, v[4:7]\n\ts_mov_b64 exec, -1\n\t"
          "s_setpc_b64 s[50:51]\n"
          PSTEP("1", "v8", "v9", "v[8:9]")
          PSTEP("2", "v10", "v11", "v[10:11]")
          PSTEP("3", "v14", "v15", "v[14:15]")
          "s_sub_u32 s41, s41, 1\n\ts_cbranch_scc0 10b\n\ts_swappc_b64 s[50:51], s[48:49]\n"
          "9:\n\tv_or_b32 v7, 4, v7\n\ts_mov_b64 exec, s[44:45]\n\tds_write_b32 v13, v7 offset:12\n\ts_mov_b64 exec, -1\n\tds_write_b32 %[at], v6\n\ts_waitcnt vmcnt(0)"
          ::[cap] "n"(SPIN_CAP), [gap] "n"(PGAP), [vk] "v"((u32)(lane * 6144)), [kb] "s"(ws + WS_PK + fb * 128), [tag] "v"(tag), [z] "v"(0ull), [ln] "v"(lane),
            [msk] "s"(~1023u), [lid] "v"(64u | lane), [rb] "v"(2047 - ((lane >> 1) * 80 + (lane & 1) * 48)),
            [shr] "s"((u32)((1.0 - PTHR) * 4294967296.0)), [pa] "s"(POST_OFF), [at] "v"((u32)(KEY_OFF - 256 + lane * 4))
          : "memory", "vcc", "scc", "s40", "s41", "s42", "s43", "s44", "s45", "s46", "s47", "s48", "s49", "s50", "s51", "s52", "s54", "s55",
            "v4", "v5", "v6", "v7", "v8", "v9", "v10", "v11", "v12", "v13", "v14", "v15");
      LSTAMP(3)
      LDS_BARRIER();
      const u32x2 cho = *(const u32x2*)(smem + POST_OFF + (*(const u32*)(smem + CTL_OFF + 124) & 3u) * 16);
#define STP12
#define STP12O
      asm volatile(
          "s_mov_b32 s41, %[cap]\n\ts_mov_b32 s54, -1\n\ts_mov_b32 s55, 0x7ffff\n\tv_mov_b32 v12, 0\n\tv_mov_b32 v5, 1\n\tv_mov_b32 v6, %[cg]\n\t"
          PKLD("v[8:9]") "s_sleep %[gap]\n\t" PKLD("v[10:11]") "s_sleep %[gap]\n\t" PKLD("v[14:15]")
#define KSTEP(L, D0, D1, DR)                                                                        \
          L "0:\n\t"                                                                               \
          "s_waitcnt vmcnt(2)\n\t"                                                                 \
          "v_cmp_eq_u32 s[42:43], " D1 ", %[tag]\n\t"                                              \
          "s_and_b64 exec, s[42:43], s[54:55]\n\tv_mov_b32 v12, " D0 "\n\ts_mov_b64 exec, -1\n\t"   \
          "s_andn2_b64 s[54:55], s[54:55], s[42:43]\n\ts_cbranch_scc0 9f\n\t"                     \
          PKLD(DR)
          KSTEP("1", "v8", "v9", "v[8:9]")
          KSTEP("2", "v10", "v11", "v[10:11]")
          KSTEP("3", "v14", "v15", "v[14:15]")
          "s_sub_u32 s41, s41, 1\n\ts_cbranch_scc0 10b\n"
          "9:\n\t"
          "v_readlane_b32 s40, v12, %[cg]\n\tds_write_b32 %[ak], v12\n\tv_mov_b32 v13, v12\n\t"
          "s_and_b32 s45, s40, 0x7ff\n\tv_cmp_lt_u32 vcc, s40, v12\n\ts_cmp_eq_u32 s45, %[cr]\n\ts_cselect_b64 vcc, vcc, -1\n\ts_cbranch_vccz 8f\n\t"
          MDPP("quad_perm:[1,0,3,2]") MDPP("quad_perm:[2,3,0,1]") MDPP("row_half_mirror") MDPP("row_mirror")
          "s_nop 0\n\tv_readlane_b32 s44, v13, 0\n\tv_readlane_b32 s45, v13, 16\n\tv_readlane_b32 s46, v13, 32\n\tv_readlane_b32 s47, v13, 48\n\t"
          "s_max_u32 s44, s44, s45\n\ts_max_u32 s46, s46, s47\n\ts_max_u32 s40, s44, s46\n\t"
          "v_cmp_eq_u32 vcc, s40, v12\n\tv_mov_b32 v5, 2\n\ts_ff1_i32_b64 s44, vcc\n\tv_mov_b32 v6, s44\n"
          "8:\n\tv_mov_b32 v4, s40\n\ts_mov_b64 exec, 1\n\tds_write_b96 %[ctl], v[4:6]\n\ts_mov_b64 exec, -1\n\t"
          STP12 "s_waitcnt vmcnt(0) lgkmcnt(0)"
          : STP12O : [cap] "n"(SPIN_CAP), [gap] "n"(PGAP), [vk] "v"((u32)(lane * 6144)), [kb] "s"(ws + WS_PK + fb * 128 + 8), [tag] "v"(tag),
            [ctl] "v"((u32)(CTL_OFF + 32)), [ak] "v"((u32)(KEY_OFF + lane * 4)), [cr] "s"(__builtin_amdgcn_readfirstlane(cho[0])),
            [cg] "s"(__builtin_amdgcn_readfirstlane(cho[1])), [z] "v"(0ull)
          : "memory", "vcc", "scc", "s40", "s41", "s42", "s43", "s44", "s45", "s46", "s47", "s54", "s55", "v4", "v5", "v6", "v8", "v9", "v10",
            "v11", "v12", "v13", "v14", "v15");
      if (fb == 0 && lane == 0) GST2(0u, ws + WS_EPOCH, 0, tg);
      FLUSH_STAMPS
      return;
    }
    float swr = dot2b(rb5, 0x3F803F80u, 0.f);
#pragma unroll
    for (int e = 0; e < 4; ++e) swr = dot2b(ra[e], 0x3F803F80u, swr);
    swr = wave_sum(swr);
    u32 ah16 = HN_OFF + (w - 1) * HSL + l16, ah4 = HN_OFF + (w - 1) * HSL + 1024 + lane * 4, l4 = lane * 4;
    asm volatile("" : "+v"(swr), "+v"(ah16), "+v"(ah4), "+v"(l4));
    u32x4 wst, h4, n4;
    u32 h1, n1;
    auto row_stats = [&]() __attribute__((always_inline)) {
      float s1 = dot2b(h1, 0x3F803F80u, 0.f), s2 = dot2b(h1, h1, 0.f), s3 = dot2b(h1, rb5, 0.f);
#pragma unroll
      for (int e = 0; e < 4; ++e) {
        s1 = dot2b(h4[e], 0x3F803F80u, s1);
        s2 = dot2b(h4[e], h4[e], s2);
        s3 = dot2b(h4[e], ra[e], s3);
      }
      const u32 y = __float_as_uint(row16_sum(tr_sum((f32x4){s1, s2, s3, s3})));
      const float mean = __uint_as_float(__builtin_amdgcn_readlane(y, 0)) * (1.0f / DM);
      const float var = fmaxf(__uint_as_float(__builtin_amdgcn_readlane(y, 32)) * (1.0f / DM) - mean * mean, 0.f);
      const u32 md = cvt2((f32x2){mean, __builtin_amdgcn_sqrtf(rbf(rbf(var) + 1e-5f))});
      const float sc = (__uint_as_float(__builtin_amdgcn_readlane(y, 16)) - swr * lo_f(md)) * __builtin_amdgcn_rcpf(hi_f(md));
      return (u32x2){md, (mono(sc) & ~3u) | (u32)(w - 1)};
    };
    {
      u32 have = 255u, st = 0;
      u32x2 mine = {0u, 0u};
      bool sig = false;
#pragma nounroll
      for (int it = 0; it < SPIN_CAP * 8; ++it) {
        u32x4 po = lds_ld128(POST_OFF + (w - 1) * 16);
        st = __builtin_amdgcn_readfirstlane(po[3]);
        const u32 gid = __builtin_amdgcn_readfirstlane(po[1]);
        if ((st & 1u) && gid != have) {
          if (((((u32)__builtin_amdgcn_s_memrealtime() >> 1) - __builtin_amdgcn_readfirstlane(po[2])) & 1023u) < PCD / 2) continue;
          LSTAMP(7)
          asm volatile("global_load_dwordx4 %0, %3, %5 sc1\n\tglobal_load_dword %1, %4, %5 offset:1024 sc1\n\ts_waitcnt vmcnt(0)\n\t"
                       "ds_read_b128 %2, %8\n\tds_write_b128 %6, %0\n\tds_write_b32 %7, %1\n\ts_waitcnt lgkmcnt(2)"
                       : "=&v"(h4), "=&v"(h1), "=&v"(po) : "v"(l16), "v"(l4), "s"(ws + WS_PC + gid * 1280), "v"(ah16), "v"(ah4),
                         "v"((u32)(POST_OFF + (w - 1) * 16)) : "memory");
          if (__any(max(max(max(h4[0], h4[1]), max(h4[2], h4[3])), h1) == SENT)) continue;
          if (__builtin_amdgcn_readfirstlane(po[1]) == gid) st = __builtin_amdgcn_readfirstlane(po[3]);
          if (st & 4u) {
            if (lane == 0) asm volatile("ds_add_u32 %0, %1" ::"v"(CTL_OFF + 112), "v"(1u) : "memory");
            sig = true;
          }
          LSTAMP(4)
          mine = row_stats();
          have = gid;
          LSTAMP(5)
        } else if (!(st & 4u))
          __builtin_amdgcn_s_sleep(1);
        if ((st & 4u) && (!(st & 1u) || gid == have)) break;
      }
      if (lane == 0) {
        if (!sig) asm volatile("ds_add_u32 %0, %1" ::"v"(CTL_OFF + 112), "v"(1u) : "memory");
        if (st & 1u) asm volatile("ds_max_u64 %0, %1" ::"v"(CTL_OFF + 120), "v"(mine) : "memory");
      }
    }
    LDS_BARRIER();
#define WAIT_FINAL                                                                         \
  _Pragma("nounroll") for (int it = 0; it < SPIN_CAP * 8; ++it) {                          \
    wst = lds_ld128(CTL_OFF + 32);                                                         \
    if (__builtin_amdgcn_readfirstlane(wst[1])) break;                                     \
    __builtin_amdgcn_s_sleep(1);                                                           \
  }
#define LOAD_CAND(CP)                                                                      \
  _Pragma("nounroll") for (int it = 0; it < SPIN_CAP; ++it) {                              \
    asm volatile("global_load_dwordx4 %0, %4, %6 sc1\n\tglobal_load_dword %1, %5, %6 offset:1024 sc1\n\t" \
                 "global_load_dwordx4 %2, %4, %6 offset:1280 sc1\n\tglobal_load_dword %3, %5, %6 offset:2304 sc1\n\ts_waitcnt vmcnt(0)" \
                 : "=&v"(h4), "=&v"(h1), "=&v"(n4), "=&v"(n1) : "v"(l16), "v"(l4), "s"(CP) : "memory"); \
    if (!__any(h4[0] == SENT || h4[1] == SENT || h4[2] == SENT || h4[3] == SENT || h1 == SENT || n4[0] == SENT || \
               n4[1] == SENT || n4[2] == SENT || n4[3] == SENT || n1 == SENT)) break;      \
    __builtin_amdgcn_s_sleep(2);                                                           \
  }
    if (w == 1) {
      WAIT_FINAL
      if (__builtin_amdgcn_readfirstlane(wst[1]) == 2u) {
        LOAD_CAND(ws + WS_CAND + __builtin_amdgcn_readfirstlane(wst[2]) * 2560)
        const u32x2 mine = row_stats();
        asm volatile("s_barrier" ::: "memory");
        *(u32x4*)(smem + ah16) = h4;
        *(u32*)(smem + ah4) = h1;
        if (lane == 0) *(u32x2*)(smem + CTL_OFF + 120) = mine;
        asm volatile("s_waitcnt lgkmcnt(0)" ::: "memory");
      }
    } else if (w == 3 || fb >= 40) {
      const int c = w == 3 ? fb : fb + 8;
      if (c < 51) {
        const u32 t0 = __builtin_amdgcn_readfirstlane(*(const u32*)(smem + KEY_OFF - 256 + c * 4));
#pragma nounroll
        for (int it = 0; it < 256; ++it) {
          if (((((u32)__builtin_amdgcn_s_memrealtime() >> 1) - t0) & 1023u) >= CCD / 2) break;
          __builtin_amdgcn_s_sleep(1);
        }
        LOAD_CAND(ws + WS_CAND + c * 2560)
        u32x4 o4;
#pragma unroll
        for (int e = 0; e < 4; ++e) o4[e] = cvt2((f32x2){lo_f(h4[e]) + lo_f(n4[e]), hi_f(h4[e]) + hi_f(n4[e])});
        u32 o1 = cvt2((f32x2){lo_f(h1) + lo_f(n1), hi_f(h1) + hi_f(n1)});
        asm volatile("" : "+v"(o4), "+v"(o1));
        WAIT_FINAL
        if ((u32)c != __builtin_amdgcn_readfirstlane(wst[2])) {
          const int crow = 2047 - (int)(__builtin_amdgcn_readfirstlane(lds_ld32(KEY_OFF + c * 4)) & 2047u);
          *(u32x4*)(out + (size_t)crow * DM + lane * 8) = o4;
          *(u32*)(out + (size_t)crow * DM + 512 + lane * 2) = o1;
        }
      }
    } else {
      const char* pp = ws + WS_PART + fb * 64;
#pragma nounroll
      for (int pass = 0; pass < 2; ++pass) {
        const u32x2 best = *(const u32x2*)(smem + CTL_OFF + 120);
        const u32 slot = best[1] & 3u;
        const u32 wkey = __builtin_amdgcn_readfirstlane(*(const u32*)(smem + (pass ? CTL_OFF + 32 : POST_OFF + slot * 16)));
        const float rr = rbf(__uint_as_float((best[1] & ~3u) ^ ((u32)((int)~best[1] >> 31) | 0x80000000u)));
        float rs = rbf(__builtin_amdgcn_rcpf(1.0f + __expf(-rr)));
        const int ccol = (g & 1) * 2 + (g >> 1);
        float hv = bf2f(*(const u16*)(smem + HN_OFF + slot * HSL + fb * 32 + p4 * 8 + ccol * 2));
        float hnv = rbf(rbf(hv - lo_f(best[0])) * __builtin_amdgcn_rcpf(hi_f(best[0])));
        const char* op_ = (const char*)out + (size_t)(2047 - (int)(wkey & 2047u)) * 1280 + fb * 32;
        u32 vo_ = p4 * 8 + ccol * 2;
        asm volatile("" : "+v"(vo_), "+v"(hnv), "+v"(hv), "+v"(rs));
        asm volatile("" : "+s"(op_));
        const u32 vp = (lane >> 2) * 2560 + p4 * 16;
        typedef __attribute__((ext_vector_type(16))) unsigned int u32x16;
        u32x16 keepa, keepb;
        f32x4 ys;
        u32 fin;
        asm volatile(
#define PLD(B0, B1, B2)                                                                             \
            "global_load_dwordx4 " B0 ", %[vp], %[p0] sc1\n\t"                                       \
            "global_load_dwordx4 " B1 ", %[vp], %[p1] sc1\n\tglobal_load_dwordx4 " B2 ", %[vp], %[p2] sc1\n\t"
#define PCHK(A0, A1, B0, B1, C0, C1)                                                                \
            "s_waitcnt vmcnt(3)\n\t"                                                                 \
            "v_pk_add_f32 v[96:97], " A0 ", " B0 "\n\tv_pk_add_f32 v[98:99], " A1 ", " B1 "\n\t"    \
            "v_pk_add_f32 v[96:97], v[96:97], " C0 "\n\tv_pk_add_f32 v[98:99], v[98:99], " C1 "\n\t" \
            "v_pk_add_f32 v[102:103], v[96:97], v[98:99]\n\t"                                        \
            "v_cmp_u_f32 vcc, v102, v103\n\ts_cbranch_vccz 9f\n\t"
            "s_mov_b32 s42, %[cap]\n\ts_sleep %[cdel]\n\t"
            PLD("v[64:67]", "v[68:71]", "v[72:75]")
            "s_sleep %[gap]\n\t"
            PLD("v[80:83]", "v[84:87]", "v[88:91]")
            "1:\n\t"
            PCHK("v[64:65]", "v[66:67]", "v[68:69]", "v[70:71]", "v[72:73]", "v[74:75]")
            PLD("v[64:67]", "v[68:71]", "v[72:75]")
            PCHK("v[80:81]", "v[82:83]", "v[84:85]", "v[86:87]", "v[88:89]", "v[90:91]")
            PLD("v[80:83]", "v[84:87]", "v[88:91]")
            "s_sub_u32 s42, s42, 1\n\ts_cbranch_scc0 1b\n"
            "9:\n\tds_read_b32 %[fin], %[af]"
            : "=&{v[96:99]}"(ys), "=&{v[64:79]}"(keepa), "=&{v[80:95]}"(keepb), [fin] "=&v"(fin)
            : [cap] "n"(SPIN_CAP), [gap] "n"(CGAP), [cdel] "n"(CDEL), [vp] "v"(vp), [p0] "s"(pp),
              [p1] "s"(pp + 40960), [p2] "s"(pp + 81920), [af] "v"((u32)(CTL_OFF + 36))
            : "memory", "vcc", "scc", "s42", "v102", "v103");
        LSTAMP(16)
        float y = tr_sum(ys);
        y += dppf<0x124>(y);
        y += dppf<0x128>(y);
        const u32 res = f2bf(hv + rbf(hnv + rbf(rbf(y) * rs)));
        asm volatile("s_mov_b32 s42, %[cap]\n"
                     "1:\n\ts_waitcnt lgkmcnt(0)\n\tv_cmp_eq_u32 vcc, %[ok], %[fin]\n\ts_cbranch_vccz 2f\n\t"
                     "s_mov_b32 exec_lo, 0x000f000f\n\ts_mov_b32 exec_hi, 0x000f000f\n\tglobal_store_short %[vo], %[res], %[op]\n\ts_mov_b64 exec, -1\n"
                     "2:\n\tv_cmp_eq_u32 vcc, 0, %[fin]\n\ts_cbranch_vccz 3f\n\tds_read_b32 %[fin], %[af]\n\ts_sub_u32 s42, s42, 1\n\ts_cbranch_scc0 1b\n"
                     "3:\n\ts_waitcnt lgkmcnt(0)\n\tv_cmp_lt_u32 vcc, %[ok], %[fin]\n\ts_cbranch_vccz 4f\n\ts_waitcnt vmcnt(0)\n4:"
                     : [fin] "+v"(fin) : [cap] "n"(SPIN_CAP * 8), [ok] "s"(pass + 1), [vo] "v"(vo_), [res] "v"(res), [op] "s"(op_),
                       [af] "v"((u32)(CTL_OFF + 36)), "{v[64:79]}"(keepa), "{v[80:95]}"(keepb) : "memory", "vcc", "scc", "s42");
        if (pass || __builtin_amdgcn_readfirstlane(fin) != 2u) break;
        asm volatile("s_barrier\n\ts_barrier\n\ts_barrier" ::: "memory");
        pp += WS_PART2 - WS_PART;
      }
    }
    LSTAMP(17)
    FLUSH_STAMPS
    return;
  }

  const int idx2 = (xcd & 1) * 26 + bidx;
  const int hg = ((xcd >> 1) & 1) * 4 + (idx2 & 3);
  const int mt = (xcd >> 2) * 13 + (idx2 >> 2);
#define ROTF (((idx2 >> 2) * 3 / 2 + (idx2 & 3) * 3) % 10)
  const int kb0 = ROTF;
  const int r0 = mt * 80;
  if (w >= 4) {
    const int cw = w - 4;
    u32 vo = w_off(cw, lane, hg), vx = x_off(cw, lane, r0), vx2 = x_off(8 + (cw & 3), lane, r0);
    asm volatile("" : "+v"(vo), "+v"(vx), "+v"(vx2));
    __builtin_amdgcn_sched_barrier(0);
    const char* wb = (const char*)wqkv + (size_t)kb0 * 245760 + W_ROW(cw);
    dma_m0<0>(vo, wb, cw * 1024);
    dma_m0<0>(vx, (const char*)x + kb0 * 128, XRING + cw * 1024);
    dma_m0<0>(vo, wb + 61440, 8192 + cw * 1024);
    if (cw < 4) dma_m0<0>(vx2, (const char*)x + kb0 * 128, XRING + 8192 + cw * 1024);
  }
  LSTAMP(40)
  const int rt = w >= 4 ? (w & 3) : 4, kh = w >= 4 ? (w - 4) >> 2 : (w & 1);
  float bias[2];
  u32 sel[4], e4, eh, aq, aka, akb, pm, pf, hi, arec, vpp;
  const char *os, *os4, *sst, *oa[10];
  int so_[10];
  int lane_ = lane, mt_ = mt, hg_ = hg;
  auto prepv = [&](const int lane) __attribute__((always_inline)) {
    const int g = lane >> 4, r16 = lane & 15, q4 = (lane & 15) >> 2, p4 = lane & 3;
    const int par = lane & 1;
    const bool ca = r16 >= 4 * g, cb = r16 >= 4 * g + 2;
    bias[0] = (ca || r0 + rt > 0) ? 0.f : -__builtin_inff();
    bias[1] = (cb || r0 + rt > 0) ? 0.f : -__builtin_inff();
    const u32 sa = par ? 0x01000C0Cu : 0x0C0C0100u, sb = par ? 0x03020C0Cu : 0x0C0C0302u;
    sel[0] = ca ? 0x0C0C0C0Cu : sa;
    sel[1] = cb ? 0x0C0C0C0Cu : sb;
    sel[2] = ca ? sa : 0x0C0C0C0Cu;
    sel[3] = cb ? sb : 0x0C0C0C0Cu;
    e4 = EXO + rt * 512 + lane * 8;
    eh = e4 + kh * 6144;
    aq = TAB_OFF + 384 + (rt * 16 + r16) * 4;
    aka = TAB_OFF + 384 + (rt * 16 + 4 * g + par + (ca ? 16 : 0)) * 4;
    akb = TAB_OFF + 384 + (rt * 16 + 4 * g + par + 2 + (cb ? 16 : 0)) * 4;
    pm = (4 * g + q4) * 160 + p4 * 40;
    pf = (20 * g + 16 + q4) * 160 + p4 * 40;
    hi = HIM + (rt * 16 + r16) * 160 + g * 40;
    arec = REC_OFF + (r16 == 0 ? kh * 192 + g * 48 : (r16 == 8 ? 384 : 432));
    vpp = lane + kh * 64 + (rt == 0 ? 768 : (rt == 2 ? 8192 : (rt == 4 ? 512 : 640)));
    asm volatile("" : "+v"(arec), "+v"(vpp));
    asm volatile("" : "+v"(bias[0]), "+v"(bias[1]), "+v"(sel[0]), "+v"(sel[1]), "+v"(sel[2]), "+v"(sel[3]), "+v"(e4), "+v"(eh),
                 "+v"(aq), "+v"(aka), "+v"(akb), "+v"(pm), "+v"(pf), "+v"(hi));
  };
  auto preps = [&](const int mt, const int hg) __attribute__((always_inline)) {
    const char* ob = ws + WS_O + (size_t)mt * 102400;
    os = ob + rt * 20480 + (2 * hg + kh) * 1024;
    os4 = ob + rt * 20480 + (16 + (hg >> 1)) * 1024 + (hg & 1) * 8;
    sst = ws + WS_STAT + (size_t)(2 * mt + (rt < 3 ? 0 : 1)) * 8192 + hg * 1024 + (rt < 3 ? rt : rt - 3) * 256;
    asm volatile("" : "+s"(os), "+s"(os4), "+s"(sst));
    const int k2 = w & 1;
#pragma unroll
    for (int j = 0; j < 10; ++j) {
      const bool oth = j >= 5 || w < 2;
      const int mo[8] = {OORD};
      const int ch = j % 5 < 4 ? (2 * hg + k2) ^ (oth ? 2 * mo[4 + j % 5] : 2 * mo[j % 5]) : (hg >> 1) ^ ((oth != ((hg & 1) != 0)) ? (k2 ? 0 : 3) : 1 + k2);
      oa[j] = ob + (j % 5 < 4 ? ch : 16 + ch) * 1024;
      so_[j] = ch * (j % 5 < 4 ? 6400 : 25600);
      asm volatile("" : "+s"(oa[j]), "+s"(so_[j]));
    }
  };
#define WO_OFFS                                                                                    \
  u32 woff[5];                                                                                     \
  _Pragma("unroll") for (int i = 0; i < 5; ++i) {                                                  \
    const int c = i * 64 + lane;                                                                   \
    const int row = c / 10;                                                                        \
    woff[i] = row * 1120 + hg * 160 + c * 16 + (4 - i) * 1024;                                     \
  }
#define WO_RUN(RHO)                                                                                \
  {                                                                                                \
    const char* wq_ = (const char*)wo + (RHO) * 40960; \
    dma_m0<-4096>(woff[0], wq_, (RHO) * 5120 + 4096);                                               \
    dma_nx<-3072>(woff[1], wq_); dma_nx<-2048>(woff[2], wq_); dma_nx<-1024>(woff[3], wq_); dma_nx<0>(woff[4], wq_); \
  }

#define BAR asm volatile("s_barrier" ::: "memory");
#define PREP_V __builtin_amdgcn_sched_barrier(0); asm volatile("" : "+v"(lane_)); prepv(lane_); __builtin_amdgcn_sched_barrier(0);
#define PREP_S __builtin_amdgcn_sched_barrier(0); asm volatile("" : "+s"(mt_), "+s"(hg_)); preps(mt_, hg_); __builtin_amdgcn_sched_barrier(0);
  if (w < 2) {
    const int l = w;
    __builtin_amdgcn_s_setprio(3);
    u32 vw[4];
#pragma unroll
    for (int i = 0; i < 4; ++i) vw[i] = w_off(i, lane, hg) + 4096;
    u32 go = (u32)kb0 * 245760u + 122880u + l * 61440u;
    u32 so = HSLOT + l * 8192 + 4096;
#define W1(I) dma_nx<((I) - 4) * 1024>(vw[(I) & 3], wb + (W_ROW(I) - (I) * 1024));
#define ISSUE_W                                                                                    \
  {                                                                                                \
    const char* wb = (const char*)wqkv + go;                                                       \
    dma_m0<-4096>(vw[0], wb, so);                                                                  \
    W1(1) W1(2) W1(3) W1(4) W1(5) W1(6) W1(7)                                                      \
    go += 122880u;                                                                                 \
    if (go >= 2457600u) go -= 2457600u;                                                            \
    so = so >= 5 * HSLOT ? so - 5 * HSLOT : so + HSLOT;                                            \
  }
    __builtin_amdgcn_s_sleep(2);
    ISSUE_W
    __builtin_amdgcn_sched_barrier(0);
    WO_OFFS
    asm volatile("" : "+v"(woff[0]), "+v"(woff[1]), "+v"(woff[2]), "+v"(woff[3]), "+v"(woff[4]));
    BAR
    ISSUE_W
    asm volatile("s_waitcnt vmcnt(8)\n\ts_barrier" ::: "memory");
    LSTAMP(30)
#pragma unroll 1
    for (int h = 0; h < 17; ++h) {
      ISSUE_W
      asm volatile("s_waitcnt vmcnt(8)\n\ts_barrier" ::: "memory");
      if (h & 1) { LSTAMP(31 + (h >> 1)) }
    }
    asm volatile("s_waitcnt vmcnt(0)\n\ts_barrier" ::: "memory");
    LSTAMP(39)
    WO_RUN(12 + l)
    const u32 tagw = lds_ld32(CTL_OFF);
    const char* bxf = ws + WS_BXF + mt * 1024;
    BAR
    const u32 town = (u32)__builtin_amdgcn_s_memrealtime();
    if (l == 0) {
      if (lane == 0) GST2((u32)(hg * 128), bxf, 0, ((u32x2){town, tagw}));
    }
    WO_RUN(18 + l)
    WO_RUN(8 + l)
    WO_RUN(l)
    const u32 vfl = (((lane & 7) == hg ? hg ^ 1 : (lane & 7))) * 128;
    u32x2 f;
    asm volatile("" : "=v"(f));
    BAR
    int gate = (int)((u32)__builtin_amdgcn_s_memrealtime() - town) + GADD;
    gate = gate > 200 ? 200 : gate;
    WO_RUN(2 + l)
    WO_RUN(4 + l)
    WO_RUN(6 + l)
    if (l == 1) asm volatile("global_load_dwordx2 %0, %1, %2 sc1" : "+v"(f) : "v"(vfl), "s"(bxf) : "memory");
    __builtin_amdgcn_s_setprio(0);
    pm = (4 * g + q4) * 160 + p4 * 40;
    pf = (20 * g + 16 + q4) * 160 + p4 * 40;
    asm volatile("" : "=v"(bias[0]), "=v"(bias[1]), "=v"(sel[0]), "=v"(sel[1]), "=v"(sel[2]), "=v"(sel[3]), "=v"(e4), "=v"(eh),
                 "=v"(aq), "=v"(aka), "=v"(akb), "+v"(pm), "+v"(pf), "=v"(hi), "=v"(arec), "=v"(vpp));
    PREP_S
    if (l == 1) {
      asm volatile("s_waitcnt vmcnt(0)" : "+v"(f)::"memory");
#pragma nounroll
      for (int it = 0; it < SPIN_CAP; ++it) {
        if (__all(f[1] == tagw)) break;
        asm volatile("global_load_dwordx2 %0, %1, %2 sc1\n\ts_waitcnt vmcnt(0)" : "=&v"(f) : "v"(vfl), "s"(bxf) : "memory");
      }
#pragma nounroll
      for (int it = 0; it < 64; ++it) {
        if (__all((int)((u32)__builtin_amdgcn_s_memrealtime() - f[0]) >= gate)) break;
      }
    }
    __builtin_amdgcn_s_setprio(0);
  } else if (w < 4) {
    __builtin_amdgcn_s_setprio(3);
    if (w == 2) {
      u32 vx[12];
#pragma unroll
      for (int i = 0; i < 12; ++i) vx[i] = x_off(i, lane, r0) + (4 - (i % 6)) * 1024;
      int kb = kb0 == 9 ? 0 : kb0 + 1;
      u32 xo = XRING + XSLOT + 4096;
#define ISSUE_X0                                                                                   \
  {                                                                                                \
    const char* xb = (const char*)x + kb * 128;                                                    \
    dma_m0<-4096>(vx[0], xb, xo);                                                                  \
    dma_nx<-3072>(vx[1], xb); dma_nx<-2048>(vx[2], xb); dma_nx<-1024>(vx[3], xb); dma_nx<0>(vx[4], xb); dma_nx<1024>(vx[5], xb); \
  }
#define ISSUE_X1                                                                                   \
  {                                                                                                \
    const char* xb = (const char*)x + kb * 128;                                                    \
    dma_m0<-4096>(vx[6], xb, xo + 6144);                                                           \
    dma_nx<-3072>(vx[7], xb); dma_nx<-2048>(vx[8], xb); dma_nx<-1024>(vx[9], xb); dma_nx<0>(vx[10], xb); dma_nx<1024>(vx[11], xb); \
    kb = kb == 9 ? 0 : kb + 1;                                                                     \
    xo = xo >= XRING + 3 * XSLOT ? xo - 3 * XSLOT : xo + XSLOT;                                    \
  }
      ISSUE_X0
      __builtin_amdgcn_sched_barrier(0);
      WO_OFFS
      asm volatile("" : "+v"(woff[0]), "+v"(woff[1]), "+v"(woff[2]), "+v"(woff[3]), "+v"(woff[4]));
      BAR
      ISSUE_X1
      ISSUE_X0
      BAR
      ISSUE_X1
      asm volatile("s_waitcnt vmcnt(12)\n\ts_barrier" ::: "memory");
#pragma unroll 1
      for (int j = 3; j < 10; ++j) {
        ISSUE_X0
        BAR
        ISSUE_X1
        asm volatile("s_waitcnt vmcnt(12)\n\ts_barrier" ::: "memory");
      }
      const char* xq = (const char*)x + (size_t)r0 * 1280;
      const int nv = mt == 25 ? 480 : 800;
#define RES1(I) if ((I) < 7 || (I) * 64 + lane < nv) dma_m0<(((I) % 5) - 4) * 1024>(woff[(I) % 5], xq + ((I) / 5) * 40960, HIM + ((I) / 5) * 5120 + 4096);
      PREP_V
      BAR
      PREP_S
      asm volatile("s_waitcnt vmcnt(0)\n\ts_barrier" ::: "memory");
      RES1(0) RES1(1) RES1(2) RES1(3) RES1(4) RES1(5) RES1(6)
      BAR
      RES1(7) RES1(8) RES1(9) RES1(10) RES1(11) RES1(12)
      if (lane < 10) dma_m0<0>(l16, (const char*)w_r + hg * 160, WR_OFF);
      BAR
    } else {
      u32x2 ep;
      u32x4 ra;
      u32 rb5;
      if (lane < 8) {
        *(u32x4*)(smem + ONES_OFF + lane * 16) = (u32x4){0x3F803F80u, 0x3F803F80u, 0x3F803F80u, 0x3F803F80u};
        *(u32x4*)(smem + CTL_OFF + lane * 16) = (u32x4){0u, 0u, 0u, 0u};
      }
      {
        const u32 o1 = lane == 26 ? 0x3F803F80u : 0u;
        if (lane < 30) *(u32x4*)(smem + REC_OFF + lane * 16) = (u32x4){o1, o1, o1, o1};
      }
      WO_OFFS
      asm volatile("" : "+v"(woff[0]), "+v"(woff[1]), "+v"(woff[2]), "+v"(woff[3]), "+v"(woff[4]));
      LDS_BARRIER();
      BAR BAR
      asm volatile("global_load_dwordx2 %0, %1, off sc1" : "=v"(ep) : "v"(ws + WS_EPOCH) : "memory");
      asm volatile("global_load_dwordx4 %0, %2, %4\n\tglobal_load_dword %1, %3, %4 offset:1024"
                   : "=&v"(ra), "=&v"(rb5) : "v"(l16), "v"((u32)(lane * 4)), "s"(w_r) : "memory");
      BAR BAR BAR
      PREP_V
      BAR
      PREP_S
      BAR
      asm volatile("s_waitcnt vmcnt(0)" : "+v"(ep), "+v"(ra), "+v"(rb5)::"memory");
      const u64 e1 = (((u64)ep[1] << 32) | ep[0]) + (ep[0] == SENT - 1u ? 2ull : 1ull);
      float swr = lo_f(rb5) + hi_f(rb5);
#pragma unroll
      for (int e = 0; e < 4; ++e) swr += lo_f(ra[e]) + hi_f(ra[e]);
      swr = wave_sum(swr);
      if (lane == 0) {
        *(u32x2*)(smem + CTL_OFF) = (u32x2){(u32)e1, (u32)(e1 >> 32)};
        *(float*)(smem + CTL_OFF + 40) = swr;
      }
      int ra0 = r0 - 16 + lane, ra1 = r0 + 48 + (lane & 31);
      ra0 = ra0 < 0 ? 0 : (ra0 > SEQ - 1 ? SEQ - 1 : ra0);
      ra1 = ra1 > SEQ - 1 ? SEQ - 1 : ra1;
      const int hd = lane / 5, pt = lane - hd * 5;
      u32 avp = REC_OFF + (hd >> 1) * 192 + (pt < 4 ? pt * 48 + (hd & 1) * 8 : hd * 48 + 16), vvp = lane < 20 ? (lane >> 1) * 128 + (lane & 1) * 16 : 0;
      u32 vf = (lane < 48 ? lane : 0) * 128, va0 = ra0 * 8, va1 = ra1 * 8, vt0 = TAB_OFF + lane * 4, vt1 = TAB_OFF + 256 + (lane & 31) * 4;
      asm volatile("" : "+v"(avp), "+v"(vvp), "+v"(vf), "+v"(va0), "+v"(va1), "+v"(vt0), "+v"(vt1));
#pragma nounroll
      for (int h = 7; h <= HK0; ++h) BAR
      u32x2 f, t0, t1;
      u32x4 vp4;
      GLD2(f, vf, ws + WS_HFLAG, 0);
      GLD2(t0, va0, ws + WS_LN1, 0);
      GLD2(t1, va1, ws + WS_LN1, 0);
      asm volatile("global_load_dwordx4 %0, %1, %2 sc1" : "=v"(vp4) : "v"(vvp), "s"(ws + WS_VP + hg * 1280) : "memory");
#pragma unroll
      for (int h = HK0 + 1; h <= 17; ++h) BAR
      WO_RUN(10)
      WO_RUN(11)
      BAR
      asm volatile("s_waitcnt vmcnt(10)" : "+v"(t0), "+v"(t1), "+v"(f), "+v"(vp4)::"memory");
#pragma nounroll
      for (int it = 0; it < SPIN_CAP; ++it) {
        if (__all(t0[1] == (u32)e1 && t1[1] == (u32)e1 && f[0] == (u32)e1 && f[1] == (u32)(e1 >> 32))) break;
        asm volatile("global_load_dwordx2 %2, %5, %7 sc1\n\tglobal_load_dwordx2 %0, %3, %6 sc1\n\tglobal_load_dwordx2 %1, %4, %6 sc1\n\ts_waitcnt vmcnt(0)"
                     : "=&v"(t0), "=&v"(t1), "=&v"(f) : "v"(va0), "v"(va1), "v"(vf), "s"(ws + WS_LN1), "s"(ws + WS_HFLAG) : "memory");
      }
      *(u32*)(smem + vt0) = t0[0] << 16;
      *(u32*)(smem + vt1) = t1[0] << 16;
      *(float*)(smem + vt0 + 384) = __builtin_amdgcn_rcpf(hi_f(t0[0]));
      *(float*)(smem + vt1 + 384) = __builtin_amdgcn_rcpf(hi_f(t1[0]));
      if (lane < 20 && vp4[1] == (u32)e1 && vp4[3] == (u32)e1) *(u32x2*)(smem + avp) = (u32x2){vp4[0], vp4[2]};
      asm volatile("" ::: "memory");
      WO_RUN(14)
      WO_RUN(15)
      LDS_BARRIER();
      WO_RUN(16)
      WO_RUN(17)
    }
    __builtin_amdgcn_s_setprio(0);
  } else {
    const int cw = w - 4;
    prepv(lane);
    preps(mt, hg);
    if (cw >= 4) __builtin_amdgcn_s_setprio(1);
    const int xa0 = XRING + r16 * 128 + ((g ^ ((r16 >> 1) & 7)) * 16);
    const u32 m1 = r16 == 0 ? 0u : 1u;
    const u32 xz0 = r16 == 0 ? (u32)ONES_OFF : (u32)xa0;
    int wa[2];
#pragma unroll
    for (int c = 0; c < 2; ++c) {
      const int ct = cw < 7 ? 2 * cw + c : 14;
      const int seg = ct / 5, tt = ct - seg * 5;
      const int G = tt < 4 ? 5 * tt + p4 : 5 * p4 + 4;
      wa[c] = (16 * (g >> 1) + 4 * (g & 1) + q4) * 512 + ((seg * 160 + G * 8) ^ ((4 * (g & 1) + q4) << 5));
    }
    auto run = [&](auto tyc) __attribute__((always_inline)) {
      constexpr int TY = decltype(tyc)::value;
      constexpr int NW = TY == 2 ? 1 : 2;
      f32x4 acc[2][6];
#pragma unroll
      for (int c = 0; c < 2; ++c)
#pragma unroll
        for (int r = 0; r < 6; ++r) acc[c][r] = (f32x4){0.f, 0.f, 0.f, 0.f};
      u32x4 xA[6], xB[6];
      u32x2 wA[4], wB[4];
#define RX(D, A, O) asm volatile("ds_read_b128 %0, %1 offset:%2" : "=v"(D) : "v"(A), "n"(O) : "memory");
#define RW(D, A, O) asm volatile("ds_read_b64_tr_b16 %0, %1 offset:%2" : "=v"(D) : "v"(A), "n"(O) : "memory");
#define MM(C, R, XF, WF)                                                                           \
  if ((C) < NW) {                                                                                    \
    const bf16x8 wf_ = mk8(WF[2 * (C)][0], WF[2 * (C)][1], WF[2 * (C) + 1][0], WF[2 * (C) + 1][1]); \
    const bf16x8 xf_ = __builtin_bit_cast(bf16x8, XF[R]);                                          \
    acc[C][R] = TY == 0 ? MFMA(wf_, xf_, acc[C][R]) : MFMA(xf_, wf_, acc[C][R]);                   \
    __builtin_amdgcn_sched_barrier(0);                                                             \
  }
#define READS(QX, QW)                                                                              \
  RW(QW[0], aw0, 0) RW(QW[1], aw0, 4096) RX(QX[1], ax, 2048)                                       \
  if (NW == 2) { RW(QW[2], aw1, 0) RW(QW[3], aw1, 4096) }                                          \
  RX(QX[2], ax, 4096) RX(QX[3], ax, 6144) RX(QX[4], ax, 8192) RX(QX[5], ax, 10240) RX(QX[0], az, 0)
#define ARRIVED(PX, PW)                                                                            \
  if (NW == 2)                                                                                     \
    asm volatile("s_waitcnt lgkmcnt(0)" : "+v"(PX[0]), "+v"(PX[1]), "+v"(PX[2]), "+v"(PX[3]), "+v"(PX[4]), "+v"(PX[5]), \
                 "+v"(PW[0]), "+v"(PW[1]), "+v"(PW[2]), "+v"(PW[3])::"memory");                    \
  else                                                                                             \
    asm volatile("s_waitcnt lgkmcnt(0)" : "+v"(PX[0]), "+v"(PX[1]), "+v"(PX[2]), "+v"(PX[3]), "+v"(PX[4]), "+v"(PX[5]), \
                 "+v"(PW[0]), "+v"(PW[1])::"memory");                                              \
  __builtin_amdgcn_sched_barrier(0);
#define SB __builtin_amdgcn_sched_barrier(0);
#define WT(N, ...) asm volatile("s_waitcnt lgkmcnt(" #N ")" : __VA_ARGS__::"memory"); SB
#define HSTAGE2(PX, PW, QX, QW, NX)                                                                \
  BAR                                                                                              \
  WT(7, "+v"(PW[0]), "+v"(PW[1]), "+v"(PX[1]))                                                     \
  MM(0, 1, PX, PW) RW(QW[0], aw0, 0)                                                               \
  WT(6, "+v"(PW[2]), "+v"(PW[3]))                                                                  \
  MM(1, 1, PX, PW) RW(QW[1], aw0, 4096)                                                            \
  WT(6, "+v"(PX[2]))                                                                               \
  MM(0, 2, PX, PW) RX(QX[1], ax, 2048) SB                                                          \
  MM(1, 2, PX, PW) RW(QW[2], aw1, 0)                                                               \
  WT(7, "+v"(PX[3]))                                                                               \
  MM(0, 3, PX, PW) RW(QW[3], aw1, 4096) SB                                                         \
  MM(1, 3, PX, PW) RX(QX[2], ax, 4096)                                                             \
  WT(6, "+v"(PX[4]), "+v"(PX[5]), "+v"(PX[0]))                                                     \
  MM(0, 4, PX, PW) RX(QX[3], ax, 6144) SB                                                          \
  MM(1, 4, PX, PW) RX(QX[4], ax, 8192) SB                                                          \
  MM(0, 5, PX, PW) RX(QX[5], ax, 10240) SB                                                         \
  MM(1, 5, PX, PW) RX(QX[0], az, 0) SB                                                             \
  MM(0, 0, PX, PW)                                                                                 \
  NX asm volatile("" : "+v"(aw0), "+v"(aw1), "+v"(ax), "+v"(az), "+s"(so), "+s"(xo)); SB           \
  MM(1, 0, PX, PW)
#define HSTAGE1(PX, PW, QX, QW, NX)                                                                \
  BAR                                                                                              \
  ARRIVED(PX, PW)                                                                                  \
  MM(0, 1, PX, PW) RW(QW[0], aw0, 0) RW(QW[1], aw0, 4096) SB                                       \
  MM(0, 2, PX, PW) RX(QX[1], ax, 2048) RX(QX[2], ax, 4096) SB                                      \
  MM(0, 3, PX, PW) RX(QX[3], ax, 6144) RX(QX[4], ax, 8192) SB                                      \
  MM(0, 4, PX, PW) RX(QX[5], ax, 10240) RX(QX[0], az, 0) SB                                        \
  MM(0, 5, PX, PW)                                                                                 \
  NX asm volatile("" : "+v"(aw0), "+v"(ax), "+v"(az), "+s"(so), "+s"(xo)); SB                      \
  MM(0, 0, PX, PW)
#define HSTAGE(...) if (NW == 2) { HSTAGE2(__VA_ARGS__) } else { HSTAGE1(__VA_ARGS__) }
      u32 aw0 = wa[0], aw1 = wa[1], ax = xa0, az = xz0;
      asm volatile("s_waitcnt vmcnt(0)\n\ts_barrier" ::: "memory");
      LSTAMP(20)
      READS(xA, wA)
      aw0 += HSLOT; aw1 += HSLOT; ax ^= 64; az ^= 64;
      int so = 2 * HSLOT, xo = XSLOT;
#pragma unroll 1
      for (int s = 0; s < 9; ++s) {
        HSTAGE(xA, wA, xB, wB, aw0 = wa[0] + so; aw1 = wa[1] + so; ax = xa0 + xo; az = xz0 + xo * m1;
               so = so >= 4 * HSLOT ? 0 : so + 2 * HSLOT; xo = xo >= 3 * XSLOT ? 0 : xo + XSLOT;)
        HSTAGE(xB, wB, xA, wA, aw0 += HSLOT; aw1 += HSLOT; ax ^= 64; az ^= 64;)
      }
      {
        HSTAGE(xA, wA, xB, wB, )
        ARRIVED(xB, wB)
        BAR
        SB
        LSTAMP(29)
      }
      char* ex = smem + EXO + cw * 6144 + lane * 8;
      if (TY == 0) {
        float st[6];
        const u32 ta = TAB_OFF + r16 * 4;
#define RT(R) asm volatile("ds_read_b32 %0, %1 offset:%2" : "=v"(st[R]) : "v"(ta), "n"(64 * (R)) : "memory");
        RT(1) RT(2) RT(3) RT(4) RT(5) RT(0)
        float cs[2][4];
#define FMS(A, M, CS) ((A) - (M) * (CS))
#define EP0(C, R)                                                                                  \
  {                                                                                                \
    const u32 a = cvt2((f32x2){FMS(acc[C][R][0], st[R], cs[C][0]), FMS(acc[C][R][1], st[R], cs[C][1])}); \
    const u32 b = cvt2((f32x2){FMS(acc[C][R][2], st[R], cs[C][2]), FMS(acc[C][R][3], st[R], cs[C][3])}); \
    *(u32x2*)(ex + (C) * 3072 + (R) * 512) = (u32x2){a, b};                                        \
    SB                                                                                             \
  }
        MM(0, 0, xB, wB) MM(1, 0, xB, wB) MM(0, 1, xB, wB) MM(1, 1, xB, wB)
#pragma unroll
        for (int i = 0; i < 4; ++i) cs[0][i] = dppf<0x150>(acc[0][0][i]);
        SB
        MM(0, 2, xB, wB)
#pragma unroll
        for (int i = 0; i < 4; ++i) cs[1][i] = dppf<0x150>(acc[1][0][i]);
        WT(0, "+v"(st[0]), "+v"(st[1]), "+v"(st[2]), "+v"(st[3]), "+v"(st[4]), "+v"(st[5]))
        MM(1, 2, xB, wB) EP0(0, 1)
        MM(0, 3, xB, wB) EP0(1, 1)
        MM(1, 3, xB, wB) EP0(0, 2)
        MM(0, 4, xB, wB) EP0(1, 2)
        MM(1, 4, xB, wB) EP0(0, 3)
        MM(0, 5, xB, wB) EP0(1, 3)
        MM(1, 5, xB, wB)
        if (cw >= 2) {
          EP0(1, 0)
          if (cw >= 3) EP0(0, 0)
        }
        EP0(0, 4) EP0(1, 4) EP0(0, 5) EP0(1, 5)
      } else {
        f32x4 mbv[6];
        const u32 ta = TAB_OFF + g * 16;
#define RV(R) RX(mbv[R], ta, 64 * (R))
        RV(1) RV(2) RV(3) RV(4) RV(5) RV(0)
        float cs[2];
#define CS1(C)                                                                                     \
  if ((C) < NW) {                                                                                  \
    const u32 v0 = __float_as_uint(acc[C][0][0]);                                                  \
    const u32 v1 = __builtin_amdgcn_permlane16_swap(v0, v0, false, false)[0];                      \
    cs[C] = __uint_as_float(__builtin_amdgcn_permlane32_swap(v1, v1, false, false)[0]);            \
    SB                                                                                             \
  }
#define EP1(C, R)                                                                                  \
  if ((C) < NW) {                                                                                  \
    const f32x4 v = (f32x4){FMS(acc[C][R][0], mbv[R][0], cs[C]), FMS(acc[C][R][1], mbv[R][1], cs[C]), FMS(acc[C][R][2], mbv[R][2], cs[C]), FMS(acc[C][R][3], mbv[R][3], cs[C])}; \
    *(u32x2*)(ex + (C) * 3072 + (R) * 512) = (u32x2){cvt2((f32x2){v[0], v[1]}), cvt2((f32x2){v[2], v[3]})}; \
    SB                                                                                             \
  }
        MM(0, 0, xB, wB) MM(1, 0, xB, wB) MM(0, 1, xB, wB) MM(1, 1, xB, wB)
        CS1(0)
        MM(0, 2, xB, wB)
        CS1(1)
        WT(0, "+v"(mbv[0]), "+v"(mbv[1]), "+v"(mbv[2]), "+v"(mbv[3]), "+v"(mbv[4]), "+v"(mbv[5]))
        MM(1, 2, xB, wB) EP1(0, 1)
        MM(0, 3, xB, wB) EP1(1, 1)
        MM(1, 3, xB, wB) EP1(0, 2)
        MM(0, 4, xB, wB) EP1(1, 2)
        MM(1, 4, xB, wB) EP1(0, 3)
        MM(0, 5, xB, wB) EP1(1, 3)
        MM(1, 5, xB, wB)
        EP1(0, 0) EP1(1, 0) EP1(0, 4) EP1(1, 4) EP1(0, 5) EP1(1, 5)
      }
    };
    if (cw < 5) run(icst<0>{});
    else if (cw < 7) run(icst<1>{});
    else run(icst<2>{});
  }
  __builtin_amdgcn_s_setprio(0);
  const bool live = r0 + rt * 16 < SEQ;
  if (w >= 2) { LSTAMP(2) }
  if (!live) {
    if (w >= 2) LDS_BARRIER();
    if (w != 7) {
      asm volatile("s_waitcnt vmcnt(0)" ::: "memory");
      FLUSH_STAMPS
      return;
    }
  } else if (w >= 2)
    asm volatile(ATT_ASM
                 :: [kh] "s"(kh), [l16] "v"(l16), [os] "s"(os), [os4] "s"(os4), [b0] "v"(bias[0]), [b1] "v"(bias[1]),
                    [s0] "v"(sel[0]), [s1] "v"(sel[1]), [s2] "v"(sel[2]), [s3] "v"(sel[3]), [e4] "v"(e4), [eh] "v"(eh), [aq] "v"(aq),
                    [aka] "v"(aka), [akb] "v"(akb), [arec] "v"(arec), [vpp] "v"(vpp), [sst] "s"(sst)
                 : "memory", "s41", "s46", "s47", "s48", "s49", "s50", "s51", "scc", "v24", "v25", "v26", "v27", "v28", "v29", "v30", "v31", "v32", "v33", "v34", "v35", "v36", "v37", "v38", "v39", "v40", "v41", "v42", "v43", "v44", "v45", "v46", "v47", "v48", "v49", "v50", "v51", "v52", "v53", "v54", "v55", "v56", "v57", "v58", "v59", "v60", "v61", "v62", "v63", "v64", "v65", "v66", "v67", "v68", "v69", "v70", "v71", "v72", "v73", "v74", "v75", "v76", "v77", "v78", "v79", "v80", "v81", "v82", "v83", "v84", "v85", "v86", "v87");
  if (w >= 2) { LSTAMP(4) }
  u32x4 hwa, hwb;
  u32x2 hwc;
#define STP(I)
#define CNT0
#define CNT1
#define STPOUT
#define STPOUT2
#define STPEND
  const int grp = rt < 3 ? 0 : 1;
  if (w < 8) {
    const int rmax = mt == 25 ? 2 : 4;
    const int rk = rt < rmax ? rt : rmax, rd = (rt ^ 1) < rmax ? (rt ^ 1) : rmax;
    u32 ta, tk;
    if (w == 0) asm volatile("s_waitcnt vmcnt(0)" ::: "memory");
    if (w == 2 || w == 3) asm volatile("s_waitcnt vmcnt(3)" ::: "memory");
    asm volatile("s_waitcnt lgkmcnt(0)\n\ts_barrier\n\t" OP_ASM STPEND
                 : "=&{v[84:87]}"(hwa), "=&{v[88:91]}"(hwb), "=&{v[92:93]}"(hwc), [ta] "=&v"(ta), [tk] "=&v"(tk) STPOUT
                 : [w] "s"(w), [l16] "v"(l16), [oa0] "s"(oa[0]), [so0] "s"(so_[0]), [oa1] "s"(oa[1]), [so1] "s"(so_[1]), [oa2] "s"(oa[2]), [so2] "s"(so_[2]), [oa3] "s"(oa[3]), [so3] "s"(so_[3]), [oa4] "s"(oa[4]), [so4] "s"(so_[4]), [oa5] "s"(oa[5]), [so5] "s"(so_[5]), [oa6] "s"(oa[6]), [so6] "s"(so_[6]), [oa7] "s"(oa[7]), [so7] "s"(so_[7]), [oa8] "s"(oa[8]), [so8] "s"(so_[8]), [oa9] "s"(oa[9]), [so9] "s"(so_[9]),
                   [pm] "v"(pm), [pf] "v"(pf), [ra] "v"(w < 3 ? (u32)(CTL_OFF + 72) : hi), [e4] "v"(e4),
                   [aw] "v"((lane & 1) ? (u32)(WR_OFF + g * 40) : (u32)ONES_OFF),
                   [vk] "v"(l16 + rk * 20480), [vd] "v"(l16 + rd * 20480), [sk] "s"((w & 1) * 6144),
                   [sd] "s"(EXO + (w < 4 ? 4 + w : rt ^ 1) * 5120), [sr] "s"(EXO + rt * 5120),
                   [sc] "s"(CTL_OFF + 48 + (w < 4 ? 4 : rt ^ 1) * 4), [sp] "s"(PST_OFF + rt * 256), [sa] "s"(CTL_OFF + 8 + grp * 4),
                   [sst] "s"(sst), [cp] "s"(ws + WS_PC + (2 * mt + grp) * 1280 + hg * 160), [oback] "n"(OBACK), [cap] "n"(SPIN_CAP)
                 : "memory", "vcc", "scc", "s43", "v24", "v25", "v26", "v27", "v28", "v29", "v30", "v31", "v32", "v33", "v34", "v35", "v36", "v37", "v38", "v39", "v40", "v41", "v42", "v43", "v44", "v45", "v46", "v47", "v48", "v49", "v50", "v51", "v52", "v53", "v54", "v55", "v56", "v57", "v58", "v59", "v60", "v61", "v62", "v63", "v64", "v65", "v66", "v67", "v68", "v69", "v70", "v71", "v72", "v73", "v74", "v75", "v76", "v77", "v78", "v79", "v80", "v81", "v82", "v83", "v94", "v95", "v96", "v97", "v98", "v99", "v100", "v101", "v102", "v103", "v104", "v105", "v106", "v107", "v108", "v109", "v110", "v111", "v112", "v113", "v114", "v115", "v116", "v117", "v118", "v119", "v120", "v121", "v122", "v123", "v124", "v125", "v126", "v127", "v128", "v129", "v130", "v131", "v132", "v133", "v134", "v135", "v136", "v137", "v138", "v139", "v140", "v141", "v142", "v143", "v144", "v145", "v146", "v147", "v148", "v149", "v150", "v151", "v152", "v153", "v154", "v155", "v156", "v157", "v158", "v159");
    if (!live || w < 3) {
      FLUSH_STAMPS
      return;
    }
  } else {
    if (w == 8 || w == 11) {
      const int cg = w == 11, gid = 2 * mt + cg;
      const char* sp = sst - hg * 1024;
      const char* sp2 = sp + (cg ? 0 : 7936);
      const u32 vq = (lane >> 3) * 1024 + (lane & 7) * 16 + (cg ? 640 : 768);
      u32x4 pq[3];
      u64 tb;
#define PLOADS "global_load_dwordx4 %0, %4, %5 sc1\n\tglobal_load_dwordx4 %1, %4, %5 offset:128 sc1\n\tglobal_load_dwordx4 %2, %4, %6 sc1\n\t"
      asm volatile("s_setprio 3\n\ts_waitcnt lgkmcnt(0)\n\ts_barrier\n\ts_sleep %7\n\t" PLOADS "s_memrealtime %3\n\ts_setprio 1"
                   : "=&v"(pq[0]), "=&v"(pq[1]), "=&v"(pq[2]), "=&s"(tb) : "v"(vq), "s"(sp), "s"(sp2), "n"(PDEL1) : "memory");
      int lane2 = lane;
      asm volatile("" : "+v"(lane2));
      const int n16 = lane2 & 15;
      u32x4 sl;
      u32x2 km;
#pragma unroll
      for (int e = 0; e < 4; ++e) sl[e] = n16 == 2 * e ? 0x3F80u : (n16 == 2 * e + 1 ? 0x3F800000u : 0u);
      u32 rid = (u32)((((lane2 >> 4) & 1) * 2 + (lane2 >> 5)) * 4 + (lane2 & 3)) << 10;
      u32 rid1 = rid + (16 << 10), rid2 = rid + (cg ? 0 : 32 << 10), msk = n16 < 4 ? 0xFFFF0000u : 0u;
      u32 ah = HIM + cg * 7680 + (lane2 < 10 ? lane2 : 9) * 16;
      km[1] = *(const u32*)(smem + CTL_OFF);
      const char* kp = ws + WS_PK + gid * 6144 + hg * 768;
      const char* cp = ws + WS_PC + gid * 1280 + hg * 160;
      asm volatile("s_waitcnt lgkmcnt(0)" : "+s"(tb), "+s"(kp), "+s"(cp)::"memory");
      km[0] = ((u32)tb >> 1) & 1023u;
      asm volatile("s_setprio 3\n\ts_waitcnt vmcnt(0)" : "+v"(pq[0]), "+v"(pq[1]), "+v"(pq[2]), "+v"(sl), "+v"(rid), "+v"(rid1), "+v"(rid2), "+v"(msk), "+v"(ah), "+v"(km)::"memory");
      LSTAMP(44)
#define PMISS __any(max(max(max(pq[0][0], pq[0][2]), max(pq[1][0], pq[1][2])), max(pq[2][0], pq[2][2])) == SENT)
      if (__builtin_expect(PMISS, 0)) {
#pragma nounroll
        for (int it = 0; it < SPIN_CAP; ++it) {
          u64 t2;
          asm volatile(PLOADS "s_waitcnt vmcnt(0)" : "=&v"(pq[0]), "=&v"(pq[1]), "=&v"(pq[2]), "=&s"(t2) : "v"(vq), "s"(sp), "s"(sp2) : "memory");
          if (!PMISS) break;
          __builtin_amdgcn_s_sleep(2);
        }
        LSTAMP(41)
      }
      u32 pkey = 0;
#pragma unroll
      for (int t = 0; t < 3; ++t) {
        const f32x4 ys = MFMA(__builtin_bit_cast(bf16x8, pq[t]), __builtin_bit_cast(bf16x8, sl), ((f32x4){0.f, 0.f, 0.f, 0.f}));
        const float y = tr_sum(ys);
        pkey = max(pkey, (mono(y * __builtin_amdgcn_rcpf(dppf<0x104>(y))) & msk) | (t == 0 ? rid : (t == 1 ? rid1 : rid2)));
      }
      pkey = max(pkey, dppu<0xB1>(pkey));
      pkey = max(pkey, dppu<0x4E>(pkey));
      pkey = max(max((u32)__builtin_amdgcn_readlane(pkey, 0), (u32)__builtin_amdgcn_readlane(pkey, 16)),
                 max((u32)__builtin_amdgcn_readlane(pkey, 32), (u32)__builtin_amdgcn_readlane(pkey, 48)));
      km[0] |= pkey;
      asm volatile("s_mov_b32 exec_lo, 0x01010101\n\ts_mov_b32 exec_hi, 0x0101\n\tglobal_store_dwordx2 %0, %1, %2 sc1\n\ts_mov_b64 exec, -1\n\ts_setprio 2" ::"v"(l16), "v"(km), "s"(kp) : "memory");
      LSTAMP(42)
      ah += ((pkey >> 10) & 63u) * 160;
      if (lane == 0) *(u32*)(smem + CTL_OFF + 24 + cg * 4) = ah;
      __builtin_amdgcn_s_setprio(0);
#pragma nounroll
      for (int it = 0; it < 64; ++it) {
        if ((int)((u32)__builtin_amdgcn_s_memrealtime() - (u32)tb) >= PSL) break;
        __builtin_amdgcn_s_sleep(2);
      }
      asm volatile("s_mov_b32 s40, %[cap]\n"
                   "1:\n\tds_read_b32 v100, %[ac]\n\tds_read_b32 v101, %[ac] offset:24\n\tds_read_b128 v[96:99], %[ah]\n\ts_sub_u32 s40, s40, 1\n\ts_cbranch_scc1 2f\n\ts_waitcnt lgkmcnt(0)\n\t"
                   "v_cmp_gt_u32 vcc, %[nf], v100\n\ts_cbranch_vccz 2f\n\ts_sleep 2\n\ts_branch 1b\n"
                   "2:\n\ts_waitcnt lgkmcnt(0)\n\tv_cmp_eq_u32 vcc, 0, v101\n\ts_cbranch_vccz 3f\n\t"
                   "s_mov_b64 exec, 0x3ff\n\tglobal_store_dwordx4 %[l16], v[96:99], %[cp] sc1\n\ts_mov_b64 exec, -1\n3:"
                   ::[cap] "n"(SPIN_CAP * 8), [ac] "v"((u32)(CTL_OFF + 8 + cg * 4)), [ah] "v"(ah), [nf] "s"(3 - cg), [l16] "v"(l16), [cp] "s"(cp)
                   : "memory", "vcc", "scc", "s40", "v96", "v97", "v98", "v99", "v100", "v101");
      LSTAMP(43)
      FLUSH_STAMPS
      return;
    }
    LDS_BARRIER();
    if (w == 10 && mt == 25) {
      FLUSH_STAMPS
      return;
    }
    const int cg = w - 9, gid = 2 * mt + cg;
    const int nr = cg ? 31 : 47;
    const int lr = lane < nr ? lane : nr;
    const int lrow = cg * 48 + lr;
    const int li = (lane & 31) < 20 ? (lane & 31) : 19;
    asm volatile(COL_ASM STPEND
                 : STPOUT2
                 : [lq] "v"((u32)(lr * 16 + hg * 1024)), [ac] "v"((u32)(CTL_OFF + 8 + cg * 4)), [nf] "s"(3 - cg),
                   [sp] "s"(ws + WS_STAT + (size_t)gid * 8192), [ao] "v"((u32)(PST_OFF + lrow * 16)), [rdm] "s"(1.0f / DM),
                   [swr] "v"(*(const float*)(smem + CTL_OFF + 40)), [msk] "s"(0xFFFFF000u), [rid] "v"(0x800u | (u32)(2047 - (r0 + lrow))),
                   [at] "v"((u32)(TAB_OFF + lrow * 8)), [zr] "v"(l16), "{v99}"(*(const u32*)(smem + CTL_OFF)),
                   [kp] "s"(ws + WS_PK + gid * 6144 + hg * 768 + 8), [ak] "v"((u32)(CTL_OFF + 16 + cg * 4)), [rb] "s"(2047 - r0 - cg * 48),
                   [ah] "v"((u32)(HIM + cg * 7680 + li * 8)), [co] "v"((u32)((lane < 32 ? 1280 : 0) + li * 8)),
                   [cp] "s"(ws + WS_CAND + gid * 2560 + hg * 160), [pdel] "n"(PDEL2), [cap] "n"(SPIN_CAP)
                 : "memory", "vcc", "scc", "s40", "s41", "s42", "s43", "s44", "s45", "s46", "s47", "v64", "v65", "v66", "v67", "v68", "v69", "v70", "v71", "v72", "v73", "v74", "v75", "v76", "v77", "v78", "v79", "v80", "v81", "v82", "v83", "v84", "v85", "v86", "v87", "v88", "v89", "v90", "v91", "v92", "v93", "v94", "v95", "v96", "v97", "v98", "v100", "v101", "v102", "v103", "v104", "v105", "v106", "v107", "v108", "v109", "v110", "v111", "v112", "v113", "v114", "v115", "v116", "v117", "v118", "v119", "v120", "v121", "v122", "v123");
    FLUSH_STAMPS
    return;
  }
  const u32 vrow = ((rt * 16 + r16) * DM + 20 * g) * 2;
  const u32 hw[10] = {hwa[0], hwa[1], hwa[2], hwa[3], hwb[0], hwb[1], hwb[2], hwb[3], hwc[0], hwc[1]};
  u32 tilekey = 0;
#pragma nounroll
  for (int it = 0; it < SPIN_CAP; ++it) {
    tilekey = lds_ld32(CTL_OFF + 16 + grp * 4);
    if (tilekey != 0u) break;
    __builtin_amdgcn_s_sleep(1);
  }
  LSTAMP(11)
  const f32x2 st2 = *(const f32x2*)(smem + TAB_OFF + (rt * 16 + r16) * 8);
  const bool held = (tilekey & 2047u) == (u32)(2047 - (r0 + rt * 16 + r16));
  u32 ok[10];
#pragma unroll
  for (int e = 0; e < 10; ++e) ok[e] = cvt2(unpk(hw[e]) + unpk(NORM2(hw[e], st2[0], st2[1])));
  const char* obase = (const char*)out + (size_t)r0 * 1280 + hg * 160;
  __builtin_amdgcn_s_sleep(OSL);
  if (!held) {
    asm volatile("global_store_dwordx4 %0, %1, %2\n\ts_nop 1" ::"v"(vrow), "v"((u32x4){ok[0], ok[1], ok[2], ok[3]}), "s"(obase) : "memory");
    asm volatile("global_store_dwordx4 %0, %1, %2 offset:16\n\ts_nop 1" ::"v"(vrow), "v"((u32x4){ok[4], ok[5], ok[6], ok[7]}), "s"(obase) : "memory");
    asm volatile("global_store_dwordx2 %0, %1, %2 offset:32\n\ts_nop 1" ::"v"(vrow), "v"((u32x2){ok[8], ok[9]}), "s"(obase) : "memory");
  }
  LSTAMP(15)
  FLUSH_STAMPS
}

extern "C" void kernel_launch(void* const* d_in, const int* in_sizes, int n_in,
                              void* d_out, int out_size, void* d_ws, size_t ws_size,
                              hipStream_t stream) {
  const u16* x = (const u16*)d_in[0];
  const u16* wqkv = (const u16*)d_in[1];
  const u16* wo = (const u16*)d_in[2];
  const u16* w_r = (const u16*)d_in[4];
  const u16* w_gate = (const u16*)d_in[5];
  const u16* w_up = (const u16*)d_in[6];
  const u16* w_down = (const u16*)d_in[7];
  (void)hipFuncSetAttribute((const void*)fused_block, hipFuncAttributeMaxDynamicSharedMemorySize, LDS_TOTAL);
  fused_block<<<256, 768, LDS_TOTAL, stream>>>(x, wqkv, wo, w_r, w_gate, w_up, w_down, (u16*)d_out, (char*)d_ws);
}
```

```cpp
#include <hip/hip_runtime.h>
#include <hip/hip_bf16.h>
#include <stdint.h>

#define SEQ 2048
#define DM 640
#define FF 1536

typedef __attribute__((ext_vector_type(8))) short bf16x8;
typedef __attribute__((ext_vector_type(4))) short s16x4;
typedef __attribute__((ext_vector_type(4))) float f32x4;
typedef __attribute__((ext_vector_type(2))) float f32x2;
typedef __attribute__((ext_vector_type(2))) __bf16 bf16x2_t;
typedef __attribute__((address_space(3))) s16x4* lds_s16x4_p;
typedef unsigned short u16;
typedef unsigned int u32;
typedef unsigned long long u64;
typedef __attribute__((ext_vector_type(4))) unsigned int u32x4;
typedef __attribute__((ext_vector_type(2))) unsigned int u32x2;

#define LSTAMP(i)
#define FLUSH_STAMPS

#define MAPV 2
#define HK0 15
#define RGAP 6
#define CGAP 6
#define KGAP 7
#define CDEL 10
#define PDEL2 7
#define OBACK 2
#define OSL 10
#define PDELAY 220
#define FDELAY 450
#define KSTART 1050
#define PGAP 5
#define PDEL1 3
#define PSL 200
#define VDELAY 150
#define FNT " nt"
#define CCD 306
#define PPRIO "3"
#define OFL ""
#define TQD 140
#define KCD 290
#define DRY 0
#define PCD 206
#define PTHR 0.91f
#define GADD 58
#define OORD 0, 4, 5, 6, 7, 1, 2, 3
#define CODE_LINES 275

#define LDS_BARRIER() do { __builtin_amdgcn_sched_barrier(0); asm volatile("s_waitcnt lgkmcnt(0)\n\ts_barrier" ::: "memory"); __builtin_amdgcn_sched_barrier(0); } while (0)
__device__ __forceinline__ float bf2f(u16 v) { return __uint_as_float(((u32)v) << 16); }
__device__ __forceinline__ u16 f2bf(float f) { return __builtin_bit_cast(u16, (__bf16)f); }
__device__ __forceinline__ float rbf(float f) { return bf2f(f2bf(f)); }
__device__ __forceinline__ float lo_f(u32 w) { return __uint_as_float(w << 16); }
__device__ __forceinline__ float hi_f(u32 w) { return __uint_as_float(w & 0xFFFF0000u); }
__device__ __forceinline__ u32 cvt2(f32x2 v) { return __builtin_bit_cast(u32, __builtin_convertvector(v, bf16x2_t)); }
__device__ __forceinline__ f32x2 unpk(u32 w) { return (f32x2){lo_f(w), hi_f(w)}; }
#define NORM2(HV, MB, RD) cvt2(unpk(cvt2(unpk(HV) - (f32x2){MB, MB})) * (f32x2){RD, RD})
__device__ __forceinline__ s16x4 tr_read(const char* p) {
  return __builtin_amdgcn_ds_read_tr16_b64_v4i16((lds_s16x4_p)(p));
}
template <int V> struct icst { static constexpr int value = V; };
template <int CTRL>
__device__ __forceinline__ float dppf(float v) {
  return __builtin_bit_cast(float, __builtin_amdgcn_update_dpp(0, __builtin_bit_cast(int, v), CTRL, 0xf, 0xf, true));
}
__device__ __forceinline__ float row8_sum(float v) {
  v += dppf<0xB1>(v);
  v += dppf<0x4E>(v);
  v += dppf<0x141>(v);
  return v;
}
__device__ __forceinline__ float row16_sum(float v) {
  v = row8_sum(v);
  v += dppf<0x140>(v);
  return v;
}
__device__ __forceinline__ float xg_sum(float v) {
  u32 a = __float_as_uint(v);
  auto r = __builtin_amdgcn_permlane32_swap(a, a, false, false);
  v = __uint_as_float(r[0]) + __uint_as_float(r[1]);
  a = __float_as_uint(v);
  auto r2 = __builtin_amdgcn_permlane16_swap(a, a, false, false);
  return __uint_as_float(r2[0]) + __uint_as_float(r2[1]);
}
__device__ __forceinline__ float tr_sum(f32x4 ys) {
  auto r01 = __builtin_amdgcn_permlane32_swap(__float_as_uint(ys[0]), __float_as_uint(ys[1]), false, false);
  auto r23 = __builtin_amdgcn_permlane32_swap(__float_as_uint(ys[2]), __float_as_uint(ys[3]), false, false);
  const u32 u = __float_as_uint(__uint_as_float(r01[0]) + __uint_as_float(r01[1]));
  const u32 vv = __float_as_uint(__uint_as_float(r23[0]) + __uint_as_float(r23[1]));
  auto r2 = __builtin_amdgcn_permlane16_swap(u, vv, false, false);
  return __uint_as_float(r2[0]) + __uint_as_float(r2[1]);
}
__device__ __forceinline__ float wave_sum(float v) { return xg_sum(row16_sum(v)); }
template <int CTRL>
__device__ __forceinline__ u32 dppu(u32 v) { return (u32)__builtin_amdgcn_update_dpp(0, (int)v, CTRL, 0xf, 0xf, true); }
__device__ __forceinline__ u32 wave_max(u32 v) {
  v = max(v, dppu<0xB1>(v)); v = max(v, dppu<0x4E>(v)); v = max(v, dppu<0x141>(v)); v = max(v, dppu<0x140>(v));
  return max(max((u32)__builtin_amdgcn_readlane(v, 0), (u32)__builtin_amdgcn_readlane(v, 16)),
             max((u32)__builtin_amdgcn_readlane(v, 32), (u32)__builtin_amdgcn_readlane(v, 48)));
}
__device__ __forceinline__ u32 mono(float f) {
  const u32 b = __float_as_uint(f);
  return b ^ ((u32)((int)b >> 31) | 0x80000000u);
}
__device__ __forceinline__ float dot2b(u32 a, u32 b, float c) {
  return __builtin_amdgcn_fdot2_f32_bf16(__builtin_bit_cast(bf16x2_t, a), __builtin_bit_cast(bf16x2_t, b), c, false);
}
template <int OFF>
__device__ __forceinline__ void dma_m0(u32 voff, const char* sbase, u32 dst) {
  asm volatile("s_mov_b32 m0, %2\n\ts_nop 0\n\tglobal_load_lds_dwordx4 %0, %1 offset:%3"
               :: "v"(voff), "s"(sbase), "s"(dst), "n"(OFF) : "memory");
}
template <int OFF>
__device__ __forceinline__ void dma_nx(u32 voff, const char* sbase) {
  asm volatile("global_load_lds_dwordx4 %0, %1 offset:%2" :: "v"(voff), "s"(sbase), "n"(OFF) : "memory");
}
__device__ __forceinline__ u32 lds_ld32(u32 addr) {
  u32 v;
  asm volatile("ds_read_b32 %0, %1\n\ts_waitcnt lgkmcnt(0)" : "=v"(v) : "v"(addr) : "memory");
  return v;
}
__device__ __forceinline__ u32x4 lds_ld128(u32 addr) {
  u32x4 v;
  asm volatile("ds_read_b128 %0, %1\n\ts_waitcnt lgkmcnt(0)" : "=v"(v) : "v"(addr) : "memory");
  return v;
}
__device__ __forceinline__ bf16x8 mk8(u32 a, u32 b, u32 c, u32 d) {
  return __builtin_bit_cast(bf16x8, (u32x4){a, b, c, d});
}
__device__ __forceinline__ bf16x8 tr_frag(const char* p, int off2) {
  s16x4 b0 = tr_read(p);
  s16x4 b1 = tr_read(p + off2);
  bf16x8 r;
  r[0] = b0[0]; r[1] = b0[1]; r[2] = b0[2]; r[3] = b0[3];
  r[4] = b1[0]; r[5] = b1[1]; r[6] = b1[2]; r[7] = b1[3];
  return r;
}
#define SPIN_CAP 400000
#define MFMA(A, B, C) __builtin_amdgcn_mfma_f32_16x16x32_bf16(A, B, C, 0, 0, 0)

#define HSLOT 16384
#define XRING 98304
#define XSLOT 12288
#define LDS_TOTAL 163840
#define EXO 102400
#define HIM 148480
#define PST_OFF (EXO + 40960)
#define TAB_OFF 161280
#define ONES_OFF 162048
#define CTL_OFF 162176
#define WR_OFF 162304
#define REC_OFF 162816
#define HN_OFF 124928
#define HSL 1344
#define ASL 1056
#define TCH_OFF 162560
#define ACT_OFF 135168
#define KEY_OFF (ACT_OFF + 5120)
#define POST_OFF (KEY_OFF + 256)
#define DOWN_OFF 81920
#define WS_EPOCH 0
#define WS_HFLAG 4096
#define WS_VP 40960
#define WS_LN1 16384
#define WS_FILL 131072
#define WS_O WS_FILL
#define WS_STAT (WS_O + 2621440)
#define WS_CAND (WS_STAT + 425984)
#define WS_PART (WS_CAND + 133120)
#define WS_BXF (WS_PART + 122880)
#define WS_PC (WS_BXF + 26624)
#define WS_PART2 (WS_PC + 66560)
#define WS_PK (4u << 20)
#define SENT 0xFFFFFFFFu

#define GST4(VO, SB, O, V) asm volatile("global_store_dwordx4 %0, %1, %2 offset:%3 sc1\n\ts_nop 1" ::"v"(VO), "v"(V), "s"(SB), "n"(O) : "memory")
#define GST2(VO, SB, O, V) asm volatile("global_store_dwordx2 %0, %1, %2 offset:%3 sc1\n\ts_nop 1" ::"v"(VO), "v"(V), "s"(SB), "n"(O) : "memory")
#define GLD2(D, VO, SB, O) asm volatile("global_load_dwordx2 %0, %1, %2 offset:%3 sc1" : "=v"(D) : "v"(VO), "s"(SB), "n"(O) : "memory")

__device__ __forceinline__ void touch_line(const void* p) {
  asm volatile("s_mov_b32 m0, %1\n\ts_nop 0\n\tglobal_load_lds_dword %0, off" ::"v"(p), "s"(TCH_OFF) : "memory");
}
__device__ __forceinline__ u32 w_off(int i, int lane, int hg) {
  int cc = (lane & 31) ^ (((2 * i + (lane >> 5)) & 7) << 1);
  cc = cc > 29 ? 29 : cc;
  return (lane >> 5) * 3840 + ((cc >= 10 ? 1120 : 0) + (cc >= 20 ? 1120 : 0)) + hg * 160 + cc * 16;
}
#define W_ROW(I) (((((2 * (I)) & 0x13) | (((2 * (I)) & 4) << 1) | (((2 * (I)) & 8) >> 1))) * 3840)
__device__ __forceinline__ u32 x_off(int i, int lane, int r0) {
  const int xr = i * 8 + (lane >> 3);
  int grow = r0 - 16 + xr;
  grow = grow < 0 ? 0 : (grow > SEQ - 1 ? SEQ - 1 : grow);
  return grow * 1280 + (((lane & 7) ^ ((xr >> 1) & 7)) * 16);
}

#define ATT_ASM \
  "ds_read_b32 v28, %[aq] offset:64\n\t" \
  "ds_read_b32 v30, %[aka]\n\t" \
  "ds_read_b32 v31, %[akb]\n\t" \
  "v_mov_b64 v[38:39], 0\n\t" \
  "v_mov_b64 v[42:43], 0\n\t" \
  "s_lshl_b32 s41, %[kh], 5\n\t" \
  "s_lshl_b64 s[46:47], 0xffff, s41\n\t" \
  "s_lshl_b64 s[48:49], s[46:47], 16\n\t" \
  "s_mov_b32 s50, 0xaaaaaaaa\n\t" \
  "s_mov_b32 s51, 0xaaaaaaaa\n\t" \
  "s_waitcnt lgkmcnt(0)\n\t" \
  "s_barrier\n\t" \
  "ds_read_b64 v[36:37], %[eh] offset:512\n\t" \
  "ds_read_b64 v[44:45], %[eh] offset:15360\n\t" \
  "ds_read_b64 v[46:47], %[e4] offset:27648\n\t" \
  "ds_read_b64 v[48:49], %[eh] offset:15872\n\t" \
  "ds_read_b64 v[50:51], %[e4] offset:28160\n\t" \
  "s_mov_b64 exec, s[46:47]\n\t" \
  "ds_read_b64 v[38:39], %[e4] offset:12800\n\t" \
  "s_mov_b64 exec, s[48:49]\n\t" \
  "ds_read_b64 v[42:43], %[e4] offset:12800\n\t" \
  "s_mov_b64 exec, -1\n\t" \
  "ds_read_b64 v[40:41], %[eh] offset:3584\n\t" \
  "ds_read_b64 v[52:53], %[eh] offset:18432\n\t" \
  "ds_read_b64 v[54:55], %[e4] offset:27648\n\t" \
  "ds_read_b64 v[56:57], %[eh] offset:18944\n\t" \
  "ds_read_b64 v[58:59], %[e4] offset:28160\n\t" \
  "v_mul_f32 v28, 0x3ea52b5b, v28\n\t" \
  "v_pk_mul_f32 v[32:33], v[28:29], v[30:31] op_sel_hi:[0,1]\n\t" \
  "s_waitcnt lgkmcnt(6)\n\t" \
  "v_mfma_f32_16x16x32_bf16 v[44:47], v[44:47], v[36:39], 0\n\t" \
  "ds_read2st64_b64 v[60:63], %[eh] offset0:60 offset1:61\n\t" \
  "v_mfma_f32_16x16x32_bf16 v[48:51], v[48:51], v[36:39], 0\n\t" \
  "ds_read2st64_b64 v[64:67], %[eh] offset0:66 offset1:67\n\t" \
  "s_waitcnt lgkmcnt(2)\n\t" \
  "v_mfma_f32_16x16x32_bf16 v[52:55], v[52:55], v[40:43], 0\n\t" \
  "ds_read2st64_b64 v[68:71], %[e4] offset0:84 offset1:85\n\t" \
  "v_mfma_f32_16x16x32_bf16 v[56:59], v[56:59], v[40:43], 0\n\t" \
  "s_mov_b32 exec_lo, 0xfff0ffff\n\t" \
  "s_mov_b32 exec_hi, 0xf000ff00\n\t" \
  "s_nop 0\n\t" \
  "v_mov_b64 v[44:45], v[48:49]\n\t" \
  "s_mov_b32 exec_lo, 0xffc0fffc\n\t" \
  "s_mov_b32 exec_hi, 0xc000fc00\n\t" \
  "v_mov_b64 v[46:47], v[50:51]\n\t" \
  "s_nop 0\n\t" \
  "v_mov_b64 v[54:55], v[58:59]\n\t" \
  "s_mov_b32 exec_lo, 0xfff0ffff\n\t" \
  "s_mov_b32 exec_hi, 0xf000ff00\n\t" \
  "v_mov_b64 v[52:53], v[56:57]\n\t" \
  "s_mov_b64 exec, -1\n\t" \
  "v_cndmask_b32 v44, v44, v45, s[50:51]\n\t" \
  "v_cndmask_b32 v46, v46, v47, s[50:51]\n\t" \
  "v_cndmask_b32 v52, v52, v53, s[50:51]\n\t" \
  "v_cndmask_b32 v54, v54, v55, s[50:51]\n\t" \
  "v_fma_f32 v72, v44, v32, %[b0]\n\t" \
  "v_fma_f32 v73, v46, v33, %[b1]\n\t" \
  "v_fma_f32 v74, v52, v32, %[b0]\n\t" \
  "v_fma_f32 v75, v54, v33, %[b1]\n\t" \
  "v_max_f32 v34, v72, v73\n\t" \
  "v_max_f32 v35, v74, v75\n\t" \
  "ds_read_b128 v[76:79], %[arec]\n\t" \
  "ds_read_b128 v[80:83], %[arec] offset:16\n\t" \
  "v_permlane32_swap_b32 v34, v35\n\t" \
  "v_max_f32 v34, v34, v35\n\t" \
  "v_mov_b32 v35, v34\n\t" \
  "ds_read_b128 v[84:87], %[arec] offset:32\n\t" \
  "s_nop 0\n\t" \
  "v_permlane16_swap_b32 v34, v35\n\t" \
  "v_max_f32 v34, v34, v35\n\t" \
  "v_mov_b32 v35, v34\n\t" \
  "s_nop 1\n\t" \
  "v_permlane32_swap_b32 v34, v35\n\t" \
  "v_sub_f32 v72, v72, v34\n\t" \
  "v_sub_f32 v73, v73, v34\n\t" \
  "v_sub_f32 v74, v74, v35\n\t" \
  "v_sub_f32 v75, v75, v35\n\t" \
  "v_exp_f32 v72, v72\n\t" \
  "v_exp_f32 v73, v73\n\t" \
  "v_exp_f32 v74, v74\n\t" \
  "v_exp_f32 v75, v75\n\t" \
  "v_add_f32 v34, v72, v73\n\t" \
  "v_add_f32 v35, v74, v75\n\t" \
  "v_pk_mul_f32 v[72:73], v[72:73], v[30:31]\n\t" \
  "v_pk_mul_f32 v[74:75], v[74:75], v[30:31]\n\t" \
  "v_permlane32_swap_b32 v34, v35\n\t" \
  "v_add_f32 v34, v34, v35\n\t" \
  "v_mov_b32 v35, v34\n\t" \
  "s_nop 1\n\t" \
  "v_permlane16_swap_b32 v34, v35\n\t" \
  "v_add_f32 v34, v34, v35\n\t" \
  "v_rcp_f32 v34, v34\n\t" \
  "s_nop 0\n\t" \
  "v_mov_b32 v30, v34\n\t" \
  "s_waitcnt lgkmcnt(0)\n\t" \
  "s_nop 0\n\t" \
  "v_permlane32_swap_b32 v34, v30\n\t" \
  "v_pk_mul_f32 v[72:73], v[34:35], v[72:73] op_sel_hi:[0,1]\n\t" \
  "v_pk_mul_f32 v[74:75], v[30:31], v[74:75] op_sel_hi:[0,1]\n\t" \
  "v_cvt_pk_bf16_f32 v72, v72, v73\n\t" \
  "v_cvt_pk_bf16_f32 v74, v74, v75\n\t" \
  "v_perm_b32 v36, 0, v72, %[s0]\n\t" \
  "v_perm_b32 v37, 0, v72, %[s1]\n\t" \
  "v_perm_b32 v38, 0, v72, %[s2]\n\t" \
  "v_perm_b32 v39, 0, v72, %[s3]\n\t" \
  "v_perm_b32 v40, 0, v74, %[s0]\n\t" \
  "v_perm_b32 v41, 0, v74, %[s1]\n\t" \
  "v_mfma_f32_16x16x32_bf16 v[60:63], v[60:63], v[36:39], 0\n\t" \
  "v_perm_b32 v42, 0, v74, %[s2]\n\t" \
  "v_perm_b32 v43, 0, v74, %[s3]\n\t" \
  "v_mfma_f32_16x16x32_bf16 v[52:55], v[68:71], v[36:39], 0\n\t" \
  "s_nop 0\n\t" \
  "v_mfma_f32_16x16x32_bf16 v[64:67], v[64:67], v[40:43], 0\n\t" \
  "v_mfma_f32_16x16x32_bf16 v[56:59], v[68:71], v[40:43], 0\n\t" \
  "s_nop 1\n\t" \
  "v_cvt_pk_bf16_f32 v24, v60, v61\n\t" \
  "v_cvt_pk_bf16_f32 v25, v62, v63\n\t" \
  "s_nop 0\n\t" \
  "v_cvt_pk_bf16_f32 v52, v52, v53\n\t" \
  "v_cvt_pk_bf16_f32 v53, v54, v55\n\t" \
  "v_cvt_pk_bf16_f32 v26, v64, v65\n\t" \
  "v_cvt_pk_bf16_f32 v27, v66, v67\n\t" \
  "global_store_dwordx4 %[l16], v[24:27], %[os] sc1\n\t" \
  "s_mov_b64 exec, s[48:49]\n\t" \
  "v_cvt_pk_bf16_f32 v52, v56, v57\n\t" \
  "v_cvt_pk_bf16_f32 v53, v58, v59\n\t" \
  "s_or_b64 exec, s[46:47], s[48:49]\n\t" \
  "global_store_dwordx2 %[l16], v[52:53], %[os4] sc1\n\t" \
  "s_mov_b64 exec, -1\n\t" \
  "v_mfma_f32_16x16x32_bf16 v[60:63], v[24:27], v[76:79], 0\n\t" \
  "v_mov_b64 v[54:55], 0\n\t" \
  "v_and_b32 v64, 0x7fff7fff, v24\n\t" \
  "v_and_b32 v65, 0x7fff7fff, v25\n\t" \
  "v_and_b32 v66, 0x7fff7fff, v26\n\t" \
  "v_and_b32 v67, 0x7fff7fff, v27\n\t" \
  "v_mfma_f32_16x16x32_bf16 v[60:63], v[52:55], v[80:83], v[60:63]\n\t" \
  "ds_write2st64_b64 %[eh], v[24:25], v[26:27] offset0:1 offset1:7\n\t" \
  "v_mfma_f32_16x16x32_bf16 v[60:63], v[64:67], v[84:87], v[60:63]\n\t" \
  "s_mov_b32 exec_lo, 0x01010101\n\t" \
  "s_mov_b32 exec_hi, 0x01010101\n\t" \
  "s_nop 5\n\t" \
  "v_cvt_pk_bf16_f32 v60, v60, v61\n\t" \
  "v_cvt_pk_bf16_f32 v61, v62, v63\n\t" \
  "v_and_b32 v60, -2, v60\n\t" \
  "global_store_dwordx2 %[vpp], v[60:61], %[sst] sc1\n\t" \
  "s_mov_b64 exec, -1\n\t" \
  ""
#define OP_ASM \
  CNT0 \
  "s_mov_b32 s43, %[cap]\n\t" \
  STP(0) \
  "s_cmp_lt_u32 %[w], 4\n\t" \
  "s_cbranch_scc1 LH_%=\n\t" \
  "s_setprio 3\n\t" \
  "global_load_dwordx4 v[124:127], %[vd], %[oa1] sc1\n\t" \
  "global_load_dwordx4 v[128:131], %[vk], %[oa1] sc1\n\t" \
  "global_load_dwordx4 v[132:135], %[vd], %[oa2] sc1\n\t" \
  "global_load_dwordx4 v[136:139], %[vk], %[oa2] sc1\n\t" \
  "v_add_u32 %[tk], %[sk], %[e4]\n\t" \
  "v_add_u32 %[ta], %[so0], %[pm]\n\t" \
  "ds_read2st64_b64 v[104:107], %[tk] offset0:1 offset1:7\n\t" \
  "v_xor_b32 %[tk], 512, %[tk]\n\t" \
  "ds_read2st64_b64 v[108:111], %[tk] offset0:1 offset1:7\n\t" \
  "ds_read_b64_tr_b16 v[64:65], %[ta] offset:0\n\t" \
  "ds_read_b64_tr_b16 v[66:67], %[ta] offset:3200\n\t" \
  "ds_read_b64_tr_b16 v[68:69], %[ta] offset:8\n\t" \
  "ds_read_b64_tr_b16 v[70:71], %[ta] offset:3208\n\t" \
  "ds_read_b64_tr_b16 v[72:73], %[ta] offset:16\n\t" \
  "ds_read_b64_tr_b16 v[74:75], %[ta] offset:3216\n\t" \
  "ds_read_b64_tr_b16 v[76:77], %[ta] offset:24\n\t" \
  "ds_read_b64_tr_b16 v[78:79], %[ta] offset:3224\n\t" \
  "ds_read_b64_tr_b16 v[80:81], %[ta] offset:32\n\t" \
  "ds_read_b64_tr_b16 v[82:83], %[ta] offset:3232\n\t" \
  "ds_read_b64 v[112:113], %[ra] offset:0\n\t" \
  "ds_read_b64 v[114:115], %[ra] offset:8\n\t" \
  "ds_read_b64 v[116:117], %[ra] offset:16\n\t" \
  "ds_read_b64 v[118:119], %[ra] offset:24\n\t" \
  "ds_read_b64 v[120:121], %[ra] offset:32\n\t" \
  "global_load_dwordx4 v[140:143], %[vd], %[oa3] sc1\n\t" \
  "v_add_u32 %[ta], %[so1], %[pm]\n\t" \
  "s_waitcnt lgkmcnt(5)\n\t" \
  "v_mfma_f32_16x16x32_bf16 v[44:47], v[64:67], v[108:111], 0\n\t" \
  "ds_read_b64_tr_b16 v[84:85], %[ta] offset:0\n\t" \
  "ds_read_b64_tr_b16 v[86:87], %[ta] offset:3200\n\t" \
  "v_mfma_f32_16x16x32_bf16 v[48:51], v[68:71], v[108:111], 0\n\t" \
  "ds_read_b64_tr_b16 v[88:89], %[ta] offset:8\n\t" \
  "ds_read_b64_tr_b16 v[90:91], %[ta] offset:3208\n\t" \
  "v_mfma_f32_16x16x32_bf16 v[52:55], v[72:75], v[108:111], 0\n\t" \
  "ds_read_b64_tr_b16 v[92:93], %[ta] offset:16\n\t" \
  "ds_read_b64_tr_b16 v[94:95], %[ta] offset:3216\n\t" \
  "v_mfma_f32_16x16x32_bf16 v[56:59], v[76:79], v[108:111], 0\n\t" \
  "ds_read_b64_tr_b16 v[96:97], %[ta] offset:24\n\t" \
  "ds_read_b64_tr_b16 v[98:99], %[ta] offset:3224\n\t" \
  "v_mfma_f32_16x16x32_bf16 v[60:63], v[80:83], v[108:111], 0\n\t" \
  "ds_read_b64_tr_b16 v[100:101], %[ta] offset:32\n\t" \
  "ds_read_b64_tr_b16 v[102:103], %[ta] offset:3232\n\t" \
  "s_waitcnt lgkmcnt(10)\n\t" \
  "v_lshlrev_b32 v24, 16, v112\n\t" \
  "v_and_b32 v25, 0xffff0000, v112\n\t" \
  "v_lshlrev_b32 v26, 16, v113\n\t" \
  "v_and_b32 v27, 0xffff0000, v113\n\t" \
  "v_lshlrev_b32 v28, 16, v114\n\t" \
  "v_and_b32 v29, 0xffff0000, v114\n\t" \
  "v_lshlrev_b32 v30, 16, v115\n\t" \
  "v_and_b32 v31, 0xffff0000, v115\n\t" \
  "v_mfma_f32_16x16x32_bf16 v[24:27], v[64:67], v[104:107], v[24:27]\n\t" \
  "v_lshlrev_b32 v32, 16, v116\n\t" \
  "v_and_b32 v33, 0xffff0000, v116\n\t" \
  "v_lshlrev_b32 v34, 16, v117\n\t" \
  "v_and_b32 v35, 0xffff0000, v117\n\t" \
  "v_mfma_f32_16x16x32_bf16 v[28:31], v[68:71], v[104:107], v[28:31]\n\t" \
  "v_lshlrev_b32 v36, 16, v118\n\t" \
  "v_and_b32 v37, 0xffff0000, v118\n\t" \
  "v_lshlrev_b32 v38, 16, v119\n\t" \
  "v_and_b32 v39, 0xffff0000, v119\n\t" \
  "v_mfma_f32_16x16x32_bf16 v[32:35], v[72:75], v[104:107], v[32:35]\n\t" \
  "v_lshlrev_b32 v40, 16, v120\n\t" \
  "v_and_b32 v41, 0xffff0000, v120\n\t" \
  "v_lshlrev_b32 v42, 16, v121\n\t" \
  "v_and_b32 v43, 0xffff0000, v121\n\t" \
  "v_mfma_f32_16x16x32_bf16 v[36:39], v[76:79], v[104:107], v[36:39]\n\t" \
  "s_nop 0\n\t" \
  "v_mfma_f32_16x16x32_bf16 v[40:43], v[80:83], v[104:107], v[40:43]\n\t" \
  "v_add_u32 %[ta], %[so2], %[pm]\n\t" \
  "ds_read_b64_tr_b16 v[104:105], %[ta] offset:0\n\t" \
  "ds_read_b64_tr_b16 v[106:107], %[ta] offset:3200\n\t" \
  "ds_read_b64_tr_b16 v[108:109], %[ta] offset:8\n\t" \
  "ds_read_b64_tr_b16 v[110:111], %[ta] offset:3208\n\t" \
  "ds_read_b64_tr_b16 v[112:113], %[ta] offset:16\n\t" \
  "ds_read_b64_tr_b16 v[114:115], %[ta] offset:3216\n\t" \
  "ds_read_b64_tr_b16 v[116:117], %[ta] offset:24\n\t" \
  "ds_read_b64_tr_b16 v[118:119], %[ta] offset:3224\n\t" \
  "ds_read_b64_tr_b16 v[120:121], %[ta] offset:32\n\t" \
  "ds_read_b64_tr_b16 v[122:123], %[ta] offset:3232\n\t" \
  STP(1) \
  "global_load_dwordx4 v[144:147], %[vk], %[oa3] sc1\n\t" \
  "global_load_dwordx4 v[148:151], %[vd], %[oa4] sc1\n\t" \
  "s_waitcnt vmcnt(5)\n\t" \
  "v_max_u32 %[tk], v124, v128\n\t" \
  "v_cmp_eq_u32 vcc, -1, %[tk]\n\t" \
  "s_cbranch_vccnz LSs1_%=\n\t" \
  "LQs1_%=:\n\t" \
  STP(2) \
  "s_waitcnt lgkmcnt(10)\n\t" \
  "v_mfma_f32_16x16x32_bf16 v[44:47], v[84:87], v[124:127], v[44:47]\n\t" \
  "v_add_u32 %[ta], %[so3], %[pm]\n\t" \
  "ds_read_b64_tr_b16 v[64:65], %[ta] offset:0\n\t" \
  "v_mfma_f32_16x16x32_bf16 v[24:27], v[84:87], v[128:131], v[24:27]\n\t" \
  "ds_read_b64_tr_b16 v[66:67], %[ta] offset:3200\n\t" \
  "global_load_dwordx4 v[152:155], %[vk], %[oa4] sc1\n\t" \
  "v_mfma_f32_16x16x32_bf16 v[48:51], v[88:91], v[124:127], v[48:51]\n\t" \
  "ds_read_b64_tr_b16 v[68:69], %[ta] offset:8\n\t" \
  "v_mfma_f32_16x16x32_bf16 v[28:31], v[88:91], v[128:131], v[28:31]\n\t" \
  "ds_read_b64_tr_b16 v[70:71], %[ta] offset:3208\n\t" \
  "global_load_dwordx4 v[156:159], %[vd], %[oa5] sc1\n\t" \
  "v_mfma_f32_16x16x32_bf16 v[52:55], v[92:95], v[124:127], v[52:55]\n\t" \
  "ds_read_b64_tr_b16 v[72:73], %[ta] offset:16\n\t" \
  "v_mfma_f32_16x16x32_bf16 v[32:35], v[92:95], v[128:131], v[32:35]\n\t" \
  "ds_read_b64_tr_b16 v[74:75], %[ta] offset:3216\n\t" \
  "v_mfma_f32_16x16x32_bf16 v[56:59], v[96:99], v[124:127], v[56:59]\n\t" \
  "ds_read_b64_tr_b16 v[76:77], %[ta] offset:24\n\t" \
  "v_mfma_f32_16x16x32_bf16 v[36:39], v[96:99], v[128:131], v[36:39]\n\t" \
  "ds_read_b64_tr_b16 v[78:79], %[ta] offset:3224\n\t" \
  "s_waitcnt vmcnt(5)\n\t" \
  "v_max_u32 %[tk], v132, v136\n\t" \
  "v_cmp_eq_u32 vcc, -1, %[tk]\n\t" \
  "v_mfma_f32_16x16x32_bf16 v[60:63], v[100:103], v[124:127], v[60:63]\n\t" \
  "ds_read_b64_tr_b16 v[80:81], %[ta] offset:32\n\t" \
  "v_mfma_f32_16x16x32_bf16 v[40:43], v[100:103], v[128:131], v[40:43]\n\t" \
  "ds_read_b64_tr_b16 v[82:83], %[ta] offset:3232\n\t" \
  "s_cbranch_vccnz LSs2_%=\n\t" \
  "LQs2_%=:\n\t" \
  "s_waitcnt lgkmcnt(10)\n\t" \
  "v_mfma_f32_16x16x32_bf16 v[44:47], v[104:107], v[132:135], v[44:47]\n\t" \
  "v_add_u32 %[ta], %[so4], %[pf]\n\t" \
  "ds_read_b64_tr_b16 v[84:85], %[ta] offset:0\n\t" \
  "v_mfma_f32_16x16x32_bf16 v[24:27], v[104:107], v[136:139], v[24:27]\n\t" \
  "ds_read_b64_tr_b16 v[86:87], %[ta] offset:12800\n\t" \
  "global_load_dwordx4 v[124:127], %[vk], %[oa5] sc1\n\t" \
  "v_mfma_f32_16x16x32_bf16 v[48:51], v[108:111], v[132:135], v[48:51]\n\t" \
  "ds_read_b64_tr_b16 v[88:89], %[ta] offset:8\n\t" \
  "v_mfma_f32_16x16x32_bf16 v[28:31], v[108:111], v[136:139], v[28:31]\n\t" \
  "ds_read_b64_tr_b16 v[90:91], %[ta] offset:12808\n\t" \
  "global_load_dwordx4 v[128:131], %[vd], %[oa6] sc1\n\t" \
  "v_mfma_f32_16x16x32_bf16 v[52:55], v[112:115], v[132:135], v[52:55]\n\t" \
  "ds_read_b64_tr_b16 v[92:93], %[ta] offset:16\n\t" \
  "v_mfma_f32_16x16x32_bf16 v[32:35], v[112:115], v[136:139], v[32:35]\n\t" \
  "ds_read_b64_tr_b16 v[94:95], %[ta] offset:12816\n\t" \
  "v_mfma_f32_16x16x32_bf16 v[56:59], v[116:119], v[132:135], v[56:59]\n\t" \
  "ds_read_b64_tr_b16 v[96:97], %[ta] offset:24\n\t" \
  "v_mfma_f32_16x16x32_bf16 v[36:39], v[116:119], v[136:139], v[36:39]\n\t" \
  "ds_read_b64_tr_b16 v[98:99], %[ta] offset:12824\n\t" \
  "s_waitcnt vmcnt(5)\n\t" \
  "v_max_u32 %[tk], v140, v144\n\t" \
  "v_cmp_eq_u32 vcc, -1, %[tk]\n\t" \
  "v_mfma_f32_16x16x32_bf16 v[60:63], v[120:123], v[132:135], v[60:63]\n\t" \
  "ds_read_b64_tr_b16 v[100:101], %[ta] offset:32\n\t" \
  "v_mfma_f32_16x16x32_bf16 v[40:43], v[120:123], v[136:139], v[40:43]\n\t" \
  "ds_read_b64_tr_b16 v[102:103], %[ta] offset:12832\n\t" \
  "s_cbranch_vccnz LSs3_%=\n\t" \
  "LQs3_%=:\n\t" \
  "s_waitcnt lgkmcnt(10)\n\t" \
  "v_mfma_f32_16x16x32_bf16 v[44:47], v[64:67], v[140:143], v[44:47]\n\t" \
  "v_add_u32 %[ta], %[so5], %[pm]\n\t" \
  "ds_read_b64_tr_b16 v[104:105], %[ta] offset:0\n\t" \
  "v_mfma_f32_16x16x32_bf16 v[24:27], v[64:67], v[144:147], v[24:27]\n\t" \
  "ds_read_b64_tr_b16 v[106:107], %[ta] offset:3200\n\t" \
  "global_load_dwordx4 v[132:135], %[vk], %[oa6] sc1\n\t" \
  "v_mfma_f32_16x16x32_bf16 v[48:51], v[68:71], v[140:143], v[48:51]\n\t" \
  "ds_read_b64_tr_b16 v[108:109], %[ta] offset:8\n\t" \
  "v_mfma_f32_16x16x32_bf16 v[28:31], v[68:71], v[144:147], v[28:31]\n\t" \
  "ds_read_b64_tr_b16 v[110:111], %[ta] offset:3208\n\t" \
  "global_load_dwordx4 v[136:139], %[vd], %[oa7] sc1\n\t" \
  "v_mfma_f32_16x16x32_bf16 v[52:55], v[72:75], v[140:143], v[52:55]\n\t" \
  "ds_read_b64_tr_b16 v[112:113], %[ta] offset:16\n\t" \
  "v_mfma_f32_16x16x32_bf16 v[32:35], v[72:75], v[144:147], v[32:35]\n\t" \
  "ds_read_b64_tr_b16 v[114:115], %[ta] offset:3216\n\t" \
  "v_mfma_f32_16x16x32_bf16 v[56:59], v[76:79], v[140:143], v[56:59]\n\t" \
  "ds_read_b64_tr_b16 v[116:117], %[ta] offset:24\n\t" \
  "v_mfma_f32_16x16x32_bf16 v[36:39], v[76:79], v[144:147], v[36:39]\n\t" \
  "ds_read_b64_tr_b16 v[118:119], %[ta] offset:3224\n\t" \
  "s_waitcnt vmcnt(5)\n\t" \
  "v_max3_u32 %[tk], v148, v150, v152\n\t" \
  "v_max_u32 %[tk], %[tk], v154\n\t" \
  "v_cmp_eq_u32 vcc, -1, %[tk]\n\t" \
  "v_mfma_f32_16x16x32_bf16 v[60:63], v[80:83], v[140:143], v[60:63]\n\t" \
  "ds_read_b64_tr_b16 v[120:121], %[ta] offset:32\n\t" \
  "v_mfma_f32_16x16x32_bf16 v[40:43], v[80:83], v[144:147], v[40:43]\n\t" \
  "ds_read_b64_tr_b16 v[122:123], %[ta] offset:3232\n\t" \
  "s_cbranch_vccnz LSs4_%=\n\t" \
  "LQs4_%=:\n\t" \
  "s_waitcnt lgkmcnt(10)\n\t" \
  "v_mfma_f32_16x16x32_bf16 v[44:47], v[84:87], v[148:151], v[44:47]\n\t" \
  "v_add_u32 %[ta], %[so6], %[pm]\n\t" \
  "ds_read_b64_tr_b16 v[64:65], %[ta] offset:0\n\t" \
  "v_mfma_f32_16x16x32_bf16 v[24:27], v[84:87], v[152:155], v[24:27]\n\t" \
  "ds_read_b64_tr_b16 v[66:67], %[ta] offset:3200\n\t" \
  "global_load_dwordx4 v[140:143], %[vd], %[oa8] sc1\n\t" \
  "v_mfma_f32_16x16x32_bf16 v[48:51], v[88:91], v[148:151], v[48:51]\n\t" \
  "ds_read_b64_tr_b16 v[68:69], %[ta] offset:8\n\t" \
  "v_mfma_f32_16x16x32_bf16 v[28:31], v[88:91], v[152:155], v[28:31]\n\t" \
  "ds_read_b64_tr_b16 v[70:71], %[ta] offset:3208\n\t" \
  "global_load_dwordx4 v[144:147], %[vd], %[oa9] sc1\n\t" \
  "v_mfma_f32_16x16x32_bf16 v[52:55], v[92:95], v[148:151], v[52:55]\n\t" \
  "ds_read_b64_tr_b16 v[72:73], %[ta] offset:16\n\t" \
  "v_mfma_f32_16x16x32_bf16 v[32:35], v[92:95], v[152:155], v[32:35]\n\t" \
  "ds_read_b64_tr_b16 v[74:75], %[ta] offset:3216\n\t" \
  "v_mfma_f32_16x16x32_bf16 v[56:59], v[96:99], v[148:151], v[56:59]\n\t" \
  "ds_read_b64_tr_b16 v[76:77], %[ta] offset:24\n\t" \
  "v_mfma_f32_16x16x32_bf16 v[36:39], v[96:99], v[152:155], v[36:39]\n\t" \
  "ds_read_b64_tr_b16 v[78:79], %[ta] offset:3224\n\t" \
  "s_waitcnt vmcnt(5)\n\t" \
  "v_max_u32 %[tk], v156, v124\n\t" \
  "v_cmp_eq_u32 vcc, -1, %[tk]\n\t" \
  "v_mfma_f32_16x16x32_bf16 v[60:63], v[100:103], v[148:151], v[60:63]\n\t" \
  "ds_read_b64_tr_b16 v[80:81], %[ta] offset:32\n\t" \
  "v_mfma_f32_16x16x32_bf16 v[40:43], v[100:103], v[152:155], v[40:43]\n\t" \
  "ds_read_b64_tr_b16 v[82:83], %[ta] offset:3232\n\t" \
  "s_cbranch_vccnz LSs5_%=\n\t" \
  "LQs5_%=:\n\t" \
  STP(3) \
  "s_waitcnt lgkmcnt(10)\n\t" \
  "v_mfma_f32_16x16x32_bf16 v[44:47], v[104:107], v[156:159], v[44:47]\n\t" \
  "v_add_u32 %[ta], %[so7], %[pm]\n\t" \
  "ds_read_b64_tr_b16 v[84:85], %[ta] offset:0\n\t" \
  "v_mfma_f32_16x16x32_bf16 v[24:27], v[104:107], v[124:127], v[24:27]\n\t" \
  "ds_read_b64_tr_b16 v[86:87], %[ta] offset:3200\n\t" \
  "global_load_dwordx4 v[148:151], %[vk], %[oa7] sc1\n\t" \
  "v_mfma_f32_16x16x32_bf16 v[48:51], v[108:111], v[156:159], v[48:51]\n\t" \
  "ds_read_b64_tr_b16 v[88:89], %[ta] offset:8\n\t" \
  "v_mfma_f32_16x16x32_bf16 v[28:31], v[108:111], v[124:127], v[28:31]\n\t" \
  "ds_read_b64_tr_b16 v[90:91], %[ta] offset:3208\n\t" \
  "global_load_dwordx4 v[152:155], %[vk], %[oa8] sc1\n\t" \
  "v_mfma_f32_16x16x32_bf16 v[52:55], v[112:115], v[156:159], v[52:55]\n\t" \
  "ds_read_b64_tr_b16 v[92:93], %[ta] offset:16\n\t" \
  "v_mfma_f32_16x16x32_bf16 v[32:35], v[112:115], v[124:127], v[32:35]\n\t" \
  "ds_read_b64_tr_b16 v[94:95], %[ta] offset:3216\n\t" \
  "v_mfma_f32_16x16x32_bf16 v[56:59], v[116:119], v[156:159], v[56:59]\n\t" \
  "ds_read_b64_tr_b16 v[96:97], %[ta] offset:24\n\t" \
  "v_mfma_f32_16x16x32_bf16 v[36:39], v[116:119], v[124:127], v[36:39]\n\t" \
  "ds_read_b64_tr_b16 v[98:99], %[ta] offset:3224\n\t" \
  "s_waitcnt vmcnt(5)\n\t" \
  "v_max_u32 %[tk], v128, v132\n\t" \
  "v_cmp_eq_u32 vcc, -1, %[tk]\n\t" \
  "v_mfma_f32_16x16x32_bf16 v[60:63], v[120:123], v[156:159], v[60:63]\n\t" \
  "ds_read_b64_tr_b16 v[100:101], %[ta] offset:32\n\t" \
  "v_mfma_f32_16x16x32_bf16 v[40:43], v[120:123], v[124:127], v[40:43]\n\t" \
  "ds_read_b64_tr_b16 v[102:103], %[ta] offset:3232\n\t" \
  "s_cbranch_vccnz LSs6_%=\n\t" \
  "LQs6_%=:\n\t" \
  "s_waitcnt lgkmcnt(10)\n\t" \
  "v_mfma_f32_16x16x32_bf16 v[44:47], v[64:67], v[128:131], v[44:47]\n\t" \
  "v_add_u32 %[ta], %[so8], %[pm]\n\t" \
  "ds_read_b64_tr_b16 v[104:105], %[ta] offset:0\n\t" \
  "v_mfma_f32_16x16x32_bf16 v[24:27], v[64:67], v[132:135], v[24:27]\n\t" \
  "ds_read_b64_tr_b16 v[106:107], %[ta] offset:3200\n\t" \
  "global_load_dwordx4 v[156:159], %[vk], %[oa9] sc1\n\t" \
  "v_mfma_f32_16x16x32_bf16 v[48:51], v[68:71], v[128:131], v[48:51]\n\t" \
  "ds_read_b64_tr_b16 v[108:109], %[ta] offset:8\n\t" \
  "v_mfma_f32_16x16x32_bf16 v[28:31], v[68:71], v[132:135], v[28:31]\n\t" \
  "ds_read_b64_tr_b16 v[110:111], %[ta] offset:3208\n\t" \
  "v_mfma_f32_16x16x32_bf16 v[52:55], v[72:75], v[128:131], v[52:55]\n\t" \
  "ds_read_b64_tr_b16 v[112:113], %[ta] offset:16\n\t" \
  "v_mfma_f32_16x16x32_bf16 v[32:35], v[72:75], v[132:135], v[32:35]\n\t" \
  "ds_read_b64_tr_b16 v[114:115], %[ta] offset:3216\n\t" \
  "v_mfma_f32_16x16x32_bf16 v[56:59], v[76:79], v[128:131], v[56:59]\n\t" \
  "ds_read_b64_tr_b16 v[116:117], %[ta] offset:24\n\t" \
  "v_mfma_f32_16x16x32_bf16 v[36:39], v[76:79], v[132:135], v[36:39]\n\t" \
  "ds_read_b64_tr_b16 v[118:119], %[ta] offset:3224\n\t" \
  "s_waitcnt vmcnt(4)\n\t" \
  "v_max_u32 %[tk], v136, v140\n\t" \
  "v_cmp_eq_u32 vcc, -1, %[tk]\n\t" \
  "v_mfma_f32_16x16x32_bf16 v[60:63], v[80:83], v[128:131], v[60:63]\n\t" \
  "ds_read_b64_tr_b16 v[120:121], %[ta] offset:32\n\t" \
  "v_mfma_f32_16x16x32_bf16 v[40:43], v[80:83], v[132:135], v[40:43]\n\t" \
  "ds_read_b64_tr_b16 v[122:123], %[ta] offset:3232\n\t" \
  "s_cbranch_vccnz LSd7_%=\n\t" \
  "LQd7_%=:\n\t" \
  "s_waitcnt lgkmcnt(10)\n\t" \
  "v_mfma_f32_16x16x32_bf16 v[44:47], v[84:87], v[136:139], v[44:47]\n\t" \
  "v_add_u32 %[ta], %[so9], %[pf]\n\t" \
  "ds_read_b64_tr_b16 v[64:65], %[ta] offset:0\n\t" \
  "ds_read_b64_tr_b16 v[66:67], %[ta] offset:12800\n\t" \
  "v_mfma_f32_16x16x32_bf16 v[48:51], v[88:91], v[136:139], v[48:51]\n\t" \
  "ds_read_b64_tr_b16 v[68:69], %[ta] offset:8\n\t" \
  "ds_read_b64_tr_b16 v[70:71], %[ta] offset:12808\n\t" \
  "v_mfma_f32_16x16x32_bf16 v[52:55], v[92:95], v[136:139], v[52:55]\n\t" \
  "ds_read_b64_tr_b16 v[72:73], %[ta] offset:16\n\t" \
  "ds_read_b64_tr_b16 v[74:75], %[ta] offset:12816\n\t" \
  "v_mfma_f32_16x16x32_bf16 v[56:59], v[96:99], v[136:139], v[56:59]\n\t" \
  "ds_read_b64_tr_b16 v[76:77], %[ta] offset:24\n\t" \
  "ds_read_b64_tr_b16 v[78:79], %[ta] offset:12824\n\t" \
  "v_mfma_f32_16x16x32_bf16 v[60:63], v[100:103], v[136:139], v[60:63]\n\t" \
  "ds_read_b64_tr_b16 v[80:81], %[ta] offset:32\n\t" \
  "ds_read_b64_tr_b16 v[82:83], %[ta] offset:12832\n\t" \
  "s_waitcnt lgkmcnt(10)\n\t" \
  "v_mfma_f32_16x16x32_bf16 v[44:47], v[104:107], v[140:143], v[44:47]\n\t" \
  "v_mfma_f32_16x16x32_bf16 v[48:51], v[108:111], v[140:143], v[48:51]\n\t" \
  "v_mfma_f32_16x16x32_bf16 v[52:55], v[112:115], v[140:143], v[52:55]\n\t" \
  "s_waitcnt vmcnt(2)\n\t" \
  "v_max3_u32 %[tk], v144, v146, v148\n\t" \
  "v_cmp_eq_u32 vcc, -1, %[tk]\n\t" \
  "v_mfma_f32_16x16x32_bf16 v[56:59], v[116:119], v[140:143], v[56:59]\n\t" \
  "v_mfma_f32_16x16x32_bf16 v[60:63], v[120:123], v[140:143], v[60:63]\n\t" \
  "s_cbranch_vccnz LSd9_%=\n\t" \
  "LQd9_%=:\n\t" \
  "s_waitcnt lgkmcnt(0)\n\t" \
  "v_mfma_f32_16x16x32_bf16 v[44:47], v[64:67], v[144:147], v[44:47]\n\t" \
  "ds_read2_b64 v[124:127], %[aw] offset0:0 offset1:1\n\t" \
  "ds_read2_b64 v[128:131], %[aw] offset0:2 offset1:3\n\t" \
  "ds_read_b64 v[132:133], %[aw] offset:32\n\t" \
  "v_mov_b64 v[134:135], 0\n\t" \
  "v_mfma_f32_16x16x32_bf16 v[48:51], v[68:71], v[144:147], v[48:51]\n\t" \
  "v_mfma_f32_16x16x32_bf16 v[52:55], v[72:75], v[144:147], v[52:55]\n\t" \
  "v_add_u32 %[ta], %[sd], %[l16]\n\t" \
  "v_mfma_f32_16x16x32_bf16 v[56:59], v[76:79], v[144:147], v[56:59]\n\t" \
  "v_mfma_f32_16x16x32_bf16 v[60:63], v[80:83], v[144:147], v[60:63]\n\t" \
  "v_mov_b32 v137, %[sc]\n\t" \
  "v_mov_b32 v136, 1\n\t" \
  "v_mfma_f32_16x16x32_bf16 v[24:27], v[84:87], v[148:151], v[24:27]\n\t" \
  "ds_write_b128 %[ta], v[44:47] offset:0\n\t" \
  "v_mfma_f32_16x16x32_bf16 v[28:31], v[88:91], v[148:151], v[28:31]\n\t" \
  "ds_write_b128 %[ta], v[48:51] offset:1024\n\t" \
  "v_mfma_f32_16x16x32_bf16 v[32:35], v[92:95], v[148:151], v[32:35]\n\t" \
  "ds_write_b128 %[ta], v[52:55] offset:2048\n\t" \
  "s_waitcnt vmcnt(0)\n\t" \
  "v_max3_u32 %[tk], v152, v156, v158\n\t" \
  "v_cmp_eq_u32 vcc, -1, %[tk]\n\t" \
  "v_mfma_f32_16x16x32_bf16 v[36:39], v[96:99], v[148:151], v[36:39]\n\t" \
  "ds_write_b128 %[ta], v[56:59] offset:3072\n\t" \
  "v_mfma_f32_16x16x32_bf16 v[40:43], v[100:103], v[148:151], v[40:43]\n\t" \
  "ds_write_b128 %[ta], v[60:63] offset:4096\n\t" \
  "s_mov_b64 exec, 1\n\t" \
  "ds_add_u32 v137, v136\n\t" \
  "s_mov_b64 exec, -1\n\t" \
  "s_cbranch_vccnz LSk8_%=\n\t" \
  "LQk8_%=:\n\t" \
  "v_mfma_f32_16x16x32_bf16 v[24:27], v[104:107], v[152:155], v[24:27]\n\t" \
  "v_mov_b64 v[94:95], 0\n\t" \
  "v_mfma_f32_16x16x32_bf16 v[28:31], v[108:111], v[152:155], v[28:31]\n\t" \
  "v_xor_b32 v96, 4, v137\n\t" \
  "v_mfma_f32_16x16x32_bf16 v[32:35], v[112:115], v[152:155], v[32:35]\n\t" \
  "v_add_u32 %[tk], %[sr], %[l16]\n\t" \
  "v_mfma_f32_16x16x32_bf16 v[36:39], v[116:119], v[152:155], v[36:39]\n\t" \
  "v_mfma_f32_16x16x32_bf16 v[40:43], v[120:123], v[152:155], v[40:43]\n\t" \
  "v_mfma_f32_16x16x32_bf16 v[24:27], v[64:67], v[156:159], v[24:27]\n\t" \
  "ds_read_b32 v97, v96\n\t" \
  "ds_read_b128 v[44:47], %[tk] offset:0\n\t" \
  "ds_read_b128 v[48:51], %[tk] offset:1024\n\t" \
  "ds_read_b128 v[52:55], %[tk] offset:2048\n\t" \
  "ds_read_b128 v[56:59], %[tk] offset:3072\n\t" \
  "ds_read_b128 v[60:63], %[tk] offset:4096\n\t" \
  "v_mfma_f32_16x16x32_bf16 v[28:31], v[68:71], v[156:159], v[28:31]\n\t" \
  "v_mfma_f32_16x16x32_bf16 v[32:35], v[72:75], v[156:159], v[32:35]\n\t" \
  "v_mfma_f32_16x16x32_bf16 v[36:39], v[76:79], v[156:159], v[36:39]\n\t" \
  "v_mfma_f32_16x16x32_bf16 v[40:43], v[80:83], v[156:159], v[40:43]\n\t" \
  STP(4) \
  "LD1_%=:\n\t" \
  "s_waitcnt lgkmcnt(0)\n\t" \
  "v_cmp_eq_u32 vcc, 0, v97\n\t" \
  "s_cbranch_vccnz LD2_%=\n\t" \
  "LT_%=:\n\t" \
  "v_mov_b32 v137, %[sa]\n\t" \
  "ds_read_b32 v138, v137 offset:16\n\t" \
  "v_add_f32 v24, v24, v44\n\t" \
  "v_add_f32 v25, v25, v45\n\t" \
  "v_add_f32 v26, v26, v46\n\t" \
  "v_add_f32 v27, v27, v47\n\t" \
  "v_cvt_pk_bf16_f32 v84, v24, v25\n\t" \
  "v_cvt_pk_bf16_f32 v85, v26, v27\n\t" \
  "v_add_f32 v28, v28, v48\n\t" \
  "v_add_f32 v29, v29, v49\n\t" \
  "v_add_f32 v30, v30, v50\n\t" \
  "v_add_f32 v31, v31, v51\n\t" \
  "v_cvt_pk_bf16_f32 v86, v28, v29\n\t" \
  "v_cvt_pk_bf16_f32 v87, v30, v31\n\t" \
  "v_add_f32 v32, v32, v52\n\t" \
  "v_add_f32 v33, v33, v53\n\t" \
  "v_add_f32 v34, v34, v54\n\t" \
  "v_add_f32 v35, v35, v55\n\t" \
  "v_cvt_pk_bf16_f32 v88, v32, v33\n\t" \
  "v_cvt_pk_bf16_f32 v89, v34, v35\n\t" \
  "v_mfma_f32_16x16x32_bf16 v[100:103], v[84:87], v[84:87], 0\n\t" \
  "v_mfma_f32_16x16x32_bf16 v[96:99], v[124:127], v[84:87], 0\n\t" \
  "v_add_f32 v36, v36, v56\n\t" \
  "v_add_f32 v37, v37, v57\n\t" \
  "v_add_f32 v38, v38, v58\n\t" \
  "v_add_f32 v39, v39, v59\n\t" \
  "v_cvt_pk_bf16_f32 v90, v36, v37\n\t" \
  "v_cvt_pk_bf16_f32 v91, v38, v39\n\t" \
  "v_add_f32 v40, v40, v60\n\t" \
  "v_add_f32 v41, v41, v61\n\t" \
  "v_add_f32 v42, v42, v62\n\t" \
  "v_add_f32 v43, v43, v63\n\t" \
  "v_cvt_pk_bf16_f32 v92, v40, v41\n\t" \
  "v_cvt_pk_bf16_f32 v93, v42, v43\n\t" \
  "v_mfma_f32_16x16x32_bf16 v[100:103], v[88:91], v[88:91], v[100:103]\n\t" \
  "v_mfma_f32_16x16x32_bf16 v[96:99], v[128:131], v[88:91], v[96:99]\n\t" \
  "v_mfma_f32_16x16x32_bf16 v[100:103], v[92:95], v[92:95], v[100:103]\n\t" \
  "v_mfma_f32_16x16x32_bf16 v[96:99], v[132:135], v[92:95], v[96:99]\n\t" \
  "s_waitcnt lgkmcnt(0)\n\t" \
  "v_sub_u32 v138, %[ra], v138\n\t" \
  "v_cmp_gt_u32 vcc, 0xa0, v138\n\t" \
  "v_and_b32 %[ta], 0xff, %[l16]\n\t" \
  "v_add_u32 %[tk], %[sp], %[ta]\n\t" \
  "v_mov_b32 v136, 1\n\t" \
  "s_cbranch_vccz LP1_%=\n\t" \
  "s_mov_b64 exec, vcc\n\t" \
  "global_store_dwordx4 v138, v[84:87], %[cp] sc1\n\t" \
  "global_store_dwordx4 v138, v[88:91], %[cp] offset:16 sc1\n\t" \
  "global_store_dwordx2 v138, v[92:93], %[cp] offset:32 sc1\n\t" \
  "ds_write_b32 v137, v136 offset:24\n\t" \
  "LP1_%=:\n\t" \
  "s_mov_b32 exec_lo, 0x00200002\n\t" \
  "s_mov_b32 exec_hi, 0x20000200\n\t" \
  "v_mov_b32 v100, v101\n\t" \
  "s_lshl_b64 exec, exec, 1\n\t" \
  "v_mov_b32 v100, v102\n\t" \
  "s_lshl_b64 exec, exec, 1\n\t" \
  "v_mov_b32 v100, v103\n\t" \
  "s_mov_b32 exec_lo, 0x00f0000f\n\t" \
  "s_mov_b32 exec_hi, 0xf0000f00\n\t" \
  "v_mov_b32 v98, v100\n\t" \
  "global_store_dwordx4 %[ta], v[96:99], %[sst] sc1\n\t" \
  STP(5) \
  "ds_write_b128 %[tk], v[96:99]\n\t" \
  "s_mov_b64 exec, -1\n\t" \
  "ds_write_b64 %[ra], v[84:85] offset:0\n\t" \
  "ds_write_b64 %[ra], v[86:87] offset:8\n\t" \
  "ds_write_b64 %[ra], v[88:89] offset:16\n\t" \
  "ds_write_b64 %[ra], v[90:91] offset:24\n\t" \
  "ds_write_b64 %[ra], v[92:93] offset:32\n\t" \
  "s_mov_b64 exec, 1\n\t" \
  "ds_add_u32 v137, v136\n\t" \
  "s_mov_b64 exec, -1\n\t" \
  "s_setprio 0\n\t" \
  "s_branch LZ_%=\n\t" \
  "LD2_%=:\n\t" \
  "s_sub_u32 s43, s43, 1\n\t" \
  "s_cbranch_scc1 LT_%=\n\t" \
  "ds_read_b32 v97, v96\n\t" \
  "ds_read_b128 v[44:47], %[tk] offset:0\n\t" \
  "ds_read_b128 v[48:51], %[tk] offset:1024\n\t" \
  "ds_read_b128 v[52:55], %[tk] offset:2048\n\t" \
  "ds_read_b128 v[56:59], %[tk] offset:3072\n\t" \
  "ds_read_b128 v[60:63], %[tk] offset:4096\n\t" \
  "s_branch LD1_%=\n\t" \
  "LH_%=:\n\t" \
  "s_sleep 3\n\t" \
  "global_load_dwordx4 v[140:143], %[vk], %[oa1] sc1\n\t" \
  "global_load_dwordx4 v[144:147], %[vk], %[oa2] sc1\n\t" \
  "global_load_dwordx4 v[148:151], %[vk], %[oa3] sc1\n\t" \
  "global_load_dwordx4 v[152:155], %[vk], %[oa4] sc1\n\t" \
  "global_load_dwordx4 v[156:159], %[vk], %[oa0] sc1\n\t" \
  "s_cmp_eq_u32 %[w], 3\n\t" \
  "s_cbranch_scc0 LG_%=\n\t" \
  "ds_read_b64 v[112:113], %[ra] offset:0\n\t" \
  "ds_read_b64 v[114:115], %[ra] offset:8\n\t" \
  "ds_read_b64 v[116:117], %[ra] offset:16\n\t" \
  "ds_read_b64 v[118:119], %[ra] offset:24\n\t" \
  "ds_read_b64 v[120:121], %[ra] offset:32\n\t" \
  "ds_read2_b64 v[124:127], %[aw] offset0:0 offset1:1\n\t" \
  "ds_read2_b64 v[128:131], %[aw] offset0:2 offset1:3\n\t" \
  "ds_read_b64 v[132:133], %[aw] offset:32\n\t" \
  "v_mov_b64 v[134:135], 0\n\t" \
  "s_waitcnt lgkmcnt(3)\n\t" \
  "v_lshlrev_b32 v24, 16, v112\n\t" \
  "v_and_b32 v25, 0xffff0000, v112\n\t" \
  "v_lshlrev_b32 v26, 16, v113\n\t" \
  "v_and_b32 v27, 0xffff0000, v113\n\t" \
  "v_lshlrev_b32 v28, 16, v114\n\t" \
  "v_and_b32 v29, 0xffff0000, v114\n\t" \
  "v_lshlrev_b32 v30, 16, v115\n\t" \
  "v_and_b32 v31, 0xffff0000, v115\n\t" \
  "v_lshlrev_b32 v32, 16, v116\n\t" \
  "v_and_b32 v33, 0xffff0000, v116\n\t" \
  "v_lshlrev_b32 v34, 16, v117\n\t" \
  "v_and_b32 v35, 0xffff0000, v117\n\t" \
  "v_lshlrev_b32 v36, 16, v118\n\t" \
  "v_and_b32 v37, 0xffff0000, v118\n\t" \
  "v_lshlrev_b32 v38, 16, v119\n\t" \
  "v_and_b32 v39, 0xffff0000, v119\n\t" \
  "v_lshlrev_b32 v40, 16, v120\n\t" \
  "v_and_b32 v41, 0xffff0000, v120\n\t" \
  "v_lshlrev_b32 v42, 16, v121\n\t" \
  "v_and_b32 v43, 0xffff0000, v121\n\t" \
  "s_branch LG2_%=\n\t" \
  "LG_%=:\n\t" \
  "v_mov_b64 v[24:25], 0\n\t" \
  "v_mov_b64 v[26:27], 0\n\t" \
  "v_mov_b64 v[28:29], 0\n\t" \
  "v_mov_b64 v[30:31], 0\n\t" \
  "v_mov_b64 v[32:33], 0\n\t" \
  "v_mov_b64 v[34:35], 0\n\t" \
  "v_mov_b64 v[36:37], 0\n\t" \
  "v_mov_b64 v[38:39], 0\n\t" \
  "v_mov_b64 v[40:41], 0\n\t" \
  "v_mov_b64 v[42:43], 0\n\t" \
  "LG2_%=:\n\t" \
  "v_add_u32 %[ta], %[so1], %[pm]\n\t" \
  "ds_read_b64_tr_b16 v[64:65], %[ta] offset:0\n\t" \
  "ds_read_b64_tr_b16 v[66:67], %[ta] offset:3200\n\t" \
  "ds_read_b64_tr_b16 v[68:69], %[ta] offset:8\n\t" \
  "ds_read_b64_tr_b16 v[70:71], %[ta] offset:3208\n\t" \
  "ds_read_b64_tr_b16 v[72:73], %[ta] offset:16\n\t" \
  "ds_read_b64_tr_b16 v[74:75], %[ta] offset:3216\n\t" \
  "ds_read_b64_tr_b16 v[76:77], %[ta] offset:24\n\t" \
  "ds_read_b64_tr_b16 v[78:79], %[ta] offset:3224\n\t" \
  "ds_read_b64_tr_b16 v[80:81], %[ta] offset:32\n\t" \
  "ds_read_b64_tr_b16 v[82:83], %[ta] offset:3232\n\t" \
  "v_add_u32 %[ta], %[so2], %[pm]\n\t" \
  "ds_read_b64_tr_b16 v[84:85], %[ta] offset:0\n\t" \
  "ds_read_b64_tr_b16 v[86:87], %[ta] offset:3200\n\t" \
  "ds_read_b64_tr_b16 v[88:89], %[ta] offset:8\n\t" \
  "ds_read_b64_tr_b16 v[90:91], %[ta] offset:3208\n\t" \
  "ds_read_b64_tr_b16 v[92:93], %[ta] offset:16\n\t" \
  "ds_read_b64_tr_b16 v[94:95], %[ta] offset:3216\n\t" \
  "ds_read_b64_tr_b16 v[96:97], %[ta] offset:24\n\t" \
  "ds_read_b64_tr_b16 v[98:99], %[ta] offset:3224\n\t" \
  "ds_read_b64_tr_b16 v[100:101], %[ta] offset:32\n\t" \
  "ds_read_b64_tr_b16 v[102:103], %[ta] offset:3232\n\t" \
  STP(1) \
  "s_waitcnt vmcnt(4)\n\t" \
  "v_cmp_eq_u32 vcc, -1, v140\n\t" \
  "s_cbranch_vccnz LSh1_%=\n\t" \
  "LQh1_%=:\n\t" \
  STP(2) \
  "s_waitcnt lgkmcnt(10)\n\t" \
  "v_mfma_f32_16x16x32_bf16 v[24:27], v[64:67], v[140:143], v[24:27]\n\t" \
  "v_add_u32 %[ta], %[so3], %[pm]\n\t" \
  "ds_read_b64_tr_b16 v[104:105], %[ta] offset:0\n\t" \
  "ds_read_b64_tr_b16 v[106:107], %[ta] offset:3200\n\t" \
  "v_mfma_f32_16x16x32_bf16 v[28:31], v[68:71], v[140:143], v[28:31]\n\t" \
  "ds_read_b64_tr_b16 v[108:109], %[ta] offset:8\n\t" \
  "ds_read_b64_tr_b16 v[110:111], %[ta] offset:3208\n\t" \
  "v_mfma_f32_16x16x32_bf16 v[32:35], v[72:75], v[140:143], v[32:35]\n\t" \
  "ds_read_b64_tr_b16 v[112:113], %[ta] offset:16\n\t" \
  "ds_read_b64_tr_b16 v[114:115], %[ta] offset:3216\n\t" \
  "v_mfma_f32_16x16x32_bf16 v[36:39], v[76:79], v[140:143], v[36:39]\n\t" \
  "ds_read_b64_tr_b16 v[116:117], %[ta] offset:24\n\t" \
  "ds_read_b64_tr_b16 v[118:119], %[ta] offset:3224\n\t" \
  "s_waitcnt vmcnt(3)\n\t" \
  "v_cmp_eq_u32 vcc, -1, v144\n\t" \
  "v_mfma_f32_16x16x32_bf16 v[40:43], v[80:83], v[140:143], v[40:43]\n\t" \
  "ds_read_b64_tr_b16 v[120:121], %[ta] offset:32\n\t" \
  "ds_read_b64_tr_b16 v[122:123], %[ta] offset:3232\n\t" \
  "s_cbranch_vccnz LSh2_%=\n\t" \
  "LQh2_%=:\n\t" \
  "s_waitcnt lgkmcnt(10)\n\t" \
  "v_mfma_f32_16x16x32_bf16 v[24:27], v[84:87], v[144:147], v[24:27]\n\t" \
  "v_add_u32 %[ta], %[so4], %[pf]\n\t" \
  "ds_read_b64_tr_b16 v[64:65], %[ta] offset:0\n\t" \
  "ds_read_b64_tr_b16 v[66:67], %[ta] offset:12800\n\t" \
  "v_mfma_f32_16x16x32_bf16 v[28:31], v[88:91], v[144:147], v[28:31]\n\t" \
  "ds_read_b64_tr_b16 v[68:69], %[ta] offset:8\n\t" \
  "ds_read_b64_tr_b16 v[70:71], %[ta] offset:12808\n\t" \
  "v_mfma_f32_16x16x32_bf16 v[32:35], v[92:95], v[144:147], v[32:35]\n\t" \
  "ds_read_b64_tr_b16 v[72:73], %[ta] offset:16\n\t" \
  "ds_read_b64_tr_b16 v[74:75], %[ta] offset:12816\n\t" \
  "v_mfma_f32_16x16x32_bf16 v[36:39], v[96:99], v[144:147], v[36:39]\n\t" \
  "ds_read_b64_tr_b16 v[76:77], %[ta] offset:24\n\t" \
  "ds_read_b64_tr_b16 v[78:79], %[ta] offset:12824\n\t" \
  "s_waitcnt vmcnt(2)\n\t" \
  "v_cmp_eq_u32 vcc, -1, v148\n\t" \
  "v_mfma_f32_16x16x32_bf16 v[40:43], v[100:103], v[144:147], v[40:43]\n\t" \
  "ds_read_b64_tr_b16 v[80:81], %[ta] offset:32\n\t" \
  "ds_read_b64_tr_b16 v[82:83], %[ta] offset:12832\n\t" \
  "s_cbranch_vccnz LSh3_%=\n\t" \
  "LQh3_%=:\n\t" \
  "s_waitcnt lgkmcnt(10)\n\t" \
  "v_mfma_f32_16x16x32_bf16 v[24:27], v[104:107], v[148:151], v[24:27]\n\t" \
  "v_add_u32 %[ta], %[so0], %[pm]\n\t" \
  "ds_read_b64_tr_b16 v[84:85], %[ta] offset:0\n\t" \
  "ds_read_b64_tr_b16 v[86:87], %[ta] offset:3200\n\t" \
  "v_mfma_f32_16x16x32_bf16 v[28:31], v[108:111], v[148:151], v[28:31]\n\t" \
  "ds_read_b64_tr_b16 v[88:89], %[ta] offset:8\n\t" \
  "ds_read_b64_tr_b16 v[90:91], %[ta] offset:3208\n\t" \
  "v_mfma_f32_16x16x32_bf16 v[32:35], v[112:115], v[148:151], v[32:35]\n\t" \
  "ds_read_b64_tr_b16 v[92:93], %[ta] offset:16\n\t" \
  "ds_read_b64_tr_b16 v[94:95], %[ta] offset:3216\n\t" \
  "v_mfma_f32_16x16x32_bf16 v[36:39], v[116:119], v[148:151], v[36:39]\n\t" \
  "ds_read_b64_tr_b16 v[96:97], %[ta] offset:24\n\t" \
  "ds_read_b64_tr_b16 v[98:99], %[ta] offset:3224\n\t" \
  "s_waitcnt vmcnt(1)\n\t" \
  "v_max_u32 %[tk], v152, v154\n\t" \
  "v_cmp_eq_u32 vcc, -1, %[tk]\n\t" \
  "v_mfma_f32_16x16x32_bf16 v[40:43], v[120:123], v[148:151], v[40:43]\n\t" \
  "ds_read_b64_tr_b16 v[100:101], %[ta] offset:32\n\t" \
  "ds_read_b64_tr_b16 v[102:103], %[ta] offset:3232\n\t" \
  "s_cbranch_vccnz LSh4_%=\n\t" \
  "LQh4_%=:\n\t" \
  "s_waitcnt lgkmcnt(10)\n\t" \
  "v_mfma_f32_16x16x32_bf16 v[24:27], v[64:67], v[152:155], v[24:27]\n\t" \
  "v_mfma_f32_16x16x32_bf16 v[28:31], v[68:71], v[152:155], v[28:31]\n\t" \
  "v_mfma_f32_16x16x32_bf16 v[32:35], v[72:75], v[152:155], v[32:35]\n\t" \
  "v_mfma_f32_16x16x32_bf16 v[36:39], v[76:79], v[152:155], v[36:39]\n\t" \
  "s_waitcnt vmcnt(0)\n\t" \
  "v_cmp_eq_u32 vcc, -1, v156\n\t" \
  "v_mfma_f32_16x16x32_bf16 v[40:43], v[80:83], v[152:155], v[40:43]\n\t" \
  "s_cbranch_vccnz LSh0_%=\n\t" \
  "LQh0_%=:\n\t" \
  STP(3) \
  "s_waitcnt lgkmcnt(0)\n\t" \
  "v_mfma_f32_16x16x32_bf16 v[24:27], v[84:87], v[156:159], v[24:27]\n\t" \
  "v_mfma_f32_16x16x32_bf16 v[28:31], v[88:91], v[156:159], v[28:31]\n\t" \
  "v_mfma_f32_16x16x32_bf16 v[32:35], v[92:95], v[156:159], v[32:35]\n\t" \
  "v_mfma_f32_16x16x32_bf16 v[36:39], v[96:99], v[156:159], v[36:39]\n\t" \
  "v_mfma_f32_16x16x32_bf16 v[40:43], v[100:103], v[156:159], v[40:43]\n\t" \
  STP(4) \
  "s_cmp_eq_u32 %[w], 3\n\t" \
  "s_cbranch_scc1 LF_%=\n\t" \
  "s_setprio 3\n\t" \
  "v_add_u32 %[ta], %[sd], %[l16]\n\t" \
  "v_mov_b32 v137, %[sc]\n\t" \
  "v_mov_b32 v136, 1\n\t" \
  "ds_write_b128 %[ta], v[24:27] offset:0\n\t" \
  "ds_write_b128 %[ta], v[28:31] offset:1024\n\t" \
  "ds_write_b128 %[ta], v[32:35] offset:2048\n\t" \
  "ds_write_b128 %[ta], v[36:39] offset:3072\n\t" \
  "ds_write_b128 %[ta], v[40:43] offset:4096\n\t" \
  "s_mov_b64 exec, 1\n\t" \
  "ds_add_u32 v137, v136\n\t" \
  "s_mov_b64 exec, -1\n\t" \
  STP(5) \
  "s_branch LZ_%=\n\t" \
  "LF_%=:\n\t" \
  "s_setprio 3\n\t" \
  "v_mov_b64 v[94:95], 0\n\t" \
  "v_mov_b32 v96, %[sc]\n\t" \
  "v_add_u32 %[ta], %[sr], %[l16]\n\t" \
  "LF1_%=:\n\t" \
  "ds_read_b32 v97, v96\n\t" \
  "s_waitcnt lgkmcnt(0)\n\t" \
  "v_cmp_gt_u32 vcc, 3, v97\n\t" \
  "s_cbranch_vccz LF2_%=\n\t" \
  "s_sleep 1\n\t" \
  "s_sub_u32 s43, s43, 1\n\t" \
  "s_cbranch_scc0 LF1_%=\n\t" \
  "LF2_%=:\n\t" \
  "ds_read_b128 v[64:67], %[ta] offset:0\n\t" \
  "ds_read_b128 v[68:71], %[ta] offset:1024\n\t" \
  "ds_read_b128 v[72:75], %[ta] offset:2048\n\t" \
  "ds_read_b128 v[76:79], %[ta] offset:3072\n\t" \
  "ds_read_b128 v[80:83], %[ta] offset:4096\n\t" \
  "ds_read_b128 v[104:107], %[ta] offset:5120\n\t" \
  "ds_read_b128 v[108:111], %[ta] offset:6144\n\t" \
  "ds_read_b128 v[112:115], %[ta] offset:7168\n\t" \
  "ds_read_b128 v[116:119], %[ta] offset:8192\n\t" \
  "ds_read_b128 v[120:123], %[ta] offset:9216\n\t" \
  "ds_read_b128 v[44:47], %[ta] offset:10240\n\t" \
  "ds_read_b128 v[48:51], %[ta] offset:11264\n\t" \
  "ds_read_b128 v[52:55], %[ta] offset:12288\n\t" \
  "ds_read_b128 v[56:59], %[ta] offset:13312\n\t" \
  "ds_read_b128 v[60:63], %[ta] offset:14336\n\t" \
  "s_waitcnt lgkmcnt(10)\n\t" \
  "v_pk_add_f32 v[24:25], v[24:25], v[64:65]\n\t" \
  "v_pk_add_f32 v[26:27], v[26:27], v[66:67]\n\t" \
  "v_pk_add_f32 v[28:29], v[28:29], v[68:69]\n\t" \
  "v_pk_add_f32 v[30:31], v[30:31], v[70:71]\n\t" \
  "v_pk_add_f32 v[32:33], v[32:33], v[72:73]\n\t" \
  "v_pk_add_f32 v[34:35], v[34:35], v[74:75]\n\t" \
  "v_pk_add_f32 v[36:37], v[36:37], v[76:77]\n\t" \
  "v_pk_add_f32 v[38:39], v[38:39], v[78:79]\n\t" \
  "v_pk_add_f32 v[40:41], v[40:41], v[80:81]\n\t" \
  "v_pk_add_f32 v[42:43], v[42:43], v[82:83]\n\t" \
  "s_waitcnt lgkmcnt(5)\n\t" \
  "v_pk_add_f32 v[24:25], v[24:25], v[104:105]\n\t" \
  "v_pk_add_f32 v[26:27], v[26:27], v[106:107]\n\t" \
  "v_pk_add_f32 v[28:29], v[28:29], v[108:109]\n\t" \
  "v_pk_add_f32 v[30:31], v[30:31], v[110:111]\n\t" \
  "v_pk_add_f32 v[32:33], v[32:33], v[112:113]\n\t" \
  "v_pk_add_f32 v[34:35], v[34:35], v[114:115]\n\t" \
  "v_pk_add_f32 v[36:37], v[36:37], v[116:117]\n\t" \
  "v_pk_add_f32 v[38:39], v[38:39], v[118:119]\n\t" \
  "v_pk_add_f32 v[40:41], v[40:41], v[120:121]\n\t" \
  "v_pk_add_f32 v[42:43], v[42:43], v[122:123]\n\t" \
  "s_waitcnt lgkmcnt(0)\n\t" \
  "s_branch LT_%=\n\t" \
  "LSs1_%=:\n\t" \
  CNT1 \
  "s_sub_u32 s43, s43, 1\n\t" \
  "s_cbranch_scc1 LQs1_%=\n\t" \
  "s_sleep %[oback]\n\t" \
  "global_load_dwordx4 v[124:127], %[vd], %[oa1] sc1\n\t" \
  "global_load_dwordx4 v[128:131], %[vk], %[oa1] sc1\n\t" \
  "s_waitcnt vmcnt(0)\n\t" \
  "v_max_u32 %[tk], v124, v128\n\t" \
  "v_cmp_eq_u32 vcc, -1, %[tk]\n\t" \
  "s_cbranch_vccnz LSs1_%=\n\t" \
  "s_branch LQs1_%=\n\t" \
  "LSs2_%=:\n\t" \
  CNT1 \
  "s_sub_u32 s43, s43, 1\n\t" \
  "s_cbranch_scc1 LQs2_%=\n\t" \
  "s_sleep %[oback]\n\t" \
  "global_load_dwordx4 v[132:135], %[vd], %[oa2] sc1\n\t" \
  "global_load_dwordx4 v[136:139], %[vk], %[oa2] sc1\n\t" \
  "s_waitcnt vmcnt(0)\n\t" \
  "v_max_u32 %[tk], v132, v136\n\t" \
  "v_cmp_eq_u32 vcc, -1, %[tk]\n\t" \
  "s_cbranch_vccnz LSs2_%=\n\t" \
  "s_branch LQs2_%=\n\t" \
  "LSs3_%=:\n\t" \
  CNT1 \
  "s_sub_u32 s43, s43, 1\n\t" \
  "s_cbranch_scc1 LQs3_%=\n\t" \
  "s_sleep %[oback]\n\t" \
  "global_load_dwordx4 v[140:143], %[vd], %[oa3] sc1\n\t" \
  "global_load_dwordx4 v[144:147], %[vk], %[oa3] sc1\n\t" \
  "s_waitcnt vmcnt(0)\n\t" \
  "v_max_u32 %[tk], v140, v144\n\t" \
  "v_cmp_eq_u32 vcc, -1, %[tk]\n\t" \
  "s_cbranch_vccnz LSs3_%=\n\t" \
  "s_branch LQs3_%=\n\t" \
  "LSs4_%=:\n\t" \
  CNT1 \
  "s_sub_u32 s43, s43, 1\n\t" \
  "s_cbranch_scc1 LQs4_%=\n\t" \
  "s_sleep %[oback]\n\t" \
  "global_load_dwordx4 v[148:151], %[vd], %[oa4] sc1\n\t" \
  "global_load_dwordx4 v[152:155], %[vk], %[oa4] sc1\n\t" \
  "s_waitcnt vmcnt(0)\n\t" \
  "v_max3_u32 %[tk], v148, v150, v152\n\t" \
  "v_max_u32 %[tk], %[tk], v154\n\t" \
  "v_cmp_eq_u32 vcc, -1, %[tk]\n\t" \
  "s_cbranch_vccnz LSs4_%=\n\t" \
  "s_branch LQs4_%=\n\t" \
  "LSs5_%=:\n\t" \
  CNT1 \
  "s_sub_u32 s43, s43, 1\n\t" \
  "s_cbranch_scc1 LQs5_%=\n\t" \
  "s_sleep %[oback]\n\t" \
  "global_load_dwordx4 v[156:159], %[vd], %[oa5] sc1\n\t" \
  "global_load_dwordx4 v[124:127], %[vk], %[oa5] sc1\n\t" \
  "s_waitcnt vmcnt(0)\n\t" \
  "v_max_u32 %[tk], v156, v124\n\t" \
  "v_cmp_eq_u32 vcc, -1, %[tk]\n\t" \
  "s_cbranch_vccnz LSs5_%=\n\t" \
  "s_branch LQs5_%=\n\t" \
  "LSs6_%=:\n\t" \
  CNT1 \
  "s_sub_u32 s43, s43, 1\n\t" \
  "s_cbranch_scc1 LQs6_%=\n\t" \
  "s_sleep %[oback]\n\t" \
  "global_load_dwordx4 v[128:131], %[vd], %[oa6] sc1\n\t" \
  "global_load_dwordx4 v[132:135], %[vk], %[oa6] sc1\n\t" \
  "s_waitcnt vmcnt(0)\n\t" \
  "v_max_u32 %[tk], v128, v132\n\t" \
  "v_cmp_eq_u32 vcc, -1, %[tk]\n\t" \
  "s_cbranch_vccnz LSs6_%=\n\t" \
  "s_branch LQs6_%=\n\t" \
  "LSd7_%=:\n\t" \
  CNT1 \
  "s_sub_u32 s43, s43, 1\n\t" \
  "s_cbranch_scc1 LQd7_%=\n\t" \
  "s_sleep %[oback]\n\t" \
  "global_load_dwordx4 v[136:139], %[vd], %[oa7] sc1\n\t" \
  "global_load_dwordx4 v[140:143], %[vd], %[oa8] sc1\n\t" \
  "s_waitcnt vmcnt(0)\n\t" \
  "v_max_u32 %[tk], v136, v140\n\t" \
  "v_cmp_eq_u32 vcc, -1, %[tk]\n\t" \
  "s_cbranch_vccnz LSd7_%=\n\t" \
  "s_branch LQd7_%=\n\t" \
  "LSd9_%=:\n\t" \
  CNT1 \
  "s_sub_u32 s43, s43, 1\n\t" \
  "s_cbranch_scc1 LQd9_%=\n\t" \
  "s_sleep %[oback]\n\t" \
  "global_load_dwordx4 v[144:147], %[vd], %[oa9] sc1\n\t" \
  "global_load_dwordx4 v[148:151], %[vk], %[oa7] sc1\n\t" \
  "s_waitcnt vmcnt(0)\n\t" \
  "v_max3_u32 %[tk], v144, v146, v148\n\t" \
  "v_cmp_eq_u32 vcc, -1, %[tk]\n\t" \
  "s_cbranch_vccnz LSd9_%=\n\t" \
  "s_branch LQd9_%=\n\t" \
  "LSk8_%=:\n\t" \
  CNT1 \
  "s_sub_u32 s43, s43, 1\n\t" \
  "s_cbranch_scc1 LQk8_%=\n\t" \
  "s_sleep %[oback]\n\t" \
  "global_load_dwordx4 v[152:155], %[vk], %[oa8] sc1\n\t" \
  "global_load_dwordx4 v[156:159], %[vk], %[oa9] sc1\n\t" \
  "s_waitcnt vmcnt(0)\n\t" \
  "v_max3_u32 %[tk], v152, v156, v158\n\t" \
  "v_cmp_eq_u32 vcc, -1, %[tk]\n\t" \
  "s_cbranch_vccnz LSk8_%=\n\t" \
  "s_branch LQk8_%=\n\t" \
  "LSh1_%=:\n\t" \
  CNT1 \
  "s_sub_u32 s43, s43, 1\n\t" \
  "s_cbranch_scc1 LQh1_%=\n\t" \
  "s_sleep %[oback]\n\t" \
  "global_load_dwordx4 v[140:143], %[vk], %[oa1] sc1\n\t" \
  "s_waitcnt vmcnt(0)\n\t" \
  "v_cmp_eq_u32 vcc, -1, v140\n\t" \
  "s_cbranch_vccnz LSh1_%=\n\t" \
  "s_branch LQh1_%=\n\t" \
  "LSh2_%=:\n\t" \
  CNT1 \
  "s_sub_u32 s43, s43, 1\n\t" \
  "s_cbranch_scc1 LQh2_%=\n\t" \
  "s_sleep %[oback]\n\t" \
  "global_load_dwordx4 v[144:147], %[vk], %[oa2] sc1\n\t" \
  "s_waitcnt vmcnt(0)\n\t" \
  "v_cmp_eq_u32 vcc, -1, v144\n\t" \
  "s_cbranch_vccnz LSh2_%=\n\t" \
  "s_branch LQh2_%=\n\t" \
  "LSh3_%=:\n\t" \
  CNT1 \
  "s_sub_u32 s43, s43, 1\n\t" \
  "s_cbranch_scc1 LQh3_%=\n\t" \
  "s_sleep %[oback]\n\t" \
  "global_load_dwordx4 v[148:151], %[vk], %[oa3] sc1\n\t" \
  "s_waitcnt vmcnt(0)\n\t" \
  "v_cmp_eq_u32 vcc, -1, v148\n\t" \
  "s_cbranch_vccnz LSh3_%=\n\t" \
  "s_branch LQh3_%=\n\t" \
  "LSh4_%=:\n\t" \
  CNT1 \
  "s_sub_u32 s43, s43, 1\n\t" \
  "s_cbranch_scc1 LQh4_%=\n\t" \
  "s_sleep %[oback]\n\t" \
  "global_load_dwordx4 v[152:155], %[vk], %[oa4] sc1\n\t" \
  "s_waitcnt vmcnt(0)\n\t" \
  "v_max_u32 %[tk], v152, v154\n\t" \
  "v_cmp_eq_u32 vcc, -1, %[tk]\n\t" \
  "s_cbranch_vccnz LSh4_%=\n\t" \
  "s_branch LQh4_%=\n\t" \
  "LSh0_%=:\n\t" \
  CNT1 \
  "s_sub_u32 s43, s43, 1\n\t" \
  "s_cbranch_scc1 LQh0_%=\n\t" \
  "s_sleep %[oback]\n\t" \
  "global_load_dwordx4 v[156:159], %[vk], %[oa0] sc1\n\t" \
  "s_waitcnt vmcnt(0)\n\t" \
  "v_cmp_eq_u32 vcc, -1, v156\n\t" \
  "s_cbranch_vccnz LSh0_%=\n\t" \
  "s_branch LQh0_%=\n\t" \
  "LZ_%=:\n\t" \
  ""
#define COL_ASM \
  "s_mov_b32 s43, %[cap]\n\t" \
  "v_xor_b32 v101, 0x400, %[lq]\n\t" \
  "v_xor_b32 v102, 0x800, %[lq]\n\t" \
  "v_xor_b32 v103, 0xc00, %[lq]\n\t" \
  "v_xor_b32 v104, 0x1000, %[lq]\n\t" \
  "v_xor_b32 v105, 0x1400, %[lq]\n\t" \
  "v_xor_b32 v106, 0x1800, %[lq]\n\t" \
  "v_xor_b32 v107, 0x1c00, %[lq]\n\t" \
  "LC0_%=:\n\t" \
  "ds_read_b32 v96, %[ac]\n\t" \
  "s_waitcnt lgkmcnt(0)\n\t" \
  "v_cmp_gt_u32 vcc, %[nf], v96\n\t" \
  "s_cbranch_vccz LC1_%=\n\t" \
  "s_sleep 1\n\t" \
  "s_sub_u32 s43, s43, 1\n\t" \
  "s_cbranch_scc0 LC0_%=\n\t" \
  "LC1_%=:\n\t" \
  "s_setprio 2\n\t" \
  "s_sleep %[pdel]\n\t" \
  "global_load_dwordx4 v[68:71], v101, %[sp] sc1\n\t" \
  "global_load_dwordx4 v[72:75], v102, %[sp] sc1\n\t" \
  "global_load_dwordx4 v[76:79], v103, %[sp] sc1\n\t" \
  "global_load_dwordx4 v[80:83], v104, %[sp] sc1\n\t" \
  "global_load_dwordx4 v[84:87], v105, %[sp] sc1\n\t" \
  "global_load_dwordx4 v[88:91], v106, %[sp] sc1\n\t" \
  "global_load_dwordx4 v[92:95], v107, %[sp] sc1\n\t" \
  "ds_read_b128 v[64:67], %[ao]\n\t" \
  "s_waitcnt lgkmcnt(0)\n\t" \
  "LC2_%=:\n\t" \
  "s_waitcnt vmcnt(4)\n\t" \
  "v_pk_add_f32 v[108:109], v[64:65], v[68:69]\n\t" \
  "v_add_f32 v110, v66, v70\n\t" \
  "v_pk_add_f32 v[112:113], v[72:73], v[76:77]\n\t" \
  "v_add_f32 v114, v74, v78\n\t" \
  "v_pk_add_f32 v[108:109], v[108:109], v[112:113]\n\t" \
  "v_add_f32 v110, v110, v114\n\t" \
  "s_waitcnt vmcnt(0)\n\t" \
  "v_pk_add_f32 v[116:117], v[80:81], v[84:85]\n\t" \
  "v_add_f32 v118, v82, v86\n\t" \
  "v_pk_add_f32 v[120:121], v[88:89], v[92:93]\n\t" \
  "v_add_f32 v122, v90, v94\n\t" \
  "v_pk_add_f32 v[116:117], v[116:117], v[120:121]\n\t" \
  "v_add_f32 v118, v118, v122\n\t" \
  "v_pk_add_f32 v[108:109], v[108:109], v[116:117]\n\t" \
  "v_add_f32 v110, v110, v118\n\t" \
  STP(0) \
  "v_mul_f32 v112, 0x3acccccd, v108\n\t" \
  "v_add_f32 v111, v108, v109\n\t" \
  "v_mul_f32 v113, v112, v112\n\t" \
  "v_cmp_u_f32 vcc, v111, v110\n\t" \
  "v_fma_f32 v113, v110, %[rdm], -v113\n\t" \
  "s_cbranch_vccnz LC5_%=\n\t" \
  "LC3_%=:\n\t" \
  "v_max_f32 v113, 0, v113\n\t" \
  "v_cvt_pk_bf16_f32 v113, v113, v113\n\t" \
  "v_lshlrev_b32 v113, 16, v113\n\t" \
  "v_add_f32 v113, 0x3727c5ac, v113\n\t" \
  "v_cvt_pk_bf16_f32 v113, v113, v113\n\t" \
  "v_lshlrev_b32 v113, 16, v113\n\t" \
  "v_sqrt_f32 v113, v113\n\t" \
  "v_cvt_pk_bf16_f32 v112, v112, v112\n\t" \
  "v_lshlrev_b32 v112, 16, v112\n\t" \
  "v_cvt_pk_bf16_f32 v113, v113, v113\n\t" \
  "v_lshlrev_b32 v113, 16, v113\n\t" \
  "v_rcp_f32 v113, v113\n\t" \
  "v_fma_f32 v114, -%[swr], v112, v109\n\t" \
  "v_mul_f32 v114, v114, v113\n\t" \
  "v_ashrrev_i32 v115, 31, v114\n\t" \
  "v_or_b32 v115, 0x80000000, v115\n\t" \
  "v_xor_b32 v114, v114, v115\n\t" \
  "v_and_or_b32 v114, v114, %[msk], %[rid]\n\t" \
  "s_nop 1\n\t" \
  "v_max_u32_dpp v114, v114, v114 quad_perm:[1,0,3,2] row_mask:0xf bank_mask:0xf bound_ctrl:1\n\t" \
  "ds_write_b64 %[at], v[112:113]\n\t" \
  "s_nop 0\n\t" \
  "v_max_u32_dpp v114, v114, v114 quad_perm:[2,3,0,1] row_mask:0xf bank_mask:0xf bound_ctrl:1\n\t" \
  "s_nop 1\n\t" \
  "v_max_u32_dpp v114, v114, v114 row_half_mirror row_mask:0xf bank_mask:0xf bound_ctrl:1\n\t" \
  "s_nop 1\n\t" \
  "v_max_u32_dpp v114, v114, v114 row_mirror row_mask:0xf bank_mask:0xf bound_ctrl:1\n\t" \
  "s_nop 0\n\t" \
  "v_readlane_b32 s40, v114, 0\n\t" \
  "v_readlane_b32 s41, v114, 16\n\t" \
  "v_readlane_b32 s42, v114, 32\n\t" \
  "s_max_u32 s40, s40, s41\n\t" \
  "s_max_u32 s40, s40, s42\n\t" \
  "v_mov_b32 v98, s40\n\t" \
  "s_mov_b32 exec_lo, 0x01010101\n\t" \
  "s_mov_b32 exec_hi, 0x0101\n\t" \
  "global_store_dwordx2 %[zr], v[98:99], %[kp] sc1\n\t" \
  "ds_write_b32 %[ak], v98\n\t" \
  "s_mov_b64 exec, -1\n\t" \
  "s_and_b32 s41, s40, 0x7ff\n\t" \
  "s_sub_u32 s41, %[rb], s41\n\t" \
  "s_mul_i32 s42, s41, 160\n\t" \
  "v_add_u32 v115, s42, %[ah]\n\t" \
  "ds_read_b64 v[116:117], v115\n\t" \
  "v_readlane_b32 s44, v112, s41\n\t" \
  "v_readlane_b32 s46, v113, s41\n\t" \
  "s_waitcnt lgkmcnt(0)\n\t" \
  "s_mov_b32 exec_hi, 0\n\t" \
  "v_lshlrev_b32 v118, 16, v116\n\t" \
  "v_and_b32 v119, 0xffff0000, v116\n\t" \
  "v_lshlrev_b32 v120, 16, v117\n\t" \
  "v_and_b32 v121, 0xffff0000, v117\n\t" \
  "v_pk_add_f32 v[118:119], v[118:119], s[44:45] op_sel_hi:[1,0] neg_lo:[0,1] neg_hi:[0,1]\n\t" \
  "v_pk_add_f32 v[120:121], v[120:121], s[44:45] op_sel_hi:[1,0] neg_lo:[0,1] neg_hi:[0,1]\n\t" \
  "v_cvt_pk_bf16_f32 v116, v118, v119\n\t" \
  "v_cvt_pk_bf16_f32 v117, v120, v121\n\t" \
  "v_lshlrev_b32 v118, 16, v116\n\t" \
  "v_and_b32 v119, 0xffff0000, v116\n\t" \
  "v_lshlrev_b32 v120, 16, v117\n\t" \
  "v_and_b32 v121, 0xffff0000, v117\n\t" \
  "v_pk_mul_f32 v[118:119], v[118:119], s[46:47] op_sel_hi:[1,0]\n\t" \
  "v_pk_mul_f32 v[120:121], v[120:121], s[46:47] op_sel_hi:[1,0]\n\t" \
  "v_cvt_pk_bf16_f32 v116, v118, v119\n\t" \
  "v_cvt_pk_bf16_f32 v117, v120, v121\n\t" \
  "s_mov_b32 exec_hi, -1\n\t" \
  "global_store_dwordx2 %[co], v[116:117], %[cp] sc1\n\t" \
  "s_branch LC9_%=\n\t" \
  "LC5_%=:\n\t" \
  "s_sub_u32 s43, s43, 1\n\t" \
  "s_cbranch_scc1 LC3_%=\n\t" \
  "s_mov_b32 s40, 0\n\t" \
  "v_max3_u32 v96, v68, v69, v70\n\t" \
  "v_cmp_eq_u32 vcc, -1, v96\n\t" \
  "s_cbranch_vccz LC61_%=\n\t" \
  "global_load_dwordx4 v[68:71], v101, %[sp] sc1\n\t" \
  "s_mov_b32 s40, 1\n\t" \
  "LC61_%=:\n\t" \
  "v_max3_u32 v96, v72, v73, v74\n\t" \
  "v_cmp_eq_u32 vcc, -1, v96\n\t" \
  "s_cbranch_vccz LC62_%=\n\t" \
  "global_load_dwordx4 v[72:75], v102, %[sp] sc1\n\t" \
  "s_mov_b32 s40, 1\n\t" \
  "LC62_%=:\n\t" \
  "v_max3_u32 v96, v76, v77, v78\n\t" \
  "v_cmp_eq_u32 vcc, -1, v96\n\t" \
  "s_cbranch_vccz LC63_%=\n\t" \
  "global_load_dwordx4 v[76:79], v103, %[sp] sc1\n\t" \
  "s_mov_b32 s40, 1\n\t" \
  "LC63_%=:\n\t" \
  "v_max3_u32 v96, v80, v81, v82\n\t" \
  "v_cmp_eq_u32 vcc, -1, v96\n\t" \
  "s_cbranch_vccz LC64_%=\n\t" \
  "global_load_dwordx4 v[80:83], v104, %[sp] sc1\n\t" \
  "s_mov_b32 s40, 1\n\t" \
  "LC64_%=:\n\t" \
  "v_max3_u32 v96, v84, v85, v86\n\t" \
  "v_cmp_eq_u32 vcc, -1, v96\n\t" \
  "s_cbranch_vccz LC65_%=\n\t" \
  "global_load_dwordx4 v[84:87], v105, %[sp] sc1\n\t" \
  "s_mov_b32 s40, 1\n\t" \
  "LC65_%=:\n\t" \
  "v_max3_u32 v96, v88, v89, v90\n\t" \
  "v_cmp_eq_u32 vcc, -1, v96\n\t" \
  "s_cbranch_vccz LC66_%=\n\t" \
  "global_load_dwordx4 v[88:91], v106, %[sp] sc1\n\t" \
  "s_mov_b32 s40, 1\n\t" \
  "LC66_%=:\n\t" \
  "v_max3_u32 v96, v92, v93, v94\n\t" \
  "v_cmp_eq_u32 vcc, -1, v96\n\t" \
  "s_cbranch_vccz LC67_%=\n\t" \
  "global_load_dwordx4 v[92:95], v107, %[sp] sc1\n\t" \
  "s_mov_b32 s40, 1\n\t" \
  "LC67_%=:\n\t" \
  "s_cmp_eq_u32 s40, 0\n\t" \
  "s_cbranch_scc1 LC3_%=\n\t" \
  "s_waitcnt vmcnt(0)\n\t" \
  "s_branch LC2_%=\n\t" \
  "LC9_%=:\n\t" \
  STP(1) \
  ""
extern "C" __global__ __launch_bounds__(768, 3) void fused_block(const u16* x, const u16* wqkv, const u16* wo, const u16* w_r,
                                                   const u16* w_gate, const u16* w_up, const u16* w_down, u16* out,
                                                   char* ws) {
  extern __shared__ __attribute__((aligned(16))) char smem[];
  const int tid = threadIdx.x;
  const int lane = tid & 63;
  const int w = __builtin_amdgcn_readfirstlane(tid >> 6);
  const int g = lane >> 4, r16 = lane & 15, q4 = (lane & 15) >> 2, p4 = lane & 3;
  const int bid = blockIdx.x;
  const int xcd = bid & 7;
  const int bidx = bid >> 3;
  const u32 l16 = lane * 16;
  u32 tv = 0;
  LSTAMP(0)
  if (w == 11) {
    u64 pc;
    asm volatile("s_getpc_b64 %0" : "=s"(pc));
    if (lane < 10 && bidx * 10 + lane < CODE_LINES) touch_line((const void*)(pc + (bidx * 10 + lane) * 128));
  }

  if (bidx >= 26) {
    const int fb = xcd * 6 + (bidx - 26);
    const u32 tstart = (u32)__builtin_amdgcn_s_memrealtime();
    const bool hurry = ((((u32)(u64)w_gate + fb * 64) >> 7) & 7u) == 3u;
#define WAIT_UNTIL(T)                                                                      \
  _Pragma("nounroll") for (int it = 0; it < 1000; ++it) {                                  \
    if ((int)((u32)__builtin_amdgcn_s_memrealtime() - tstart) >= (T)) break;               \
    __builtin_amdgcn_s_sleep(4);                                                           \
  }
    if (w < 3) {
      int li = (bidx - 26) * 160 + w * 64 + lane;
      if (w < 2 || lane < 32) touch_line((const char*)x + (size_t)(((xcd >> 2) * 13 + 6) * 80 - 16) * 1280 + li * 128);
    }
    u32x2 ep;
    u32 tag0;
    asm volatile("global_load_dwordx2 %0, %1, off sc1" : "=v"(ep) : "v"(ws + WS_EPOCH) : "memory");
    {
      int wi = (((xcd >> 1) & 1) * 6 + (bidx - 26)) * 12 + w;
      wi = wi > 129 ? 129 : wi;
      int row = (xcd >> 2) * 1040 + (xcd & 1) * 520 + wi * 4 + g;
      row = row > SEQ - 1 ? SEQ - 1 : row;
      const u32 vo = row * 1280 + r16 * 16;
      u32x4 xr[5];
#pragma unroll
      for (int i = 0; i < 5; ++i)
        asm volatile("global_load_dwordx4 %0, %1, %2 offset:%3" : "=v"(xr[i]) : "v"(vo), "s"(x), "n"(i * 256) : "memory");
      asm volatile("s_waitcnt vmcnt(0)" ::: "memory");
      asm volatile("" : "+v"(xr[0]), "+v"(xr[1]), "+v"(xr[2]), "+v"(xr[3]), "+v"(xr[4]), "+v"(ep)::"memory");
      tag0 = ep[0] + (ep[0] == SENT - 1u ? 2u : 1u);
      float sm = 0.f;
#pragma unroll
      for (int i = 0; i < 5; ++i)
#pragma unroll
        for (int e = 0; e < 4; ++e) sm = dot2b(xr[i][e], 0x3F803F80u, sm);
      const float mean = row16_sum(sm) * (1.0f / DM);
      float ss = 0.f;
#pragma unroll
      for (int i = 0; i < 5; ++i)
#pragma unroll
        for (int e = 0; e < 4; ++e) {
          const float a = lo_f(xr[i][e]) - mean, b = hi_f(xr[i][e]) - mean;
          ss = __builtin_fmaf(a, a, ss);
          ss = __builtin_fmaf(b, b, ss);
        }
      const float var = row16_sum(ss) * (1.0f / DM);
      const float den = rbf(__builtin_amdgcn_sqrtf(rbf(rbf(var) + 1e-5f)));
      if (r16 == 0) GST2((u32)(row * 8), ws + WS_LN1, 0, ((u32x2){cvt2((f32x2){mean, den}), tag0}));
    }
    u32x4 va[2][5], vb[5];
    const int vrk = (fb >> 4) * 14 + (int)(((((u32)(u64)w_gate + fb * 64) >> 6) - 8u) & 15u);
    const int vq0 = vrk * 4 + w * 2;
    u32x4 ra;
    u32 rb5;
    asm volatile("global_load_dwordx4 %0, %2, %4\n\tglobal_load_dword %1, %3, %4 offset:1024"
                   : "=&v"(ra), "=&v"(rb5) : "v"(l16), "v"((u32)(lane * 4)), "s"(w_r) : "memory");
    if (w >= 2) {
      WAIT_UNTIL(hurry ? 0 : PDELAY + 12 * (w - 2))
      const u32x4 s4 = (u32x4){SENT, SENT, SENT, SENT};
      const char* fp = ws + WS_FILL + (size_t)fb * 73728 + (w - 2) * 7168 + 4096;
      GST4(l16, fp, -4096, s4); GST4(l16, fp, -3072, s4); GST4(l16, fp, -2048, s4); GST4(l16, fp, -1024, s4);
      GST4(l16, fp, 0, s4); GST4(l16, fp, 1024, s4); GST4(l16, fp, 2048, s4);
      if (w < 4) GST4(l16, ws + WS_FILL + (size_t)fb * 73728 + 69632 + w * 1024, 0, s4);
      if (w >= 5) GST4((u32)(((w - 5) * 8 + (lane >> 3)) * 6144 + (lane & 7) * 16), ws + WS_PK + fb * 128, 0, s4);
    }
    if (w == 0) {
      const u64 e1 = (((u64)ep[1] << 32) | ep[0]) + (ep[0] == SENT - 1u ? 2ull : 1ull);
      if (lane == 0) *(u32x4*)(smem + CTL_OFF) = (u32x4){(u32)e1, (u32)(e1 >> 32), 0u, 0u};
      if (lane >= 1 && lane < 8) *(u32x4*)(smem + CTL_OFF + lane * 16) = (u32x4){0u, 0u, 0u, 0u};
      if (lane >= 32) *(u32x4*)(smem + KEY_OFF - 512 + lane * 16) = (u32x4){0u, 0u, 0u, 0u};
    }
    if (w >= 6) {
      const u32 o1 = tid >= 636 ? 0x3F803F80u : 0u;
      if (tid < 720) *(u32x4*)(smem + HN_OFF + (tid - 384) * 16) = (u32x4){o1, o1, o1, o1};
    }
    if (w < 2 && !hurry && vrk < 40) {
      WAIT_UNTIL(VDELAY)
      const u32 vo = (vq0 * 4 + g) * 1280 + r16 * 16;
#pragma unroll
      for (int i = 0; i < 5; ++i)
        asm volatile("global_load_dwordx4 %0, %3, %4 offset:%8\n\tglobal_load_dwordx4 %1, %3, %5 offset:%8\n\tglobal_load_dwordx4 %2, %6, %7 offset:%8"
                     : "=&v"(va[0][i]), "=&v"(va[1][i]), "=&v"(vb[i]) : "v"(vo), "s"(wo), "s"((const char*)wo + 5120), "v"((u32)(r16 * 16)), "s"(w_r), "n"(i * 256) : "memory");
      asm volatile("s_waitcnt vmcnt(0)" : "+v"(va[0][0]), "+v"(va[0][1]), "+v"(va[0][2]), "+v"(va[0][3]), "+v"(va[0][4]), "+v"(va[1][0]), "+v"(va[1][1]),
                   "+v"(va[1][2]), "+v"(va[1][3]), "+v"(va[1][4]), "+v"(vb[0]), "+v"(vb[1]), "+v"(vb[2]), "+v"(vb[3]), "+v"(vb[4])::"memory");
      float sb = 0.f;
#pragma unroll
      for (int i = 0; i < 20; ++i) sb = dot2b(vb[i >> 2][i & 3], 0x3F803F80u, sb);
      sb = row16_sum(sb) * (1.0f / DM);
      u32x4 pk2[2];
#pragma unroll
      for (int k = 0; k < 2; ++k) {
        float d = 0.f, sa = 0.f;
#pragma unroll
        for (int i = 0; i < 20; ++i) {
          d = dot2b(va[k][i >> 2][i & 3], vb[i >> 2][i & 3], d);
          sa = dot2b(va[k][i >> 2][i & 3], 0x3F803F80u, sa);
        }
        const u32 vp = __float_as_uint(row16_sum(d - sa * sb));
        pk2[k] = (u32x4){cvt2((f32x2){__uint_as_float(__builtin_amdgcn_readlane(vp, 0)), __uint_as_float(__builtin_amdgcn_readlane(vp, 16))}), tag0,
                          cvt2((f32x2){__uint_as_float(__builtin_amdgcn_readlane(vp, 32)), __uint_as_float(__builtin_amdgcn_readlane(vp, 48))}), tag0};
      }
      if (lane < 8) GST4(l16, ws + WS_VP + vq0 * 64, 0, ((lane & 1) ? pk2[1] : pk2[0]));
      LSTAMP(6)
    } else
      asm volatile("s_waitcnt vmcnt(0)" ::: "memory");
    asm volatile("" : "+v"(ra), "+v"(rb5)::"memory");
    LDS_BARRIER();
    const u32x2 tg = *(const u32x2*)(smem + CTL_OFF);
    const u32 tag = tg[0];
    if (w == 2 && lane == 0) GST2(0u, ws + WS_HFLAG + fb * 128, 0, tg);
    LSTAMP(1)
    if (w >= 3) {
      int li = (bidx - 26) * 534 + (w - 3) * 64 + lane;
      li = li > 3199 ? 3199 : li;
      const int rr = li / 5;
      if (w < 11 || lane < 22) touch_line((const char*)wo + rr * 1280 + ((xcd >> 1) & 1) * 640 + (li - rr * 5) * 128);
    }
    WAIT_UNTIL(hurry ? 0 : FDELAY)
    asm volatile("s_barrier" ::: "memory");
    if (w >= 2) {
      const int ch = (lane & 7) ^ (((lane >> 4) & 1) * 2 + ((w + fb) & 1) * 4);
      const u32 vg = (lane >> 3) * (FF * 2) + fb * 64 + (ch & 3) * 16;
      const u64 gsel = (ch & 4) ? (u64)w_up : (u64)w_gate;
#pragma nounroll
      for (int n0 = w - 2; n0 < 80; n0 += 10) {
        const int n = (fb & 1) ? 79 - n0 : n0;
        const u64 p = gsel + vg + (u32)(n * 8 * FF * 2);
        asm volatile("s_mov_b32 m0, %1\n\ts_nop 0\n\tglobal_load_lds_dwordx4 %0, off" FNT ::"v"(p), "s"(n * 1024) : "memory");
      }
#pragma nounroll
      for (int n = 11 - w; n < 40; n += 10) {
        const u32 row = (u32)(n * 64 + lane) / 80u;
        asm volatile("s_mov_b32 m0, %2\n\ts_nop 0\n\tglobal_load_lds_dwordx4 %0, %1" FNT
                     ::"v"(l16 ^ ((row & 3u) * 32u + (row & 8u) * 16u)), "s"((const char*)w_down + (size_t)fb * 40960 + n * 1024), "s"(DOWN_OFF + n * 1024) : "memory");
      }
      asm volatile("s_waitcnt vmcnt(0)" ::: "memory");
      if (lane == 0) asm volatile("ds_add_u32 %0, %1" ::"v"(CTL_OFF + 48), "v"(1u) : "memory");
    } else {
      WAIT_UNTIL(KSTART)
    }
    if (w >= 4) {
      const int v = w - 4, kq = v & 3, m = v >> 2;
#pragma nounroll
      for (int it = 0; it < SPIN_CAP; ++it) {
        if (__builtin_amdgcn_readfirstlane(lds_ld32(CTL_OFF + 48)) >= 10u) break;
        __builtin_amdgcn_s_sleep(1);
      }
      LSTAMP(2)
      bf16x8 gfr[5], ufr[5], dfr[5];
      {
        const int gx = (160 * kq + 8 * g + q4) * 128 + ((m ^ (q4 >> 1) ^ ((g & 1) * 2)) * 32) + p4 * 8;
        const char* db = smem + DOWN_OFF + (8 * g + q4) * 1280 + p4 * 8;
#pragma unroll
        for (int j = 0; j < 5; ++j) {
          gfr[j] = tr_frag(smem + gx + j * 4096, 512);
          ufr[j] = tr_frag(smem + (gx ^ 64) + j * 4096, 512);
          dfr[j] = tr_frag(db + ((v * 160 + j * 32) ^ (q4 * 32 + (g & 1) * 128)), 5120);
        }
#pragma unroll
        for (int j = 0; j < 5; ++j) asm volatile("" : "+v"(gfr[j]), "+v"(ufr[j]), "+v"(dfr[j]));
      }
      u32 ahn = HN_OFF + p4 * HSL + kq * 320 + g * 16, asw = ACT_OFF + p4 * ASL + kq * 256 + (q4 & 1) * 128 + m * 64 + g * 16, asr = ACT_OFF + (lane & 31) * 4;
      u32 aav = ACT_OFF + 4352 + v * 64 + (lane & 31) * 2, aar = ACT_OFF + 4352 + v * 64 + g * 16, vpo = v * 320 + r16 * 64 + g * 16, up3 = lane >= 32 ? 3u : 0u;
      asm volatile("" : "+v"(up3), "+v"(aar));
      const char* pdst = ws + WS_PART + fb * 2560;
      asm volatile("" : "+v"(ahn), "+v"(asw), "+v"(asr), "+v"(aav), "+v"(vpo));
      asm volatile("" : "+s"(pdst));
      int pass = DRY ? -1 : 0;
      asm volatile("" : "+s"(pass));
#pragma nounroll
      for (; pass < 2; ++pass) {
        asm volatile("s_cmp_lt_i32 %0, 0\n\ts_cbranch_scc1 1f\n\ts_barrier\n1:" ::"s"(pass) : "memory", "scc");
        LSTAMP(15)
        u32x4 bs;
#define GU(J, C0, C1) "v_mfma_f32_16x16x32_bf16 v[120:123], %[g" #J "], v[100+4*" #J ":103+4*" #J "], " C0 "\n\t" \
                      "v_mfma_f32_16x16x32_bf16 v[124:127], %[u" #J "], v[100+4*" #J ":103+4*" #J "], " C1 "\n\t"
#define GU1(J) GU(J, "v[120:123]", "v[124:127]")
        asm volatile("ds_read_b128 v[100:103], %[ah]\n\tds_read_b128 v[104:107], %[ah] offset:64\n\tds_read_b128 v[108:111], %[ah] offset:128\n\t"
                     "ds_read_b128 v[112:115], %[ah] offset:192\n\tds_read_b128 v[116:119], %[ah] offset:256\n\t"
                     "s_waitcnt lgkmcnt(4)\n\t" GU(0, "0", "0") "s_waitcnt lgkmcnt(3)\n\t" GU1(1) "s_waitcnt lgkmcnt(2)\n\t" GU1(2)
                     "s_waitcnt lgkmcnt(1)\n\t" GU1(3) "s_waitcnt lgkmcnt(0)\n\t" GU1(4)
                     "ds_read_b128 %[bs], %[ab]\n\ts_mov_b32 exec_lo, 0x00f000f0\n\ts_mov_b32 exec_hi, 0x00f000f0\n\ts_cmp_lt_i32 %[ps], 0\n\ts_nop 3\n\t"
                     "v_mov_b64 v[120:121], v[124:125]\n\tv_mov_b64 v[122:123], v[126:127]\n\t"
                     "s_mov_b32 exec_lo, 0x00ff00ff\n\ts_mov_b32 exec_hi, 0x00ff00ff\n\tds_write_b128 %[aw], v[120:123]\n\ts_mov_b64 exec, -1\n\t"
                     "s_setprio 3\n\ts_waitcnt lgkmcnt(0)\n\tv_cmp_eq_u32 vcc, 3, v129\n\ts_cbranch_scc1 2f\n\ts_barrier\n"
                     "2:\n\ts_cbranch_vccnz 1f\n\tds_read_b128 %[bs], %[ab]\n\ts_waitcnt lgkmcnt(0)\n1:"
                     : [bs] "=&{v[128:131]}"(bs)
                     : [ps] "s"(pass), [ah] "v"(ahn), [ab] "v"((u32)(CTL_OFF + 112)), [aw] "v"(asw), [g0] "v"(gfr[0]), [g1] "v"(gfr[1]), [g2] "v"(gfr[2]),
                       [g3] "v"(gfr[3]), [g4] "v"(gfr[4]), [u0] "v"(ufr[0]), [u1] "v"(ufr[1]), [u2] "v"(ufr[2]), [u3] "v"(ufr[3]), [u4] "v"(ufr[4])
                     : "memory", "vcc", "scc", "v100", "v101", "v102", "v103", "v104", "v105", "v106", "v107", "v108", "v109", "v110", "v111", "v112", "v113",
                       "v114", "v115", "v116", "v117", "v118", "v119", "v120", "v121", "v122", "v123", "v124", "v125", "v126", "v127");
        LSTAMP(14)
        const u32x2 best = {bs[2], bs[3]};
        f32x2 s0, s1, s2, s3;
        asm volatile("ds_read2_b32 %0, %4 offset0:0 offset1:32\n\tds_read2_b32 %1, %4 offset0:64 offset1:96\n\t"
                     "ds_read2_b32 %2, %4 offset0:128 offset1:160\n\tds_read2_b32 %3, %4 offset0:192 offset1:224"
                     : "=&v"(s0), "=&v"(s1), "=&v"(s2), "=&v"(s3) : "v"(asr + ((best[1] & 3u) | up3) * ASL) : "memory");
        float rd = __builtin_amdgcn_rcpf(hi_f(best[0])), mb = lo_f(best[0]);
        asm volatile("s_waitcnt lgkmcnt(0)" : "+v"(s0), "+v"(s1), "+v"(s2), "+v"(s3), "+v"(rd), "+v"(mb)::"memory");
        const f32x2 sm = (s0 + s1) + (s2 + s3);
        auto xg = __builtin_amdgcn_permlane32_swap(__float_as_uint(sm[0]), __float_as_uint(sm[0]), false, false);
        auto xu = __builtin_amdgcn_permlane32_swap(__float_as_uint(sm[1]), __float_as_uint(sm[1]), false, false);
        const f32x2 gu = unpk(cvt2((f32x2){(__uint_as_float(xg[0]) - mb * __uint_as_float(xg[1])) * rd,
                                          (__uint_as_float(xu[0]) - mb * __uint_as_float(xu[1])) * rd}));
        const float sg = rbf(gu[0] * __builtin_amdgcn_rcpf(1.0f + __expf(-gu[0])));
        const u32 av = f2bf(sg * gu[1]);
#define DN(J) "v_mfma_f32_16x16x32_bf16 v[104+4*" #J ":107+4*" #J "], %[d" #J "], v[100:103], 0\n\t"
#define MV(J) "s_lshl_b64 exec, exec, 1\n\tv_mov_b64 v[104:105], v[104+4*" #J ":105+4*" #J "]\n\tv_mov_b64 v[106:107], v[106+4*" #J ":107+4*" #J "]\n\t"
        asm volatile("ds_write_b16 %[aw], %[av]\n\tds_read_b128 v[100:103], %[ar]\n\ts_waitcnt lgkmcnt(0)\n\t" DN(0) DN(1) DN(2) DN(3) DN(4)
                     "s_mov_b32 exec_lo, 0x00010001\n\ts_mov_b32 exec_hi, 0x00010001\n\ts_nop 1\n\t" MV(1) MV(2) MV(3) MV(4)
                     "s_mov_b32 exec_lo, 0x001f001f\n\ts_mov_b32 exec_hi, 0x001f001f\n\ts_cmp_lt_i32 %[ps], 0\n\ts_cbranch_scc1 1f\n\t"
                     "global_store_dwordx4 %[vp], v[104:107], %[pd] sc1\n"
                     "1:\n\ts_mov_b64 exec, -1\n\ts_setprio 0"
                     ::[ps] "s"(pass), [aw] "v"(aav), [av] "v"(av), [ar] "v"(aar), [vp] "v"(vpo), [pd] "s"(pdst), [d0] "v"(dfr[0]), [d1] "v"(dfr[1]),
                       [d2] "v"(dfr[2]), [d3] "v"(dfr[3]), [d4] "v"(dfr[4])
                     : "memory", "scc", "v100", "v101", "v102", "v103", "v104", "v105", "v106", "v107", "v108", "v109", "v110", "v111", "v112", "v113",
                       "v114", "v115", "v116", "v117", "v118", "v119", "v120", "v121", "v122", "v123");
        if (pass) continue;
        LSTAMP(16)
        u32 fin = 0;
#pragma nounroll
        for (int it = 0; it < SPIN_CAP * 8; ++it) {
          fin = __builtin_amdgcn_readfirstlane(lds_ld32(CTL_OFF + 40));
          if (fin) break;
          __builtin_amdgcn_s_sleep(4);
        }
        if (fin != 2u) break;
        asm volatile("s_barrier" ::: "memory");
        pdst += WS_PART2 - WS_PART;
      }
      FLUSH_STAMPS
      return;
    }
    if (w == 0) {
      LSTAMP(8)
      u32 pk6;
#define STP3
#define STP3O
      asm volatile(
          "s_mov_b32 s41, %[cap]\n\ts_mov_b32 s40, 1\n\ts_mov_b64 s[46:47], 0\n\tv_mov_b32 v5, %[ln]\n\t"
          "s_mov_b32 s54, -1\n\ts_mov_b32 s55, 0x7ffff\n\tv_mov_b32 v12, 0\n\t"
#define PKLD(D) "s_mov_b64 exec, s[54:55]\n\tglobal_atomic_or_x2 " D ", %[vk], %[z], %[kb] sc0 sc1\n\ts_mov_b64 exec, -1\n\t"
          PKLD("v[8:9]") "s_sleep %[gap]\n\t" PKLD("v[10:11]") "s_sleep %[gap]\n\t" PKLD("v[14:15]")
#define MDPP(C) "s_nop 1\n\tv_max_u32_dpp v13, v13, v13 " C " row_mask:0xf bank_mask:0xf bound_ctrl:1\n\t"
#define PSTEP(L, D0, D1, DR)                                                                        \
          L "0:\n\t"                                                                               \
          "s_waitcnt vmcnt(2)\n\t"                                                                 \
          "v_cmp_eq_u32 s[42:43], " D1 ", %[tag]\n\t"                                              \
          "s_and_b64 exec, s[42:43], s[54:55]\n\ts_andn2_b64 s[54:55], s[54:55], s[42:43]\n\t"      \
          "v_and_or_b32 v12, " D0 ", %[msk], %[lid]\n\t"                                           \
          "v_mov_b32 v6, " D0 "\n\ts_mov_b64 exec, -1\n\t"                                         \
          "v_cmp_le_u32 vcc, s40, v12\n\t"                                                         \
          "s_cmp_eq_u64 vcc, s[46:47]\n\ts_cbranch_scc1 " L "1f\n\t"                              \
          "s_swappc_b64 s[50:51], s[48:49]\n"                                                      \
          L "1:\n\t"                                                                               \
          "s_cmp_eq_u64 s[54:55], 0\n\ts_cbranch_scc1 9f\n\t"                                     \
          PKLD(DR)
          "s_getpc_b64 s[48:49]\n\ts_add_u32 s48, s48, 12\n\ts_addc_u32 s49, s49, 0\n\ts_branch 10f\n\t"
          "v_mov_b32 v13, v12\n\t"
          MDPP("quad_perm:[1,0,3,2]") MDPP("quad_perm:[2,3,0,1]") MDPP("row_half_mirror") MDPP("row_mirror")
          "v_bfe_u32 v4, v6, 10, 6\n\tv_readlane_b32 s44, v13, 0\n\tv_readlane_b32 s45, v13, 16\n\tv_readlane_b32 s46, v13, 32\n\tv_readlane_b32 s47, v13, 48\n\t"
          "v_sub_u32 v4, %[rb], v4\n\ts_max_u32 s44, s44, s45\n\ts_max_u32 s46, s46, s47\n\ts_max_u32 s44, s44, s46\n\t"
          "s_and_b32 s45, s44, 0x7fffff\n\ts_bitset1_b32 s45, 23\n\ts_mul_hi_u32 s45, s45, %[shr]\n"
          "5:\n\ts_sub_u32 s40, s44, s45\n\ts_cselect_b32 s40, 1, s40\n\ts_max_u32 s40, s40, 1\n\tv_cmp_le_u32 s[46:47], s40, v12\n\ts_mul_i32 s45, s45, 3\n\ts_lshr_b32 s45, s45, 2\n\t"
          "s_bcnt1_i32_b64 s52, s[46:47]\n\ts_cmp_gt_u32 s52, 3\n\ts_cbranch_scc1 5b\n\t"
          "v_mbcnt_lo_u32_b32 v13, s46, 0\n\tv_mbcnt_hi_u32_b32 v13, s47, v13\n\tv_sub_u32 v7, %[ln], v13\n\tv_add_u32 v7, s52, v7\n\t"
          "v_cndmask_b32 v13, v7, v13, s[46:47]\n\tv_cndmask_b32 v7, 2, 1, s[46:47]\n\tv_cmp_gt_u32 s[44:45], 3, v13\n\t"
          "v_lshl_add_u32 v13, v13, 4, %[pa]\n\ts_mov_b64 exec, s[44:45]\n\tds_write_b128 v13, v[4:7]\n\ts_mov_b64 exec, -1\n\t"
          "s_setpc_b64 s[50:51]\n"
          PSTEP("1", "v8", "v9", "v[8:9]")
          PSTEP("2", "v10", "v11", "v[10:11]")
          PSTEP("3", "v14", "v15", "v[14:15]")
          "s_sub_u32 s41, s41, 1\n\ts_cbranch_scc0 10b\n\ts_swappc_b64 s[50:51], s[48:49]\n"
          "9:\n\t" STP3 "v_or_b32 v7, 4, v7\n\ts_mov_b64 exec, s[44:45]\n\tds_write_b32 v13, v7 offset:12\n\ts_mov_b64 exec, -1\n\tds_write_b32 %[at], v6\n\ts_waitcnt lgkmcnt(0)\n\ts_barrier\n\ts_waitcnt vmcnt(0)"
          : "=&{v6}"(pk6) STP3O : [cap] "n"(SPIN_CAP), [gap] "n"(PGAP), [vk] "v"((u32)(lane * 6144)), [kb] "s"(ws + WS_PK + fb * 128), [tag] "v"(tag), [z] "v"(0ull), [ln] "v"(lane),
            [msk] "s"(~1023u), [lid] "v"(64u | lane), [rb] "v"(2047 - ((lane >> 1) * 80 + (lane & 1) * 48)),
            [shr] "s"((u32)((1.0 - PTHR) * 4294967296.0)), [pa] "s"(POST_OFF), [at] "v"((u32)(KEY_OFF - 256 + lane * 4))
          : "memory", "vcc", "scc", "s40", "s41", "s42", "s43", "s44", "s45", "s46", "s47", "s48", "s49", "s50", "s51", "s52", "s54", "s55",
            "v4", "v5", "v7", "v8", "v9", "v10", "v11", "v12", "v13", "v14", "v15");
      const u32 now = (u32)__builtin_amdgcn_s_memrealtime();
      u32 kgo = __builtin_amdgcn_readfirstlane(now + KCD - 2 * (1023u - wave_max(lane < 51 ? (pk6 - (now >> 1) - 1u) & 1023u : 0u)));
      asm volatile("" : "+s"(kgo));
      asm volatile("s_barrier" ::: "memory");
      const u32x2 cho = *(const u32x2*)(smem + POST_OFF + (*(const u32*)(smem + CTL_OFF + 124) & 3u) * 16);
#define STP12
#define STP12O
      asm volatile(
          "s_mov_b32 s41, %[cap]\n\ts_mov_b32 s54, -1\n\ts_mov_b32 s55, 0x7ffff\n\tv_mov_b32 v12, 0\n\tv_mov_b32 v6, 1\n\tv_mov_b32 v7, %[cg]\n\t"
          "4:\n\ts_memrealtime s[42:43]\n\ts_waitcnt lgkmcnt(0)\n\ts_sub_u32 s42, s42, %[go]\n\ts_cmp_lt_i32 s42, 0\n\ts_cbranch_scc1 4b\n\t"
          "s_mov_b64 exec, s[54:55]\n\tglobal_load_dwordx2 v[8:9], %[vk], %[kb] sc1\n\ts_mov_b64 exec, -1\n\t"
#define KSTEP(L, D0, D1, DR)                                                                        \
          L "0:\n\t"                                                                               \
          "s_waitcnt vmcnt(" KW ")\n\t"                                                            \
          "v_cmp_eq_u32 s[42:43], " D1 ", %[tag]\n\t"                                              \
          "s_and_b64 exec, s[42:43], s[54:55]\n\tv_mov_b32 v12, " D0 "\n\ts_mov_b64 exec, -1\n\t"   \
          "s_andn2_b64 s[54:55], s[54:55], s[42:43]\n\ts_cbranch_scc0 9f\n\t"                     \
          PKLD(DR)
#define KW "0"
          KSTEP("3", "v8", "v9", "v[8:9]")
          "s_sleep %[gap]\n\t" PKLD("v[10:11]")
#undef KW
#define KW "1"
          KSTEP("1", "v8", "v9", "v[8:9]")
          KSTEP("2", "v10", "v11", "v[10:11]")
          "s_sub_u32 s41, s41, 1\n\ts_cbranch_scc0 10b\n"
          "9:\n\t"
          "v_readlane_b32 s40, v12, %[cg]\n\tds_write_b32 %[ak], v12\n\tv_mov_b32 v13, v12\n\t"
          "s_and_b32 s45, s40, 0x7ff\n\tv_cmp_lt_u32 vcc, s40, v12\n\ts_cmp_eq_u32 s45, %[cr]\n\ts_cselect_b64 vcc, vcc, -1\n\ts_cbranch_vccz 8f\n\t"
          MDPP("quad_perm:[1,0,3,2]") MDPP("quad_perm:[2,3,0,1]") MDPP("row_half_mirror") MDPP("row_mirror")
          "s_nop 0\n\tv_readlane_b32 s44, v13, 0\n\tv_readlane_b32 s45, v13, 16\n\tv_readlane_b32 s46, v13, 32\n\tv_readlane_b32 s47, v13, 48\n\t"
          "s_max_u32 s44, s44, s45\n\ts_max_u32 s46, s46, s47\n\ts_max_u32 s40, s44, s46\n\t"
          "v_cmp_eq_u32 vcc, s40, v12\n\tv_mov_b32 v6, 2\n\ts_ff1_i32_b64 s44, vcc\n\tv_mov_b32 v7, s44\n"
          "8:\n\tv_mov_b32 v4, s40\n\ts_mov_b64 exec, 1\n\tds_write_b128 %[ctl], v[4:7]\n\ts_mov_b64 exec, -1\n\t"
          STP12 "s_waitcnt vmcnt(0) lgkmcnt(0)"
          : STP12O : [cap] "n"(SPIN_CAP), [gap] "n"(KGAP), [go] "s"(kgo), [vk] "v"((u32)(lane * 6144)), [kb] "s"(ws + WS_PK + fb * 128 + 8), [tag] "v"(tag),
            [ctl] "v"((u32)(CTL_OFF + 32)), [ak] "v"((u32)(KEY_OFF + lane * 4)), [cr] "s"(__builtin_amdgcn_readfirstlane(cho[0])),
            [cg] "s"(__builtin_amdgcn_readfirstlane(cho[1])), [z] "v"(0ull)
          : "memory", "vcc", "scc", "s40", "s41", "s42", "s43", "s44", "s45", "s46", "s47", "s54", "s55", "v4", "v5", "v6", "v7", "v8", "v9", "v10",
            "v11", "v12", "v13", "v14", "v15");
      if (fb == 0 && lane == 0) GST2(0u, ws + WS_EPOCH, 0, tg);
      FLUSH_STAMPS
      return;
    }
    float swr = dot2b(rb5, 0x3F803F80u, 0.f);
#pragma unroll
    for (int e = 0; e < 4; ++e) swr = dot2b(ra[e], 0x3F803F80u, swr);
    swr = wave_sum(swr);
    u32 ah16 = HN_OFF + (w - 1) * HSL + l16, ah4 = HN_OFF + (w - 1) * HSL + 1024 + lane * 4, l4 = lane * 4;
    asm volatile("" : "+v"(swr), "+v"(ah16), "+v"(ah4), "+v"(l4));
    u32x4 h4, n4;
    u32 h1, n1;
#define DOT3(H, W) "v_dot2c_f32_bf16 v102, 0x3f803f80, " H "\n\tv_dot2c_f32_bf16 v103, " H ", " H "\n\tv_dot2c_f32_bf16 v104, " H ", " W "\n\t"
#define ADPP(C) "s_nop 1\n\tv_add_f32_dpp v102, v102, v102 " C " row_mask:0xf bank_mask:0xf bound_ctrl:1\n\t"
#define XDPP(C) "s_nop 1\n\tv_max_u32_dpp v102, v102, v102 " C " row_mask:0xf bank_mask:0xf bound_ctrl:1\n\t"
#define FPR
#define FPRO
    const char* rp = (const char*)w_r;
    u32 second = 0;
    u32x2 mine;
#pragma nounroll
    for (;;) {
      asm volatile(
          "s_mov_b32 s47, %[cap]\n\ts_mov_b32 s43, -1\n\ts_mov_b32 s46, -1\n"
          "7:\n\ts_mov_b64 s[48:49], %[rp]\n\ts_mov_b32 s45, 0\n\ts_branch 4f\n"
          "18:\n\ts_sleep 1\n"
          "1:\n\ts_sub_u32 s47, s47, 1\n\ts_cbranch_scc1 8f\n\ts_cmp_lg_u32 %[sec], 0\n\ts_cbranch_scc1 7b\n\t"
          "ds_read_b128 v[108:111], %[ap]\n\ts_memrealtime s[40:41]\n\ts_waitcnt lgkmcnt(0)\n\t"
          "v_readfirstlane_b32 s45, v111\n\tv_readfirstlane_b32 s46, v109\n\tv_readfirstlane_b32 s50, v110\n\ts_lshr_b32 s41, s40, 1\n\t"
          "s_cmp_eq_u32 s45, 6\n\ts_cbranch_scc1 8f\n\ts_bitcmp0_b32 s45, 0\n\ts_cbranch_scc1 18b\n\t"
          "s_cmp_eq_u32 s46, s42\n\ts_cbranch_scc1 6f\n\ts_cmp_eq_u32 s46, s43\n\ts_cbranch_scc1 2f\n\t"
          "s_sub_u32 s41, s41, s50\n\ts_and_b32 s41, s41, 0x3ff\n\ts_cmp_lt_u32 s41, %[tq]\n\ts_cbranch_scc1 18b\n\t"
          "s_mov_b32 s43, s46\n\ts_add_u32 s44, s50, 8\n\ts_cmp_ge_u32 s41, %[pc] - 20\n\ts_cbranch_scc1 1b\n\t"
          "s_mul_i32 s48, s46, 6144\n\tv_mov_b64 v[104:105], 0\n\tv_mov_b32 v101, s48\n\tv_mad_u32_u24 v101, %[l16], 48, v101\n\t"
          "s_mov_b64 exec, 0xff\n\tglobal_atomic_or_x2 v[102:103], v101, v[104:105], %[pk] sc0 sc1\n\ts_sub_u32 s50, s50, 512\n\ts_waitcnt vmcnt(0)\n\t"
          "v_cmp_eq_u32 vcc, v103, %[tag]\n\tv_subrev_u32 v102, s50, v102\n\tv_and_b32 v102, 0x3ff, v102\n\tv_cndmask_b32 v102, 0, v102, vcc\n\t"
          XDPP("quad_perm:[1,0,3,2]") XDPP("quad_perm:[2,3,0,1]") XDPP("row_half_mirror")
          "s_mov_b64 exec, -1\n\tv_readfirstlane_b32 s44, v102\n\ts_min_u32 s44, s44, 562\n\ts_add_u32 s44, s44, s50\n\ts_branch 1b\n"
          "2:\n\ts_sub_u32 s41, s41, s44\n\ts_and_b32 s41, s41, 0x3ff\n\ts_cmp_lt_u32 s41, %[pc] - 8\n\ts_cbranch_scc1 1b\n\t"
          "s_sub_u32 s41, %[pc], s41\n\ts_lshl1_add_u32 s51, s41, s40\n\ts_mul_i32 s48, s46, 1280\n\ts_add_u32 s48, s48, %[lo]\n\ts_addc_u32 s49, %[hi], 0\n\t" FPR
          "3:\n\ts_memrealtime s[40:41]\n\ts_waitcnt lgkmcnt(0)\n\ts_sub_u32 s40, s40, s51\n\ts_cmp_lt_i32 s40, 0\n\ts_cbranch_scc1 3b\n"
          "4:\n\tglobal_load_dwordx4 v[96:99], %[l16], s[48:49] sc1\n\tglobal_load_dword v100, %[l4], s[48:49] offset:1024 sc1\n\t"
          "v_mov_b64 v[102:103], 0\n\tv_mov_b32 v104, 0\n\ts_mov_b32 s42, -2\n\ts_waitcnt vmcnt(0)\n\t"
          "ds_read_b128 v[108:111], %[ap]\n\tds_read_b32 v105, %[ab] offset:4\n\tds_write_b128 %[a16], v[96:99]\n\tds_write_b32 %[a4], v100\n\t"
          "v_max3_u32 v101, v96, v97, v98\n\tv_max3_u32 v101, v101, v99, v100\n\tv_cmp_eq_u32 vcc, -1, v101\n\t"
          DOT3("v96", "v92") DOT3("v97", "v93")
          "s_cbranch_vccnz 1b\n\ts_mov_b32 s42, s46\n\ts_waitcnt lgkmcnt(2)\n\tv_lshl_or_b32 v109, v111, 8, v109\n\tv_lshl_or_b32 v109, v105, 16, v109\n\t"
          "s_or_b32 s45, s46, 0x20500\n\tv_cmp_eq_u32 vcc, s45, v109\n\ts_cbranch_vccz 5f\n\ts_waitcnt lgkmcnt(0)\n\ts_barrier\n"
          "5:\n\t" DOT3("v98", "v94") DOT3("v99", "v95") DOT3("v100", "%[rb]")
          "s_nop 2\n\tv_mov_b32 v105, v104\n\tv_permlane32_swap_b32 v102, v103\n\tv_add_f32 v102, v102, v103\n\tv_permlane32_swap_b32 v104, v105\n\t"
          "v_add_f32 v104, v104, v105\n\ts_nop 1\n\tv_permlane16_swap_b32 v102, v104\n\tv_add_f32 v102, v102, v104\n\t"
          ADPP("quad_perm:[1,0,3,2]") ADPP("quad_perm:[2,3,0,1]") ADPP("row_half_mirror") ADPP("row_mirror")
          "v_mul_f32 v103, 0x3acccccd, v102\n\tv_readlane_b32 s41, v102, 16\n\tv_readlane_b32 s40, v103, 32\n\t"
          "s_cmp_lg_u32 %[sec], 0\n\ts_nop 0\n\tv_fma_f32 v102, -v103, v103, s40\n\tv_max_f32 v102, 0, v102\n\tv_cvt_pk_bf16_f32 v102, 0, v102\n\t"
          "v_add_f32 v102, 0x3727c5ac, v102\n\tv_cvt_pk_bf16_f32 v102, 0, v102\n\tv_sqrt_f32 v102, v102\n\tv_cvt_pk_bf16_f32 v105, 0, v103\n\t"
          "v_fma_f32 v105, -%[swr], v105, s41\n\tv_cvt_pk_bf16_f32 v106, v103, v102\n\tv_and_b32 v102, 0xffff0000, v106\n\tv_rcp_f32 v102, v102\n\t"
          "v_mov_b32 v104, 1\n\tv_mul_f32 v102, v102, v105\n\tv_ashrrev_i32 v103, 31, v102\n\tv_or_b32 v103, 0x80000000, v103\n\t"
          "v_xor_b32 v102, v102, v103\n\tv_and_or_b32 v107, v102, -4, %[sl]\n\ts_cbranch_scc1 10f\n\ts_cbranch_vccz 1b\n\t"
          "s_mov_b64 exec, 1\n\tds_max_u64 %[ab], v[106:107] offset:8\n\tds_add_u32 %[ab], v104 offset:4\n\ts_branch 9f\n"
          "6:\n\ts_bitcmp0_b32 s45, 2\n\ts_cbranch_scc1 1b\n\ts_mov_b64 exec, 1\n\tds_max_u64 %[ab], v[106:107] offset:8\n"
          "8:\n\ts_cmp_lg_u32 %[sec], 0\n\ts_cbranch_scc1 10f\n\tv_mov_b32 v104, 1\n\ts_mov_b64 exec, 1\n\tds_add_u32 %[ab], v104 offset:4\n\ts_barrier\n"
          "9:\n\ts_mov_b64 exec, -1\n\ts_waitcnt lgkmcnt(0)\n\ts_barrier\n"
          "10:"
          : "=&{v[106:107]}"(mine) FPRO
          : [cap] "n"(SPIN_CAP * 8), [rp] "s"(rp), [sec] "s"(second), [ap] "v"((u32)(POST_OFF + (w - 1) * 16)), [tq] "n"(TQD / 2), [pc] "n"(PCD / 2),
            [pk] "s"(ws + WS_PK + (bidx - 26) * 128), [tag] "v"(tag), [lo] "s"((u32)(u64)(ws + WS_PC)), [hi] "s"((u32)((u64)(ws + WS_PC) >> 32)),
            [l16] "v"(l16), [l4] "v"(l4), [a16] "v"(ah16), [a4] "v"(ah4), "{v[92:95]}"(ra), [rb] "v"(rb5), [swr] "v"(swr), [sl] "s"(w - 1),
            [ab] "v"((u32)(CTL_OFF + 112))
          : "memory", "vcc", "scc", "s40", "s41", "s42", "s43", "s44", "s45", "s46", "s47", "s48", "s49", "s50", "s51", "v96", "v97", "v98", "v99",
            "v100", "v101", "v102", "v103", "v104", "v105", "v108", "v109", "v110", "v111");
      if (w != 1) break;
      if (second) {
        if (lane == 0) *(u32x2*)(smem + CTL_OFF + 120) = mine;
        asm volatile("s_waitcnt lgkmcnt(0)" ::: "memory");
        break;
      }
      u32x4 wst;
#pragma nounroll
      for (int it = 0; it < SPIN_CAP * 8; ++it) {
        wst = lds_ld128(CTL_OFF + 32);
        if (__builtin_amdgcn_readfirstlane(wst[2])) break;
        __builtin_amdgcn_s_sleep(1);
      }
      if (__builtin_amdgcn_readfirstlane(wst[2]) != 2u) break;
      asm volatile("s_barrier" ::: "memory");
      rp = ws + WS_CAND + __builtin_amdgcn_readfirstlane(wst[3]) * 2560;
      second = 1;
    }
#define LOAD_CAND(CP)                                                                      \
  _Pragma("nounroll") for (int it = 0; it < SPIN_CAP; ++it) {                              \
    asm volatile("global_load_dwordx4 %0, %4, %6 sc1\n\tglobal_load_dword %1, %5, %6 offset:1024 sc1\n\t" \
                 "global_load_dwordx4 %2, %4, %6 offset:1280 sc1\n\tglobal_load_dword %3, %5, %6 offset:2304 sc1\n\ts_waitcnt vmcnt(0)" \
                 : "=&v"(h4), "=&v"(h1), "=&v"(n4), "=&v"(n1) : "v"(l16), "v"(l4), "s"(CP) : "memory"); \
    if (!__any(h4[0] == SENT || h4[1] == SENT || h4[2] == SENT || h4[3] == SENT || h1 == SENT || n4[0] == SENT || \
               n4[1] == SENT || n4[2] == SENT || n4[3] == SENT || n1 == SENT)) break;      \
    __builtin_amdgcn_s_sleep(2);                                                           \
  }
    if (w == 1) {
    } else if (w == 3 || fb >= 40) {
      const int c = w == 3 ? fb : fb + 8;
      if (c < 51) {
        const u32 t0 = __builtin_amdgcn_readfirstlane(*(const u32*)(smem + KEY_OFF - 256 + c * 4));
#pragma nounroll
        for (int it = 0; it < 256; ++it) {
          if (((((u32)__builtin_amdgcn_s_memrealtime() >> 1) - t0) & 1023u) >= CCD / 2) break;
          __builtin_amdgcn_s_sleep(1);
        }
        LOAD_CAND(ws + WS_CAND + c * 2560)
        u32x4 o4;
#pragma unroll
        for (int e = 0; e < 4; ++e) o4[e] = cvt2((f32x2){lo_f(h4[e]) + lo_f(n4[e]), hi_f(h4[e]) + hi_f(n4[e])});
        u32 o1 = cvt2((f32x2){lo_f(h1) + lo_f(n1), hi_f(h1) + hi_f(n1)});
        asm volatile("s_mov_b32 s40, %[cap]\n"
                     "1:\n\tds_read_b64 v[100:101], %[af]\n\tds_read_b32 v102, %[ak]\n\ts_sub_u32 s40, s40, 1\n\ts_cbranch_scc1 2f\n\ts_waitcnt lgkmcnt(0)\n\t"
                     "v_cmp_eq_u32 vcc, 0, v100\n\ts_cbranch_vccnz 1b\n\t"
                     "v_cmp_eq_u32 vcc, %[c], v101\n\tv_bfi_b32 v102, v102, 0, %[m]\n\ts_cbranch_vccnz 2f\n\t"
                     "v_mad_u32_u24 v100, v102, %[rs], %[l16]\n\tv_mad_u32_u24 v101, v102, %[rs], %[l4]\n\t"
                     "global_store_dwordx4 v100, %[o4], %[out]\n\tglobal_store_dword v101, %[o1], %[out] offset:1024\n"
                     "2:\n\ts_waitcnt lgkmcnt(0)"
                     ::[cap] "n"(SPIN_CAP * 8), [af] "v"((u32)(CTL_OFF + 40)), [ak] "v"((u32)(KEY_OFF + c * 4)), [c] "s"(c), [m] "s"(2047), [rs] "s"(1280),
                       [l16] "v"(l16), [l4] "v"(l4), [o4] "v"(o4), [o1] "v"(o1), [out] "s"(out)
                     : "memory", "vcc", "scc", "s40", "v100", "v101", "v102");
      }
    } else {
      const char* pp = ws + WS_PART + fb * 64;
      int pass = 0;
      asm volatile("" : "+s"(pass));
#pragma nounroll
      for (; pass < 2; ++pass) {
        const u32x2 best = *(const u32x2*)(smem + CTL_OFF + 120);
        const u32 slot = best[1] & 3u;
        const u32 wkey = __builtin_amdgcn_readfirstlane(*(const u32*)(smem + (pass ? CTL_OFF + 32 : POST_OFF + slot * 16)));
        const float rr = rbf(__uint_as_float((best[1] & ~3u) ^ ((u32)((int)~best[1] >> 31) | 0x80000000u)));
        float rs = rbf(__builtin_amdgcn_rcpf(1.0f + __expf(-rr)));
        const int ccol = (g & 1) * 2 + (g >> 1);
        float hv = bf2f(*(const u16*)(smem + HN_OFF + slot * HSL + fb * 32 + p4 * 8 + ccol * 2));
        float hnv = rbf(rbf(hv - lo_f(best[0])) * __builtin_amdgcn_rcpf(hi_f(best[0])));
        const char* op_ = (const char*)out + (size_t)(2047 - (int)(wkey & 2047u)) * 1280 + fb * 32;
        u32 vo_ = p4 * 8 + ccol * 2;
        asm volatile("" : "+v"(vo_), "+v"(hnv), "+v"(hv), "+v"(rs));
        asm volatile("" : "+s"(op_));
        const u32 vp = (lane >> 2) * 2560 + p4 * 16;
        typedef __attribute__((ext_vector_type(16))) unsigned int u32x16;
        u32x16 keepa, keepb;
        f32x4 ys;
        u32 fin;
        asm volatile(
#define PLD(B0, B1, B2)                                                                             \
            "global_load_dwordx4 " B0 ", %[vp], %[p0] sc1\n\t"                                       \
            "global_load_dwordx4 " B1 ", %[vp], %[p1] sc1\n\tglobal_load_dwordx4 " B2 ", %[vp], %[p2] sc1\n\t"
#define PCHK(A0, A1, B0, B1, C0, C1)                                                                \
            "s_waitcnt vmcnt(3)\n\t"                                                                 \
            "v_pk_add_f32 v[96:97], " A0 ", " B0 "\n\tv_pk_add_f32 v[98:99], " A1 ", " B1 "\n\t"    \
            "v_pk_add_f32 v[96:97], v[96:97], " C0 "\n\tv_pk_add_f32 v[98:99], v[98:99], " C1 "\n\t" \
            "v_pk_add_f32 v[102:103], v[96:97], v[98:99]\n\t"                                        \
            "v_cmp_u_f32 vcc, v102, v103\n\ts_cbranch_vccz 9f\n\t"
            "s_mov_b32 s42, %[cap]\n\ts_sleep %[cdel]\n\t"
            PLD("v[64:67]", "v[68:71]", "v[72:75]")
            "s_sleep %[gap]\n\t"
            PLD("v[80:83]", "v[84:87]", "v[88:91]")
            "1:\n\t"
            PCHK("v[64:65]", "v[66:67]", "v[68:69]", "v[70:71]", "v[72:73]", "v[74:75]")
            PLD("v[64:67]", "v[68:71]", "v[72:75]")
            PCHK("v[80:81]", "v[82:83]", "v[84:85]", "v[86:87]", "v[88:89]", "v[90:91]")
            PLD("v[80:83]", "v[84:87]", "v[88:91]")
            "s_sub_u32 s42, s42, 1\n\ts_cbranch_scc0 1b\n"
            "9:\n\tds_read_b32 %[fin], %[af]"
            : "=&{v[96:99]}"(ys), "=&{v[64:79]}"(keepa), "=&{v[80:95]}"(keepb), [fin] "=&v"(fin)
            : [cap] "n"(SPIN_CAP), [gap] "n"(CGAP), [cdel] "n"(CDEL), [vp] "v"(vp), [p0] "s"(pp),
              [p1] "s"(pp + 40960), [p2] "s"(pp + 81920), [af] "v"((u32)(CTL_OFF + 40))
            : "memory", "vcc", "scc", "s42", "v102", "v103");
        LSTAMP(16)
        float y = tr_sum(ys);
        y += dppf<0x124>(y);
        y += dppf<0x128>(y);
        const u32 res = f2bf(hv + rbf(hnv + rbf(rbf(y) * rs)));
        asm volatile("s_mov_b32 s42, %[cap]\n"
                     "1:\n\ts_waitcnt lgkmcnt(0)\n\tv_cmp_eq_u32 vcc, %[ok], %[fin]\n\ts_cbranch_vccz 2f\n\t"
                     "s_mov_b32 exec_lo, 0x000f000f\n\ts_mov_b32 exec_hi, 0x000f000f\n\tglobal_store_short %[vo], %[res], %[op]\n\ts_mov_b64 exec, -1\n"
                     "2:\n\tv_cmp_eq_u32 vcc, 0, %[fin]\n\ts_cbranch_vccz 3f\n\tds_read_b32 %[fin], %[af]\n\ts_sub_u32 s42, s42, 1\n\ts_cbranch_scc0 1b\n"
                     "3:\n\ts_waitcnt lgkmcnt(0)\n\tv_cmp_lt_u32 vcc, %[ok], %[fin]\n\ts_cbranch_vccz 4f\n\ts_waitcnt vmcnt(0)\n4:"
                     : [fin] "+v"(fin) : [cap] "n"(SPIN_CAP * 8), [ok] "s"(pass + 1), [vo] "v"(vo_), [res] "v"(res), [op] "s"(op_),
                       [af] "v"((u32)(CTL_OFF + 40)), "{v[64:79]}"(keepa), "{v[80:95]}"(keepb) : "memory", "vcc", "scc", "s42");
        if (pass || __builtin_amdgcn_readfirstlane(fin) != 2u) break;
        asm volatile("s_barrier\n\ts_barrier\n\ts_barrier" ::: "memory");
        pp += WS_PART2 - WS_PART;
      }
    }
    LSTAMP(17)
    FLUSH_STAMPS
    return;
  }

  const int idx2 = (xcd & 1) * 26 + bidx;
  const int hg = ((xcd >> 1) & 1) * 4 + (idx2 & 3);
  const int mt = (xcd >> 2) * 13 + (idx2 >> 2);
#define ROTF (((idx2 >> 2) * 3 / 2 + (idx2 & 3) * 3) % 10)
  const int kb0 = ROTF;
  const int r0 = mt * 80;
  if (w >= 4) {
    const int cw = w - 4;
    u32 vo = w_off(cw, lane, hg), vx = x_off(cw, lane, r0), vx2 = x_off(8 + (cw & 3), lane, r0);
    asm volatile("" : "+v"(vo), "+v"(vx), "+v"(vx2));
    __builtin_amdgcn_sched_barrier(0);
    const char* wb = (const char*)wqkv + (size_t)kb0 * 245760 + W_ROW(cw);
    dma_m0<0>(vo, wb, cw * 1024);
    dma_m0<0>(vx, (const char*)x + kb0 * 128, XRING + cw * 1024);
    dma_m0<0>(vo, wb + 61440, 8192 + cw * 1024);
    if (cw < 4) dma_m0<0>(vx2, (const char*)x + kb0 * 128, XRING + 8192 + cw * 1024);
  }
  LSTAMP(40)
  const int rt = w >= 4 ? (w & 3) : 4, kh = w >= 4 ? (w - 4) >> 2 : (w & 1);
  float bias[2];
  u32 sel[4], e4, eh, aq, aka, akb, pm, pf, hi, arec, vpp;
  const char *os, *os4, *sst, *oa[10];
  int so_[10];
  int lane_ = lane, mt_ = mt, hg_ = hg;
  auto prepv = [&](const int lane) __attribute__((always_inline)) {
    const int g = lane >> 4, r16 = lane & 15, q4 = (lane & 15) >> 2, p4 = lane & 3;
    const int par = lane & 1;
    const bool ca = r16 >= 4 * g, cb = r16 >= 4 * g + 2;
    bias[0] = (ca || r0 + rt > 0) ? 0.f : -__builtin_inff();
    bias[1] = (cb || r0 + rt > 0) ? 0.f : -__builtin_inff();
    const u32 sa = par ? 0x01000C0Cu : 0x0C0C0100u, sb = par ? 0x03020C0Cu : 0x0C0C0302u;
    sel[0] = ca ? 0x0C0C0C0Cu : sa;
    sel[1] = cb ? 0x0C0C0C0Cu : sb;
    sel[2] = ca ? sa : 0x0C0C0C0Cu;
    sel[3] = cb ? sb : 0x0C0C0C0Cu;
    e4 = EXO + rt * 512 + lane * 8;
    eh = e4 + kh * 6144;
    aq = TAB_OFF + 384 + (rt * 16 + r16) * 4;
    aka = TAB_OFF + 384 + (rt * 16 + 4 * g + par + (ca ? 16 : 0)) * 4;
    akb = TAB_OFF + 384 + (rt * 16 + 4 * g + par + 2 + (cb ? 16 : 0)) * 4;
    pm = (4 * g + q4) * 160 + p4 * 40;
    pf = (20 * g + 16 + q4) * 160 + p4 * 40;
    hi = HIM + (rt * 16 + r16) * 160 + g * 40;
    arec = REC_OFF + (r16 == 0 ? kh * 192 + g * 48 : (r16 == 8 ? 384 : 432));
    vpp = lane + kh * 64 + (rt == 0 ? 768 : (rt == 2 ? 8192 : (rt == 4 ? 512 : 640)));
    asm volatile("" : "+v"(arec), "+v"(vpp));
    asm volatile("" : "+v"(bias[0]), "+v"(bias[1]), "+v"(sel[0]), "+v"(sel[1]), "+v"(sel[2]), "+v"(sel[3]), "+v"(e4), "+v"(eh),
                 "+v"(aq), "+v"(aka), "+v"(akb), "+v"(pm), "+v"(pf), "+v"(hi));
  };
  auto preps = [&](const int mt, const int hg) __attribute__((always_inline)) {
    const char* ob = ws + WS_O + (size_t)mt * 102400;
    os = ob + rt * 20480 + (2 * hg + kh) * 1024;
    os4 = ob + rt * 20480 + (16 + (hg >> 1)) * 1024 + (hg & 1) * 8;
    sst = ws + WS_STAT + (size_t)(2 * mt + (rt < 3 ? 0 : 1)) * 8192 + hg * 1024 + (rt < 3 ? rt : rt - 3) * 256;
    asm volatile("" : "+s"(os), "+s"(os4), "+s"(sst));
    const int k2 = w & 1;
#pragma unroll
    for (int j = 0; j < 10; ++j) {
      const bool oth = j >= 5 || w < 2;
      const int mo[8] = {OORD};
      const int ch = j % 5 < 4 ? (2 * hg + k2) ^ (oth ? 2 * mo[4 + j % 5] : 2 * mo[j % 5]) : (hg >> 1) ^ ((oth != ((hg & 1) != 0)) ? (k2 ? 0 : 3) : 1 + k2);
      oa[j] = ob + (j % 5 < 4 ? ch : 16 + ch) * 1024;
      so_[j] = ch * (j % 5 < 4 ? 6400 : 25600);
      asm volatile("" : "+s"(oa[j]), "+s"(so_[j]));
    }
  };
#define WO_OFFS                                                                                    \
  u32 woff[5];                                                                                     \
  _Pragma("unroll") for (int i = 0; i < 5; ++i) {                                                  \
    const int c = i * 64 + lane;                                                                   \
    const int row = c / 10;                                                                        \
    woff[i] = row * 1120 + hg * 160 + c * 16 + (4 - i) * 1024;                                     \
  }
#define WO_RUN(RHO)                                                                                \
  {                                                                                                \
    const char* wq_ = (const char*)wo + (RHO) * 40960; \
    dma_m0<-4096>(woff[0], wq_, (RHO) * 5120 + 4096);                                               \
    dma_nx<-3072>(woff[1], wq_); dma_nx<-2048>(woff[2], wq_); dma_nx<-1024>(woff[3], wq_); dma_nx<0>(woff[4], wq_); \
  }

#define BAR asm volatile("s_barrier" ::: "memory");
#define PREP_V __builtin_amdgcn_sched_barrier(0); asm volatile("" : "+v"(lane_)); prepv(lane_); __builtin_amdgcn_sched_barrier(0);
#define PREP_S __builtin_amdgcn_sched_barrier(0); asm volatile("" : "+s"(mt_), "+s"(hg_)); preps(mt_, hg_); __builtin_amdgcn_sched_barrier(0);
  if (w < 2) {
    const int l = w;
    __builtin_amdgcn_s_setprio(3);
    u32 vw[4];
#pragma unroll
    for (int i = 0; i < 4; ++i) vw[i] = w_off(i, lane, hg) + 4096;
    u32 go = (u32)kb0 * 245760u + 122880u + l * 61440u;
    u32 so = HSLOT + l * 8192 + 4096;
#define W1(I) dma_nx<((I) - 4) * 1024>(vw[(I) & 3], wb + (W_ROW(I) - (I) * 1024));
#define ISSUE_W                                                                                    \
  {                                                                                                \
    const char* wb = (const char*)wqkv + go;                                                       \
    dma_m0<-4096>(vw[0], wb, so);                                                                  \
    W1(1) W1(2) W1(3) W1(4) W1(5) W1(6) W1(7)                                                      \
    go += 122880u;                                                                                 \
    if (go >= 2457600u) go -= 2457600u;                                                            \
    so = so >= 5 * HSLOT ? so - 5 * HSLOT : so + HSLOT;                                            \
  }
    __builtin_amdgcn_s_sleep(2);
    ISSUE_W
    __builtin_amdgcn_sched_barrier(0);
    WO_OFFS
    asm volatile("" : "+v"(woff[0]), "+v"(woff[1]), "+v"(woff[2]), "+v"(woff[3]), "+v"(woff[4]));
    BAR
    ISSUE_W
    asm volatile("s_waitcnt vmcnt(8)\n\ts_barrier" ::: "memory");
    LSTAMP(30)
#pragma unroll 1
    for (int h = 0; h < 17; ++h) {
      ISSUE_W
      asm volatile("s_waitcnt vmcnt(8)\n\ts_barrier" ::: "memory");
      if (h & 1) { LSTAMP(31 + (h >> 1)) }
    }
    asm volatile("s_waitcnt vmcnt(0)\n\ts_barrier" ::: "memory");
    LSTAMP(39)
    WO_RUN(12 + l)
    const u32 tagw = lds_ld32(CTL_OFF);
    const char* bxf = ws + WS_BXF + mt * 1024;
    BAR
    const u32 town = (u32)__builtin_amdgcn_s_memrealtime();
    if (l == 0) {
      if (lane == 0) GST2((u32)(hg * 128), bxf, 0, ((u32x2){town, tagw}));
    }
    WO_RUN(18 + l)
    WO_RUN(8 + l)
    WO_RUN(l)
    const u32 vfl = (((lane & 7) == hg ? hg ^ 1 : (lane & 7))) * 128;
    u32x2 f;
    asm volatile("" : "=v"(f));
    BAR
    int gate = (int)((u32)__builtin_amdgcn_s_memrealtime() - town) + GADD;
    gate = gate > 200 ? 200 : gate;
    WO_RUN(2 + l)
    WO_RUN(4 + l)
    WO_RUN(6 + l)
    if (l == 1) asm volatile("global_load_dwordx2 %0, %1, %2 sc1" : "+v"(f) : "v"(vfl), "s"(bxf) : "memory");
    __builtin_amdgcn_s_setprio(0);
    pm = (4 * g + q4) * 160 + p4 * 40;
    pf = (20 * g + 16 + q4) * 160 + p4 * 40;
    asm volatile("" : "=v"(bias[0]), "=v"(bias[1]), "=v"(sel[0]), "=v"(sel[1]), "=v"(sel[2]), "=v"(sel[3]), "=v"(e4), "=v"(eh),
                 "=v"(aq), "=v"(aka), "=v"(akb), "+v"(pm), "+v"(pf), "=v"(hi), "=v"(arec), "=v"(vpp));
    PREP_S
    if (l == 1) {
      asm volatile("s_waitcnt vmcnt(0)" : "+v"(f)::"memory");
#pragma nounroll
      for (int it = 0; it < SPIN_CAP; ++it) {
        if (__all(f[1] == tagw)) break;
        asm volatile("global_load_dwordx2 %0, %1, %2 sc1\n\ts_waitcnt vmcnt(0)" : "=&v"(f) : "v"(vfl), "s"(bxf) : "memory");
      }
#pragma nounroll
      for (int it = 0; it < 64; ++it) {
        if (__all((int)((u32)__builtin_amdgcn_s_memrealtime() - f[0]) >= gate)) break;
      }
    }
    __builtin_amdgcn_s_setprio(0);
  } else if (w < 4) {
    __builtin_amdgcn_s_setprio(3);
    if (w == 2) {
      u32 vx[12];
#pragma unroll
      for (int i = 0; i < 12; ++i) vx[i] = x_off(i, lane, r0) + (4 - (i % 6)) * 1024;
      int kb = kb0 == 9 ? 0 : kb0 + 1;
      u32 xo = XRING + XSLOT + 4096;
#define ISSUE_X0                                                                                   \
  {                                                                                                \
    const char* xb = (const char*)x + kb * 128;                                                    \
    dma_m0<-4096>(vx[0], xb, xo);                                                                  \
    dma_nx<-3072>(vx[1], xb); dma_nx<-2048>(vx[2], xb); dma_nx<-1024>(vx[3], xb); dma_nx<0>(vx[4], xb); dma_nx<1024>(vx[5], xb); \
  }
#define ISSUE_X1                                                                                   \
  {                                                                                                \
    const char* xb = (const char*)x + kb * 128;                                                    \
    dma_m0<-4096>(vx[6], xb, xo + 6144);                                                           \
    dma_nx<-3072>(vx[7], xb); dma_nx<-2048>(vx[8], xb); dma_nx<-1024>(vx[9], xb); dma_nx<0>(vx[10], xb); dma_nx<1024>(vx[11], xb); \
    kb = kb == 9 ? 0 : kb + 1;                                                                     \
    xo = xo >= XRING + 3 * XSLOT ? xo - 3 * XSLOT : xo + XSLOT;                                    \
  }
      ISSUE_X0
      __builtin_amdgcn_sched_barrier(0);
      WO_OFFS
      asm volatile("" : "+v"(woff[0]), "+v"(woff[1]), "+v"(woff[2]), "+v"(woff[3]), "+v"(woff[4]));
      BAR
      ISSUE_X1
      ISSUE_X0
      BAR
      ISSUE_X1
      asm volatile("s_waitcnt vmcnt(12)\n\ts_barrier" ::: "memory");
#pragma unroll 1
      for (int j = 3; j < 10; ++j) {
        ISSUE_X0
        BAR
        ISSUE_X1
        asm volatile("s_waitcnt vmcnt(12)\n\ts_barrier" ::: "memory");
      }
      const char* xq = (const char*)x + (size_t)r0 * 1280;
      const int nv = mt == 25 ? 480 : 800;
#define RES1(I) if ((I) < 7 || (I) * 64 + lane < nv) dma_m0<(((I) % 5) - 4) * 1024>(woff[(I) % 5], xq + ((I) / 5) * 40960, HIM + ((I) / 5) * 5120 + 4096);
      PREP_V
      BAR
      PREP_S
      asm volatile("s_waitcnt vmcnt(0)\n\ts_barrier" ::: "memory");
      RES1(0) RES1(1) RES1(2) RES1(3) RES1(4) RES1(5) RES1(6)
      BAR
      RES1(7) RES1(8) RES1(9) RES1(10) RES1(11) RES1(12)
      if (lane < 10) dma_m0<0>(l16, (const char*)w_r + hg * 160, WR_OFF);
      BAR
    } else {
      u32x2 ep;
      u32x4 ra;
      u32 rb5;
      if (lane < 8) {
        *(u32x4*)(smem + ONES_OFF + lane * 16) = (u32x4){0x3F803F80u, 0x3F803F80u, 0x3F803F80u, 0x3F803F80u};
        *(u32x4*)(smem + CTL_OFF + lane * 16) = (u32x4){0u, 0u, 0u, 0u};
      }
      {
        const u32 o1 = lane == 26 ? 0x3F803F80u : 0u;
        if (lane < 30) *(u32x4*)(smem + REC_OFF + lane * 16) = (u32x4){o1, o1, o1, o1};
      }
      WO_OFFS
      asm volatile("" : "+v"(woff[0]), "+v"(woff[1]), "+v"(woff[2]), "+v"(woff[3]), "+v"(woff[4]));
      LDS_BARRIER();
      BAR BAR
      asm volatile("global_load_dwordx2 %0, %1, off sc1" : "=v"(ep) : "v"(ws + WS_EPOCH) : "memory");
      asm volatile("global_load_dwordx4 %0, %2, %4\n\tglobal_load_dword %1, %3, %4 offset:1024"
                   : "=&v"(ra), "=&v"(rb5) : "v"(l16), "v"((u32)(lane * 4)), "s"(w_r) : "memory");
      BAR BAR BAR
      PREP_V
      BAR
      PREP_S
      BAR
      asm volatile("s_waitcnt vmcnt(0)" : "+v"(ep), "+v"(ra), "+v"(rb5)::"memory");
      const u64 e1 = (((u64)ep[1] << 32) | ep[0]) + (ep[0] == SENT - 1u ? 2ull : 1ull);
      float swr = lo_f(rb5) + hi_f(rb5);
#pragma unroll
      for (int e = 0; e < 4; ++e) swr += lo_f(ra[e]) + hi_f(ra[e]);
      swr = wave_sum(swr);
      if (lane == 0) {
        *(u32x2*)(smem + CTL_OFF) = (u32x2){(u32)e1, (u32)(e1 >> 32)};
        *(float*)(smem + CTL_OFF + 40) = swr;
      }
      int ra0 = r0 - 16 + lane, ra1 = r0 + 48 + (lane & 31);
      ra0 = ra0 < 0 ? 0 : (ra0 > SEQ - 1 ? SEQ - 1 : ra0);
      ra1 = ra1 > SEQ - 1 ? SEQ - 1 : ra1;
      const int hd = lane / 5, pt = lane - hd * 5;
      u32 avp = REC_OFF + (hd >> 1) * 192 + (pt < 4 ? pt * 48 + (hd & 1) * 8 : hd * 48 + 16), vvp = lane < 20 ? (lane >> 1) * 128 + (lane & 1) * 16 : 0;
      u32 vf = (lane < 48 ? lane : 0) * 128, va0 = ra0 * 8, va1 = ra1 * 8, vt0 = TAB_OFF + lane * 4, vt1 = TAB_OFF + 256 + (lane & 31) * 4;
      asm volatile("" : "+v"(avp), "+v"(vvp), "+v"(vf), "+v"(va0), "+v"(va1), "+v"(vt0), "+v"(vt1));
#pragma nounroll
      for (int h = 7; h <= HK0; ++h) BAR
      u32x2 f, t0, t1;
      u32x4 vp4;
      GLD2(f, vf, ws + WS_HFLAG, 0);
      GLD2(t0, va0, ws + WS_LN1, 0);
      GLD2(t1, va1, ws + WS_LN1, 0);
      asm volatile("global_load_dwordx4 %0, %1, %2 sc1" : "=v"(vp4) : "v"(vvp), "s"(ws + WS_VP + hg * 1280) : "memory");
#pragma unroll
      for (int h = HK0 + 1; h <= 17; ++h) BAR
      WO_RUN(10)
      WO_RUN(11)
      BAR
      asm volatile("s_waitcnt vmcnt(10)" : "+v"(t0), "+v"(t1), "+v"(f), "+v"(vp4)::"memory");
#pragma nounroll
      for (int it = 0; it < SPIN_CAP; ++it) {
        if (__all(t0[1] == (u32)e1 && t1[1] == (u32)e1 && f[0] == (u32)e1 && f[1] == (u32)(e1 >> 32))) break;
        asm volatile("global_load_dwordx2 %2, %5, %7 sc1\n\tglobal_load_dwordx2 %0, %3, %6 sc1\n\tglobal_load_dwordx2 %1, %4, %6 sc1\n\ts_waitcnt vmcnt(0)"
                     : "=&v"(t0), "=&v"(t1), "=&v"(f) : "v"(va0), "v"(va1), "v"(vf), "s"(ws + WS_LN1), "s"(ws + WS_HFLAG) : "memory");
      }
      *(u32*)(smem + vt0) = t0[0] << 16;
      *(u32*)(smem + vt1) = t1[0] << 16;
      *(float*)(smem + vt0 + 384) = __builtin_amdgcn_rcpf(hi_f(t0[0]));
      *(float*)(smem + vt1 + 384) = __builtin_amdgcn_rcpf(hi_f(t1[0]));
      if (lane < 20 && vp4[1] == (u32)e1 && vp4[3] == (u32)e1) *(u32x2*)(smem + avp) = (u32x2){vp4[0], vp4[2]};
      asm volatile("" ::: "memory");
      WO_RUN(14)
      WO_RUN(15)
      LDS_BARRIER();
      WO_RUN(16)
      WO_RUN(17)
    }
    __builtin_amdgcn_s_setprio(0);
  } else {
    const int cw = w - 4;
    prepv(lane);
    preps(mt, hg);
    if (cw >= 4) __builtin_amdgcn_s_setprio(1);
    const int xa0 = XRING + r16 * 128 + ((g ^ ((r16 >> 1) & 7)) * 16);
    const u32 m1 = r16 == 0 ? 0u : 1u;
    const u32 xz0 = r16 == 0 ? (u32)ONES_OFF : (u32)xa0;
    int wa[2];
#pragma unroll
    for (int c = 0; c < 2; ++c) {
      const int ct = cw < 7 ? 2 * cw + c : 14;
      const int seg = ct / 5, tt = ct - seg * 5;
      const int G = tt < 4 ? 5 * tt + p4 : 5 * p4 + 4;
      wa[c] = (16 * (g >> 1) + 4 * (g & 1) + q4) * 512 + ((seg * 160 + G * 8) ^ ((4 * (g & 1) + q4) << 5));
    }
    auto run = [&](auto tyc) __attribute__((always_inline)) {
      constexpr int TY = decltype(tyc)::value;
      constexpr int NW = TY == 2 ? 1 : 2;
      f32x4 acc[2][6];
#pragma unroll
      for (int c = 0; c < 2; ++c)
#pragma unroll
        for (int r = 0; r < 6; ++r) acc[c][r] = (f32x4){0.f, 0.f, 0.f, 0.f};
      u32x4 xA[6], xB[6];
      u32x2 wA[4], wB[4];
#define RX(D, A, O) asm volatile("ds_read_b128 %0, %1 offset:%2" : "=v"(D) : "v"(A), "n"(O) : "memory");
#define RW(D, A, O) asm volatile("ds_read_b64_tr_b16 %0, %1 offset:%2" : "=v"(D) : "v"(A), "n"(O) : "memory");
#define MM(C, R, XF, WF)                                                                           \
  if ((C) < NW) {                                                                                    \
    const bf16x8 wf_ = mk8(WF[2 * (C)][0], WF[2 * (C)][1], WF[2 * (C) + 1][0], WF[2 * (C) + 1][1]); \
    const bf16x8 xf_ = __builtin_bit_cast(bf16x8, XF[R]);                                          \
    acc[C][R] = TY == 0 ? MFMA(wf_, xf_, acc[C][R]) : MFMA(xf_, wf_, acc[C][R]);                   \
    __builtin_amdgcn_sched_barrier(0);                                                             \
  }
#define READS(QX, QW)                                                                              \
  RW(QW[0], aw0, 0) RW(QW[1], aw0, 4096) RX(QX[1], ax, 2048)                                       \
  if (NW == 2) { RW(QW[2], aw1, 0) RW(QW[3], aw1, 4096) }                                          \
  RX(QX[2], ax, 4096) RX(QX[3], ax, 6144) RX(QX[4], ax, 8192) RX(QX[5], ax, 10240) RX(QX[0], az, 0)
#define ARRIVED(PX, PW)                                                                            \
  if (NW == 2)                                                                                     \
    asm volatile("s_waitcnt lgkmcnt(0)" : "+v"(PX[0]), "+v"(PX[1]), "+v"(PX[2]), "+v"(PX[3]), "+v"(PX[4]), "+v"(PX[5]), \
                 "+v"(PW[0]), "+v"(PW[1]), "+v"(PW[2]), "+v"(PW[3])::"memory");                    \
  else                                                                                             \
    asm volatile("s_waitcnt lgkmcnt(0)" : "+v"(PX[0]), "+v"(PX[1]), "+v"(PX[2]), "+v"(PX[3]), "+v"(PX[4]), "+v"(PX[5]), \
                 "+v"(PW[0]), "+v"(PW[1])::"memory");                                              \
  __builtin_amdgcn_sched_barrier(0);
#define SB __builtin_amdgcn_sched_barrier(0);
#define WT(N, ...) asm volatile("s_waitcnt lgkmcnt(" #N ")" : __VA_ARGS__::"memory"); SB
#define HSTAGE2(PX, PW, QX, QW, NX)                                                                \
  BAR                                                                                              \
  WT(7, "+v"(PW[0]), "+v"(PW[1]), "+v"(PX[1]))                                                     \
  MM(0, 1, PX, PW) RW(QW[0], aw0, 0)                                                               \
  WT(6, "+v"(PW[2]), "+v"(PW[3]))                                                                  \
  MM(1, 1, PX, PW) RW(QW[1], aw0, 4096)                                                            \
  WT(6, "+v"(PX[2]))                                                                               \
  MM(0, 2, PX, PW) RX(QX[1], ax, 2048) SB                                                          \
  MM(1, 2, PX, PW) RW(QW[2], aw1, 0)                                                               \
  WT(7, "+v"(PX[3]))                                                                               \
  MM(0, 3, PX, PW) RW(QW[3], aw1, 4096) SB                                                         \
  MM(1, 3, PX, PW) RX(QX[2], ax, 4096)                                                             \
  WT(6, "+v"(PX[4]), "+v"(PX[5]), "+v"(PX[0]))                                                     \
  MM(0, 4, PX, PW) RX(QX[3], ax, 6144) SB                                                          \
  MM(1, 4, PX, PW) RX(QX[4], ax, 8192) SB                                                          \
  MM(0, 5, PX, PW) RX(QX[5], ax, 10240) SB                                                         \
  MM(1, 5, PX, PW) RX(QX[0], az, 0) SB                                                             \
  MM(0, 0, PX, PW)                                                                                 \
  NX asm volatile("" : "+v"(aw0), "+v"(aw1), "+v"(ax), "+v"(az), "+s"(so), "+s"(xo)); SB           \
  MM(1, 0, PX, PW)
#define HSTAGE1(PX, PW, QX, QW, NX)                                                                \
  BAR                                                                                              \
  ARRIVED(PX, PW)                                                                                  \
  MM(0, 1, PX, PW) RW(QW[0], aw0, 0) RW(QW[1], aw0, 4096) SB                                       \
  MM(0, 2, PX, PW) RX(QX[1], ax, 2048) RX(QX[2], ax, 4096) SB                                      \
  MM(0, 3, PX, PW) RX(QX[3], ax, 6144) RX(QX[4], ax, 8192) SB                                      \
  MM(0, 4, PX, PW) RX(QX[5], ax, 10240) RX(QX[0], az, 0) SB                                        \
  MM(0, 5, PX, PW)                                                                                 \
  NX asm volatile("" : "+v"(aw0), "+v"(ax), "+v"(az), "+s"(so), "+s"(xo)); SB                      \
  MM(0, 0, PX, PW)
#define HSTAGE(...) if (NW == 2) { HSTAGE2(__VA_ARGS__) } else { HSTAGE1(__VA_ARGS__) }
      u32 aw0 = wa[0], aw1 = wa[1], ax = xa0, az = xz0;
      asm volatile("s_waitcnt vmcnt(0)\n\ts_barrier" ::: "memory");
      LSTAMP(20)
      READS(xA, wA)
      aw0 += HSLOT; aw1 += HSLOT; ax ^= 64; az ^= 64;
      int so = 2 * HSLOT, xo = XSLOT;
#pragma unroll 1
      for (int s = 0; s < 9; ++s) {
        HSTAGE(xA, wA, xB, wB, aw0 = wa[0] + so; aw1 = wa[1] + so; ax = xa0 + xo; az = xz0 + xo * m1;
               so = so >= 4 * HSLOT ? 0 : so + 2 * HSLOT; xo = xo >= 3 * XSLOT ? 0 : xo + XSLOT;)
        HSTAGE(xB, wB, xA, wA, aw0 += HSLOT; aw1 += HSLOT; ax ^= 64; az ^= 64;)
      }
      {
        HSTAGE(xA, wA, xB, wB, )
        ARRIVED(xB, wB)
        BAR
        SB
        LSTAMP(29)
      }
      char* ex = smem + EXO + cw * 6144 + lane * 8;
      if (TY == 0) {
        float st[6];
        const u32 ta = TAB_OFF + r16 * 4;
#define RT(R) asm volatile("ds_read_b32 %0, %1 offset:%2" : "=v"(st[R]) : "v"(ta), "n"(64 * (R)) : "memory");
        RT(1) RT(2) RT(3) RT(4) RT(5) RT(0)
        float cs[2][4];
#define FMS(A, M, CS) ((A) - (M) * (CS))
#define EP0(C, R)                                                                                  \
  {                                                                                                \
    const u32 a = cvt2((f32x2){FMS(acc[C][R][0], st[R], cs[C][0]), FMS(acc[C][R][1], st[R], cs[C][1])}); \
    const u32 b = cvt2((f32x2){FMS(acc[C][R][2], st[R], cs[C][2]), FMS(acc[C][R][3], st[R], cs[C][3])}); \
    *(u32x2*)(ex + (C) * 3072 + (R) * 512) = (u32x2){a, b};                                        \
    SB                                                                                             \
  }
        MM(0, 0, xB, wB) MM(1, 0, xB, wB) MM(0, 1, xB, wB) MM(1, 1, xB, wB)
#pragma unroll
        for (int i = 0; i < 4; ++i) cs[0][i] = dppf<0x150>(acc[0][0][i]);
        SB
        MM(0, 2, xB, wB)
#pragma unroll
        for (int i = 0; i < 4; ++i) cs[1][i] = dppf<0x150>(acc[1][0][i]);
        WT(0, "+v"(st[0]), "+v"(st[1]), "+v"(st[2]), "+v"(st[3]), "+v"(st[4]), "+v"(st[5]))
        MM(1, 2, xB, wB) EP0(0, 1)
        MM(0, 3, xB, wB) EP0(1, 1)
        MM(1, 3, xB, wB) EP0(0, 2)
        MM(0, 4, xB, wB) EP0(1, 2)
        MM(1, 4, xB, wB) EP0(0, 3)
        MM(0, 5, xB, wB) EP0(1, 3)
        MM(1, 5, xB, wB)
        if (cw >= 2) {
          EP0(1, 0)
          if (cw >= 3) EP0(0, 0)
        }
        EP0(0, 4) EP0(1, 4) EP0(0, 5) EP0(1, 5)
      } else {
        f32x4 mbv[6];
        const u32 ta = TAB_OFF + g * 16;
#define RV(R) RX(mbv[R], ta, 64 * (R))
        RV(1) RV(2) RV(3) RV(4) RV(5) RV(0)
        float cs[2];
#define CS1(C)                                                                                     \
  if ((C) < NW) {                                                                                  \
    const u32 v0 = __float_as_uint(acc[C][0][0]);                                                  \
    const u32 v1 = __builtin_amdgcn_permlane16_swap(v0, v0, false, false)[0];                      \
    cs[C] = __uint_as_float(__builtin_amdgcn_permlane32_swap(v1, v1, false, false)[0]);            \
    SB                                                                                             \
  }
#define EP1(C, R)                                                                                  \
  if ((C) < NW) {                                                                                  \
    const f32x4 v = (f32x4){FMS(acc[C][R][0], mbv[R][0], cs[C]), FMS(acc[C][R][1], mbv[R][1], cs[C]), FMS(acc[C][R][2], mbv[R][2], cs[C]), FMS(acc[C][R][3], mbv[R][3], cs[C])}; \
    *(u32x2*)(ex + (C) * 3072 + (R) * 512) = (u32x2){cvt2((f32x2){v[0], v[1]}), cvt2((f32x2){v[2], v[3]})}; \
    SB                                                                                             \
  }
        MM(0, 0, xB, wB) MM(1, 0, xB, wB) MM(0, 1, xB, wB) MM(1, 1, xB, wB)
        CS1(0)
        MM(0, 2, xB, wB)
        CS1(1)
        WT(0, "+v"(mbv[0]), "+v"(mbv[1]), "+v"(mbv[2]), "+v"(mbv[3]), "+v"(mbv[4]), "+v"(mbv[5]))
        MM(1, 2, xB, wB) EP1(0, 1)
        MM(0, 3, xB, wB) EP1(1, 1)
        MM(1, 3, xB, wB) EP1(0, 2)
        MM(0, 4, xB, wB) EP1(1, 2)
        MM(1, 4, xB, wB) EP1(0, 3)
        MM(0, 5, xB, wB) EP1(1, 3)
        MM(1, 5, xB, wB)
        EP1(0, 0) EP1(1, 0) EP1(0, 4) EP1(1, 4) EP1(0, 5) EP1(1, 5)
      }
    };
    if (cw < 5) run(icst<0>{});
    else if (cw < 7) run(icst<1>{});
    else run(icst<2>{});
  }
  __builtin_amdgcn_s_setprio(0);
  const bool live = r0 + rt * 16 < SEQ;
  if (w >= 2) { LSTAMP(2) }
  if (!live) {
    if (w >= 2) LDS_BARRIER();
    if (w != 7) {
      asm volatile("s_waitcnt vmcnt(0)" ::: "memory");
      FLUSH_STAMPS
      return;
    }
  } else if (w >= 2)
    asm volatile(ATT_ASM
                 :: [kh] "s"(kh), [l16] "v"(l16), [os] "s"(os), [os4] "s"(os4), [b0] "v"(bias[0]), [b1] "v"(bias[1]),
                    [s0] "v"(sel[0]), [s1] "v"(sel[1]), [s2] "v"(sel[2]), [s3] "v"(sel[3]), [e4] "v"(e4), [eh] "v"(eh), [aq] "v"(aq),
                    [aka] "v"(aka), [akb] "v"(akb), [arec] "v"(arec), [vpp] "v"(vpp), [sst] "s"(sst)
                 : "memory", "s41", "s46", "s47", "s48", "s49", "s50", "s51", "scc", "v24", "v25", "v26", "v27", "v28", "v29", "v30", "v31", "v32", "v33", "v34", "v35", "v36", "v37", "v38", "v39", "v40", "v41", "v42", "v43", "v44", "v45", "v46", "v47", "v48", "v49", "v50", "v51", "v52", "v53", "v54", "v55", "v56", "v57", "v58", "v59", "v60", "v61", "v62", "v63", "v64", "v65", "v66", "v67", "v68", "v69", "v70", "v71", "v72", "v73", "v74", "v75", "v76", "v77", "v78", "v79", "v80", "v81", "v82", "v83", "v84", "v85", "v86", "v87");
  if (w >= 2) { LSTAMP(4) }
  u32x4 hwa, hwb;
  u32x2 hwc;
#define STP(I)
#define CNT0
#define CNT1
#define STPOUT
#define STPOUT2
#define STPEND
  const int grp = rt < 3 ? 0 : 1;
  if (w < 8) {
    const int rmax = mt == 25 ? 2 : 4;
    const int rk = rt < rmax ? rt : rmax, rd = (rt ^ 1) < rmax ? (rt ^ 1) : rmax;
    u32 ta, tk;
    if (w == 0) asm volatile("s_waitcnt vmcnt(0)" ::: "memory");
    if (w == 2 || w == 3) asm volatile("s_waitcnt vmcnt(3)" ::: "memory");
    asm volatile("s_waitcnt lgkmcnt(0)\n\ts_barrier\n\t" OP_ASM STPEND
                 : "=&{v[84:87]}"(hwa), "=&{v[88:91]}"(hwb), "=&{v[92:93]}"(hwc), [ta] "=&v"(ta), [tk] "=&v"(tk) STPOUT
                 : [w] "s"(w), [l16] "v"(l16), [oa0] "s"(oa[0]), [so0] "s"(so_[0]), [oa1] "s"(oa[1]), [so1] "s"(so_[1]), [oa2] "s"(oa[2]), [so2] "s"(so_[2]), [oa3] "s"(oa[3]), [so3] "s"(so_[3]), [oa4] "s"(oa[4]), [so4] "s"(so_[4]), [oa5] "s"(oa[5]), [so5] "s"(so_[5]), [oa6] "s"(oa[6]), [so6] "s"(so_[6]), [oa7] "s"(oa[7]), [so7] "s"(so_[7]), [oa8] "s"(oa[8]), [so8] "s"(so_[8]), [oa9] "s"(oa[9]), [so9] "s"(so_[9]),
                   [pm] "v"(pm), [pf] "v"(pf), [ra] "v"(w < 3 ? (u32)(CTL_OFF + 72) : hi), [e4] "v"(e4),
                   [aw] "v"((lane & 1) ? (u32)(WR_OFF + g * 40) : (u32)ONES_OFF),
                   [vk] "v"(l16 + rk * 20480), [vd] "v"(l16 + rd * 20480), [sk] "s"((w & 1) * 6144),
                   [sd] "s"(EXO + (w < 4 ? 4 + w : rt ^ 1) * 5120), [sr] "s"(EXO + rt * 5120),
                   [sc] "s"(CTL_OFF + 48 + (w < 4 ? 4 : rt ^ 1) * 4), [sp] "s"(PST_OFF + rt * 256), [sa] "s"(CTL_OFF + 8 + grp * 4),
                   [sst] "s"(sst), [cp] "s"(ws + WS_PC + (2 * mt + grp) * 1280 + hg * 160), [oback] "n"(OBACK), [cap] "n"(SPIN_CAP)
                 : "memory", "vcc", "scc", "s43", "v24", "v25", "v26", "v27", "v28", "v29", "v30", "v31", "v32", "v33", "v34", "v35", "v36", "v37", "v38", "v39", "v40", "v41", "v42", "v43", "v44", "v45", "v46", "v47", "v48", "v49", "v50", "v51", "v52", "v53", "v54", "v55", "v56", "v57", "v58", "v59", "v60", "v61", "v62", "v63", "v64", "v65", "v66", "v67", "v68", "v69", "v70", "v71", "v72", "v73", "v74", "v75", "v76", "v77", "v78", "v79", "v80", "v81", "v82", "v83", "v94", "v95", "v96", "v97", "v98", "v99", "v100", "v101", "v102", "v103", "v104", "v105", "v106", "v107", "v108", "v109", "v110", "v111", "v112", "v113", "v114", "v115", "v116", "v117", "v118", "v119", "v120", "v121", "v122", "v123", "v124", "v125", "v126", "v127", "v128", "v129", "v130", "v131", "v132", "v133", "v134", "v135", "v136", "v137", "v138", "v139", "v140", "v141", "v142", "v143", "v144", "v145", "v146", "v147", "v148", "v149", "v150", "v151", "v152", "v153", "v154", "v155", "v156", "v157", "v158", "v159");
    if (!live || w < 3) {
      FLUSH_STAMPS
      return;
    }
  } else {
    if (w == 8 || w == 11) {
      const int cg = w == 11, gid = 2 * mt + cg;
      const char* sp = sst - hg * 1024;
      const char* sp2 = sp + (cg ? 0 : 7936);
      const u32 vq = (lane >> 3) * 1024 + (lane & 7) * 16 + (cg ? 640 : 768);
      u32x4 pq[3];
      u64 tb;
#define PLOADS "global_load_dwordx4 %0, %4, %5 sc1\n\tglobal_load_dwordx4 %1, %4, %5 offset:128 sc1\n\tglobal_load_dwordx4 %2, %4, %6 sc1\n\t"
      asm volatile("s_setprio 3\n\ts_waitcnt lgkmcnt(0)\n\ts_barrier\n\ts_sleep %7\n\t" PLOADS "s_memrealtime %3\n\ts_setprio 1"
                   : "=&v"(pq[0]), "=&v"(pq[1]), "=&v"(pq[2]), "=&s"(tb) : "v"(vq), "s"(sp), "s"(sp2), "n"(PDEL1) : "memory");
      int lane2 = lane;
      asm volatile("" : "+v"(lane2));
      const int n16 = lane2 & 15;
      u32x4 sl;
      u32x2 km;
#pragma unroll
      for (int e = 0; e < 4; ++e) sl[e] = n16 == 2 * e ? 0x3F80u : (n16 == 2 * e + 1 ? 0x3F800000u : 0u);
      u32 rid = (u32)((((lane2 >> 4) & 1) * 2 + (lane2 >> 5)) * 4 + (lane2 & 3)) << 10;
      u32 rid1 = rid + (16 << 10), rid2 = rid + (cg ? 0 : 32 << 10), msk = n16 < 4 ? 0xFFFF0000u : 0u;
      u32 ah = HIM + cg * 7680 + (lane2 < 10 ? lane2 : 9) * 16;
      km[1] = *(const u32*)(smem + CTL_OFF);
      const char* kp = ws + WS_PK + gid * 6144 + hg * 768;
      const char* cp = ws + WS_PC + gid * 1280 + hg * 160;
      asm volatile("s_waitcnt lgkmcnt(0)" : "+s"(tb), "+s"(kp), "+s"(cp)::"memory");
      km[0] = ((u32)tb >> 1) & 1023u;
      asm volatile("s_setprio " PPRIO "\n\ts_waitcnt vmcnt(0)" : "+v"(pq[0]), "+v"(pq[1]), "+v"(pq[2]), "+v"(sl), "+v"(rid), "+v"(rid1), "+v"(rid2), "+v"(msk), "+v"(ah), "+v"(km)::"memory");
      LSTAMP(44)
#define PMISS __any(max(max(max(pq[0][0], pq[0][2]), max(pq[1][0], pq[1][2])), max(pq[2][0], pq[2][2])) == SENT)
      if (__builtin_expect(PMISS, 0)) {
#pragma nounroll
        for (int it = 0; it < SPIN_CAP; ++it) {
          u64 t2;
          asm volatile(PLOADS "s_waitcnt vmcnt(0)" : "=&v"(pq[0]), "=&v"(pq[1]), "=&v"(pq[2]), "=&s"(t2) : "v"(vq), "s"(sp), "s"(sp2) : "memory");
          if (!PMISS) break;
          __builtin_amdgcn_s_sleep(2);
        }
        LSTAMP(41)
      }
      u32 pkey = 0;
#pragma unroll
      for (int t = 0; t < 3; ++t) {
        const f32x4 ys = MFMA(__builtin_bit_cast(bf16x8, pq[t]), __builtin_bit_cast(bf16x8, sl), ((f32x4){0.f, 0.f, 0.f, 0.f}));
        const float y = tr_sum(ys);
        pkey = max(pkey, (mono(y * __builtin_amdgcn_rcpf(dppf<0x104>(y))) & msk) | (t == 0 ? rid : (t == 1 ? rid1 : rid2)));
      }
      pkey = max(pkey, dppu<0xB1>(pkey));
      pkey = max(pkey, dppu<0x4E>(pkey));
      pkey = max(max((u32)__builtin_amdgcn_readlane(pkey, 0), (u32)__builtin_amdgcn_readlane(pkey, 16)),
                 max((u32)__builtin_amdgcn_readlane(pkey, 32), (u32)__builtin_amdgcn_readlane(pkey, 48)));
      km[0] |= pkey;
      asm volatile("s_mov_b32 exec_lo, 0x01010101\n\ts_mov_b32 exec_hi, 0x0101\n\tglobal_store_dwordx2 %0, %1, %2 sc1\n\ts_mov_b64 exec, -1\n\ts_setprio 2" ::"v"(l16), "v"(km), "s"(kp) : "memory");
      LSTAMP(42)
      ah += ((pkey >> 10) & 63u) * 160;
      if (lane == 0) *(u32*)(smem + CTL_OFF + 24 + cg * 4) = ah;
      __builtin_amdgcn_s_setprio(0);
#pragma nounroll
      for (int it = 0; it < 64; ++it) {
        if ((int)((u32)__builtin_amdgcn_s_memrealtime() - (u32)tb) >= PSL) break;
        __builtin_amdgcn_s_sleep(2);
      }
      asm volatile("s_mov_b32 s40, %[cap]\n"
                   "1:\n\tds_read_b32 v100, %[ac]\n\tds_read_b32 v101, %[ac] offset:24\n\tds_read_b128 v[96:99], %[ah]\n\ts_sub_u32 s40, s40, 1\n\ts_cbranch_scc1 2f\n\ts_waitcnt lgkmcnt(0)\n\t"
                   "v_cmp_gt_u32 vcc, %[nf], v100\n\ts_cbranch_vccz 2f\n\ts_sleep 2\n\ts_branch 1b\n"
                   "2:\n\ts_waitcnt lgkmcnt(0)\n\tv_cmp_eq_u32 vcc, 0, v101\n\ts_cbranch_vccz 3f\n\t"
                   "s_mov_b64 exec, 0x3ff\n\tglobal_store_dwordx4 %[l16], v[96:99], %[cp] sc1\n\ts_mov_b64 exec, -1\n3:"
                   ::[cap] "n"(SPIN_CAP * 8), [ac] "v"((u32)(CTL_OFF + 8 + cg * 4)), [ah] "v"(ah), [nf] "s"(3 - cg), [l16] "v"(l16), [cp] "s"(cp)
                   : "memory", "vcc", "scc", "s40", "v96", "v97", "v98", "v99", "v100", "v101");
      LSTAMP(43)
      FLUSH_STAMPS
      return;
    }
    LDS_BARRIER();
    if (w == 10 && mt == 25) {
      FLUSH_STAMPS
      return;
    }
    const int cg = w - 9, gid = 2 * mt + cg;
    const int nr = cg ? 31 : 47;
    const int lr = lane < nr ? lane : nr;
    const int lrow = cg * 48 + lr;
    const int li = (lane & 31) < 20 ? (lane & 31) : 19;
    asm volatile(COL_ASM STPEND
                 : STPOUT2
                 : [lq] "v"((u32)(lr * 16 + hg * 1024)), [ac] "v"((u32)(CTL_OFF + 8 + cg * 4)), [nf] "s"(3 - cg),
                   [sp] "s"(ws + WS_STAT + (size_t)gid * 8192), [ao] "v"((u32)(PST_OFF + lrow * 16)), [rdm] "s"(1.0f / DM),
                   [swr] "v"(*(const float*)(smem + CTL_OFF + 40)), [msk] "s"(0xFFFFF000u), [rid] "v"(0x800u | (u32)(2047 - (r0 + lrow))),
                   [at] "v"((u32)(TAB_OFF + lrow * 8)), [zr] "v"(l16), "{v99}"(*(const u32*)(smem + CTL_OFF)),
                   [kp] "s"(ws + WS_PK + gid * 6144 + hg * 768 + 8), [ak] "v"((u32)(CTL_OFF + 16 + cg * 4)), [rb] "s"(2047 - r0 - cg * 48),
                   [ah] "v"((u32)(HIM + cg * 7680 + li * 8)), [co] "v"((u32)((lane < 32 ? 1280 : 0) + li * 8)),
                   [cp] "s"(ws + WS_CAND + gid * 2560 + hg * 160), [pdel] "n"(PDEL2), [cap] "n"(SPIN_CAP)
                 : "memory", "vcc", "scc", "s40", "s41", "s42", "s43", "s44", "s45", "s46", "s47", "v64", "v65", "v66", "v67", "v68", "v69", "v70", "v71", "v72", "v73", "v74", "v75", "v76", "v77", "v78", "v79", "v80", "v81", "v82", "v83", "v84", "v85", "v86", "v87", "v88", "v89", "v90", "v91", "v92", "v93", "v94", "v95", "v96", "v97", "v98", "v100", "v101", "v102", "v103", "v104", "v105", "v106", "v107", "v108", "v109", "v110", "v111", "v112", "v113", "v114", "v115", "v116", "v117", "v118", "v119", "v120", "v121", "v122", "v123");
    FLUSH_STAMPS
    return;
  }
  const u32 vrow = ((rt * 16 + r16) * DM + 20 * g) * 2;
  const u32 hw[10] = {hwa[0], hwa[1], hwa[2], hwa[3], hwb[0], hwb[1], hwb[2], hwb[3], hwc[0], hwc[1]};
  u32 tilekey = 0;
#pragma nounroll
  for (int it = 0; it < SPIN_CAP; ++it) {
    tilekey = lds_ld32(CTL_OFF + 16 + grp * 4);
    if (tilekey != 0u) break;
    __builtin_amdgcn_s_sleep(1);
  }
  LSTAMP(11)
  const f32x2 st2 = *(const f32x2*)(smem + TAB_OFF + (rt * 16 + r16) * 8);
  const bool held = (tilekey & 2047u) == (u32)(2047 - (r0 + rt * 16 + r16));
  u32 ok[10];
#pragma unroll
  for (int e = 0; e < 10; ++e) ok[e] = cvt2(unpk(hw[e]) + unpk(NORM2(hw[e], st2[0], st2[1])));
  const char* obase = (const char*)out + (size_t)r0 * 1280 + hg * 160;
  __builtin_amdgcn_s_sleep(OSL);
  if (!held) {
    asm volatile("global_store_dwordx4 %0, %1, %2" OFL "\n\ts_nop 1" ::"v"(vrow), "v"((u32x4){ok[0], ok[1], ok[2], ok[3]}), "s"(obase) : "memory");
    asm volatile("global_store_dwordx4 %0, %1, %2 offset:16" OFL "\n\ts_nop 1" ::"v"(vrow), "v"((u32x4){ok[4], ok[5], ok[6], ok[7]}), "s"(obase) : "memory");
    asm volatile("global_store_dwordx2 %0, %1, %2 offset:32" OFL "\n\ts_nop 1" ::"v"(vrow), "v"((u32x2){ok[8], ok[9]}), "s"(obase) : "memory");
  }
  LSTAMP(15)
  FLUSH_STAMPS
}

extern "C" void kernel_launch(void* const* d_in, const int* in_sizes, int n_in,
                              void* d_out, int out_size, void* d_ws, size_t ws_size,
                              hipStream_t stream) {
  const u16* x = (const u16*)d_in[0];
  const u16* wqkv = (const u16*)d_in[1];
  const u16* wo = (const u16*)d_in[2];
  const u16* w_r = (const u16*)d_in[4];
  const u16* w_gate = (const u16*)d_in[5];
  const u16* w_up = (const u16*)d_in[6];
  const u16* w_down = (const u16*)d_in[7];
  (void)hipFuncSetAttribute((const void*)fused_block, hipFuncAttributeMaxDynamicSharedMemorySize, LDS_TOTAL);
  fused_block<<<256, 768, LDS_TOTAL, stream>>>(x, wqkv, wo, w_r, w_gate, w_up, w_down, (u16*)d_out, (char*)d_ws);
}
```

```cpp
#include <hip/hip_runtime.h>
#include <hip/hip_bf16.h>
#include <stdint.h>

#define SEQ 2048
#define DM 640
#define FF 1536

typedef __attribute__((ext_vector_type(8))) short bf16x8;
typedef __attribute__((ext_vector_type(4))) short s16x4;
typedef __attribute__((ext_vector_type(4))) float f32x4;
typedef __attribute__((ext_vector_type(2))) float f32x2;
typedef __attribute__((ext_vector_type(2))) __bf16 bf16x2_t;
typedef __attribute__((address_space(3))) s16x4* lds_s16x4_p;
typedef unsigned short u16;
typedef unsigned int u32;
typedef unsigned long long u64;
typedef __attribute__((ext_vector_type(4))) unsigned int u32x4;
typedef __attribute__((ext_vector_type(2))) unsigned int u32x2;

#define LSTAMP(i)
#define FLUSH_STAMPS

#define MAPV 2
#define HK0 15
#define RGAP 6
#define CGAP 6
#define KGAP 7
#define CDEL 10
#define PDEL2 7
#define OBACK 2
#define OSL 8
#define PDELAY 220
#define FDELAY 450
#define KSTART 1050
#define PGAP 5
#define PDEL1 3
#define PSL 200
#define VDELAY 150
#define FNT ""
#define CCD 296
#define PPRIO "3"
#define OFL ""
#define TQD 140
#define KCD 288
#define B2A "s_setprio 3\n\ts_waitcnt lgkmcnt(0)\n\ts_barrier\n\t"
#define AL1 ""
#define AL2 ""
#define AL3 ""
#define KOLD 290
#define PCD 202
#define PTHR 0.91f
#define GADD 58
#define OORD 0, 4, 5, 6, 7, 1, 2, 3
#define CODE_LINES 270

#define LDS_BARRIER() do { __builtin_amdgcn_sched_barrier(0); asm volatile("s_waitcnt lgkmcnt(0)\n\ts_barrier" ::: "memory"); __builtin_amdgcn_sched_barrier(0); } while (0)
__device__ __forceinline__ float bf2f(u16 v) { return __uint_as_float(((u32)v) << 16); }
__device__ __forceinline__ u16 f2bf(float f) { return __builtin_bit_cast(u16, (__bf16)f); }
__device__ __forceinline__ float rbf(float f) { return bf2f(f2bf(f)); }
__device__ __forceinline__ float lo_f(u32 w) { return __uint_as_float(w << 16); }
__device__ __forceinline__ float hi_f(u32 w) { return __uint_as_float(w & 0xFFFF0000u); }
__device__ __forceinline__ u32 cvt2(f32x2 v) { return __builtin_bit_cast(u32, __builtin_convertvector(v, bf16x2_t)); }
__device__ __forceinline__ f32x2 unpk(u32 w) { return (f32x2){lo_f(w), hi_f(w)}; }
#define NORM2(HV, MB, RD) cvt2(unpk(cvt2(unpk(HV) - (f32x2){MB, MB})) * (f32x2){RD, RD})
__device__ __forceinline__ s16x4 tr_read(const char* p) {
  return __builtin_amdgcn_ds_read_tr16_b64_v4i16((lds_s16x4_p)(p));
}
template <int V> struct icst { static constexpr int value = V; };
template <int CTRL>
__device__ __forceinline__ float dppf(float v) {
  return __builtin_bit_cast(float, __builtin_amdgcn_update_dpp(0, __builtin_bit_cast(int, v), CTRL, 0xf, 0xf, true));
}
__device__ __forceinline__ float row8_sum(float v) {
  v += dppf<0xB1>(v);
  v += dppf<0x4E>(v);
  v += dppf<0x141>(v);
  return v;
}
__device__ __forceinline__ float row16_sum(float v) {
  v = row8_sum(v);
  v += dppf<0x140>(v);
  return v;
}
__device__ __forceinline__ float xg_sum(float v) {
  u32 a = __float_as_uint(v);
  auto r = __builtin_amdgcn_permlane32_swap(a, a, false, false);
  v = __uint_as_float(r[0]) + __uint_as_float(r[1]);
  a = __float_as_uint(v);
  auto r2 = __builtin_amdgcn_permlane16_swap(a, a, false, false);
  return __uint_as_float(r2[0]) + __uint_as_float(r2[1]);
}
__device__ __forceinline__ float tr_sum(f32x4 ys) {
  auto r01 = __builtin_amdgcn_permlane32_swap(__float_as_uint(ys[0]), __float_as_uint(ys[1]), false, false);
  auto r23 = __builtin_amdgcn_permlane32_swap(__float_as_uint(ys[2]), __float_as_uint(ys[3]), false, false);
  const u32 u = __float_as_uint(__uint_as_float(r01[0]) + __uint_as_float(r01[1]));
  const u32 vv = __float_as_uint(__uint_as_float(r23[0]) + __uint_as_float(r23[1]));
  auto r2 = __builtin_amdgcn_permlane16_swap(u, vv, false, false);
  return __uint_as_float(r2[0]) + __uint_as_float(r2[1]);
}
__device__ __forceinline__ float wave_sum(float v) { return xg_sum(row16_sum(v)); }
template <int CTRL>
__device__ __forceinline__ u32 dppu(u32 v) { return (u32)__builtin_amdgcn_update_dpp(0, (int)v, CTRL, 0xf, 0xf, true); }
__device__ __forceinline__ u32 wave_max(u32 v) {
  v = max(v, dppu<0xB1>(v)); v = max(v, dppu<0x4E>(v)); v = max(v, dppu<0x141>(v)); v = max(v, dppu<0x140>(v));
  return max(max((u32)__builtin_amdgcn_readlane(v, 0), (u32)__builtin_amdgcn_readlane(v, 16)),
             max((u32)__builtin_amdgcn_readlane(v, 32), (u32)__builtin_amdgcn_readlane(v, 48)));
}
__device__ __forceinline__ u32 mono(float f) {
  const u32 b = __float_as_uint(f);
  return b ^ ((u32)((int)b >> 31) | 0x80000000u);
}
__device__ __forceinline__ float dot2b(u32 a, u32 b, float c) {
  return __builtin_amdgcn_fdot2_f32_bf16(__builtin_bit_cast(bf16x2_t, a), __builtin_bit_cast(bf16x2_t, b), c, false);
}
template <int OFF>
__device__ __forceinline__ void dma_m0(u32 voff, const char* sbase, u32 dst) {
  asm volatile("s_mov_b32 m0, %2\n\ts_nop 0\n\tglobal_load_lds_dwordx4 %0, %1 offset:%3"
               :: "v"(voff), "s"(sbase), "s"(dst), "n"(OFF) : "memory");
}
template <int OFF>
__device__ __forceinline__ void dma_nx(u32 voff, const char* sbase) {
  asm volatile("global_load_lds_dwordx4 %0, %1 offset:%2" :: "v"(voff), "s"(sbase), "n"(OFF) : "memory");
}
__device__ __forceinline__ u32 lds_ld32(u32 addr) {
  u32 v;
  asm volatile("ds_read_b32 %0, %1\n\ts_waitcnt lgkmcnt(0)" : "=v"(v) : "v"(addr) : "memory");
  return v;
}
__device__ __forceinline__ u32x4 lds_ld128(u32 addr) {
  u32x4 v;
  asm volatile("ds_read_b128 %0, %1\n\ts_waitcnt lgkmcnt(0)" : "=v"(v) : "v"(addr) : "memory");
  return v;
}
__device__ __forceinline__ bf16x8 mk8(u32 a, u32 b, u32 c, u32 d) {
  return __builtin_bit_cast(bf16x8, (u32x4){a, b, c, d});
}
__device__ __forceinline__ bf16x8 tr_frag(const char* p, int off2) {
  s16x4 b0 = tr_read(p);
  s16x4 b1 = tr_read(p + off2);
  bf16x8 r;
  r[0] = b0[0]; r[1] = b0[1]; r[2] = b0[2]; r[3] = b0[3];
  r[4] = b1[0]; r[5] = b1[1]; r[6] = b1[2]; r[7] = b1[3];
  return r;
}
#define SPIN_CAP 400000
#define MFMA(A, B, C) __builtin_amdgcn_mfma_f32_16x16x32_bf16(A, B, C, 0, 0, 0)

#define HSLOT 16384
#define XRING 98304
#define XSLOT 12288
#define LDS_TOTAL 163840
#define EXO 102400
#define HIM 148480
#define PST_OFF (EXO + 40960)
#define TAB_OFF 161280
#define ONES_OFF 162048
#define CTL_OFF 162176
#define WR_OFF 162304
#define REC_OFF 162816
#define HN_OFF 124928
#define HSL 1344
#define ASL 1056
#define TCH_OFF 162560
#define ACT_OFF 135168
#define KEY_OFF (ACT_OFF + 5120)
#define POST_OFF (KEY_OFF + 256)
#define DOWN_OFF 81920
#define WS_EPOCH 0
#define WS_HFLAG 4096
#define WS_VP 40960
#define WS_LN1 16384
#define WS_FILL 131072
#define WS_O WS_FILL
#define WS_STAT (WS_O + 2621440)
#define WS_CAND (WS_STAT + 425984)
#define WS_PART (WS_CAND + 133120)
#define WS_BXF (WS_PART + 122880)
#define WS_PC (WS_BXF + 26624)
#define WS_PART2 (WS_PC + 66560)
#define WS_PK (4u << 20)
#define SENT 0xFFFFFFFFu

#define GST4(VO, SB, O, V) asm volatile("global_store_dwordx4 %0, %1, %2 offset:%3 sc1\n\ts_nop 1" ::"v"(VO), "v"(V), "s"(SB), "n"(O) : "memory")
#define GST2(VO, SB, O, V) asm volatile("global_store_dwordx2 %0, %1, %2 offset:%3 sc1\n\ts_nop 1" ::"v"(VO), "v"(V), "s"(SB), "n"(O) : "memory")
#define GLD2(D, VO, SB, O) asm volatile("global_load_dwordx2 %0, %1, %2 offset:%3 sc1" : "=v"(D) : "v"(VO), "s"(SB), "n"(O) : "memory")

__device__ __forceinline__ void touch_line(const void* p) {
  asm volatile("s_mov_b32 m0, %1\n\ts_nop 0\n\tglobal_load_lds_dword %0, off" ::"v"(p), "s"(TCH_OFF) : "memory");
}
__device__ __forceinline__ u32 w_off(int i, int lane, int hg) {
  int cc = (lane & 31) ^ (((2 * i + (lane >> 5)) & 7) << 1);
  cc = cc > 29 ? 29 : cc;
  return (lane >> 5) * 3840 + ((cc >= 10 ? 1120 : 0) + (cc >= 20 ? 1120 : 0)) + hg * 160 + cc * 16;
}
#define W_ROW(I) (((((2 * (I)) & 0x13) | (((2 * (I)) & 4) << 1) | (((2 * (I)) & 8) >> 1))) * 3840)
__device__ __forceinline__ u32 x_off(int i, int lane, int r0) {
  const int xr = i * 8 + (lane >> 3);
  int grow = r0 - 16 + xr;
  grow = grow < 0 ? 0 : (grow > SEQ - 1 ? SEQ - 1 : grow);
  return grow * 1280 + (((lane & 7) ^ ((xr >> 1) & 7)) * 16);
}

#define ATT_ASM \
  "ds_read_b32 v28, %[aq] offset:64\n\t" \
  "ds_read_b32 v30, %[aka]\n\t" \
  "ds_read_b32 v31, %[akb]\n\t" \
  "v_mov_b64 v[38:39], 0\n\t" \
  "v_mov_b64 v[42:43], 0\n\t" \
  "s_lshl_b32 s41, %[kh], 5\n\t" \
  "s_lshl_b64 s[46:47], 0xffff, s41\n\t" \
  "s_lshl_b64 s[48:49], s[46:47], 16\n\t" \
  "s_mov_b32 s50, 0xaaaaaaaa\n\t" \
  "s_mov_b32 s51, 0xaaaaaaaa\n\t" \
  "s_waitcnt lgkmcnt(0)\n\t" \
  "s_barrier\n\t" \
  "ds_read_b64 v[36:37], %[eh] offset:512\n\t" \
  "ds_read_b64 v[44:45], %[eh] offset:15360\n\t" \
  "ds_read_b64 v[46:47], %[e4] offset:27648\n\t" \
  "ds_read_b64 v[48:49], %[eh] offset:15872\n\t" \
  "ds_read_b64 v[50:51], %[e4] offset:28160\n\t" \
  "s_mov_b64 exec, s[46:47]\n\t" \
  "ds_read_b64 v[38:39], %[e4] offset:12800\n\t" \
  "s_mov_b64 exec, s[48:49]\n\t" \
  "ds_read_b64 v[42:43], %[e4] offset:12800\n\t" \
  "s_mov_b64 exec, -1\n\t" \
  "ds_read_b64 v[40:41], %[eh] offset:3584\n\t" \
  "ds_read_b64 v[52:53], %[eh] offset:18432\n\t" \
  "ds_read_b64 v[54:55], %[e4] offset:27648\n\t" \
  "ds_read_b64 v[56:57], %[eh] offset:18944\n\t" \
  "ds_read_b64 v[58:59], %[e4] offset:28160\n\t" \
  "v_mul_f32 v28, 0x3ea52b5b, v28\n\t" \
  "v_pk_mul_f32 v[32:33], v[28:29], v[30:31] op_sel_hi:[0,1]\n\t" \
  "s_waitcnt lgkmcnt(6)\n\t" \
  "v_mfma_f32_16x16x32_bf16 v[44:47], v[44:47], v[36:39], 0\n\t" \
  "ds_read2st64_b64 v[60:63], %[eh] offset0:60 offset1:61\n\t" \
  "v_mfma_f32_16x16x32_bf16 v[48:51], v[48:51], v[36:39], 0\n\t" \
  "ds_read2st64_b64 v[64:67], %[eh] offset0:66 offset1:67\n\t" \
  "s_waitcnt lgkmcnt(2)\n\t" \
  "v_mfma_f32_16x16x32_bf16 v[52:55], v[52:55], v[40:43], 0\n\t" \
  "ds_read2st64_b64 v[68:71], %[e4] offset0:84 offset1:85\n\t" \
  "v_mfma_f32_16x16x32_bf16 v[56:59], v[56:59], v[40:43], 0\n\t" \
  "s_mov_b32 exec_lo, 0xfff0ffff\n\t" \
  "s_mov_b32 exec_hi, 0xf000ff00\n\t" \
  "s_nop 0\n\t" \
  "v_mov_b64 v[44:45], v[48:49]\n\t" \
  "s_mov_b32 exec_lo, 0xffc0fffc\n\t" \
  "s_mov_b32 exec_hi, 0xc000fc00\n\t" \
  "v_mov_b64 v[46:47], v[50:51]\n\t" \
  "s_nop 0\n\t" \
  "v_mov_b64 v[54:55], v[58:59]\n\t" \
  "s_mov_b32 exec_lo, 0xfff0ffff\n\t" \
  "s_mov_b32 exec_hi, 0xf000ff00\n\t" \
  "v_mov_b64 v[52:53], v[56:57]\n\t" \
  "s_mov_b64 exec, -1\n\t" \
  "v_cndmask_b32 v44, v44, v45, s[50:51]\n\t" \
  "v_cndmask_b32 v46, v46, v47, s[50:51]\n\t" \
  "v_cndmask_b32 v52, v52, v53, s[50:51]\n\t" \
  "v_cndmask_b32 v54, v54, v55, s[50:51]\n\t" \
  "v_fma_f32 v72, v44, v32, %[b0]\n\t" \
  "v_fma_f32 v73, v46, v33, %[b1]\n\t" \
  "v_fma_f32 v74, v52, v32, %[b0]\n\t" \
  "v_fma_f32 v75, v54, v33, %[b1]\n\t" \
  "v_max_f32 v34, v72, v73\n\t" \
  "v_max_f32 v35, v74, v75\n\t" \
  "ds_read_b128 v[76:79], %[arec]\n\t" \
  "ds_read_b128 v[80:83], %[arec] offset:16\n\t" \
  "v_permlane32_swap_b32 v34, v35\n\t" \
  "v_max_f32 v34, v34, v35\n\t" \
  "v_mov_b32 v35, v34\n\t" \
  "ds_read_b128 v[84:87], %[arec] offset:32\n\t" \
  "s_nop 0\n\t" \
  "v_permlane16_swap_b32 v34, v35\n\t" \
  "v_max_f32 v34, v34, v35\n\t" \
  "v_mov_b32 v35, v34\n\t" \
  "s_nop 1\n\t" \
  "v_permlane32_swap_b32 v34, v35\n\t" \
  "v_sub_f32 v72, v72, v34\n\t" \
  "v_sub_f32 v73, v73, v34\n\t" \
  "v_sub_f32 v74, v74, v35\n\t" \
  "v_sub_f32 v75, v75, v35\n\t" \
  "v_exp_f32 v72, v72\n\t" \
  "v_exp_f32 v73, v73\n\t" \
  "v_exp_f32 v74, v74\n\t" \
  "v_exp_f32 v75, v75\n\t" \
  "v_add_f32 v34, v72, v73\n\t" \
  "v_add_f32 v35, v74, v75\n\t" \
  "v_pk_mul_f32 v[72:73], v[72:73], v[30:31]\n\t" \
  "v_pk_mul_f32 v[74:75], v[74:75], v[30:31]\n\t" \
  "v_permlane32_swap_b32 v34, v35\n\t" \
  "v_add_f32 v34, v34, v35\n\t" \
  "v_mov_b32 v35, v34\n\t" \
  "s_nop 1\n\t" \
  "v_permlane16_swap_b32 v34, v35\n\t" \
  "v_add_f32 v34, v34, v35\n\t" \
  "v_rcp_f32 v34, v34\n\t" \
  "s_nop 0\n\t" \
  "v_mov_b32 v30, v34\n\t" \
  "s_waitcnt lgkmcnt(0)\n\t" \
  "s_nop 0\n\t" \
  "v_permlane32_swap_b32 v34, v30\n\t" \
  "v_pk_mul_f32 v[72:73], v[34:35], v[72:73] op_sel_hi:[0,1]\n\t" \
  "v_pk_mul_f32 v[74:75], v[30:31], v[74:75] op_sel_hi:[0,1]\n\t" \
  "v_cvt_pk_bf16_f32 v72, v72, v73\n\t" \
  "v_cvt_pk_bf16_f32 v74, v74, v75\n\t" \
  "v_perm_b32 v36, 0, v72, %[s0]\n\t" \
  "v_perm_b32 v37, 0, v72, %[s1]\n\t" \
  "v_perm_b32 v38, 0, v72, %[s2]\n\t" \
  "v_perm_b32 v39, 0, v72, %[s3]\n\t" \
  "v_perm_b32 v40, 0, v74, %[s0]\n\t" \
  "v_perm_b32 v41, 0, v74, %[s1]\n\t" \
  "v_mfma_f32_16x16x32_bf16 v[60:63], v[60:63], v[36:39], 0\n\t" \
  "v_perm_b32 v42, 0, v74, %[s2]\n\t" \
  "v_perm_b32 v43, 0, v74, %[s3]\n\t" \
  "v_mfma_f32_16x16x32_bf16 v[52:55], v[68:71], v[36:39], 0\n\t" \
  "s_nop 0\n\t" \
  "v_mfma_f32_16x16x32_bf16 v[64:67], v[64:67], v[40:43], 0\n\t" \
  "v_mfma_f32_16x16x32_bf16 v[56:59], v[68:71], v[40:43], 0\n\t" \
  "s_nop 1\n\t" \
  "v_cvt_pk_bf16_f32 v24, v60, v61\n\t" \
  "v_cvt_pk_bf16_f32 v25, v62, v63\n\t" \
  "s_nop 0\n\t" \
  "v_cvt_pk_bf16_f32 v52, v52, v53\n\t" \
  "v_cvt_pk_bf16_f32 v53, v54, v55\n\t" \
  "v_cvt_pk_bf16_f32 v26, v64, v65\n\t" \
  "v_cvt_pk_bf16_f32 v27, v66, v67\n\t" \
  "global_store_dwordx4 %[l16], v[24:27], %[os] sc1\n\t" \
  "s_mov_b64 exec, s[48:49]\n\t" \
  "v_cvt_pk_bf16_f32 v52, v56, v57\n\t" \
  "v_cvt_pk_bf16_f32 v53, v58, v59\n\t" \
  "s_or_b64 exec, s[46:47], s[48:49]\n\t" \
  "global_store_dwordx2 %[l16], v[52:53], %[os4] sc1\n\t" \
  "s_mov_b64 exec, -1\n\t" \
  "v_mfma_f32_16x16x32_bf16 v[60:63], v[24:27], v[76:79], 0\n\t" \
  "v_and_b32 v54, 0x7fff7fff, v52\n\t" \
  "v_and_b32 v55, 0x7fff7fff, v53\n\t" \
  "v_and_b32 v64, 0x7fff7fff, v24\n\t" \
  "v_and_b32 v65, 0x7fff7fff, v25\n\t" \
  "v_and_b32 v66, 0x7fff7fff, v26\n\t" \
  "v_and_b32 v67, 0x7fff7fff, v27\n\t" \
  "v_mfma_f32_16x16x32_bf16 v[60:63], v[52:55], v[80:83], v[60:63]\n\t" \
  "ds_write2st64_b64 %[eh], v[24:25], v[26:27] offset0:1 offset1:7\n\t" \
  "v_mfma_f32_16x16x32_bf16 v[60:63], v[64:67], v[84:87], v[60:63]\n\t" \
  "s_mov_b32 exec_lo, 0x01010101\n\t" \
  "s_mov_b32 exec_hi, 0x01010101\n\t" \
  "s_nop 5\n\t" \
  "v_cvt_pk_bf16_f32 v60, v60, v61\n\t" \
  "v_cvt_pk_bf16_f32 v61, v62, v63\n\t" \
  "v_and_b32 v60, -2, v60\n\t" \
  "global_store_dwordx2 %[vpp], v[60:61], %[sst] sc1\n\t" \
  "s_mov_b64 exec, -1\n\t" \
  ""
#define OP_ASM \
  CNT0 \
  "s_mov_b32 s43, %[cap]\n\t" \
  STP(0) \
  "s_cmp_lt_u32 %[w], 4\n\t" \
  "s_cbranch_scc1 LH_%=\n\t" \
  "s_setprio 3\n\t" \
  "global_load_dwordx4 v[124:127], %[vd], %[oa1] sc1\n\t" \
  "global_load_dwordx4 v[128:131], %[vk], %[oa1] sc1\n\t" \
  "global_load_dwordx4 v[132:135], %[vd], %[oa2] sc1\n\t" \
  "global_load_dwordx4 v[136:139], %[vk], %[oa2] sc1\n\t" \
  "v_add_u32 %[tk], %[sk], %[e4]\n\t" \
  "v_add_u32 %[ta], %[so0], %[pm]\n\t" \
  "ds_read2st64_b64 v[104:107], %[tk] offset0:1 offset1:7\n\t" \
  "v_xor_b32 %[tk], 512, %[tk]\n\t" \
  "ds_read2st64_b64 v[108:111], %[tk] offset0:1 offset1:7\n\t" \
  "ds_read_b64_tr_b16 v[64:65], %[ta] offset:0\n\t" \
  "ds_read_b64_tr_b16 v[66:67], %[ta] offset:3200\n\t" \
  "ds_read_b64_tr_b16 v[68:69], %[ta] offset:8\n\t" \
  "ds_read_b64_tr_b16 v[70:71], %[ta] offset:3208\n\t" \
  "ds_read_b64_tr_b16 v[72:73], %[ta] offset:16\n\t" \
  "ds_read_b64_tr_b16 v[74:75], %[ta] offset:3216\n\t" \
  "ds_read_b64_tr_b16 v[76:77], %[ta] offset:24\n\t" \
  "ds_read_b64_tr_b16 v[78:79], %[ta] offset:3224\n\t" \
  "ds_read_b64_tr_b16 v[80:81], %[ta] offset:32\n\t" \
  "ds_read_b64_tr_b16 v[82:83], %[ta] offset:3232\n\t" \
  "ds_read_b64 v[112:113], %[ra] offset:0\n\t" \
  "ds_read_b64 v[114:115], %[ra] offset:8\n\t" \
  "ds_read_b64 v[116:117], %[ra] offset:16\n\t" \
  "ds_read_b64 v[118:119], %[ra] offset:24\n\t" \
  "ds_read_b64 v[120:121], %[ra] offset:32\n\t" \
  "global_load_dwordx4 v[140:143], %[vd], %[oa3] sc1\n\t" \
  "v_add_u32 %[ta], %[so1], %[pm]\n\t" \
  "s_waitcnt lgkmcnt(5)\n\t" \
  "v_mfma_f32_16x16x32_bf16 v[44:47], v[64:67], v[108:111], 0\n\t" \
  "ds_read_b64_tr_b16 v[84:85], %[ta] offset:0\n\t" \
  "ds_read_b64_tr_b16 v[86:87], %[ta] offset:3200\n\t" \
  "v_mfma_f32_16x16x32_bf16 v[48:51], v[68:71], v[108:111], 0\n\t" \
  "ds_read_b64_tr_b16 v[88:89], %[ta] offset:8\n\t" \
  "ds_read_b64_tr_b16 v[90:91], %[ta] offset:3208\n\t" \
  "v_mfma_f32_16x16x32_bf16 v[52:55], v[72:75], v[108:111], 0\n\t" \
  "ds_read_b64_tr_b16 v[92:93], %[ta] offset:16\n\t" \
  "ds_read_b64_tr_b16 v[94:95], %[ta] offset:3216\n\t" \
  "v_mfma_f32_16x16x32_bf16 v[56:59], v[76:79], v[108:111], 0\n\t" \
  "ds_read_b64_tr_b16 v[96:97], %[ta] offset:24\n\t" \
  "ds_read_b64_tr_b16 v[98:99], %[ta] offset:3224\n\t" \
  "v_mfma_f32_16x16x32_bf16 v[60:63], v[80:83], v[108:111], 0\n\t" \
  "ds_read_b64_tr_b16 v[100:101], %[ta] offset:32\n\t" \
  "ds_read_b64_tr_b16 v[102:103], %[ta] offset:3232\n\t" \
  "s_waitcnt lgkmcnt(10)\n\t" \
  "v_lshlrev_b32 v24, 16, v112\n\t" \
  "v_and_b32 v25, 0xffff0000, v112\n\t" \
  "v_lshlrev_b32 v26, 16, v113\n\t" \
  "v_and_b32 v27, 0xffff0000, v113\n\t" \
  "v_lshlrev_b32 v28, 16, v114\n\t" \
  "v_and_b32 v29, 0xffff0000, v114\n\t" \
  "v_lshlrev_b32 v30, 16, v115\n\t" \
  "v_and_b32 v31, 0xffff0000, v115\n\t" \
  "v_mfma_f32_16x16x32_bf16 v[24:27], v[64:67], v[104:107], v[24:27]\n\t" \
  "v_lshlrev_b32 v32, 16, v116\n\t" \
  "v_and_b32 v33, 0xffff0000, v116\n\t" \
  "v_lshlrev_b32 v34, 16, v117\n\t" \
  "v_and_b32 v35, 0xffff0000, v117\n\t" \
  "v_mfma_f32_16x16x32_bf16 v[28:31], v[68:71], v[104:107], v[28:31]\n\t" \
  "v_lshlrev_b32 v36, 16, v118\n\t" \
  "v_and_b32 v37, 0xffff0000, v118\n\t" \
  "v_lshlrev_b32 v38, 16, v119\n\t" \
  "v_and_b32 v39, 0xffff0000, v119\n\t" \
  "v_mfma_f32_16x16x32_bf16 v[32:35], v[72:75], v[104:107], v[32:35]\n\t" \
  "v_lshlrev_b32 v40, 16, v120\n\t" \
  "v_and_b32 v41, 0xffff0000, v120\n\t" \
  "v_lshlrev_b32 v42, 16, v121\n\t" \
  "v_and_b32 v43, 0xffff0000, v121\n\t" \
  "v_mfma_f32_16x16x32_bf16 v[36:39], v[76:79], v[104:107], v[36:39]\n\t" \
  "s_nop 0\n\t" \
  "v_mfma_f32_16x16x32_bf16 v[40:43], v[80:83], v[104:107], v[40:43]\n\t" \
  "v_add_u32 %[ta], %[so2], %[pm]\n\t" \
  "ds_read_b64_tr_b16 v[104:105], %[ta] offset:0\n\t" \
  "ds_read_b64_tr_b16 v[106:107], %[ta] offset:3200\n\t" \
  "ds_read_b64_tr_b16 v[108:109], %[ta] offset:8\n\t" \
  "ds_read_b64_tr_b16 v[110:111], %[ta] offset:3208\n\t" \
  "ds_read_b64_tr_b16 v[112:113], %[ta] offset:16\n\t" \
  "ds_read_b64_tr_b16 v[114:115], %[ta] offset:3216\n\t" \
  "ds_read_b64_tr_b16 v[116:117], %[ta] offset:24\n\t" \
  "ds_read_b64_tr_b16 v[118:119], %[ta] offset:3224\n\t" \
  "ds_read_b64_tr_b16 v[120:121], %[ta] offset:32\n\t" \
  "ds_read_b64_tr_b16 v[122:123], %[ta] offset:3232\n\t" \
  STP(1) \
  "global_load_dwordx4 v[144:147], %[vk], %[oa3] sc1\n\t" \
  "global_load_dwordx4 v[148:151], %[vd], %[oa4] sc1\n\t" \
  "s_waitcnt vmcnt(5)\n\t" \
  "v_max_u32 %[tk], v124, v128\n\t" \
  "v_cmp_eq_u32 vcc, -1, %[tk]\n\t" \
  "s_cbranch_vccnz LSs1_%=\n\t" \
  "LQs1_%=:\n\t" \
  STP(2) \
  "s_waitcnt lgkmcnt(10)\n\t" \
  "v_mfma_f32_16x16x32_bf16 v[44:47], v[84:87], v[124:127], v[44:47]\n\t" \
  "v_add_u32 %[ta], %[so3], %[pm]\n\t" \
  "ds_read_b64_tr_b16 v[64:65], %[ta] offset:0\n\t" \
  "v_mfma_f32_16x16x32_bf16 v[24:27], v[84:87], v[128:131], v[24:27]\n\t" \
  "ds_read_b64_tr_b16 v[66:67], %[ta] offset:3200\n\t" \
  "global_load_dwordx4 v[152:155], %[vk], %[oa4] sc1\n\t" \
  "v_mfma_f32_16x16x32_bf16 v[48:51], v[88:91], v[124:127], v[48:51]\n\t" \
  "ds_read_b64_tr_b16 v[68:69], %[ta] offset:8\n\t" \
  "v_mfma_f32_16x16x32_bf16 v[28:31], v[88:91], v[128:131], v[28:31]\n\t" \
  "ds_read_b64_tr_b16 v[70:71], %[ta] offset:3208\n\t" \
  "global_load_dwordx4 v[156:159], %[vd], %[oa5] sc1\n\t" \
  "v_mfma_f32_16x16x32_bf16 v[52:55], v[92:95], v[124:127], v[52:55]\n\t" \
  "ds_read_b64_tr_b16 v[72:73], %[ta] offset:16\n\t" \
  "v_mfma_f32_16x16x32_bf16 v[32:35], v[92:95], v[128:131], v[32:35]\n\t" \
  "ds_read_b64_tr_b16 v[74:75], %[ta] offset:3216\n\t" \
  "v_mfma_f32_16x16x32_bf16 v[56:59], v[96:99], v[124:127], v[56:59]\n\t" \
  "ds_read_b64_tr_b16 v[76:77], %[ta] offset:24\n\t" \
  "v_mfma_f32_16x16x32_bf16 v[36:39], v[96:99], v[128:131], v[36:39]\n\t" \
  "ds_read_b64_tr_b16 v[78:79], %[ta] offset:3224\n\t" \
  "s_waitcnt vmcnt(5)\n\t" \
  "v_max_u32 %[tk], v132, v136\n\t" \
  "v_cmp_eq_u32 vcc, -1, %[tk]\n\t" \
  "v_mfma_f32_16x16x32_bf16 v[60:63], v[100:103], v[124:127], v[60:63]\n\t" \
  "ds_read_b64_tr_b16 v[80:81], %[ta] offset:32\n\t" \
  "v_mfma_f32_16x16x32_bf16 v[40:43], v[100:103], v[128:131], v[40:43]\n\t" \
  "ds_read_b64_tr_b16 v[82:83], %[ta] offset:3232\n\t" \
  "s_cbranch_vccnz LSs2_%=\n\t" \
  "LQs2_%=:\n\t" \
  "s_waitcnt lgkmcnt(10)\n\t" \
  "v_mfma_f32_16x16x32_bf16 v[44:47], v[104:107], v[132:135], v[44:47]\n\t" \
  "v_add_u32 %[ta], %[so4], %[pf]\n\t" \
  "ds_read_b64_tr_b16 v[84:85], %[ta] offset:0\n\t" \
  "v_mfma_f32_16x16x32_bf16 v[24:27], v[104:107], v[136:139], v[24:27]\n\t" \
  "ds_read_b64_tr_b16 v[86:87], %[ta] offset:12800\n\t" \
  "global_load_dwordx4 v[124:127], %[vk], %[oa5] sc1\n\t" \
  "v_mfma_f32_16x16x32_bf16 v[48:51], v[108:111], v[132:135], v[48:51]\n\t" \
  "ds_read_b64_tr_b16 v[88:89], %[ta] offset:8\n\t" \
  "v_mfma_f32_16x16x32_bf16 v[28:31], v[108:111], v[136:139], v[28:31]\n\t" \
  "ds_read_b64_tr_b16 v[90:91], %[ta] offset:12808\n\t" \
  "global_load_dwordx4 v[128:131], %[vd], %[oa6] sc1\n\t" \
  "v_mfma_f32_16x16x32_bf16 v[52:55], v[112:115], v[132:135], v[52:55]\n\t" \
  "ds_read_b64_tr_b16 v[92:93], %[ta] offset:16\n\t" \
  "v_mfma_f32_16x16x32_bf16 v[32:35], v[112:115], v[136:139], v[32:35]\n\t" \
  "ds_read_b64_tr_b16 v[94:95], %[ta] offset:12816\n\t" \
  "v_mfma_f32_16x16x32_bf16 v[56:59], v[116:119], v[132:135], v[56:59]\n\t" \
  "ds_read_b64_tr_b16 v[96:97], %[ta] offset:24\n\t" \
  "v_mfma_f32_16x16x32_bf16 v[36:39], v[116:119], v[136:139], v[36:39]\n\t" \
  "ds_read_b64_tr_b16 v[98:99], %[ta] offset:12824\n\t" \
  "s_waitcnt vmcnt(5)\n\t" \
  "v_max_u32 %[tk], v140, v144\n\t" \
  "v_cmp_eq_u32 vcc, -1, %[tk]\n\t" \
  "v_mfma_f32_16x16x32_bf16 v[60:63], v[120:123], v[132:135], v[60:63]\n\t" \
  "ds_read_b64_tr_b16 v[100:101], %[ta] offset:32\n\t" \
  "v_mfma_f32_16x16x32_bf16 v[40:43], v[120:123], v[136:139], v[40:43]\n\t" \
  "ds_read_b64_tr_b16 v[102:103], %[ta] offset:12832\n\t" \
  "s_cbranch_vccnz LSs3_%=\n\t" \
  "LQs3_%=:\n\t" \
  "s_waitcnt lgkmcnt(10)\n\t" \
  "v_mfma_f32_16x16x32_bf16 v[44:47], v[64:67], v[140:143], v[44:47]\n\t" \
  "v_add_u32 %[ta], %[so5], %[pm]\n\t" \
  "ds_read_b64_tr_b16 v[104:105], %[ta] offset:0\n\t" \
  "v_mfma_f32_16x16x32_bf16 v[24:27], v[64:67], v[144:147], v[24:27]\n\t" \
  "ds_read_b64_tr_b16 v[106:107], %[ta] offset:3200\n\t" \
  "global_load_dwordx4 v[132:135], %[vk], %[oa6] sc1\n\t" \
  "v_mfma_f32_16x16x32_bf16 v[48:51], v[68:71], v[140:143], v[48:51]\n\t" \
  "ds_read_b64_tr_b16 v[108:109], %[ta] offset:8\n\t" \
  "v_mfma_f32_16x16x32_bf16 v[28:31], v[68:71], v[144:147], v[28:31]\n\t" \
  "ds_read_b64_tr_b16 v[110:111], %[ta] offset:3208\n\t" \
  "global_load_dwordx4 v[136:139], %[vd], %[oa7] sc1\n\t" \
  "v_mfma_f32_16x16x32_bf16 v[52:55], v[72:75], v[140:143], v[52:55]\n\t" \
  "ds_read_b64_tr_b16 v[112:113], %[ta] offset:16\n\t" \
  "v_mfma_f32_16x16x32_bf16 v[32:35], v[72:75], v[144:147], v[32:35]\n\t" \
  "ds_read_b64_tr_b16 v[114:115], %[ta] offset:3216\n\t" \
  "v_mfma_f32_16x16x32_bf16 v[56:59], v[76:79], v[140:143], v[56:59]\n\t" \
  "ds_read_b64_tr_b16 v[116:117], %[ta] offset:24\n\t" \
  "v_mfma_f32_16x16x32_bf16 v[36:39], v[76:79], v[144:147], v[36:39]\n\t" \
  "ds_read_b64_tr_b16 v[118:119], %[ta] offset:3224\n\t" \
  "s_waitcnt vmcnt(5)\n\t" \
  "v_max3_u32 %[tk], v148, v150, v152\n\t" \
  "v_max_u32 %[tk], %[tk], v154\n\t" \
  "v_cmp_eq_u32 vcc, -1, %[tk]\n\t" \
  "v_mfma_f32_16x16x32_bf16 v[60:63], v[80:83], v[140:143], v[60:63]\n\t" \
  "ds_read_b64_tr_b16 v[120:121], %[ta] offset:32\n\t" \
  "v_mfma_f32_16x16x32_bf16 v[40:43], v[80:83], v[144:147], v[40:43]\n\t" \
  "ds_read_b64_tr_b16 v[122:123], %[ta] offset:3232\n\t" \
  "s_cbranch_vccnz LSs4_%=\n\t" \
  "LQs4_%=:\n\t" \
  "s_waitcnt lgkmcnt(10)\n\t" \
  "v_mfma_f32_16x16x32_bf16 v[44:47], v[84:87], v[148:151], v[44:47]\n\t" \
  "v_add_u32 %[ta], %[so6], %[pm]\n\t" \
  "ds_read_b64_tr_b16 v[64:65], %[ta] offset:0\n\t" \
  "v_mfma_f32_16x16x32_bf16 v[24:27], v[84:87], v[152:155], v[24:27]\n\t" \
  "ds_read_b64_tr_b16 v[66:67], %[ta] offset:3200\n\t" \
  "global_load_dwordx4 v[140:143], %[vd], %[oa8] sc1\n\t" \
  "v_mfma_f32_16x16x32_bf16 v[48:51], v[88:91], v[148:151], v[48:51]\n\t" \
  "ds_read_b64_tr_b16 v[68:69], %[ta] offset:8\n\t" \
  "v_mfma_f32_16x16x32_bf16 v[28:31], v[88:91], v[152:155], v[28:31]\n\t" \
  "ds_read_b64_tr_b16 v[70:71], %[ta] offset:3208\n\t" \
  "global_load_dwordx4 v[144:147], %[vd], %[oa9] sc1\n\t" \
  "v_mfma_f32_16x16x32_bf16 v[52:55], v[92:95], v[148:151], v[52:55]\n\t" \
  "ds_read_b64_tr_b16 v[72:73], %[ta] offset:16\n\t" \
  "v_mfma_f32_16x16x32_bf16 v[32:35], v[92:95], v[152:155], v[32:35]\n\t" \
  "ds_read_b64_tr_b16 v[74:75], %[ta] offset:3216\n\t" \
  "v_mfma_f32_16x16x32_bf16 v[56:59], v[96:99], v[148:151], v[56:59]\n\t" \
  "ds_read_b64_tr_b16 v[76:77], %[ta] offset:24\n\t" \
  "v_mfma_f32_16x16x32_bf16 v[36:39], v[96:99], v[152:155], v[36:39]\n\t" \
  "ds_read_b64_tr_b16 v[78:79], %[ta] offset:3224\n\t" \
  "s_waitcnt vmcnt(5)\n\t" \
  "v_max_u32 %[tk], v156, v124\n\t" \
  "v_cmp_eq_u32 vcc, -1, %[tk]\n\t" \
  "v_mfma_f32_16x16x32_bf16 v[60:63], v[100:103], v[148:151], v[60:63]\n\t" \
  "ds_read_b64_tr_b16 v[80:81], %[ta] offset:32\n\t" \
  "v_mfma_f32_16x16x32_bf16 v[40:43], v[100:103], v[152:155], v[40:43]\n\t" \
  "ds_read_b64_tr_b16 v[82:83], %[ta] offset:3232\n\t" \
  "s_cbranch_vccnz LSs5_%=\n\t" \
  "LQs5_%=:\n\t" \
  STP(3) \
  "s_waitcnt lgkmcnt(10)\n\t" \
  "v_mfma_f32_16x16x32_bf16 v[44:47], v[104:107], v[156:159], v[44:47]\n\t" \
  "v_add_u32 %[ta], %[so7], %[pm]\n\t" \
  "ds_read_b64_tr_b16 v[84:85], %[ta] offset:0\n\t" \
  "v_mfma_f32_16x16x32_bf16 v[24:27], v[104:107], v[124:127], v[24:27]\n\t" \
  "ds_read_b64_tr_b16 v[86:87], %[ta] offset:3200\n\t" \
  "global_load_dwordx4 v[148:151], %[vk], %[oa7] sc1\n\t" \
  "v_mfma_f32_16x16x32_bf16 v[48:51], v[108:111], v[156:159], v[48:51]\n\t" \
  "ds_read_b64_tr_b16 v[88:89], %[ta] offset:8\n\t" \
  "v_mfma_f32_16x16x32_bf16 v[28:31], v[108:111], v[124:127], v[28:31]\n\t" \
  "ds_read_b64_tr_b16 v[90:91], %[ta] offset:3208\n\t" \
  "global_load_dwordx4 v[152:155], %[vk], %[oa8] sc1\n\t" \
  "v_mfma_f32_16x16x32_bf16 v[52:55], v[112:115], v[156:159], v[52:55]\n\t" \
  "ds_read_b64_tr_b16 v[92:93], %[ta] offset:16\n\t" \
  "v_mfma_f32_16x16x32_bf16 v[32:35], v[112:115], v[124:127], v[32:35]\n\t" \
  "ds_read_b64_tr_b16 v[94:95], %[ta] offset:3216\n\t" \
  "v_mfma_f32_16x16x32_bf16 v[56:59], v[116:119], v[156:159], v[56:59]\n\t" \
  "ds_read_b64_tr_b16 v[96:97], %[ta] offset:24\n\t" \
  "v_mfma_f32_16x16x32_bf16 v[36:39], v[116:119], v[124:127], v[36:39]\n\t" \
  "ds_read_b64_tr_b16 v[98:99], %[ta] offset:3224\n\t" \
  "s_waitcnt vmcnt(5)\n\t" \
  "v_max_u32 %[tk], v128, v132\n\t" \
  "v_cmp_eq_u32 vcc, -1, %[tk]\n\t" \
  "v_mfma_f32_16x16x32_bf16 v[60:63], v[120:123], v[156:159], v[60:63]\n\t" \
  "ds_read_b64_tr_b16 v[100:101], %[ta] offset:32\n\t" \
  "v_mfma_f32_16x16x32_bf16 v[40:43], v[120:123], v[124:127], v[40:43]\n\t" \
  "ds_read_b64_tr_b16 v[102:103], %[ta] offset:3232\n\t" \
  "s_cbranch_vccnz LSs6_%=\n\t" \
  "LQs6_%=:\n\t" \
  "s_waitcnt lgkmcnt(10)\n\t" \
  "v_mfma_f32_16x16x32_bf16 v[44:47], v[64:67], v[128:131], v[44:47]\n\t" \
  "v_add_u32 %[ta], %[so8], %[pm]\n\t" \
  "ds_read_b64_tr_b16 v[104:105], %[ta] offset:0\n\t" \
  "v_mfma_f32_16x16x32_bf16 v[24:27], v[64:67], v[132:135], v[24:27]\n\t" \
  "ds_read_b64_tr_b16 v[106:107], %[ta] offset:3200\n\t" \
  "global_load_dwordx4 v[156:159], %[vk], %[oa9] sc1\n\t" \
  "v_mfma_f32_16x16x32_bf16 v[48:51], v[68:71], v[128:131], v[48:51]\n\t" \
  "ds_read_b64_tr_b16 v[108:109], %[ta] offset:8\n\t" \
  "v_mfma_f32_16x16x32_bf16 v[28:31], v[68:71], v[132:135], v[28:31]\n\t" \
  "ds_read_b64_tr_b16 v[110:111], %[ta] offset:3208\n\t" \
  "v_mfma_f32_16x16x32_bf16 v[52:55], v[72:75], v[128:131], v[52:55]\n\t" \
  "ds_read_b64_tr_b16 v[112:113], %[ta] offset:16\n\t" \
  "v_mfma_f32_16x16x32_bf16 v[32:35], v[72:75], v[132:135], v[32:35]\n\t" \
  "ds_read_b64_tr_b16 v[114:115], %[ta] offset:3216\n\t" \
  "v_mfma_f32_16x16x32_bf16 v[56:59], v[76:79], v[128:131], v[56:59]\n\t" \
  "ds_read_b64_tr_b16 v[116:117], %[ta] offset:24\n\t" \
  "v_mfma_f32_16x16x32_bf16 v[36:39], v[76:79], v[132:135], v[36:39]\n\t" \
  "ds_read_b64_tr_b16 v[118:119], %[ta] offset:3224\n\t" \
  "s_waitcnt vmcnt(4)\n\t" \
  "v_max_u32 %[tk], v136, v140\n\t" \
  "v_cmp_eq_u32 vcc, -1, %[tk]\n\t" \
  "v_mfma_f32_16x16x32_bf16 v[60:63], v[80:83], v[128:131], v[60:63]\n\t" \
  "ds_read_b64_tr_b16 v[120:121], %[ta] offset:32\n\t" \
  "v_mfma_f32_16x16x32_bf16 v[40:43], v[80:83], v[132:135], v[40:43]\n\t" \
  "ds_read_b64_tr_b16 v[122:123], %[ta] offset:3232\n\t" \
  "s_cbranch_vccnz LSd7_%=\n\t" \
  "LQd7_%=:\n\t" \
  "s_waitcnt lgkmcnt(10)\n\t" \
  "v_mfma_f32_16x16x32_bf16 v[44:47], v[84:87], v[136:139], v[44:47]\n\t" \
  "v_add_u32 %[ta], %[so9], %[pf]\n\t" \
  "ds_read_b64_tr_b16 v[64:65], %[ta] offset:0\n\t" \
  "ds_read_b64_tr_b16 v[66:67], %[ta] offset:12800\n\t" \
  "v_mfma_f32_16x16x32_bf16 v[48:51], v[88:91], v[136:139], v[48:51]\n\t" \
  "ds_read_b64_tr_b16 v[68:69], %[ta] offset:8\n\t" \
  "ds_read_b64_tr_b16 v[70:71], %[ta] offset:12808\n\t" \
  "v_mfma_f32_16x16x32_bf16 v[52:55], v[92:95], v[136:139], v[52:55]\n\t" \
  "ds_read_b64_tr_b16 v[72:73], %[ta] offset:16\n\t" \
  "ds_read_b64_tr_b16 v[74:75], %[ta] offset:12816\n\t" \
  "v_mfma_f32_16x16x32_bf16 v[56:59], v[96:99], v[136:139], v[56:59]\n\t" \
  "ds_read_b64_tr_b16 v[76:77], %[ta] offset:24\n\t" \
  "ds_read_b64_tr_b16 v[78:79], %[ta] offset:12824\n\t" \
  "v_mfma_f32_16x16x32_bf16 v[60:63], v[100:103], v[136:139], v[60:63]\n\t" \
  "ds_read_b64_tr_b16 v[80:81], %[ta] offset:32\n\t" \
  "ds_read_b64_tr_b16 v[82:83], %[ta] offset:12832\n\t" \
  "s_waitcnt lgkmcnt(10)\n\t" \
  "v_mfma_f32_16x16x32_bf16 v[44:47], v[104:107], v[140:143], v[44:47]\n\t" \
  "v_mfma_f32_16x16x32_bf16 v[48:51], v[108:111], v[140:143], v[48:51]\n\t" \
  "v_mfma_f32_16x16x32_bf16 v[52:55], v[112:115], v[140:143], v[52:55]\n\t" \
  "s_waitcnt vmcnt(2)\n\t" \
  "v_max3_u32 %[tk], v144, v146, v148\n\t" \
  "v_cmp_eq_u32 vcc, -1, %[tk]\n\t" \
  "v_mfma_f32_16x16x32_bf16 v[56:59], v[116:119], v[140:143], v[56:59]\n\t" \
  "v_mfma_f32_16x16x32_bf16 v[60:63], v[120:123], v[140:143], v[60:63]\n\t" \
  "s_cbranch_vccnz LSd9_%=\n\t" \
  "LQd9_%=:\n\t" \
  "s_waitcnt lgkmcnt(0)\n\t" \
  "v_mfma_f32_16x16x32_bf16 v[44:47], v[64:67], v[144:147], v[44:47]\n\t" \
  "ds_read2_b64 v[124:127], %[aw] offset0:0 offset1:1\n\t" \
  "ds_read2_b64 v[128:131], %[aw] offset0:2 offset1:3\n\t" \
  "ds_read_b64 v[132:133], %[aw] offset:32\n\t" \
  "v_mov_b64 v[134:135], 0\n\t" \
  "v_mfma_f32_16x16x32_bf16 v[48:51], v[68:71], v[144:147], v[48:51]\n\t" \
  "v_mfma_f32_16x16x32_bf16 v[52:55], v[72:75], v[144:147], v[52:55]\n\t" \
  "v_add_u32 %[ta], %[sd], %[l16]\n\t" \
  "v_mfma_f32_16x16x32_bf16 v[56:59], v[76:79], v[144:147], v[56:59]\n\t" \
  "v_mfma_f32_16x16x32_bf16 v[60:63], v[80:83], v[144:147], v[60:63]\n\t" \
  "v_mov_b32 v137, %[sc]\n\t" \
  "v_mov_b32 v136, 1\n\t" \
  "v_mfma_f32_16x16x32_bf16 v[24:27], v[84:87], v[148:151], v[24:27]\n\t" \
  "ds_write_b128 %[ta], v[44:47] offset:0\n\t" \
  "v_mfma_f32_16x16x32_bf16 v[28:31], v[88:91], v[148:151], v[28:31]\n\t" \
  "ds_write_b128 %[ta], v[48:51] offset:1024\n\t" \
  "v_mfma_f32_16x16x32_bf16 v[32:35], v[92:95], v[148:151], v[32:35]\n\t" \
  "ds_write_b128 %[ta], v[52:55] offset:2048\n\t" \
  "s_waitcnt vmcnt(0)\n\t" \
  "v_max3_u32 %[tk], v152, v156, v158\n\t" \
  "v_cmp_eq_u32 vcc, -1, %[tk]\n\t" \
  "v_mfma_f32_16x16x32_bf16 v[36:39], v[96:99], v[148:151], v[36:39]\n\t" \
  "ds_write_b128 %[ta], v[56:59] offset:3072\n\t" \
  "v_mfma_f32_16x16x32_bf16 v[40:43], v[100:103], v[148:151], v[40:43]\n\t" \
  "ds_write_b128 %[ta], v[60:63] offset:4096\n\t" \
  "s_mov_b64 exec, 1\n\t" \
  "ds_add_u32 v137, v136\n\t" \
  "s_mov_b64 exec, -1\n\t" \
  "s_cbranch_vccnz LSk8_%=\n\t" \
  "LQk8_%=:\n\t" \
  "v_mfma_f32_16x16x32_bf16 v[24:27], v[104:107], v[152:155], v[24:27]\n\t" \
  "v_mov_b64 v[94:95], 0\n\t" \
  "v_mfma_f32_16x16x32_bf16 v[28:31], v[108:111], v[152:155], v[28:31]\n\t" \
  "v_xor_b32 v96, 4, v137\n\t" \
  "v_mfma_f32_16x16x32_bf16 v[32:35], v[112:115], v[152:155], v[32:35]\n\t" \
  "v_add_u32 %[tk], %[sr], %[l16]\n\t" \
  "v_mfma_f32_16x16x32_bf16 v[36:39], v[116:119], v[152:155], v[36:39]\n\t" \
  "v_mfma_f32_16x16x32_bf16 v[40:43], v[120:123], v[152:155], v[40:43]\n\t" \
  "v_mfma_f32_16x16x32_bf16 v[24:27], v[64:67], v[156:159], v[24:27]\n\t" \
  "ds_read_b32 v97, v96\n\t" \
  "ds_read_b128 v[44:47], %[tk] offset:0\n\t" \
  "ds_read_b128 v[48:51], %[tk] offset:1024\n\t" \
  "ds_read_b128 v[52:55], %[tk] offset:2048\n\t" \
  "ds_read_b128 v[56:59], %[tk] offset:3072\n\t" \
  "ds_read_b128 v[60:63], %[tk] offset:4096\n\t" \
  "v_mfma_f32_16x16x32_bf16 v[28:31], v[68:71], v[156:159], v[28:31]\n\t" \
  "v_mfma_f32_16x16x32_bf16 v[32:35], v[72:75], v[156:159], v[32:35]\n\t" \
  "v_mfma_f32_16x16x32_bf16 v[36:39], v[76:79], v[156:159], v[36:39]\n\t" \
  "v_mfma_f32_16x16x32_bf16 v[40:43], v[80:83], v[156:159], v[40:43]\n\t" \
  STP(4) \
  "LD1_%=:\n\t" \
  "s_waitcnt lgkmcnt(0)\n\t" \
  "v_cmp_eq_u32 vcc, 0, v97\n\t" \
  "s_cbranch_vccnz LD2_%=\n\t" \
  "LT_%=:\n\t" \
  "v_mov_b32 v137, %[sa]\n\t" \
  "ds_read_b32 v138, v137 offset:16\n\t" \
  "v_add_f32 v24, v24, v44\n\t" \
  "v_add_f32 v25, v25, v45\n\t" \
  "v_add_f32 v26, v26, v46\n\t" \
  "v_add_f32 v27, v27, v47\n\t" \
  "v_cvt_pk_bf16_f32 v84, v24, v25\n\t" \
  "v_cvt_pk_bf16_f32 v85, v26, v27\n\t" \
  "v_add_f32 v28, v28, v48\n\t" \
  "v_add_f32 v29, v29, v49\n\t" \
  "v_add_f32 v30, v30, v50\n\t" \
  "v_add_f32 v31, v31, v51\n\t" \
  "v_cvt_pk_bf16_f32 v86, v28, v29\n\t" \
  "v_cvt_pk_bf16_f32 v87, v30, v31\n\t" \
  "v_add_f32 v32, v32, v52\n\t" \
  "v_add_f32 v33, v33, v53\n\t" \
  "v_add_f32 v34, v34, v54\n\t" \
  "v_add_f32 v35, v35, v55\n\t" \
  "v_cvt_pk_bf16_f32 v88, v32, v33\n\t" \
  "v_cvt_pk_bf16_f32 v89, v34, v35\n\t" \
  "v_mfma_f32_16x16x32_bf16 v[100:103], v[84:87], v[84:87], 0\n\t" \
  "v_mfma_f32_16x16x32_bf16 v[96:99], v[124:127], v[84:87], 0\n\t" \
  "v_add_f32 v36, v36, v56\n\t" \
  "v_add_f32 v37, v37, v57\n\t" \
  "v_add_f32 v38, v38, v58\n\t" \
  "v_add_f32 v39, v39, v59\n\t" \
  "v_cvt_pk_bf16_f32 v90, v36, v37\n\t" \
  "v_cvt_pk_bf16_f32 v91, v38, v39\n\t" \
  "v_add_f32 v40, v40, v60\n\t" \
  "v_add_f32 v41, v41, v61\n\t" \
  "v_add_f32 v42, v42, v62\n\t" \
  "v_add_f32 v43, v43, v63\n\t" \
  "v_cvt_pk_bf16_f32 v92, v40, v41\n\t" \
  "v_cvt_pk_bf16_f32 v93, v42, v43\n\t" \
  "v_mfma_f32_16x16x32_bf16 v[100:103], v[88:91], v[88:91], v[100:103]\n\t" \
  "v_mfma_f32_16x16x32_bf16 v[96:99], v[128:131], v[88:91], v[96:99]\n\t" \
  "v_mfma_f32_16x16x32_bf16 v[100:103], v[92:95], v[92:95], v[100:103]\n\t" \
  "v_mfma_f32_16x16x32_bf16 v[96:99], v[132:135], v[92:95], v[96:99]\n\t" \
  "s_waitcnt lgkmcnt(0)\n\t" \
  "v_sub_u32 v138, %[ra], v138\n\t" \
  "v_cmp_gt_u32 vcc, 0xa0, v138\n\t" \
  "v_and_b32 %[ta], 0xff, %[l16]\n\t" \
  "v_add_u32 %[tk], %[sp], %[ta]\n\t" \
  "v_mov_b32 v136, 1\n\t" \
  "s_cbranch_vccz LP1_%=\n\t" \
  "s_mov_b64 exec, vcc\n\t" \
  "global_store_dwordx4 v138, v[84:87], %[cp] sc1\n\t" \
  "global_store_dwordx4 v138, v[88:91], %[cp] offset:16 sc1\n\t" \
  "global_store_dwordx2 v138, v[92:93], %[cp] offset:32 sc1\n\t" \
  "ds_write_b32 v137, v136 offset:24\n\t" \
  "LP1_%=:\n\t" \
  "s_mov_b32 exec_lo, 0x00200002\n\t" \
  "s_mov_b32 exec_hi, 0x20000200\n\t" \
  "v_mov_b32 v100, v101\n\t" \
  "s_lshl_b64 exec, exec, 1\n\t" \
  "v_mov_b32 v100, v102\n\t" \
  "s_lshl_b64 exec, exec, 1\n\t" \
  "v_mov_b32 v100, v103\n\t" \
  "s_mov_b32 exec_lo, 0x00f0000f\n\t" \
  "s_mov_b32 exec_hi, 0xf0000f00\n\t" \
  "v_mov_b32 v98, v100\n\t" \
  "global_store_dwordx4 %[ta], v[96:99], %[sst] sc1\n\t" \
  STP(5) \
  "ds_write_b128 %[tk], v[96:99]\n\t" \
  "s_mov_b64 exec, -1\n\t" \
  "ds_write_b64 %[ra], v[84:85] offset:0\n\t" \
  "ds_write_b64 %[ra], v[86:87] offset:8\n\t" \
  "ds_write_b64 %[ra], v[88:89] offset:16\n\t" \
  "ds_write_b64 %[ra], v[90:91] offset:24\n\t" \
  "ds_write_b64 %[ra], v[92:93] offset:32\n\t" \
  "s_mov_b64 exec, 1\n\t" \
  "ds_add_u32 v137, v136\n\t" \
  "s_mov_b64 exec, -1\n\t" \
  "s_setprio 0\n\t" \
  "s_branch LZ_%=\n\t" \
  "LD2_%=:\n\t" \
  "s_sub_u32 s43, s43, 1\n\t" \
  "s_cbranch_scc1 LT_%=\n\t" \
  "ds_read_b32 v97, v96\n\t" \
  "ds_read_b128 v[44:47], %[tk] offset:0\n\t" \
  "ds_read_b128 v[48:51], %[tk] offset:1024\n\t" \
  "ds_read_b128 v[52:55], %[tk] offset:2048\n\t" \
  "ds_read_b128 v[56:59], %[tk] offset:3072\n\t" \
  "ds_read_b128 v[60:63], %[tk] offset:4096\n\t" \
  "s_branch LD1_%=\n\t" \
  "LH_%=:\n\t" \
  "s_sleep 3\n\t" \
  "global_load_dwordx4 v[140:143], %[vk], %[oa1] sc1\n\t" \
  "global_load_dwordx4 v[144:147], %[vk], %[oa2] sc1\n\t" \
  "global_load_dwordx4 v[148:151], %[vk], %[oa3] sc1\n\t" \
  "global_load_dwordx4 v[152:155], %[vk], %[oa4] sc1\n\t" \
  "global_load_dwordx4 v[156:159], %[vk], %[oa0] sc1\n\t" \
  "s_cmp_eq_u32 %[w], 3\n\t" \
  "s_cbranch_scc0 LG_%=\n\t" \
  "ds_read_b64 v[112:113], %[ra] offset:0\n\t" \
  "ds_read_b64 v[114:115], %[ra] offset:8\n\t" \
  "ds_read_b64 v[116:117], %[ra] offset:16\n\t" \
  "ds_read_b64 v[118:119], %[ra] offset:24\n\t" \
  "ds_read_b64 v[120:121], %[ra] offset:32\n\t" \
  "ds_read2_b64 v[124:127], %[aw] offset0:0 offset1:1\n\t" \
  "ds_read2_b64 v[128:131], %[aw] offset0:2 offset1:3\n\t" \
  "ds_read_b64 v[132:133], %[aw] offset:32\n\t" \
  "v_mov_b64 v[134:135], 0\n\t" \
  "s_waitcnt lgkmcnt(3)\n\t" \
  "v_lshlrev_b32 v24, 16, v112\n\t" \
  "v_and_b32 v25, 0xffff0000, v112\n\t" \
  "v_lshlrev_b32 v26, 16, v113\n\t" \
  "v_and_b32 v27, 0xffff0000, v113\n\t" \
  "v_lshlrev_b32 v28, 16, v114\n\t" \
  "v_and_b32 v29, 0xffff0000, v114\n\t" \
  "v_lshlrev_b32 v30, 16, v115\n\t" \
  "v_and_b32 v31, 0xffff0000, v115\n\t" \
  "v_lshlrev_b32 v32, 16, v116\n\t" \
  "v_and_b32 v33, 0xffff0000, v116\n\t" \
  "v_lshlrev_b32 v34, 16, v117\n\t" \
  "v_and_b32 v35, 0xffff0000, v117\n\t" \
  "v_lshlrev_b32 v36, 16, v118\n\t" \
  "v_and_b32 v37, 0xffff0000, v118\n\t" \
  "v_lshlrev_b32 v38, 16, v119\n\t" \
  "v_and_b32 v39, 0xffff0000, v119\n\t" \
  "v_lshlrev_b32 v40, 16, v120\n\t" \
  "v_and_b32 v41, 0xffff0000, v120\n\t" \
  "v_lshlrev_b32 v42, 16, v121\n\t" \
  "v_and_b32 v43, 0xffff0000, v121\n\t" \
  "s_branch LG2_%=\n\t" \
  "LG_%=:\n\t" \
  "v_mov_b64 v[24:25], 0\n\t" \
  "v_mov_b64 v[26:27], 0\n\t" \
  "v_mov_b64 v[28:29], 0\n\t" \
  "v_mov_b64 v[30:31], 0\n\t" \
  "v_mov_b64 v[32:33], 0\n\t" \
  "v_mov_b64 v[34:35], 0\n\t" \
  "v_mov_b64 v[36:37], 0\n\t" \
  "v_mov_b64 v[38:39], 0\n\t" \
  "v_mov_b64 v[40:41], 0\n\t" \
  "v_mov_b64 v[42:43], 0\n\t" \
  "LG2_%=:\n\t" \
  "v_add_u32 %[ta], %[so1], %[pm]\n\t" \
  "ds_read_b64_tr_b16 v[64:65], %[ta] offset:0\n\t" \
  "ds_read_b64_tr_b16 v[66:67], %[ta] offset:3200\n\t" \
  "ds_read_b64_tr_b16 v[68:69], %[ta] offset:8\n\t" \
  "ds_read_b64_tr_b16 v[70:71], %[ta] offset:3208\n\t" \
  "ds_read_b64_tr_b16 v[72:73], %[ta] offset:16\n\t" \
  "ds_read_b64_tr_b16 v[74:75], %[ta] offset:3216\n\t" \
  "ds_read_b64_tr_b16 v[76:77], %[ta] offset:24\n\t" \
  "ds_read_b64_tr_b16 v[78:79], %[ta] offset:3224\n\t" \
  "ds_read_b64_tr_b16 v[80:81], %[ta] offset:32\n\t" \
  "ds_read_b64_tr_b16 v[82:83], %[ta] offset:3232\n\t" \
  "v_add_u32 %[ta], %[so2], %[pm]\n\t" \
  "ds_read_b64_tr_b16 v[84:85], %[ta] offset:0\n\t" \
  "ds_read_b64_tr_b16 v[86:87], %[ta] offset:3200\n\t" \
  "ds_read_b64_tr_b16 v[88:89], %[ta] offset:8\n\t" \
  "ds_read_b64_tr_b16 v[90:91], %[ta] offset:3208\n\t" \
  "ds_read_b64_tr_b16 v[92:93], %[ta] offset:16\n\t" \
  "ds_read_b64_tr_b16 v[94:95], %[ta] offset:3216\n\t" \
  "ds_read_b64_tr_b16 v[96:97], %[ta] offset:24\n\t" \
  "ds_read_b64_tr_b16 v[98:99], %[ta] offset:3224\n\t" \
  "ds_read_b64_tr_b16 v[100:101], %[ta] offset:32\n\t" \
  "ds_read_b64_tr_b16 v[102:103], %[ta] offset:3232\n\t" \
  STP(1) \
  "s_waitcnt vmcnt(4)\n\t" \
  "v_cmp_eq_u32 vcc, -1, v140\n\t" \
  "s_cbranch_vccnz LSh1_%=\n\t" \
  "LQh1_%=:\n\t" \
  STP(2) \
  "s_waitcnt lgkmcnt(10)\n\t" \
  "v_mfma_f32_16x16x32_bf16 v[24:27], v[64:67], v[140:143], v[24:27]\n\t" \
  "v_add_u32 %[ta], %[so3], %[pm]\n\t" \
  "ds_read_b64_tr_b16 v[104:105], %[ta] offset:0\n\t" \
  "ds_read_b64_tr_b16 v[106:107], %[ta] offset:3200\n\t" \
  "v_mfma_f32_16x16x32_bf16 v[28:31], v[68:71], v[140:143], v[28:31]\n\t" \
  "ds_read_b64_tr_b16 v[108:109], %[ta] offset:8\n\t" \
  "ds_read_b64_tr_b16 v[110:111], %[ta] offset:3208\n\t" \
  "v_mfma_f32_16x16x32_bf16 v[32:35], v[72:75], v[140:143], v[32:35]\n\t" \
  "ds_read_b64_tr_b16 v[112:113], %[ta] offset:16\n\t" \
  "ds_read_b64_tr_b16 v[114:115], %[ta] offset:3216\n\t" \
  "v_mfma_f32_16x16x32_bf16 v[36:39], v[76:79], v[140:143], v[36:39]\n\t" \
  "ds_read_b64_tr_b16 v[116:117], %[ta] offset:24\n\t" \
  "ds_read_b64_tr_b16 v[118:119], %[ta] offset:3224\n\t" \
  "s_waitcnt vmcnt(3)\n\t" \
  "v_cmp_eq_u32 vcc, -1, v144\n\t" \
  "v_mfma_f32_16x16x32_bf16 v[40:43], v[80:83], v[140:143], v[40:43]\n\t" \
  "ds_read_b64_tr_b16 v[120:121], %[ta] offset:32\n\t" \
  "ds_read_b64_tr_b16 v[122:123], %[ta] offset:3232\n\t" \
  "s_cbranch_vccnz LSh2_%=\n\t" \
  "LQh2_%=:\n\t" \
  "s_waitcnt lgkmcnt(10)\n\t" \
  "v_mfma_f32_16x16x32_bf16 v[24:27], v[84:87], v[144:147], v[24:27]\n\t" \
  "v_add_u32 %[ta], %[so4], %[pf]\n\t" \
  "ds_read_b64_tr_b16 v[64:65], %[ta] offset:0\n\t" \
  "ds_read_b64_tr_b16 v[66:67], %[ta] offset:12800\n\t" \
  "v_mfma_f32_16x16x32_bf16 v[28:31], v[88:91], v[144:147], v[28:31]\n\t" \
  "ds_read_b64_tr_b16 v[68:69], %[ta] offset:8\n\t" \
  "ds_read_b64_tr_b16 v[70:71], %[ta] offset:12808\n\t" \
  "v_mfma_f32_16x16x32_bf16 v[32:35], v[92:95], v[144:147], v[32:35]\n\t" \
  "ds_read_b64_tr_b16 v[72:73], %[ta] offset:16\n\t" \
  "ds_read_b64_tr_b16 v[74:75], %[ta] offset:12816\n\t" \
  "v_mfma_f32_16x16x32_bf16 v[36:39], v[96:99], v[144:147], v[36:39]\n\t" \
  "ds_read_b64_tr_b16 v[76:77], %[ta] offset:24\n\t" \
  "ds_read_b64_tr_b16 v[78:79], %[ta] offset:12824\n\t" \
  "s_waitcnt vmcnt(2)\n\t" \
  "v_cmp_eq_u32 vcc, -1, v148\n\t" \
  "v_mfma_f32_16x16x32_bf16 v[40:43], v[100:103], v[144:147], v[40:43]\n\t" \
  "ds_read_b64_tr_b16 v[80:81], %[ta] offset:32\n\t" \
  "ds_read_b64_tr_b16 v[82:83], %[ta] offset:12832\n\t" \
  "s_cbranch_vccnz LSh3_%=\n\t" \
  "LQh3_%=:\n\t" \
  "s_waitcnt lgkmcnt(10)\n\t" \
  "v_mfma_f32_16x16x32_bf16 v[24:27], v[104:107], v[148:151], v[24:27]\n\t" \
  "v_add_u32 %[ta], %[so0], %[pm]\n\t" \
  "ds_read_b64_tr_b16 v[84:85], %[ta] offset:0\n\t" \
  "ds_read_b64_tr_b16 v[86:87], %[ta] offset:3200\n\t" \
  "v_mfma_f32_16x16x32_bf16 v[28:31], v[108:111], v[148:151], v[28:31]\n\t" \
  "ds_read_b64_tr_b16 v[88:89], %[ta] offset:8\n\t" \
  "ds_read_b64_tr_b16 v[90:91], %[ta] offset:3208\n\t" \
  "v_mfma_f32_16x16x32_bf16 v[32:35], v[112:115], v[148:151], v[32:35]\n\t" \
  "ds_read_b64_tr_b16 v[92:93], %[ta] offset:16\n\t" \
  "ds_read_b64_tr_b16 v[94:95], %[ta] offset:3216\n\t" \
  "v_mfma_f32_16x16x32_bf16 v[36:39], v[116:119], v[148:151], v[36:39]\n\t" \
  "ds_read_b64_tr_b16 v[96:97], %[ta] offset:24\n\t" \
  "ds_read_b64_tr_b16 v[98:99], %[ta] offset:3224\n\t" \
  "s_waitcnt vmcnt(1)\n\t" \
  "v_max_u32 %[tk], v152, v154\n\t" \
  "v_cmp_eq_u32 vcc, -1, %[tk]\n\t" \
  "v_mfma_f32_16x16x32_bf16 v[40:43], v[120:123], v[148:151], v[40:43]\n\t" \
  "ds_read_b64_tr_b16 v[100:101], %[ta] offset:32\n\t" \
  "ds_read_b64_tr_b16 v[102:103], %[ta] offset:3232\n\t" \
  "s_cbranch_vccnz LSh4_%=\n\t" \
  "LQh4_%=:\n\t" \
  "s_waitcnt lgkmcnt(10)\n\t" \
  "v_mfma_f32_16x16x32_bf16 v[24:27], v[64:67], v[152:155], v[24:27]\n\t" \
  "v_mfma_f32_16x16x32_bf16 v[28:31], v[68:71], v[152:155], v[28:31]\n\t" \
  "v_mfma_f32_16x16x32_bf16 v[32:35], v[72:75], v[152:155], v[32:35]\n\t" \
  "v_mfma_f32_16x16x32_bf16 v[36:39], v[76:79], v[152:155], v[36:39]\n\t" \
  "s_waitcnt vmcnt(0)\n\t" \
  "v_cmp_eq_u32 vcc, -1, v156\n\t" \
  "v_mfma_f32_16x16x32_bf16 v[40:43], v[80:83], v[152:155], v[40:43]\n\t" \
  "s_cbranch_vccnz LSh0_%=\n\t" \
  "LQh0_%=:\n\t" \
  STP(3) \
  "s_waitcnt lgkmcnt(0)\n\t" \
  "v_mfma_f32_16x16x32_bf16 v[24:27], v[84:87], v[156:159], v[24:27]\n\t" \
  "v_mfma_f32_16x16x32_bf16 v[28:31], v[88:91], v[156:159], v[28:31]\n\t" \
  "v_mfma_f32_16x16x32_bf16 v[32:35], v[92:95], v[156:159], v[32:35]\n\t" \
  "v_mfma_f32_16x16x32_bf16 v[36:39], v[96:99], v[156:159], v[36:39]\n\t" \
  "v_mfma_f32_16x16x32_bf16 v[40:43], v[100:103], v[156:159], v[40:43]\n\t" \
  STP(4) \
  "s_cmp_eq_u32 %[w], 3\n\t" \
  "s_cbranch_scc1 LF_%=\n\t" \
  "s_setprio 3\n\t" \
  "v_add_u32 %[ta], %[sd], %[l16]\n\t" \
  "v_mov_b32 v137, %[sc]\n\t" \
  "v_mov_b32 v136, 1\n\t" \
  "ds_write_b128 %[ta], v[24:27] offset:0\n\t" \
  "ds_write_b128 %[ta], v[28:31] offset:1024\n\t" \
  "ds_write_b128 %[ta], v[32:35] offset:2048\n\t" \
  "ds_write_b128 %[ta], v[36:39] offset:3072\n\t" \
  "ds_write_b128 %[ta], v[40:43] offset:4096\n\t" \
  "s_mov_b64 exec, 1\n\t" \
  "ds_add_u32 v137, v136\n\t" \
  "s_mov_b64 exec, -1\n\t" \
  STP(5) \
  "s_branch LZ_%=\n\t" \
  "LF_%=:\n\t" \
  "s_setprio 3\n\t" \
  "v_mov_b64 v[94:95], 0\n\t" \
  "v_mov_b32 v96, %[sc]\n\t" \
  "v_add_u32 %[ta], %[sr], %[l16]\n\t" \
  "LF1_%=:\n\t" \
  "ds_read_b32 v97, v96\n\t" \
  "s_waitcnt lgkmcnt(0)\n\t" \
  "v_cmp_gt_u32 vcc, 3, v97\n\t" \
  "s_cbranch_vccz LF2_%=\n\t" \
  "s_sleep 1\n\t" \
  "s_sub_u32 s43, s43, 1\n\t" \
  "s_cbranch_scc0 LF1_%=\n\t" \
  "LF2_%=:\n\t" \
  "ds_read_b128 v[64:67], %[ta] offset:0\n\t" \
  "ds_read_b128 v[68:71], %[ta] offset:1024\n\t" \
  "ds_read_b128 v[72:75], %[ta] offset:2048\n\t" \
  "ds_read_b128 v[76:79], %[ta] offset:3072\n\t" \
  "ds_read_b128 v[80:83], %[ta] offset:4096\n\t" \
  "ds_read_b128 v[104:107], %[ta] offset:5120\n\t" \
  "ds_read_b128 v[108:111], %[ta] offset:6144\n\t" \
  "ds_read_b128 v[112:115], %[ta] offset:7168\n\t" \
  "ds_read_b128 v[116:119], %[ta] offset:8192\n\t" \
  "ds_read_b128 v[120:123], %[ta] offset:9216\n\t" \
  "ds_read_b128 v[44:47], %[ta] offset:10240\n\t" \
  "ds_read_b128 v[48:51], %[ta] offset:11264\n\t" \
  "ds_read_b128 v[52:55], %[ta] offset:12288\n\t" \
  "ds_read_b128 v[56:59], %[ta] offset:13312\n\t" \
  "ds_read_b128 v[60:63], %[ta] offset:14336\n\t" \
  "s_waitcnt lgkmcnt(10)\n\t" \
  "v_pk_add_f32 v[24:25], v[24:25], v[64:65]\n\t" \
  "v_pk_add_f32 v[26:27], v[26:27], v[66:67]\n\t" \
  "v_pk_add_f32 v[28:29], v[28:29], v[68:69]\n\t" \
  "v_pk_add_f32 v[30:31], v[30:31], v[70:71]\n\t" \
  "v_pk_add_f32 v[32:33], v[32:33], v[72:73]\n\t" \
  "v_pk_add_f32 v[34:35], v[34:35], v[74:75]\n\t" \
  "v_pk_add_f32 v[36:37], v[36:37], v[76:77]\n\t" \
  "v_pk_add_f32 v[38:39], v[38:39], v[78:79]\n\t" \
  "v_pk_add_f32 v[40:41], v[40:41], v[80:81]\n\t" \
  "v_pk_add_f32 v[42:43], v[42:43], v[82:83]\n\t" \
  "s_waitcnt lgkmcnt(5)\n\t" \
  "v_pk_add_f32 v[24:25], v[24:25], v[104:105]\n\t" \
  "v_pk_add_f32 v[26:27], v[26:27], v[106:107]\n\t" \
  "v_pk_add_f32 v[28:29], v[28:29], v[108:109]\n\t" \
  "v_pk_add_f32 v[30:31], v[30:31], v[110:111]\n\t" \
  "v_pk_add_f32 v[32:33], v[32:33], v[112:113]\n\t" \
  "v_pk_add_f32 v[34:35], v[34:35], v[114:115]\n\t" \
  "v_pk_add_f32 v[36:37], v[36:37], v[116:117]\n\t" \
  "v_pk_add_f32 v[38:39], v[38:39], v[118:119]\n\t" \
  "v_pk_add_f32 v[40:41], v[40:41], v[120:121]\n\t" \
  "v_pk_add_f32 v[42:43], v[42:43], v[122:123]\n\t" \
  "s_waitcnt lgkmcnt(0)\n\t" \
  "s_branch LT_%=\n\t" \
  "LSs1_%=:\n\t" \
  CNT1 \
  "s_sub_u32 s43, s43, 1\n\t" \
  "s_cbranch_scc1 LQs1_%=\n\t" \
  "s_sleep %[oback]\n\t" \
  "global_load_dwordx4 v[124:127], %[vd], %[oa1] sc1\n\t" \
  "global_load_dwordx4 v[128:131], %[vk], %[oa1] sc1\n\t" \
  "s_waitcnt vmcnt(0)\n\t" \
  "v_max_u32 %[tk], v124, v128\n\t" \
  "v_cmp_eq_u32 vcc, -1, %[tk]\n\t" \
  "s_cbranch_vccnz LSs1_%=\n\t" \
  "s_branch LQs1_%=\n\t" \
  "LSs2_%=:\n\t" \
  CNT1 \
  "s_sub_u32 s43, s43, 1\n\t" \
  "s_cbranch_scc1 LQs2_%=\n\t" \
  "s_sleep %[oback]\n\t" \
  "global_load_dwordx4 v[132:135], %[vd], %[oa2] sc1\n\t" \
  "global_load_dwordx4 v[136:139], %[vk], %[oa2] sc1\n\t" \
  "s_waitcnt vmcnt(0)\n\t" \
  "v_max_u32 %[tk], v132, v136\n\t" \
  "v_cmp_eq_u32 vcc, -1, %[tk]\n\t" \
  "s_cbranch_vccnz LSs2_%=\n\t" \
  "s_branch LQs2_%=\n\t" \
  "LSs3_%=:\n\t" \
  CNT1 \
  "s_sub_u32 s43, s43, 1\n\t" \
  "s_cbranch_scc1 LQs3_%=\n\t" \
  "s_sleep %[oback]\n\t" \
  "global_load_dwordx4 v[140:143], %[vd], %[oa3] sc1\n\t" \
  "global_load_dwordx4 v[144:147], %[vk], %[oa3] sc1\n\t" \
  "s_waitcnt vmcnt(0)\n\t" \
  "v_max_u32 %[tk], v140, v144\n\t" \
  "v_cmp_eq_u32 vcc, -1, %[tk]\n\t" \
  "s_cbranch_vccnz LSs3_%=\n\t" \
  "s_branch LQs3_%=\n\t" \
  "LSs4_%=:\n\t" \
  CNT1 \
  "s_sub_u32 s43, s43, 1\n\t" \
  "s_cbranch_scc1 LQs4_%=\n\t" \
  "s_sleep %[oback]\n\t" \
  "global_load_dwordx4 v[148:151], %[vd], %[oa4] sc1\n\t" \
  "global_load_dwordx4 v[152:155], %[vk], %[oa4] sc1\n\t" \
  "s_waitcnt vmcnt(0)\n\t" \
  "v_max3_u32 %[tk], v148, v150, v152\n\t" \
  "v_max_u32 %[tk], %[tk], v154\n\t" \
  "v_cmp_eq_u32 vcc, -1, %[tk]\n\t" \
  "s_cbranch_vccnz LSs4_%=\n\t" \
  "s_branch LQs4_%=\n\t" \
  "LSs5_%=:\n\t" \
  CNT1 \
  "s_sub_u32 s43, s43, 1\n\t" \
  "s_cbranch_scc1 LQs5_%=\n\t" \
  "s_sleep %[oback]\n\t" \
  "global_load_dwordx4 v[156:159], %[vd], %[oa5] sc1\n\t" \
  "global_load_dwordx4 v[124:127], %[vk], %[oa5] sc1\n\t" \
  "s_waitcnt vmcnt(0)\n\t" \
  "v_max_u32 %[tk], v156, v124\n\t" \
  "v_cmp_eq_u32 vcc, -1, %[tk]\n\t" \
  "s_cbranch_vccnz LSs5_%=\n\t" \
  "s_branch LQs5_%=\n\t" \
  "LSs6_%=:\n\t" \
  CNT1 \
  "s_sub_u32 s43, s43, 1\n\t" \
  "s_cbranch_scc1 LQs6_%=\n\t" \
  "s_sleep %[oback]\n\t" \
  "global_load_dwordx4 v[128:131], %[vd], %[oa6] sc1\n\t" \
  "global_load_dwordx4 v[132:135], %[vk], %[oa6] sc1\n\t" \
  "s_waitcnt vmcnt(0)\n\t" \
  "v_max_u32 %[tk], v128, v132\n\t" \
  "v_cmp_eq_u32 vcc, -1, %[tk]\n\t" \
  "s_cbranch_vccnz LSs6_%=\n\t" \
  "s_branch LQs6_%=\n\t" \
  "LSd7_%=:\n\t" \
  CNT1 \
  "s_sub_u32 s43, s43, 1\n\t" \
  "s_cbranch_scc1 LQd7_%=\n\t" \
  "s_sleep %[oback]\n\t" \
  "global_load_dwordx4 v[136:139], %[vd], %[oa7] sc1\n\t" \
  "global_load_dwordx4 v[140:143], %[vd], %[oa8] sc1\n\t" \
  "s_waitcnt vmcnt(0)\n\t" \
  "v_max_u32 %[tk], v136, v140\n\t" \
  "v_cmp_eq_u32 vcc, -1, %[tk]\n\t" \
  "s_cbranch_vccnz LSd7_%=\n\t" \
  "s_branch LQd7_%=\n\t" \
  "LSd9_%=:\n\t" \
  CNT1 \
  "s_sub_u32 s43, s43, 1\n\t" \
  "s_cbranch_scc1 LQd9_%=\n\t" \
  "s_sleep %[oback]\n\t" \
  "global_load_dwordx4 v[144:147], %[vd], %[oa9] sc1\n\t" \
  "global_load_dwordx4 v[148:151], %[vk], %[oa7] sc1\n\t" \
  "s_waitcnt vmcnt(0)\n\t" \
  "v_max3_u32 %[tk], v144, v146, v148\n\t" \
  "v_cmp_eq_u32 vcc, -1, %[tk]\n\t" \
  "s_cbranch_vccnz LSd9_%=\n\t" \
  "s_branch LQd9_%=\n\t" \
  "LSk8_%=:\n\t" \
  CNT1 \
  "s_sub_u32 s43, s43, 1\n\t" \
  "s_cbranch_scc1 LQk8_%=\n\t" \
  "s_sleep %[oback]\n\t" \
  "global_load_dwordx4 v[152:155], %[vk], %[oa8] sc1\n\t" \
  "global_load_dwordx4 v[156:159], %[vk], %[oa9] sc1\n\t" \
  "s_waitcnt vmcnt(0)\n\t" \
  "v_max3_u32 %[tk], v152, v156, v158\n\t" \
  "v_cmp_eq_u32 vcc, -1, %[tk]\n\t" \
  "s_cbranch_vccnz LSk8_%=\n\t" \
  "s_branch LQk8_%=\n\t" \
  "LSh1_%=:\n\t" \
  CNT1 \
  "s_sub_u32 s43, s43, 1\n\t" \
  "s_cbranch_scc1 LQh1_%=\n\t" \
  "s_sleep %[oback]\n\t" \
  "global_load_dwordx4 v[140:143], %[vk], %[oa1] sc1\n\t" \
  "s_waitcnt vmcnt(0)\n\t" \
  "v_cmp_eq_u32 vcc, -1, v140\n\t" \
  "s_cbranch_vccnz LSh1_%=\n\t" \
  "s_branch LQh1_%=\n\t" \
  "LSh2_%=:\n\t" \
  CNT1 \
  "s_sub_u32 s43, s43, 1\n\t" \
  "s_cbranch_scc1 LQh2_%=\n\t" \
  "s_sleep %[oback]\n\t" \
  "global_load_dwordx4 v[144:147], %[vk], %[oa2] sc1\n\t" \
  "s_waitcnt vmcnt(0)\n\t" \
  "v_cmp_eq_u32 vcc, -1, v144\n\t" \
  "s_cbranch_vccnz LSh2_%=\n\t" \
  "s_branch LQh2_%=\n\t" \
  "LSh3_%=:\n\t" \
  CNT1 \
  "s_sub_u32 s43, s43, 1\n\t" \
  "s_cbranch_scc1 LQh3_%=\n\t" \
  "s_sleep %[oback]\n\t" \
  "global_load_dwordx4 v[148:151], %[vk], %[oa3] sc1\n\t" \
  "s_waitcnt vmcnt(0)\n\t" \
  "v_cmp_eq_u32 vcc, -1, v148\n\t" \
  "s_cbranch_vccnz LSh3_%=\n\t" \
  "s_branch LQh3_%=\n\t" \
  "LSh4_%=:\n\t" \
  CNT1 \
  "s_sub_u32 s43, s43, 1\n\t" \
  "s_cbranch_scc1 LQh4_%=\n\t" \
  "s_sleep %[oback]\n\t" \
  "global_load_dwordx4 v[152:155], %[vk], %[oa4] sc1\n\t" \
  "s_waitcnt vmcnt(0)\n\t" \
  "v_max_u32 %[tk], v152, v154\n\t" \
  "v_cmp_eq_u32 vcc, -1, %[tk]\n\t" \
  "s_cbranch_vccnz LSh4_%=\n\t" \
  "s_branch LQh4_%=\n\t" \
  "LSh0_%=:\n\t" \
  CNT1 \
  "s_sub_u32 s43, s43, 1\n\t" \
  "s_cbranch_scc1 LQh0_%=\n\t" \
  "s_sleep %[oback]\n\t" \
  "global_load_dwordx4 v[156:159], %[vk], %[oa0] sc1\n\t" \
  "s_waitcnt vmcnt(0)\n\t" \
  "v_cmp_eq_u32 vcc, -1, v156\n\t" \
  "s_cbranch_vccnz LSh0_%=\n\t" \
  "s_branch LQh0_%=\n\t" \
  "LZ_%=:\n\t" \
  ""
#define COL_ASM \
  "s_mov_b32 s43, %[cap]\n\t" \
  "v_xor_b32 v101, 0x400, %[lq]\n\t" \
  "v_xor_b32 v102, 0x800, %[lq]\n\t" \
  "v_xor_b32 v103, 0xc00, %[lq]\n\t" \
  "v_xor_b32 v104, 0x1000, %[lq]\n\t" \
  "v_xor_b32 v105, 0x1400, %[lq]\n\t" \
  "v_xor_b32 v106, 0x1800, %[lq]\n\t" \
  "v_xor_b32 v107, 0x1c00, %[lq]\n\t" \
  "LC0_%=:\n\t" \
  "ds_read_b32 v96, %[ac]\n\t" \
  "s_waitcnt lgkmcnt(0)\n\t" \
  "v_cmp_gt_u32 vcc, %[nf], v96\n\t" \
  "s_cbranch_vccz LC1_%=\n\t" \
  "s_sleep 1\n\t" \
  "s_sub_u32 s43, s43, 1\n\t" \
  "s_cbranch_scc0 LC0_%=\n\t" \
  "LC1_%=:\n\t" \
  "s_setprio 2\n\t" \
  "s_sleep %[pdel]\n\t" \
  "global_load_dwordx4 v[68:71], v101, %[sp] sc1\n\t" \
  "global_load_dwordx4 v[72:75], v102, %[sp] sc1\n\t" \
  "global_load_dwordx4 v[76:79], v103, %[sp] sc1\n\t" \
  "global_load_dwordx4 v[80:83], v104, %[sp] sc1\n\t" \
  "global_load_dwordx4 v[84:87], v105, %[sp] sc1\n\t" \
  "global_load_dwordx4 v[88:91], v106, %[sp] sc1\n\t" \
  "global_load_dwordx4 v[92:95], v107, %[sp] sc1\n\t" \
  "ds_read_b128 v[64:67], %[ao]\n\t" \
  "s_waitcnt lgkmcnt(0)\n\t" \
  "LC2_%=:\n\t" \
  "s_waitcnt vmcnt(4)\n\t" \
  "v_pk_add_f32 v[108:109], v[64:65], v[68:69]\n\t" \
  "v_add_f32 v110, v66, v70\n\t" \
  "v_pk_add_f32 v[112:113], v[72:73], v[76:77]\n\t" \
  "v_add_f32 v114, v74, v78\n\t" \
  "v_pk_add_f32 v[108:109], v[108:109], v[112:113]\n\t" \
  "v_add_f32 v110, v110, v114\n\t" \
  "s_waitcnt vmcnt(0)\n\t" \
  "v_pk_add_f32 v[116:117], v[80:81], v[84:85]\n\t" \
  "v_add_f32 v118, v82, v86\n\t" \
  "v_pk_add_f32 v[120:121], v[88:89], v[92:93]\n\t" \
  "v_add_f32 v122, v90, v94\n\t" \
  "v_pk_add_f32 v[116:117], v[116:117], v[120:121]\n\t" \
  "v_add_f32 v118, v118, v122\n\t" \
  "v_pk_add_f32 v[108:109], v[108:109], v[116:117]\n\t" \
  "v_add_f32 v110, v110, v118\n\t" \
  STP(0) \
  "v_mul_f32 v112, 0x3acccccd, v108\n\t" \
  "v_add_f32 v111, v108, v109\n\t" \
  "v_mul_f32 v113, v112, v112\n\t" \
  "v_cmp_u_f32 vcc, v111, v110\n\t" \
  "v_fma_f32 v113, v110, %[rdm], -v113\n\t" \
  "s_cbranch_vccnz LC5_%=\n\t" \
  "LC3_%=:\n\t" \
  "v_max_f32 v113, 0, v113\n\t" \
  "v_cvt_pk_bf16_f32 v113, 0, v113\n\t" \
  "v_add_f32 v113, 0x3727c5ac, v113\n\t" \
  "v_cvt_pk_bf16_f32 v113, 0, v113\n\t" \
  "v_sqrt_f32 v113, v113\n\t" \
  "v_cvt_pk_bf16_f32 v112, 0, v112\n\t" \
  "v_cvt_pk_bf16_f32 v113, 0, v113\n\t" \
  "v_rcp_f32 v113, v113\n\t" \
  "v_fma_f32 v114, -%[swr], v112, v109\n\t" \
  "v_mul_f32 v114, v114, v113\n\t" \
  "v_ashrrev_i32 v115, 31, v114\n\t" \
  "v_or_b32 v115, 0x80000000, v115\n\t" \
  "v_xor_b32 v114, v114, v115\n\t" \
  "v_and_or_b32 v114, v114, %[msk], %[rid]\n\t" \
  "s_nop 1\n\t" \
  "v_max_u32_dpp v114, v114, v114 quad_perm:[1,0,3,2] row_mask:0xf bank_mask:0xf bound_ctrl:1\n\t" \
  "ds_write_b64 %[at], v[112:113]\n\t" \
  "s_nop 0\n\t" \
  "v_max_u32_dpp v114, v114, v114 quad_perm:[2,3,0,1] row_mask:0xf bank_mask:0xf bound_ctrl:1\n\t" \
  "s_nop 1\n\t" \
  "v_max_u32_dpp v114, v114, v114 row_half_mirror row_mask:0xf bank_mask:0xf bound_ctrl:1\n\t" \
  "s_nop 1\n\t" \
  "v_max_u32_dpp v114, v114, v114 row_mirror row_mask:0xf bank_mask:0xf bound_ctrl:1\n\t" \
  "s_nop 0\n\t" \
  "v_readlane_b32 s40, v114, 0\n\t" \
  "v_readlane_b32 s41, v114, 16\n\t" \
  "v_readlane_b32 s42, v114, 32\n\t" \
  "s_max_u32 s40, s40, s41\n\t" \
  "s_max_u32 s40, s40, s42\n\t" \
  "v_mov_b32 v98, s40\n\t" \
  "s_mov_b32 exec_lo, 0x01010101\n\t" \
  "s_mov_b32 exec_hi, 0x0101\n\t" \
  "global_store_dwordx2 %[zr], v[98:99], %[kp] sc1\n\t" \
  "ds_write_b32 %[ak], v98\n\t" \
  "s_mov_b64 exec, -1\n\t" \
  "s_and_b32 s41, s40, 0x7ff\n\t" \
  "s_sub_u32 s41, %[rb], s41\n\t" \
  "s_mul_i32 s42, s41, 160\n\t" \
  "v_add_u32 v115, s42, %[ah]\n\t" \
  "ds_read_b64 v[116:117], v115\n\t" \
  "v_readlane_b32 s44, v112, s41\n\t" \
  "v_readlane_b32 s46, v113, s41\n\t" \
  "s_waitcnt lgkmcnt(0)\n\t" \
  "s_mov_b32 exec_hi, 0\n\t" \
  "v_lshlrev_b32 v118, 16, v116\n\t" \
  "v_and_b32 v119, 0xffff0000, v116\n\t" \
  "v_lshlrev_b32 v120, 16, v117\n\t" \
  "v_and_b32 v121, 0xffff0000, v117\n\t" \
  "v_pk_add_f32 v[122:123], v[118:119], s[44:45] op_sel_hi:[1,0] neg_lo:[0,1] neg_hi:[0,1]\n\t" \
  "v_pk_add_f32 v[114:115], v[120:121], s[44:45] op_sel_hi:[1,0] neg_lo:[0,1] neg_hi:[0,1]\n\t" \
  "v_pk_fma_f32 v[118:119], v[122:123], s[46:47], v[118:119] op_sel_hi:[1,0,1]\n\t" \
  "v_pk_fma_f32 v[120:121], v[114:115], s[46:47], v[120:121] op_sel_hi:[1,0,1]\n\t" \
  "v_cvt_pk_bf16_f32 v116, v118, v119\n\t" \
  "v_cvt_pk_bf16_f32 v117, v120, v121\n\t" \
  "s_mov_b32 exec_hi, -1\n\t" \
  "global_store_dwordx2 %[co], v[116:117], %[cp] sc1\n\t" \
  "s_branch LC9_%=\n\t" \
  "LC5_%=:\n\t" \
  "s_sub_u32 s43, s43, 1\n\t" \
  "s_cbranch_scc1 LC3_%=\n\t" \
  "s_mov_b32 s40, 0\n\t" \
  "v_max3_u32 v96, v68, v69, v70\n\t" \
  "v_cmp_eq_u32 vcc, -1, v96\n\t" \
  "s_cbranch_vccz LC61_%=\n\t" \
  "global_load_dwordx4 v[68:71], v101, %[sp] sc1\n\t" \
  "s_mov_b32 s40, 1\n\t" \
  "LC61_%=:\n\t" \
  "v_max3_u32 v96, v72, v73, v74\n\t" \
  "v_cmp_eq_u32 vcc, -1, v96\n\t" \
  "s_cbranch_vccz LC62_%=\n\t" \
  "global_load_dwordx4 v[72:75], v102, %[sp] sc1\n\t" \
  "s_mov_b32 s40, 1\n\t" \
  "LC62_%=:\n\t" \
  "v_max3_u32 v96, v76, v77, v78\n\t" \
  "v_cmp_eq_u32 vcc, -1, v96\n\t" \
  "s_cbranch_vccz LC63_%=\n\t" \
  "global_load_dwordx4 v[76:79], v103, %[sp] sc1\n\t" \
  "s_mov_b32 s40, 1\n\t" \
  "LC63_%=:\n\t" \
  "v_max3_u32 v96, v80, v81, v82\n\t" \
  "v_cmp_eq_u32 vcc, -1, v96\n\t" \
  "s_cbranch_vccz LC64_%=\n\t" \
  "global_load_dwordx4 v[80:83], v104, %[sp] sc1\n\t" \
  "s_mov_b32 s40, 1\n\t" \
  "LC64_%=:\n\t" \
  "v_max3_u32 v96, v84, v85, v86\n\t" \
  "v_cmp_eq_u32 vcc, -1, v96\n\t" \
  "s_cbranch_vccz LC65_%=\n\t" \
  "global_load_dwordx4 v[84:87], v105, %[sp] sc1\n\t" \
  "s_mov_b32 s40, 1\n\t" \
  "LC65_%=:\n\t" \
  "v_max3_u32 v96, v88, v89, v90\n\t" \
  "v_cmp_eq_u32 vcc, -1, v96\n\t" \
  "s_cbranch_vccz LC66_%=\n\t" \
  "global_load_dwordx4 v[88:91], v106, %[sp] sc1\n\t" \
  "s_mov_b32 s40, 1\n\t" \
  "LC66_%=:\n\t" \
  "v_max3_u32 v96, v92, v93, v94\n\t" \
  "v_cmp_eq_u32 vcc, -1, v96\n\t" \
  "s_cbranch_vccz LC67_%=\n\t" \
  "global_load_dwordx4 v[92:95], v107, %[sp] sc1\n\t" \
  "s_mov_b32 s40, 1\n\t" \
  "LC67_%=:\n\t" \
  "s_cmp_eq_u32 s40, 0\n\t" \
  "s_cbranch_scc1 LC3_%=\n\t" \
  "s_waitcnt vmcnt(0)\n\t" \
  "s_branch LC2_%=\n\t" \
  "LC9_%=:\n\t" \
  STP(1) \
  ""
extern "C" __global__ __launch_bounds__(768, 3) void fused_block(const u16* x, const u16* wqkv, const u16* wo, const u16* w_r,
                                                   const u16* w_gate, const u16* w_up, const u16* w_down, u16* out,
                                                   char* ws) {
  extern __shared__ __attribute__((aligned(16))) char smem[];
  const int tid = threadIdx.x;
  const int lane = tid & 63;
  const int w = __builtin_amdgcn_readfirstlane(tid >> 6);
  const int g = lane >> 4, r16 = lane & 15, q4 = (lane & 15) >> 2, p4 = lane & 3;
  const int bid = blockIdx.x;
  const int xcd = bid & 7;
  const int bidx = bid >> 3;
  const u32 l16 = lane * 16;
  u32 tv = 0;
  LSTAMP(0)
  if (w == 11) {
    u64 pc;
    asm volatile("s_getpc_b64 %0" : "=s"(pc));
    if (lane < 10 && bidx * 10 + lane < CODE_LINES) touch_line((const void*)(pc + (bidx * 10 + lane) * 128));
  }

  if (bidx >= 26) {
    const int fb = xcd * 6 + (bidx - 26);
    const u32 tstart = (u32)__builtin_amdgcn_s_memrealtime();
    const bool hurry = ((((u32)(u64)w_gate + fb * 64) >> 7) & 7u) == 3u;
#define WAIT_UNTIL(T)                                                                      \
  _Pragma("nounroll") for (int it = 0; it < 1000; ++it) {                                  \
    if ((int)((u32)__builtin_amdgcn_s_memrealtime() - tstart) >= (T)) break;               \
    __builtin_amdgcn_s_sleep(4);                                                           \
  }
    if (w < 3) {
      int li = (bidx - 26) * 160 + w * 64 + lane;
      if (w < 2 || lane < 32) touch_line((const char*)x + (size_t)(((xcd >> 2) * 13 + 6) * 80 - 16) * 1280 + li * 128);
    }
    u32x2 ep;
    u32 tag0;
    asm volatile("global_load_dwordx2 %0, %1, off sc1" : "=v"(ep) : "v"(ws + WS_EPOCH) : "memory");
    {
      int wi = (((xcd >> 1) & 1) * 6 + (bidx - 26)) * 12 + w;
      wi = wi > 129 ? 129 : wi;
      int row = (xcd >> 2) * 1040 + (xcd & 1) * 520 + wi * 4 + g;
      row = row > SEQ - 1 ? SEQ - 1 : row;
      const u32 vo = row * 1280 + r16 * 16;
      u32x4 xr[5];
#pragma unroll
      for (int i = 0; i < 5; ++i)
        asm volatile("global_load_dwordx4 %0, %1, %2 offset:%3" : "=v"(xr[i]) : "v"(vo), "s"(x), "n"(i * 256) : "memory");
      asm volatile("s_waitcnt vmcnt(0)" ::: "memory");
      asm volatile("" : "+v"(xr[0]), "+v"(xr[1]), "+v"(xr[2]), "+v"(xr[3]), "+v"(xr[4]), "+v"(ep)::"memory");
      tag0 = ep[0] + (ep[0] == SENT - 1u ? 2u : 1u);
      float sm = 0.f;
#pragma unroll
      for (int i = 0; i < 5; ++i)
#pragma unroll
        for (int e = 0; e < 4; ++e) sm = dot2b(xr[i][e], 0x3F803F80u, sm);
      const float mean = row16_sum(sm) * (1.0f / DM);
      float ss = 0.f;
#pragma unroll
      for (int i = 0; i < 5; ++i)
#pragma unroll
        for (int e = 0; e < 4; ++e) {
          const float a = lo_f(xr[i][e]) - mean, b = hi_f(xr[i][e]) - mean;
          ss = __builtin_fmaf(a, a, ss);
          ss = __builtin_fmaf(b, b, ss);
        }
      const float var = row16_sum(ss) * (1.0f / DM);
      const float den = rbf(__builtin_amdgcn_sqrtf(rbf(rbf(var) + 1e-5f)));
      if (r16 == 0) GST2((u32)(row * 8), ws + WS_LN1, 0, ((u32x2){cvt2((f32x2){mean, den}), tag0}));
    }
    u32x4 va[2][5], vb[5];
    const int vrk = (fb >> 4) * 14 + (int)(((((u32)(u64)w_gate + fb * 64) >> 6) - 8u) & 15u);
    const int vq0 = vrk * 4 + w * 2;
    u32x4 ra;
    u32 rb5;
    asm volatile("global_load_dwordx4 %0, %2, %4\n\tglobal_load_dword %1, %3, %4 offset:1024"
                   : "=&v"(ra), "=&v"(rb5) : "v"(l16), "v"((u32)(lane * 4)), "s"(w_r) : "memory");
    if (w >= 2) {
      WAIT_UNTIL(hurry ? 0 : PDELAY + 12 * (w - 2))
      const u32x4 s4 = (u32x4){SENT, SENT, SENT, SENT};
      const char* fp = ws + WS_FILL + (size_t)fb * 73728 + (w - 2) * 7168 + 4096;
      GST4(l16, fp, -4096, s4); GST4(l16, fp, -3072, s4); GST4(l16, fp, -2048, s4); GST4(l16, fp, -1024, s4);
      GST4(l16, fp, 0, s4); GST4(l16, fp, 1024, s4); GST4(l16, fp, 2048, s4);
      if (w < 4) GST4(l16, ws + WS_FILL + (size_t)fb * 73728 + 69632 + w * 1024, 0, s4);
      if (w >= 5) GST4((u32)(((w - 5) * 8 + (lane >> 3)) * 6144 + (lane & 7) * 16), ws + WS_PK + fb * 128, 0, s4);
    }
    if (w == 0) {
      const u64 e1 = (((u64)ep[1] << 32) | ep[0]) + (ep[0] == SENT - 1u ? 2ull : 1ull);
      if (lane == 0) *(u32x4*)(smem + CTL_OFF) = (u32x4){(u32)e1, (u32)(e1 >> 32), 0u, 0u};
      if (lane >= 1 && lane < 8) *(u32x4*)(smem + CTL_OFF + lane * 16) = (u32x4){0u, 0u, 0u, 0u};
      if (lane >= 32) *(u32x4*)(smem + KEY_OFF - 512 + lane * 16) = (u32x4){0u, 0u, 0u, 0u};
    }
    if (w >= 6) {
      const u32 o1 = tid >= 636 ? 0x3F803F80u : 0u;
      if (tid < 720) *(u32x4*)(smem + HN_OFF + (tid - 384) * 16) = (u32x4){o1, o1, o1, o1};
    }
    if (w < 2 && !hurry && vrk < 40) {
      WAIT_UNTIL(VDELAY)
      const u32 vo = (vq0 * 4 + g) * 1280 + r16 * 16;
#pragma unroll
      for (int i = 0; i < 5; ++i)
        asm volatile("global_load_dwordx4 %0, %3, %4 offset:%8\n\tglobal_load_dwordx4 %1, %3, %5 offset:%8\n\tglobal_load_dwordx4 %2, %6, %7 offset:%8"
                     : "=&v"(va[0][i]), "=&v"(va[1][i]), "=&v"(vb[i]) : "v"(vo), "s"(wo), "s"((const char*)wo + 5120), "v"((u32)(r16 * 16)), "s"(w_r), "n"(i * 256) : "memory");
      asm volatile("s_waitcnt vmcnt(0)" : "+v"(va[0][0]), "+v"(va[0][1]), "+v"(va[0][2]), "+v"(va[0][3]), "+v"(va[0][4]), "+v"(va[1][0]), "+v"(va[1][1]),
                   "+v"(va[1][2]), "+v"(va[1][3]), "+v"(va[1][4]), "+v"(vb[0]), "+v"(vb[1]), "+v"(vb[2]), "+v"(vb[3]), "+v"(vb[4])::"memory");
      float sb = 0.f;
#pragma unroll
      for (int i = 0; i < 20; ++i) sb = dot2b(vb[i >> 2][i & 3], 0x3F803F80u, sb);
      sb = row16_sum(sb) * (1.0f / DM);
      u32x4 pk2[2];
#pragma unroll
      for (int k = 0; k < 2; ++k) {
        float d = 0.f, sa = 0.f;
#pragma unroll
        for (int i = 0; i < 20; ++i) {
          d = dot2b(va[k][i >> 2][i & 3], vb[i >> 2][i & 3], d);
          sa = dot2b(va[k][i >> 2][i & 3], 0x3F803F80u, sa);
        }
        const u32 vp = __float_as_uint(row16_sum(d - sa * sb));
        pk2[k] = (u32x4){cvt2((f32x2){__uint_as_float(__builtin_amdgcn_readlane(vp, 0)), __uint_as_float(__builtin_amdgcn_readlane(vp, 16))}), tag0,
                          cvt2((f32x2){__uint_as_float(__builtin_amdgcn_readlane(vp, 32)), __uint_as_float(__builtin_amdgcn_readlane(vp, 48))}), tag0};
      }
      if (lane < 8) GST4(l16, ws + WS_VP + vq0 * 64, 0, ((lane & 1) ? pk2[1] : pk2[0]));
      LSTAMP(6)
    } else
      asm volatile("s_waitcnt vmcnt(0)" ::: "memory");
    asm volatile("" : "+v"(ra), "+v"(rb5)::"memory");
    LDS_BARRIER();
    const u32x2 tg = *(const u32x2*)(smem + CTL_OFF);
    const u32 tag = tg[0];
    if (w == 2 && lane == 0) GST2(0u, ws + WS_HFLAG + fb * 128, 0, tg);
    LSTAMP(1)
    if (w >= 3) {
      int li = (bidx - 26) * 534 + (w - 3) * 64 + lane;
      li = li > 3199 ? 3199 : li;
      const int rr = li / 5;
      if (w < 11 || lane < 22) touch_line((const char*)wo + rr * 1280 + ((xcd >> 1) & 1) * 640 + (li - rr * 5) * 128);
    }
    WAIT_UNTIL(hurry ? 0 : FDELAY)
    asm volatile("s_barrier" ::: "memory");
    if (w >= 2) {
      const int ch = (lane & 7) ^ (((lane >> 4) & 1) * 2 + ((w + fb) & 1) * 4);
      const u32 vg = (lane >> 3) * (FF * 2) + fb * 64 + (ch & 3) * 16;
      const u64 gsel = (ch & 4) ? (u64)w_up : (u64)w_gate;
#pragma nounroll
      for (int n0 = w - 2; n0 < 80; n0 += 10) {
        const int n = (fb & 1) ? 79 - n0 : n0;
        const u64 p = gsel + vg + (u32)(n * 8 * FF * 2);
        asm volatile("s_mov_b32 m0, %1\n\ts_nop 0\n\tglobal_load_lds_dwordx4 %0, off" FNT ::"v"(p), "s"(n * 1024) : "memory");
      }
#pragma nounroll
      for (int n = 11 - w; n < 40; n += 10) {
        const u32 row = (u32)(n * 64 + lane) / 80u;
        asm volatile("s_mov_b32 m0, %2\n\ts_nop 0\n\tglobal_load_lds_dwordx4 %0, %1" FNT
                     ::"v"(l16 ^ ((row & 3u) * 32u + (row & 8u) * 16u)), "s"((const char*)w_down + (size_t)fb * 40960 + n * 1024), "s"(DOWN_OFF + n * 1024) : "memory");
      }
      asm volatile("s_waitcnt vmcnt(0)" ::: "memory");
      if (lane == 0) asm volatile("ds_add_u32 %0, %1" ::"v"(CTL_OFF + 48), "v"(1u) : "memory");
    } else {
      WAIT_UNTIL(KSTART)
    }
    if (w >= 4) {
      const int v = w - 4, kq = v & 3, m = v >> 2;
#pragma nounroll
      for (int it = 0; it < SPIN_CAP; ++it) {
        if (__builtin_amdgcn_readfirstlane(lds_ld32(CTL_OFF + 48)) >= 10u) break;
        __builtin_amdgcn_s_sleep(1);
      }
      LSTAMP(2)
      bf16x8 gfr[5], ufr[5], dfr[5];
      {
        const int gx = (160 * kq + 8 * g + q4) * 128 + ((m ^ (q4 >> 1) ^ ((g & 1) * 2)) * 32) + p4 * 8;
        const char* db = smem + DOWN_OFF + (8 * g + q4) * 1280 + p4 * 8;
#pragma unroll
        for (int j = 0; j < 5; ++j) {
          gfr[j] = tr_frag(smem + gx + j * 4096, 512);
          ufr[j] = tr_frag(smem + (gx ^ 64) + j * 4096, 512);
          dfr[j] = tr_frag(db + ((v * 160 + j * 32) ^ (q4 * 32 + (g & 1) * 128)), 5120);
        }
#pragma unroll
        for (int j = 0; j < 5; ++j) asm volatile("" : "+v"(gfr[j]), "+v"(ufr[j]), "+v"(dfr[j]));
      }
      u32 ahn = HN_OFF + p4 * HSL + kq * 320 + g * 16, asw = ACT_OFF + p4 * ASL + kq * 256 + (q4 & 1) * 128 + m * 64 + g * 16, asr = ACT_OFF + (lane & 31) * 4;
      u32 aav = ACT_OFF + 4352 + v * 64 + (lane & 31) * 2, aar = ACT_OFF + 4352 + v * 64 + g * 16, vpo = v * 320 + r16 * 64 + g * 16, up3 = lane >= 32 ? 3u : 0u;
      asm volatile("" : "+v"(up3), "+v"(aar));
      const char* pdst = ws + WS_PART + fb * 2560;
      asm volatile("" : "+v"(ahn), "+v"(asw), "+v"(asr), "+v"(aav), "+v"(vpo));
      asm volatile("" : "+s"(pdst));
      int pass = 0;
      asm volatile("" : "+s"(pass));
#pragma nounroll
      for (; pass < 2; ++pass) {
        asm volatile("s_barrier" ::: "memory");
        LSTAMP(15)
        u32x4 bs;
#define GU(J, C0, C1) "v_mfma_f32_16x16x32_bf16 v[120:123], %[g" #J "], v[100+4*" #J ":103+4*" #J "], " C0 "\n\t" \
                      "v_mfma_f32_16x16x32_bf16 v[124:127], %[u" #J "], v[100+4*" #J ":103+4*" #J "], " C1 "\n\t"
#define GU1(J) GU(J, "v[120:123]", "v[124:127]")
        asm volatile("ds_read_b128 v[100:103], %[ah]\n\tds_read_b128 v[104:107], %[ah] offset:64\n\tds_read_b128 v[108:111], %[ah] offset:128\n\t"
                     "ds_read_b128 v[112:115], %[ah] offset:192\n\tds_read_b128 v[116:119], %[ah] offset:256\n\t"
                     "s_waitcnt lgkmcnt(4)\n\t" GU(0, "0", "0") "s_waitcnt lgkmcnt(3)\n\t" GU1(1) "s_waitcnt lgkmcnt(2)\n\t" GU1(2)
                     "s_waitcnt lgkmcnt(1)\n\t" GU1(3) "s_waitcnt lgkmcnt(0)\n\t" GU1(4)
                     "ds_read_b128 %[bs], %[ab]\n\ts_mov_b32 exec_lo, 0x00f000f0\n\ts_mov_b32 exec_hi, 0x00f000f0\n\ts_nop 4\n\t"
                     "v_mov_b64 v[120:121], v[124:125]\n\tv_mov_b64 v[122:123], v[126:127]\n\t"
                     "s_mov_b32 exec_lo, 0x00ff00ff\n\ts_mov_b32 exec_hi, 0x00ff00ff\n\tds_write_b128 %[aw], v[120:123]\n\ts_mov_b64 exec, -1\n\t"
                     "s_setprio 3\n\ts_waitcnt lgkmcnt(0)\n\tv_cmp_eq_u32 vcc, 3, v129\n\ts_barrier\n\t"
                     "s_cbranch_vccnz 1f\n\tds_read_b128 %[bs], %[ab]\n\ts_waitcnt lgkmcnt(0)\n1:"
                     : [bs] "=&{v[128:131]}"(bs)
                     : [ah] "v"(ahn), [ab] "v"((u32)(CTL_OFF + 112)), [aw] "v"(asw), [g0] "v"(gfr[0]), [g1] "v"(gfr[1]), [g2] "v"(gfr[2]),
                       [g3] "v"(gfr[3]), [g4] "v"(gfr[4]), [u0] "v"(ufr[0]), [u1] "v"(ufr[1]), [u2] "v"(ufr[2]), [u3] "v"(ufr[3]), [u4] "v"(ufr[4])
                     : "memory", "vcc", "scc", "v100", "v101", "v102", "v103", "v104", "v105", "v106", "v107", "v108", "v109", "v110", "v111", "v112", "v113",
                       "v114", "v115", "v116", "v117", "v118", "v119", "v120", "v121", "v122", "v123", "v124", "v125", "v126", "v127");
        LSTAMP(14)
        const u32x2 best = {bs[2], bs[3]};
        f32x2 s0, s1, s2, s3;
        asm volatile("ds_read2_b32 %0, %4 offset0:0 offset1:32\n\tds_read2_b32 %1, %4 offset0:64 offset1:96\n\t"
                     "ds_read2_b32 %2, %4 offset0:128 offset1:160\n\tds_read2_b32 %3, %4 offset0:192 offset1:224"
                     : "=&v"(s0), "=&v"(s1), "=&v"(s2), "=&v"(s3) : "v"(asr + ((best[1] & 3u) | up3) * ASL) : "memory");
        float rd = __builtin_amdgcn_rcpf(hi_f(best[0])), mb = lo_f(best[0]);
        asm volatile("s_waitcnt lgkmcnt(0)" : "+v"(s0), "+v"(s1), "+v"(s2), "+v"(s3), "+v"(rd), "+v"(mb)::"memory");
        const f32x2 sm = (s0 + s1) + (s2 + s3);
        auto xg = __builtin_amdgcn_permlane32_swap(__float_as_uint(sm[0]), __float_as_uint(sm[0]), false, false);
        auto xu = __builtin_amdgcn_permlane32_swap(__float_as_uint(sm[1]), __float_as_uint(sm[1]), false, false);
        const f32x2 gu = unpk(cvt2((f32x2){(__uint_as_float(xg[0]) - mb * __uint_as_float(xg[1])) * rd,
                                          (__uint_as_float(xu[0]) - mb * __uint_as_float(xu[1])) * rd}));
        const float sg = rbf(gu[0] * __builtin_amdgcn_rcpf(1.0f + __expf(-gu[0])));
        const u32 av = f2bf(sg * gu[1]);
#define DN(J) "v_mfma_f32_16x16x32_bf16 v[104+4*" #J ":107+4*" #J "], %[d" #J "], v[100:103], 0\n\t"
#define MV(J) "s_lshl_b64 exec, exec, 1\n\tv_mov_b64 v[104:105], v[104+4*" #J ":105+4*" #J "]\n\tv_mov_b64 v[106:107], v[106+4*" #J ":107+4*" #J "]\n\t"
        asm volatile("ds_write_b16 %[aw], %[av]\n\tds_read_b128 v[100:103], %[ar]\n\ts_waitcnt lgkmcnt(0)\n\t" DN(0) DN(1) DN(2) DN(3) DN(4)
                     "s_mov_b32 exec_lo, 0x00010001\n\ts_mov_b32 exec_hi, 0x00010001\n\ts_nop 1\n\t" MV(1) MV(2) MV(3) MV(4)
                     "s_mov_b32 exec_lo, 0x001f001f\n\ts_mov_b32 exec_hi, 0x001f001f\n\tglobal_store_dwordx4 %[vp], v[104:107], %[pd] sc1\n\t"
                     "s_mov_b64 exec, -1\n\ts_setprio 0"
                     ::[aw] "v"(aav), [av] "v"(av), [ar] "v"(aar), [vp] "v"(vpo), [pd] "s"(pdst), [d0] "v"(dfr[0]), [d1] "v"(dfr[1]),
                       [d2] "v"(dfr[2]), [d3] "v"(dfr[3]), [d4] "v"(dfr[4])
                     : "memory", "scc", "v100", "v101", "v102", "v103", "v104", "v105", "v106", "v107", "v108", "v109", "v110", "v111", "v112", "v113",
                       "v114", "v115", "v116", "v117", "v118", "v119", "v120", "v121", "v122", "v123");
        if (pass) break;
        LSTAMP(16)
        u32 fin = 0;
#pragma nounroll
        for (int it = 0; it < SPIN_CAP * 8; ++it) {
          fin = __builtin_amdgcn_readfirstlane(lds_ld32(CTL_OFF + 40));
          if (fin) break;
          __builtin_amdgcn_s_sleep(4);
        }
        if (fin != 2u) break;
        asm volatile("s_barrier" ::: "memory");
        pdst += WS_PART2 - WS_PART;
      }
      FLUSH_STAMPS
      return;
    }
    if (w == 0) {
      LSTAMP(8)
      u32 pk6;
#define STP3
#define STP3O
      asm volatile(
          "s_mov_b32 s41, %[cap]\n\ts_mov_b32 s40, 1\n\ts_mov_b64 s[46:47], 0\n\tv_mov_b32 v5, %[ln]\n\t"
          "s_mov_b32 s54, -1\n\ts_mov_b32 s55, 0x7ffff\n\tv_mov_b32 v12, 0\n\t"
#define PKLD(D) "s_mov_b64 exec, s[54:55]\n\tglobal_atomic_or_x2 " D ", %[vk], %[z], %[kb] sc0 sc1\n\ts_mov_b64 exec, -1\n\t"
          PKLD("v[8:9]") "s_sleep %[gap]\n\t" PKLD("v[10:11]") "s_sleep %[gap]\n\t" PKLD("v[14:15]")
#define MDPP(C) "s_nop 1\n\tv_max_u32_dpp v13, v13, v13 " C " row_mask:0xf bank_mask:0xf bound_ctrl:1\n\t"
#define PSTEP(L, D0, D1, DR)                                                                        \
          L "0:\n\t"                                                                               \
          "s_waitcnt vmcnt(2)\n\t"                                                                 \
          "v_cmp_eq_u32 s[42:43], " D1 ", %[tag]\n\t"                                              \
          "s_and_b64 exec, s[42:43], s[54:55]\n\ts_andn2_b64 s[54:55], s[54:55], s[42:43]\n\t"      \
          "v_and_or_b32 v12, " D0 ", %[msk], %[lid]\n\t"                                           \
          "v_mov_b32 v6, " D0 "\n\ts_mov_b64 exec, -1\n\t"                                         \
          "v_cmp_le_u32 vcc, s40, v12\n\t"                                                         \
          "s_cmp_eq_u64 vcc, s[46:47]\n\ts_cbranch_scc1 " L "1f\n\t"                              \
          "s_swappc_b64 s[50:51], s[48:49]\n"                                                      \
          L "1:\n\t"                                                                               \
          "s_cmp_eq_u64 s[54:55], 0\n\ts_cbranch_scc1 9f\n\t"                                     \
          PKLD(DR)
          "s_getpc_b64 s[48:49]\n\ts_add_u32 s48, s48, 12\n\ts_addc_u32 s49, s49, 0\n\ts_branch 10f\n\t"
          "v_mov_b32 v13, v12\n\t"
          MDPP("quad_perm:[1,0,3,2]") MDPP("quad_perm:[2,3,0,1]") MDPP("row_half_mirror") MDPP("row_mirror")
          "v_bfe_u32 v4, v6, 10, 6\n\tv_readlane_b32 s44, v13, 0\n\tv_readlane_b32 s45, v13, 16\n\tv_readlane_b32 s46, v13, 32\n\tv_readlane_b32 s47, v13, 48\n\t"
          "v_sub_u32 v4, %[rb], v4\n\ts_max_u32 s44, s44, s45\n\ts_max_u32 s46, s46, s47\n\ts_max_u32 s44, s44, s46\n\t"
          "s_and_b32 s45, s44, 0x7fffff\n\ts_bitset1_b32 s45, 23\n\ts_mul_hi_u32 s45, s45, %[shr]\n"
          "5:\n\ts_sub_u32 s40, s44, s45\n\ts_cselect_b32 s40, 1, s40\n\ts_max_u32 s40, s40, 1\n\tv_cmp_le_u32 s[46:47], s40, v12\n\ts_mul_i32 s45, s45, 3\n\ts_lshr_b32 s45, s45, 2\n\t"
          "s_bcnt1_i32_b64 s52, s[46:47]\n\ts_cmp_gt_u32 s52, 3\n\ts_cbranch_scc1 5b\n\t"
          "v_mbcnt_lo_u32_b32 v13, s46, 0\n\tv_mbcnt_hi_u32_b32 v13, s47, v13\n\tv_sub_u32 v7, %[ln], v13\n\tv_add_u32 v7, s52, v7\n\t"
          "v_cndmask_b32 v13, v7, v13, s[46:47]\n\tv_cndmask_b32 v7, 2, 1, s[46:47]\n\tv_cmp_gt_u32 s[44:45], 3, v13\n\t"
          "v_lshl_add_u32 v13, v13, 4, %[pa]\n\ts_mov_b64 exec, s[44:45]\n\tds_write_b128 v13, v[4:7]\n\ts_mov_b64 exec, -1\n\t"
          "s_setpc_b64 s[50:51]\n"
          PSTEP("1", "v8", "v9", "v[8:9]")
          PSTEP("2", "v10", "v11", "v[10:11]")
          PSTEP("3", "v14", "v15", "v[14:15]")
          "s_sub_u32 s41, s41, 1\n\ts_cbranch_scc0 10b\n\ts_swappc_b64 s[50:51], s[48:49]\n"
          "9:\n\t" STP3 "v_or_b32 v7, 4, v7\n\ts_mov_b64 exec, s[44:45]\n\tds_write_b32 v13, v7 offset:12\n\ts_mov_b64 exec, -1\n\tds_write_b32 %[at], v6\n\ts_waitcnt lgkmcnt(0)\n\ts_barrier\n\ts_waitcnt vmcnt(0)"
          : "=&{v6}"(pk6) STP3O : [cap] "n"(SPIN_CAP), [gap] "n"(PGAP), [vk] "v"((u32)(lane * 6144)), [kb] "s"(ws + WS_PK + fb * 128), [tag] "v"(tag), [z] "v"(0ull), [ln] "v"(lane),
            [msk] "s"(~1023u), [lid] "v"(64u | lane), [rb] "v"(2047 - ((lane >> 1) * 80 + (lane & 1) * 48)),
            [shr] "s"((u32)((1.0 - PTHR) * 4294967296.0)), [pa] "s"(POST_OFF), [at] "v"((u32)(KEY_OFF - 256 + lane * 4))
          : "memory", "vcc", "scc", "s40", "s41", "s42", "s43", "s44", "s45", "s46", "s47", "s48", "s49", "s50", "s51", "s52", "s54", "s55",
            "v4", "v5", "v7", "v8", "v9", "v10", "v11", "v12", "v13", "v14", "v15");
      const u32 now = (u32)__builtin_amdgcn_s_memrealtime();
      const u32 nag = (pk6 - (now >> 1) - 1u) & 1023u;
      u32 kgo = __builtin_amdgcn_readfirstlane(now + KCD - 2 * (1023u - wave_max(lane < 51 ? nag : 0u)));
      u64 old = __ballot(lane < 51 && nag < 1023u - (KOLD - 8) / 2);
      asm volatile("" : "+s"(kgo), "+s"(old));
#define STP12
#define STP12O
      asm volatile(
          "s_mov_b32 s41, %[cap]\n\ts_mov_b32 s54, -1\n\ts_mov_b32 s55, 0x7ffff\n\tv_mov_b32 v12, 0\n\t"
          "s_mov_b64 exec, %[old]\n\ts_cbranch_execz 3f\n\tglobal_load_dwordx2 v[14:15], %[vk], %[kb] sc1\n"
          "3:\n\ts_setprio 3\n\ts_barrier\n"
          "4:\n\ts_memrealtime s[42:43]\n\ts_waitcnt lgkmcnt(0)\n\ts_sub_u32 s42, s42, %[go]\n\ts_cmp_lt_i32 s42, 0\n\ts_cbranch_scc1 4b\n\t"
          "s_andn2_b64 exec, s[54:55], %[old]\n\ts_cbranch_execz 5f\n\tglobal_load_dwordx2 v[14:15], %[vk], %[kb] sc1\n"
          "5:\n\ts_mov_b64 exec, s[54:55]\n\tds_read_b32 v4, %[ctl] offset:92\n\ts_waitcnt lgkmcnt(0)\n\tv_and_b32 v4, 3, v4\n\t"
          "v_lshl_add_u32 v4, v4, 4, %[pa]\n\tds_read_b64 v[6:7], v4\n\ts_waitcnt lgkmcnt(0)\n\t"
          "v_readfirstlane_b32 s48, v6\n\tv_readfirstlane_b32 s49, v7\n\tv_mov_b32 v6, 1\n\ts_waitcnt vmcnt(0)\n\tv_cmp_ne_u32 s[54:55], v15, %[tag]\n\tv_mov_b32 v12, v14\n\ts_mov_b64 exec, -1\n\t"
          "s_cmp_eq_u64 s[54:55], 0\n\ts_cbranch_scc1 9f\n\t"
          PKLD("v[8:9]") "s_sleep %[gap]\n\t" PKLD("v[10:11]")
#define KSTEP(L, D0, D1, DR)                                                                        \
          L "0:\n\t"                                                                               \
          "s_waitcnt vmcnt(1)\n\t"                                                                 \
          "v_cmp_eq_u32 s[42:43], " D1 ", %[tag]\n\t"                                              \
          "s_and_b64 exec, s[42:43], s[54:55]\n\tv_mov_b32 v12, " D0 "\n\ts_mov_b64 exec, -1\n\t"   \
          "s_andn2_b64 s[54:55], s[54:55], s[42:43]\n\ts_cbranch_scc0 9f\n\t"                     \
          PKLD(DR)
          KSTEP("1", "v8", "v9", "v[8:9]")
          KSTEP("2", "v10", "v11", "v[10:11]")
          "s_sub_u32 s41, s41, 1\n\ts_cbranch_scc0 10b\n"
          "9:\n\t"
          "v_readlane_b32 s40, v12, s49\n\tds_write_b32 %[ak], v12\n\tv_mov_b32 v13, v12\n\t"
          "s_and_b32 s45, s40, 0x7ff\n\tv_cmp_lt_u32 vcc, s40, v12\n\ts_cmp_eq_u32 s45, s48\n\ts_cselect_b64 vcc, vcc, -1\n\ts_cbranch_vccz 8f\n\t"
          MDPP("quad_perm:[1,0,3,2]") MDPP("quad_perm:[2,3,0,1]") MDPP("row_half_mirror") MDPP("row_mirror")
          "s_nop 0\n\tv_readlane_b32 s44, v13, 0\n\tv_readlane_b32 s45, v13, 16\n\tv_readlane_b32 s46, v13, 32\n\tv_readlane_b32 s47, v13, 48\n\t"
          "s_max_u32 s44, s44, s45\n\ts_max_u32 s46, s46, s47\n\ts_max_u32 s40, s44, s46\n\t"
          "v_cmp_eq_u32 vcc, s40, v12\n\tv_mov_b32 v6, 2\n\ts_ff1_i32_b64 s44, vcc\n\tv_mov_b32 v7, s44\n"
          "8:\n\tv_mov_b32 v4, s40\n\ts_mov_b64 exec, 1\n\tds_write_b128 %[ctl], v[4:7]\n\ts_mov_b64 exec, -1\n\t"
          STP12 "s_waitcnt vmcnt(0) lgkmcnt(0)"
          : STP12O : [cap] "n"(SPIN_CAP), [gap] "n"(KGAP), [go] "s"(kgo), [old] "s"(old), [vk] "v"((u32)(lane * 6144)), [kb] "s"(ws + WS_PK + fb * 128 + 8), [tag] "v"(tag),
            [ctl] "v"((u32)(CTL_OFF + 32)), [ak] "v"((u32)(KEY_OFF + lane * 4)), [pa] "s"(POST_OFF), [z] "v"(0ull)
          : "memory", "vcc", "scc", "s40", "s41", "s42", "s43", "s44", "s45", "s46", "s47", "s48", "s49", "s54", "s55", "v4", "v5", "v6", "v7", "v8", "v9", "v10",
            "v11", "v12", "v13", "v14", "v15");
      if (fb == 0 && lane == 0) GST2(0u, ws + WS_EPOCH, 0, tg);
      FLUSH_STAMPS
      return;
    }
    float swr = dot2b(rb5, 0x3F803F80u, 0.f);
#pragma unroll
    for (int e = 0; e < 4; ++e) swr = dot2b(ra[e], 0x3F803F80u, swr);
    swr = wave_sum(swr);
    u32 ah16 = HN_OFF + (w - 1) * HSL + l16, ah4 = HN_OFF + (w - 1) * HSL + 1024 + lane * 4, l4 = lane * 4;
    asm volatile("" : "+v"(swr), "+v"(ah16), "+v"(ah4), "+v"(l4));
#define DOT3(H, W) "v_dot2c_f32_bf16 v102, 0x3f803f80, " H "\n\tv_dot2c_f32_bf16 v103, " H ", " H "\n\tv_dot2c_f32_bf16 v104, " H ", " W "\n\t"
#define ADPP(C) "s_nop 1\n\tv_add_f32_dpp v102, v102, v102 " C " row_mask:0xf bank_mask:0xf bound_ctrl:1\n\t"
#define XDPP(C) "s_nop 1\n\tv_max_u32_dpp v102, v102, v102 " C " row_mask:0xf bank_mask:0xf bound_ctrl:1\n\t"
#define FPR
#define FPRO
    const char* rp = (const char*)w_r;
    u32 second = 0;
    u32x2 mine;
#pragma nounroll
    for (;;) {
      asm volatile(
          "s_mov_b32 s47, %[cap]\n\ts_mov_b32 s43, -1\n\ts_mov_b32 s46, -1\n"
          "7:\n\ts_mov_b64 s[48:49], %[rp]\n\ts_mov_b32 s45, 0\n\ts_branch 4f\n"
          "18:\n\ts_sleep 1\n"
          "1:\n\ts_sub_u32 s47, s47, 1\n\ts_cbranch_scc1 8f\n\ts_cmp_lg_u32 %[sec], 0\n\ts_cbranch_scc1 7b\n\t"
          "ds_read_b128 v[108:111], %[ap]\n\ts_memrealtime s[40:41]\n\ts_waitcnt lgkmcnt(0)\n\t"
          "v_readfirstlane_b32 s45, v111\n\tv_readfirstlane_b32 s46, v109\n\tv_readfirstlane_b32 s50, v110\n\ts_lshr_b32 s41, s40, 1\n\t"
          "s_cmp_eq_u32 s45, 6\n\ts_cbranch_scc1 8f\n\ts_bitcmp0_b32 s45, 0\n\ts_cbranch_scc1 18b\n\t"
          "s_cmp_eq_u32 s46, s42\n\ts_cbranch_scc1 6f\n\ts_cmp_eq_u32 s46, s43\n\ts_cbranch_scc1 2f\n\t"
          "s_sub_u32 s41, s41, s50\n\ts_and_b32 s41, s41, 0x3ff\n\ts_cmp_lt_u32 s41, %[tq]\n\ts_cbranch_scc1 18b\n\t"
          "s_mov_b32 s43, s46\n\ts_add_u32 s44, s50, 8\n\ts_cmp_ge_u32 s41, %[pc] - 20\n\ts_cbranch_scc1 1b\n\t"
          "s_mul_i32 s48, s46, 6144\n\tv_mov_b64 v[104:105], 0\n\tv_mov_b32 v101, s48\n\tv_mad_u32_u24 v101, %[l16], 48, v101\n\t"
          "s_mov_b64 exec, 0xff\n\tglobal_atomic_or_x2 v[102:103], v101, v[104:105], %[pk] sc0 sc1\n\ts_sub_u32 s50, s50, 512\n\ts_waitcnt vmcnt(0)\n\t"
          "v_cmp_eq_u32 vcc, v103, %[tag]\n\tv_subrev_u32 v102, s50, v102\n\tv_and_b32 v102, 0x3ff, v102\n\tv_cndmask_b32 v102, 0, v102, vcc\n\t"
          XDPP("quad_perm:[1,0,3,2]") XDPP("quad_perm:[2,3,0,1]") XDPP("row_half_mirror")
          "s_mov_b64 exec, -1\n\tv_readfirstlane_b32 s44, v102\n\ts_min_u32 s44, s44, 562\n\ts_add_u32 s44, s44, s50\n\ts_branch 1b\n"
          "2:\n\ts_sub_u32 s41, s41, s44\n\ts_and_b32 s41, s41, 0x3ff\n\ts_cmp_lt_u32 s41, %[pc] - 8\n\ts_cbranch_scc1 1b\n\t"
          "s_sub_u32 s41, %[pc], s41\n\ts_lshl1_add_u32 s51, s41, s40\n\ts_mul_i32 s48, s46, 1280\n\ts_add_u32 s48, s48, %[lo]\n\ts_addc_u32 s49, %[hi], 0\n\t" FPR
          "3:\n\ts_memrealtime s[40:41]\n\ts_waitcnt lgkmcnt(0)\n\ts_sub_u32 s40, s40, s51\n\ts_cmp_lt_i32 s40, 0\n\ts_cbranch_scc1 3b\n"
          "4:\n\tglobal_load_dwordx4 v[96:99], %[l16], s[48:49] sc1\n\tglobal_load_dword v100, %[l4], s[48:49] offset:1024 sc1\n\t"
          "v_mov_b64 v[102:103], 0\n\tv_mov_b32 v104, 0\n\ts_mov_b32 s42, -2\n\ts_waitcnt vmcnt(0)\n\t"
          "ds_read_b128 v[108:111], %[ap]\n\tds_read_b32 v105, %[ab] offset:4\n\tds_write_b128 %[a16], v[96:99]\n\tds_write_b32 %[a4], v100\n\t"
          "v_max3_u32 v101, v96, v97, v98\n\tv_max3_u32 v101, v101, v99, v100\n\tv_cmp_eq_u32 vcc, -1, v101\n\t"
          DOT3("v96", "v92") DOT3("v97", "v93")
          "s_cbranch_vccnz 1b\n\ts_mov_b32 s42, s46\n\ts_waitcnt lgkmcnt(2)\n\tv_lshl_or_b32 v109, v111, 8, v109\n\tv_lshl_or_b32 v109, v105, 16, v109\n\t"
          "s_or_b32 s45, s46, 0x20500\n\tv_cmp_eq_u32 vcc, s45, v109\n\ts_cbranch_vccz 5f\n\ts_waitcnt lgkmcnt(0)\n\ts_barrier\n"
          "5:\n\t" DOT3("v98", "v94") DOT3("v99", "v95") DOT3("v100", "%[rb]")
          "s_nop 2\n\tv_mov_b32 v105, v104\n\tv_permlane32_swap_b32 v102, v103\n\tv_add_f32 v102, v102, v103\n\tv_permlane32_swap_b32 v104, v105\n\t"
          "v_add_f32 v104, v104, v105\n\ts_nop 1\n\tv_permlane16_swap_b32 v102, v104\n\tv_add_f32 v102, v102, v104\n\t"
          ADPP("quad_perm:[1,0,3,2]") ADPP("quad_perm:[2,3,0,1]") ADPP("row_half_mirror") ADPP("row_mirror")
          "v_mul_f32 v103, 0x3acccccd, v102\n\tv_readlane_b32 s41, v102, 16\n\tv_readlane_b32 s40, v103, 32\n\t"
          "s_cmp_lg_u32 %[sec], 0\n\ts_nop 0\n\tv_fma_f32 v102, -v103, v103, s40\n\tv_max_f32 v102, 0, v102\n\tv_cvt_pk_bf16_f32 v102, 0, v102\n\t"
          "v_add_f32 v102, 0x3727c5ac, v102\n\tv_cvt_pk_bf16_f32 v102, 0, v102\n\tv_sqrt_f32 v102, v102\n\tv_cvt_pk_bf16_f32 v105, 0, v103\n\t"
          "v_fma_f32 v105, -%[swr], v105, s41\n\tv_cvt_pk_bf16_f32 v106, v103, v102\n\tv_and_b32 v102, 0xffff0000, v106\n\tv_rcp_f32 v102, v102\n\t"
          "v_mov_b32 v104, 1\n\tv_mul_f32 v102, v102, v105\n\tv_ashrrev_i32 v103, 31, v102\n\tv_or_b32 v103, 0x80000000, v103\n\t"
          "v_xor_b32 v102, v102, v103\n\tv_and_or_b32 v107, v102, -4, %[sl]\n\ts_cbranch_scc1 10f\n\ts_cbranch_vccz 1b\n\t"
          "s_mov_b64 exec, 1\n\tds_max_u64 %[ab], v[106:107] offset:8\n\tds_add_u32 %[ab], v104 offset:4\n\ts_branch 9f\n"
          "6:\n\ts_bitcmp0_b32 s45, 2\n\ts_cbranch_scc1 1b\n\ts_mov_b64 exec, 1\n\tds_max_u64 %[ab], v[106:107] offset:8\n"
          "8:\n\ts_cmp_lg_u32 %[sec], 0\n\ts_cbranch_scc1 10f\n\tv_mov_b32 v104, 1\n\ts_mov_b64 exec, 1\n\tds_add_u32 %[ab], v104 offset:4\n\ts_barrier\n"
          "9:\n\ts_mov_b64 exec, -1\n\ts_waitcnt lgkmcnt(0)\n\ts_barrier\n"
          "10:"
          : "=&{v[106:107]}"(mine) FPRO
          : [cap] "n"(SPIN_CAP * 8), [rp] "s"(rp), [sec] "s"(second), [ap] "v"((u32)(POST_OFF + (w - 1) * 16)), [tq] "n"(TQD / 2), [pc] "n"(PCD / 2),
            [pk] "s"(ws + WS_PK + (bidx - 26) * 128), [tag] "v"(tag), [lo] "s"((u32)(u64)(ws + WS_PC)), [hi] "s"((u32)((u64)(ws + WS_PC) >> 32)),
            [l16] "v"(l16), [l4] "v"(l4), [a16] "v"(ah16), [a4] "v"(ah4), "{v[92:95]}"(ra), [rb] "v"(rb5), [swr] "v"(swr), [sl] "s"(w - 1),
            [ab] "v"((u32)(CTL_OFF + 112))
          : "memory", "vcc", "scc", "s40", "s41", "s42", "s43", "s44", "s45", "s46", "s47", "s48", "s49", "s50", "s51", "v96", "v97", "v98", "v99",
            "v100", "v101", "v102", "v103", "v104", "v105", "v108", "v109", "v110", "v111");
      if (w != 1) break;
      if (second) {
        if (lane == 0) *(u32x2*)(smem + CTL_OFF + 120) = mine;
        asm volatile("s_waitcnt lgkmcnt(0)" ::: "memory");
        break;
      }
      u32x4 wst;
#pragma nounroll
      for (int it = 0; it < SPIN_CAP * 8; ++it) {
        wst = lds_ld128(CTL_OFF + 32);
        if (__builtin_amdgcn_readfirstlane(wst[2])) break;
        __builtin_amdgcn_s_sleep(1);
      }
      if (__builtin_amdgcn_readfirstlane(wst[2]) != 2u) break;
      asm volatile("s_barrier" ::: "memory");
      rp = ws + WS_CAND + __builtin_amdgcn_readfirstlane(wst[3]) * 2560;
      second = 1;
    }
    if (w == 1) {
    } else if (w == 3 || fb >= 40) {
      const int c = w == 3 ? fb : fb + 8;
      if (c < 51) {
        const u32 now = (u32)__builtin_amdgcn_s_memrealtime();
        const u32 age = ((now >> 1) - __builtin_amdgcn_readfirstlane(*(const u32*)(smem + KEY_OFF - 256 + c * 4))) & 1023u;
        asm volatile("s_mov_b32 s42, %[cap]\n"
                     "3:\n\ts_memrealtime s[40:41]\n\ts_waitcnt lgkmcnt(0)\n\ts_sub_u32 s40, s40, %[go]\n\ts_cmp_lt_i32 s40, 0\n\ts_cbranch_scc1 3b\n"
                     "4:\n\tglobal_load_dwordx4 v[96:99], %[l16], %[cp] sc1\n\tglobal_load_dword v103, %[l4], %[cp] offset:1024 sc1\n\t"
                     "s_sub_u32 s42, s42, 8\n\ts_waitcnt vmcnt(0)\n\ts_cbranch_scc1 2f\n\t"
                     "v_max3_u32 v101, v96, v98, v103\n\tv_cmp_eq_u32 vcc, -1, v101\n\ts_cbranch_vccnz 4b\n"
                     "1:\n\tds_read_b64 v[100:101], %[af]\n\tds_read_b32 v102, %[ak]\n\ts_sub_u32 s42, s42, 1\n\ts_cbranch_scc1 2f\n\ts_waitcnt lgkmcnt(0)\n\t"
                     "v_cmp_eq_u32 vcc, 0, v100\n\ts_cbranch_vccnz 1b\n\t"
                     "v_cmp_eq_u32 vcc, %[c], v101\n\tv_bfi_b32 v102, v102, 0, %[m]\n\ts_cbranch_vccnz 2f\n\t"
                     "v_mad_u32_u24 v100, v102, %[rs], %[l16]\n\tv_mad_u32_u24 v101, v102, %[rs], %[l4]\n\t"
                     "global_store_dwordx4 v100, v[96:99], %[out]\n\tglobal_store_dword v101, v103, %[out] offset:1024\n"
                     "2:\n\ts_waitcnt lgkmcnt(0)"
                     ::[cap] "n"(SPIN_CAP * 8), [go] "s"(now + CCD - 2 * age), [cp] "s"(ws + WS_CAND + c * 2560 + 1280), [af] "v"((u32)(CTL_OFF + 40)),
                       [ak] "v"((u32)(KEY_OFF + c * 4)), [c] "s"(c), [m] "s"(2047), [rs] "s"(1280), [l16] "v"(l16), [l4] "v"(l4), [out] "s"(out)
                     : "memory", "vcc", "scc", "s40", "s41", "s42", "v96", "v97", "v98", "v99", "v100", "v101", "v102", "v103");
      }
    } else {
      const char* pp = ws + WS_PART + fb * 64;
      int pass = 0;
      asm volatile("" : "+s"(pass));
#pragma nounroll
      for (; pass < 2; ++pass) {
        const u32x2 best = *(const u32x2*)(smem + CTL_OFF + 120);
        const u32 slot = best[1] & 3u;
        const u32 wkey = __builtin_amdgcn_readfirstlane(*(const u32*)(smem + (pass ? CTL_OFF + 32 : POST_OFF + slot * 16)));
        const float rr = rbf(__uint_as_float((best[1] & ~3u) ^ ((u32)((int)~best[1] >> 31) | 0x80000000u)));
        float rs = rbf(__builtin_amdgcn_rcpf(1.0f + __expf(-rr)));
        const int ccol = (g & 1) * 2 + (g >> 1);
        float hv = bf2f(*(const u16*)(smem + HN_OFF + slot * HSL + fb * 32 + p4 * 8 + ccol * 2));
        hv = __builtin_fmaf(hv - lo_f(best[0]), __builtin_amdgcn_rcpf(hi_f(best[0])), hv);
        const char* op_ = (const char*)out + (size_t)(2047 - (int)(wkey & 2047u)) * 1280 + fb * 32;
        u32 vo_ = p4 * 8 + ccol * 2;
        asm volatile("" : "+v"(vo_), "+v"(hv), "+v"(rs));
        asm volatile("" : "+s"(op_));
        const u32 vp = (lane >> 2) * 2560 + p4 * 16;
        typedef __attribute__((ext_vector_type(16))) unsigned int u32x16;
        u32x16 keepa, keepb;
        f32x4 ys;
        u32 fin;
        asm volatile(
#define PLD(B0, B1, B2)                                                                             \
            "global_load_dwordx4 " B0 ", %[vp], %[p0] sc1\n\t"                                       \
            "global_load_dwordx4 " B1 ", %[vp], %[p1] sc1\n\tglobal_load_dwordx4 " B2 ", %[vp], %[p2] sc1\n\t"
#define PCHK(A0, A1, B0, B1, C0, C1)                                                                \
            "s_waitcnt vmcnt(3)\n\t"                                                                 \
            "v_pk_add_f32 v[96:97], " A0 ", " B0 "\n\tv_pk_add_f32 v[98:99], " A1 ", " B1 "\n\t"    \
            "v_pk_add_f32 v[96:97], v[96:97], " C0 "\n\tv_pk_add_f32 v[98:99], v[98:99], " C1 "\n\t" \
            "v_pk_add_f32 v[102:103], v[96:97], v[98:99]\n\t"                                        \
            "v_cmp_u_f32 vcc, v102, v103\n\ts_cbranch_vccz 9f\n\t"
            "s_mov_b32 s42, %[cap]\n\ts_sleep %[cdel]\n\t"
            PLD("v[64:67]", "v[68:71]", "v[72:75]")
            "s_sleep %[gap]\n\t"
            PLD("v[80:83]", "v[84:87]", "v[88:91]")
            "1:\n\t"
            PCHK("v[64:65]", "v[66:67]", "v[68:69]", "v[70:71]", "v[72:73]", "v[74:75]")
            PLD("v[64:67]", "v[68:71]", "v[72:75]")
            PCHK("v[80:81]", "v[82:83]", "v[84:85]", "v[86:87]", "v[88:89]", "v[90:91]")
            PLD("v[80:83]", "v[84:87]", "v[88:91]")
            "s_sub_u32 s42, s42, 1\n\ts_cbranch_scc0 1b\n"
            "9:\n\tds_read_b32 %[fin], %[af]"
            : "=&{v[96:99]}"(ys), "=&{v[64:79]}"(keepa), "=&{v[80:95]}"(keepb), [fin] "=&v"(fin)
            : [cap] "n"(SPIN_CAP), [gap] "n"(CGAP), [cdel] "n"(CDEL), [vp] "v"(vp), [p0] "s"(pp),
              [p1] "s"(pp + 40960), [p2] "s"(pp + 81920), [af] "v"((u32)(CTL_OFF + 40))
            : "memory", "vcc", "scc", "s42", "v102", "v103");
        LSTAMP(16)
        float y = tr_sum(ys);
        y += dppf<0x124>(y);
        y += dppf<0x128>(y);
        const u32 res = cvt2((f32x2){__builtin_fmaf(y, rs, hv), 0.f});
        asm volatile("s_mov_b32 s42, %[cap]\n"
                     "1:\n\ts_waitcnt lgkmcnt(0)\n\tv_cmp_eq_u32 vcc, %[ok], %[fin]\n\ts_cbranch_vccz 2f\n\t"
                     "s_mov_b32 exec_lo, 0x000f000f\n\ts_mov_b32 exec_hi, 0x000f000f\n\tglobal_store_short %[vo], %[res], %[op]\n\ts_mov_b64 exec, -1\n"
                     "2:\n\tv_cmp_eq_u32 vcc, 0, %[fin]\n\ts_cbranch_vccz 3f\n\tds_read_b32 %[fin], %[af]\n\ts_sub_u32 s42, s42, 1\n\ts_cbranch_scc0 1b\n"
                     "3:\n\ts_waitcnt lgkmcnt(0)\n\tv_cmp_lt_u32 vcc, %[ok], %[fin]\n\ts_cbranch_vccz 4f\n\ts_waitcnt vmcnt(0)\n4:"
                     : [fin] "+v"(fin) : [cap] "n"(SPIN_CAP * 8), [ok] "s"(pass + 1), [vo] "v"(vo_), [res] "v"(res), [op] "s"(op_),
                       [af] "v"((u32)(CTL_OFF + 40)), "{v[64:79]}"(keepa), "{v[80:95]}"(keepb) : "memory", "vcc", "scc", "s42");
        if (pass || __builtin_amdgcn_readfirstlane(fin) != 2u) break;
        asm volatile("s_barrier\n\ts_barrier\n\ts_barrier" ::: "memory");
        pp += WS_PART2 - WS_PART;
      }
    }
    LSTAMP(17)
    FLUSH_STAMPS
    return;
  }

  const int idx2 = (xcd & 1) * 26 + bidx;
  const int hg = ((xcd >> 1) & 1) * 4 + (idx2 & 3);
  const int mt = (xcd >> 2) * 13 + (idx2 >> 2);
#define ROTF (((idx2 >> 2) * 3 / 2 + (idx2 & 3) * 3) % 10)
  const int kb0 = ROTF;
  const int r0 = mt * 80;
  if (w >= 4) {
    const int cw = w - 4;
    u32 vo = w_off(cw, lane, hg), vx = x_off(cw, lane, r0), vx2 = x_off(8 + (cw & 3), lane, r0);
    asm volatile("" : "+v"(vo), "+v"(vx), "+v"(vx2));
    __builtin_amdgcn_sched_barrier(0);
    const char* wb = (const char*)wqkv + (size_t)kb0 * 245760 + W_ROW(cw);
    dma_m0<0>(vo, wb, cw * 1024);
    dma_m0<0>(vx, (const char*)x + kb0 * 128, XRING + cw * 1024);
    dma_m0<0>(vo, wb + 61440, 8192 + cw * 1024);
    if (cw < 4) dma_m0<0>(vx2, (const char*)x + kb0 * 128, XRING + 8192 + cw * 1024);
  }
  LSTAMP(40)
  const int rt = w >= 4 ? (w & 3) : 4, kh = w >= 4 ? (w - 4) >> 2 : (w & 1);
  float bias[2];
  u32 sel[4], e4, eh, aq, aka, akb, pm, pf, hi, arec, vpp;
  const char *os, *os4, *sst, *oa[10];
  int so_[10];
  int lane_ = lane, mt_ = mt, hg_ = hg;
  auto prepv = [&](const int lane) __attribute__((always_inline)) {
    const int g = lane >> 4, r16 = lane & 15, q4 = (lane & 15) >> 2, p4 = lane & 3;
    const int par = lane & 1;
    const bool ca = r16 >= 4 * g, cb = r16 >= 4 * g + 2;
    bias[0] = (ca || r0 + rt > 0) ? 0.f : -__builtin_inff();
    bias[1] = (cb || r0 + rt > 0) ? 0.f : -__builtin_inff();
    const u32 sa = par ? 0x01000C0Cu : 0x0C0C0100u, sb = par ? 0x03020C0Cu : 0x0C0C0302u;
    sel[0] = ca ? 0x0C0C0C0Cu : sa;
    sel[1] = cb ? 0x0C0C0C0Cu : sb;
    sel[2] = ca ? sa : 0x0C0C0C0Cu;
    sel[3] = cb ? sb : 0x0C0C0C0Cu;
    e4 = EXO + rt * 512 + lane * 8;
    eh = e4 + kh * 6144;
    aq = TAB_OFF + 384 + (rt * 16 + r16) * 4;
    aka = TAB_OFF + 384 + (rt * 16 + 4 * g + par + (ca ? 16 : 0)) * 4;
    akb = TAB_OFF + 384 + (rt * 16 + 4 * g + par + 2 + (cb ? 16 : 0)) * 4;
    pm = (4 * g + q4) * 160 + p4 * 40;
    pf = (20 * g + 16 + q4) * 160 + p4 * 40;
    hi = HIM + (rt * 16 + r16) * 160 + g * 40;
    arec = REC_OFF + (r16 == 0 ? kh * 192 + g * 48 : (r16 == 8 ? ((g >> 1) == kh ? 480 : 384) : 432));
    vpp = lane + kh * 64 + (rt == 0 ? 768 : (rt == 2 ? 8192 : (rt == 4 ? 512 : 640)));
    asm volatile("" : "+v"(arec), "+v"(vpp));
    asm volatile("" : "+v"(bias[0]), "+v"(bias[1]), "+v"(sel[0]), "+v"(sel[1]), "+v"(sel[2]), "+v"(sel[3]), "+v"(e4), "+v"(eh),
                 "+v"(aq), "+v"(aka), "+v"(akb), "+v"(pm), "+v"(pf), "+v"(hi));
  };
  auto preps = [&](const int mt, const int hg) __attribute__((always_inline)) {
    const char* ob = ws + WS_O + (size_t)mt * 102400;
    os = ob + rt * 20480 + (2 * hg + kh) * 1024;
    os4 = ob + rt * 20480 + (16 + (hg >> 1)) * 1024 + (hg & 1) * 8;
    sst = ws + WS_STAT + (size_t)(2 * mt + (rt < 3 ? 0 : 1)) * 8192 + hg * 1024 + (rt < 3 ? rt : rt - 3) * 256;
    asm volatile("" : "+s"(os), "+s"(os4), "+s"(sst));
    const int k2 = w & 1;
#pragma unroll
    for (int j = 0; j < 10; ++j) {
      const bool oth = j >= 5 || w < 2;
      const int mo[8] = {OORD};
      const int ch = j % 5 < 4 ? (2 * hg + k2) ^ (oth ? 2 * mo[4 + j % 5] : 2 * mo[j % 5]) : (hg >> 1) ^ ((oth != ((hg & 1) != 0)) ? (k2 ? 0 : 3) : 1 + k2);
      oa[j] = ob + (j % 5 < 4 ? ch : 16 + ch) * 1024;
      so_[j] = ch * (j % 5 < 4 ? 6400 : 25600);
      asm volatile("" : "+s"(oa[j]), "+s"(so_[j]));
    }
  };
#define WO_OFFS                                                                                    \
  u32 woff[5];                                                                                     \
  _Pragma("unroll") for (int i = 0; i < 5; ++i) {                                                  \
    const int c = i * 64 + lane;                                                                   \
    const int row = c / 10;                                                                        \
    woff[i] = row * 1120 + hg * 160 + c * 16 + (4 - i) * 1024;                                     \
  }
#define WO_RUN(RHO)                                                                                \
  {                                                                                                \
    const char* wq_ = (const char*)wo + (RHO) * 40960; \
    dma_m0<-4096>(woff[0], wq_, (RHO) * 5120 + 4096);                                               \
    dma_nx<-3072>(woff[1], wq_); dma_nx<-2048>(woff[2], wq_); dma_nx<-1024>(woff[3], wq_); dma_nx<0>(woff[4], wq_); \
  }

#define BAR asm volatile("s_barrier" ::: "memory");
#define PREP_V __builtin_amdgcn_sched_barrier(0); asm volatile("" : "+v"(lane_)); prepv(lane_); __builtin_amdgcn_sched_barrier(0);
#define PREP_S __builtin_amdgcn_sched_barrier(0); asm volatile("" : "+s"(mt_), "+s"(hg_)); preps(mt_, hg_); __builtin_amdgcn_sched_barrier(0);
  if (w < 2) {
    const int l = w;
    __builtin_amdgcn_s_setprio(3);
    u32 vw[4];
#pragma unroll
    for (int i = 0; i < 4; ++i) vw[i] = w_off(i, lane, hg) + 4096;
    u32 go = (u32)kb0 * 245760u + 122880u + l * 61440u;
    u32 so = HSLOT + l * 8192 + 4096;
#define W1(I) dma_nx<((I) - 4) * 1024>(vw[(I) & 3], wb + (W_ROW(I) - (I) * 1024));
#define ISSUE_W                                                                                    \
  {                                                                                                \
    const char* wb = (const char*)wqkv + go;                                                       \
    dma_m0<-4096>(vw[0], wb, so);                                                                  \
    W1(1) W1(2) W1(3) W1(4) W1(5) W1(6) W1(7)                                                      \
    go += 122880u;                                                                                 \
    if (go >= 2457600u) go -= 2457600u;                                                            \
    so = so >= 5 * HSLOT ? so - 5 * HSLOT : so + HSLOT;                                            \
  }
    __builtin_amdgcn_s_sleep(2);
    ISSUE_W
    __builtin_amdgcn_sched_barrier(0);
    WO_OFFS
    asm volatile("" : "+v"(woff[0]), "+v"(woff[1]), "+v"(woff[2]), "+v"(woff[3]), "+v"(woff[4]));
    BAR
    ISSUE_W
    ISSUE_W
    asm volatile("s_waitcnt vmcnt(16)\n\ts_barrier" ::: "memory");
    asm volatile("s_waitcnt vmcnt(8)\n\ts_barrier" ::: "memory");
#pragma unroll 1
    for (int h = 1; h < 17; ++h) {
      ISSUE_W
      asm volatile("s_waitcnt vmcnt(8)\n\ts_barrier" ::: "memory");
      if (h & 1) { LSTAMP(31 + (h >> 1)) }
    }
    asm volatile("s_waitcnt vmcnt(0)\n\ts_barrier" ::: "memory");
    LSTAMP(39)
    WO_RUN(12 + l)
    const u32 tagw = lds_ld32(CTL_OFF);
    const char* bxf = ws + WS_BXF + mt * 1024;
    BAR
    const u32 town = (u32)__builtin_amdgcn_s_memrealtime();
    if (l == 0) {
      if (lane == 0) GST2((u32)(hg * 128), bxf, 0, ((u32x2){town, tagw}));
    }
    WO_RUN(18 + l)
    WO_RUN(8 + l)
    WO_RUN(l)
    const u32 vfl = (((lane & 7) == hg ? hg ^ 1 : (lane & 7))) * 128;
    u32x2 f;
    asm volatile("" : "=v"(f));
    BAR
    int gate = (int)((u32)__builtin_amdgcn_s_memrealtime() - town) + GADD;
    gate = gate > 200 ? 200 : gate;
    WO_RUN(2 + l)
    WO_RUN(4 + l)
    WO_RUN(6 + l)
    if (l == 1) asm volatile("global_load_dwordx2 %0, %1, %2 sc1" : "+v"(f) : "v"(vfl), "s"(bxf) : "memory");
    __builtin_amdgcn_s_setprio(0);
    pm = (4 * g + q4) * 160 + p4 * 40;
    pf = (20 * g + 16 + q4) * 160 + p4 * 40;
    asm volatile("" : "=v"(bias[0]), "=v"(bias[1]), "=v"(sel[0]), "=v"(sel[1]), "=v"(sel[2]), "=v"(sel[3]), "=v"(e4), "=v"(eh),
                 "=v"(aq), "=v"(aka), "=v"(akb), "+v"(pm), "+v"(pf), "=v"(hi), "=v"(arec), "=v"(vpp));
    PREP_S
    if (l == 1) {
      asm volatile("s_waitcnt vmcnt(0)" : "+v"(f)::"memory");
#pragma nounroll
      for (int it = 0; it < SPIN_CAP; ++it) {
        if (__all(f[1] == tagw)) break;
        asm volatile("global_load_dwordx2 %0, %1, %2 sc1\n\ts_waitcnt vmcnt(0)" : "=&v"(f) : "v"(vfl), "s"(bxf) : "memory");
      }
#pragma nounroll
      for (int it = 0; it < 64; ++it) {
        if (__all((int)((u32)__builtin_amdgcn_s_memrealtime() - f[0]) >= gate)) break;
      }
    }
    __builtin_amdgcn_s_setprio(0);
  } else if (w < 4) {
    __builtin_amdgcn_s_setprio(3);
    if (w == 2) {
      u32 vx[12];
#pragma unroll
      for (int i = 0; i < 12; ++i) vx[i] = x_off(i, lane, r0) + (4 - (i % 6)) * 1024;
      int kb = kb0 == 9 ? 0 : kb0 + 1;
      u32 xo = XRING + XSLOT + 4096;
#define ISSUE_X0                                                                                   \
  {                                                                                                \
    const char* xb = (const char*)x + kb * 128;                                                    \
    dma_m0<-4096>(vx[0], xb, xo);                                                                  \
    dma_nx<-3072>(vx[1], xb); dma_nx<-2048>(vx[2], xb); dma_nx<-1024>(vx[3], xb); dma_nx<0>(vx[4], xb); dma_nx<1024>(vx[5], xb); \
  }
#define ISSUE_X1                                                                                   \
  {                                                                                                \
    const char* xb = (const char*)x + kb * 128;                                                    \
    dma_m0<-4096>(vx[6], xb, xo + 6144);                                                           \
    dma_nx<-3072>(vx[7], xb); dma_nx<-2048>(vx[8], xb); dma_nx<-1024>(vx[9], xb); dma_nx<0>(vx[10], xb); dma_nx<1024>(vx[11], xb); \
    kb = kb == 9 ? 0 : kb + 1;                                                                     \
    xo = xo >= XRING + 3 * XSLOT ? xo - 3 * XSLOT : xo + XSLOT;                                    \
  }
      ISSUE_X0
      __builtin_amdgcn_sched_barrier(0);
      WO_OFFS
      asm volatile("" : "+v"(woff[0]), "+v"(woff[1]), "+v"(woff[2]), "+v"(woff[3]), "+v"(woff[4]));
      BAR
      ISSUE_X1
      ISSUE_X0
      BAR
      ISSUE_X1
      asm volatile("s_waitcnt vmcnt(12)\n\ts_barrier" ::: "memory");
#pragma unroll 1
      for (int j = 3; j < 10; ++j) {
        ISSUE_X0
        BAR
        ISSUE_X1
        asm volatile("s_waitcnt vmcnt(12)\n\ts_barrier" ::: "memory");
      }
      const char* xq = (const char*)x + (size_t)r0 * 1280;
      const int nv = mt == 25 ? 480 : 800;
#define RES1(I) if ((I) < 7 || (I) * 64 + lane < nv) dma_m0<(((I) % 5) - 4) * 1024>(woff[(I) % 5], xq + ((I) / 5) * 40960, HIM + ((I) / 5) * 5120 + 4096);
      PREP_V
      BAR
      PREP_S
      asm volatile("s_waitcnt vmcnt(0)\n\ts_barrier" ::: "memory");
      RES1(0) RES1(1) RES1(2) RES1(3) RES1(4) RES1(5) RES1(6)
      BAR
      RES1(7) RES1(8) RES1(9) RES1(10) RES1(11) RES1(12)
      if (lane < 10) dma_m0<0>(l16, (const char*)w_r + hg * 160, WR_OFF);
      BAR
    } else {
      u32x2 ep;
      u32x4 ra;
      u32 rb5;
      if (lane < 8) {
        *(u32x4*)(smem + ONES_OFF + lane * 16) = (u32x4){0x3F803F80u, 0x3F803F80u, 0x3F803F80u, 0x3F803F80u};
        *(u32x4*)(smem + CTL_OFF + lane * 16) = (u32x4){0u, 0u, 0u, 0u};
      }
      {
        const u32 o1 = lane == 26 || lane == 32 ? 0x3F803F80u : 0u;
        const u32 o2 = lane == 31 ? 0x3F803F80u : o1;
        if (lane < 33) *(u32x4*)(smem + REC_OFF + lane * 16) = (u32x4){o1, o1, o2, o2};
      }
      WO_OFFS
      asm volatile("" : "+v"(woff[0]), "+v"(woff[1]), "+v"(woff[2]), "+v"(woff[3]), "+v"(woff[4]));
      LDS_BARRIER();
      BAR BAR
      asm volatile("global_load_dwordx2 %0, %1, off sc1" : "=v"(ep) : "v"(ws + WS_EPOCH) : "memory");
      asm volatile("global_load_dwordx4 %0, %2, %4\n\tglobal_load_dword %1, %3, %4 offset:1024"
                   : "=&v"(ra), "=&v"(rb5) : "v"(l16), "v"((u32)(lane * 4)), "s"(w_r) : "memory");
      BAR BAR BAR
      PREP_V
      BAR
      PREP_S
      BAR
      asm volatile("s_waitcnt vmcnt(0)" : "+v"(ep), "+v"(ra), "+v"(rb5)::"memory");
      const u64 e1 = (((u64)ep[1] << 32) | ep[0]) + (ep[0] == SENT - 1u ? 2ull : 1ull);
      float swr = lo_f(rb5) + hi_f(rb5);
#pragma unroll
      for (int e = 0; e < 4; ++e) swr += lo_f(ra[e]) + hi_f(ra[e]);
      swr = wave_sum(swr);
      if (lane == 0) {
        *(u32x2*)(smem + CTL_OFF) = (u32x2){(u32)e1, (u32)(e1 >> 32)};
        *(float*)(smem + CTL_OFF + 40) = swr;
      }
      int ra0 = r0 - 16 + lane, ra1 = r0 + 48 + (lane & 31);
      ra0 = ra0 < 0 ? 0 : (ra0 > SEQ - 1 ? SEQ - 1 : ra0);
      ra1 = ra1 > SEQ - 1 ? SEQ - 1 : ra1;
      const int hd = lane / 5, pt = lane - hd * 5;
      u32 avp = REC_OFF + (hd >> 1) * 192 + (pt < 4 ? pt * 48 + (hd & 1) * 8 : hd * 48 + 16), vvp = lane < 20 ? (lane >> 1) * 128 + (lane & 1) * 16 : 0;
      u32 vf = (lane < 48 ? lane : 0) * 128, va0 = ra0 * 8, va1 = ra1 * 8, vt0 = TAB_OFF + lane * 4, vt1 = TAB_OFF + 256 + (lane & 31) * 4;
      asm volatile("" : "+v"(avp), "+v"(vvp), "+v"(vf), "+v"(va0), "+v"(va1), "+v"(vt0), "+v"(vt1));
#pragma nounroll
      for (int h = 7; h <= HK0; ++h) BAR
      u32x2 f, t0, t1;
      u32x4 vp4;
      GLD2(f, vf, ws + WS_HFLAG, 0);
      GLD2(t0, va0, ws + WS_LN1, 0);
      GLD2(t1, va1, ws + WS_LN1, 0);
      asm volatile("global_load_dwordx4 %0, %1, %2 sc1" : "=v"(vp4) : "v"(vvp), "s"(ws + WS_VP + hg * 1280) : "memory");
#pragma unroll
      for (int h = HK0 + 1; h <= 17; ++h) BAR
      WO_RUN(10)
      WO_RUN(11)
      BAR
      asm volatile("s_waitcnt vmcnt(10)" : "+v"(t0), "+v"(t1), "+v"(f), "+v"(vp4)::"memory");
#pragma nounroll
      for (int it = 0; it < SPIN_CAP; ++it) {
        if (__all(t0[1] == (u32)e1 && t1[1] == (u32)e1 && f[0] == (u32)e1 && f[1] == (u32)(e1 >> 32))) break;
        asm volatile("global_load_dwordx2 %2, %5, %7 sc1\n\tglobal_load_dwordx2 %0, %3, %6 sc1\n\tglobal_load_dwordx2 %1, %4, %6 sc1\n\ts_waitcnt vmcnt(0)"
                     : "=&v"(t0), "=&v"(t1), "=&v"(f) : "v"(va0), "v"(va1), "v"(vf), "s"(ws + WS_LN1), "s"(ws + WS_HFLAG) : "memory");
      }
      *(u32*)(smem + vt0) = t0[0] << 16;
      *(u32*)(smem + vt1) = t1[0] << 16;
      *(float*)(smem + vt0 + 384) = __builtin_amdgcn_rcpf(hi_f(t0[0]));
      *(float*)(smem + vt1 + 384) = __builtin_amdgcn_rcpf(hi_f(t1[0]));
      if (lane < 20 && vp4[1] == (u32)e1 && vp4[3] == (u32)e1) *(u32x2*)(smem + avp) = (u32x2){vp4[0], vp4[2]};
      asm volatile("" ::: "memory");
      WO_RUN(14)
      WO_RUN(15)
      LDS_BARRIER();
      WO_RUN(16)
      WO_RUN(17)
    }
    __builtin_amdgcn_s_setprio(0);
  } else {
    const int cw = w - 4;
    prepv(lane);
    preps(mt, hg);
    if (cw >= 4) __builtin_amdgcn_s_setprio(1);
    const int xa0 = XRING + r16 * 128 + ((g ^ ((r16 >> 1) & 7)) * 16);
    const u32 m1 = r16 == 0 ? 0u : 1u;
    const u32 xz0 = r16 == 0 ? (u32)ONES_OFF : (u32)xa0;
    int wa[2];
#pragma unroll
    for (int c = 0; c < 2; ++c) {
      const int ct = cw < 7 ? 2 * cw + c : 14;
      const int seg = ct / 5, tt = ct - seg * 5;
      const int G = tt < 4 ? 5 * tt + p4 : 5 * p4 + 4;
      wa[c] = (16 * (g >> 1) + 4 * (g & 1) + q4) * 512 + ((seg * 160 + G * 8) ^ ((4 * (g & 1) + q4) << 5));
    }
    auto run = [&](auto tyc) __attribute__((always_inline)) {
      constexpr int TY = decltype(tyc)::value;
      constexpr int NW = TY == 2 ? 1 : 2;
      f32x4 acc[2][6];
#pragma unroll
      for (int c = 0; c < 2; ++c)
#pragma unroll
        for (int r = 0; r < 6; ++r) acc[c][r] = (f32x4){0.f, 0.f, 0.f, 0.f};
      u32x4 xA[6], xB[6];
      u32x2 wA[4], wB[4];
#define RX(D, A, O) asm volatile("ds_read_b128 %0, %1 offset:%2" : "=v"(D) : "v"(A), "n"(O) : "memory");
#define RW(D, A, O) asm volatile("ds_read_b64_tr_b16 %0, %1 offset:%2" : "=v"(D) : "v"(A), "n"(O) : "memory");
#define MM(C, R, XF, WF)                                                                           \
  if ((C) < NW) {                                                                                    \
    const bf16x8 wf_ = mk8(WF[2 * (C)][0], WF[2 * (C)][1], WF[2 * (C) + 1][0], WF[2 * (C) + 1][1]); \
    const bf16x8 xf_ = __builtin_bit_cast(bf16x8, XF[R]);                                          \
    acc[C][R] = TY == 0 ? MFMA(wf_, xf_, acc[C][R]) : MFMA(xf_, wf_, acc[C][R]);                   \
    __builtin_amdgcn_sched_barrier(0);                                                             \
  }
#define READS(QX, QW)                                                                              \
  RW(QW[0], aw0, 0) RW(QW[1], aw0, 4096) RX(QX[1], ax, 2048)                                       \
  if (NW == 2) { RW(QW[2], aw1, 0) RW(QW[3], aw1, 4096) }                                          \
  RX(QX[2], ax, 4096) RX(QX[3], ax, 6144) RX(QX[4], ax, 8192) RX(QX[5], ax, 10240) RX(QX[0], az, 0)
#define ARRIVED(PX, PW)                                                                            \
  if (NW == 2)                                                                                     \
    asm volatile("s_waitcnt lgkmcnt(0)" : "+v"(PX[0]), "+v"(PX[1]), "+v"(PX[2]), "+v"(PX[3]), "+v"(PX[4]), "+v"(PX[5]), \
                 "+v"(PW[0]), "+v"(PW[1]), "+v"(PW[2]), "+v"(PW[3])::"memory");                    \
  else                                                                                             \
    asm volatile("s_waitcnt lgkmcnt(0)" : "+v"(PX[0]), "+v"(PX[1]), "+v"(PX[2]), "+v"(PX[3]), "+v"(PX[4]), "+v"(PX[5]), \
                 "+v"(PW[0]), "+v"(PW[1])::"memory");                                              \
  __builtin_amdgcn_sched_barrier(0);
#define SB __builtin_amdgcn_sched_barrier(0);
#define WT(N, ...) asm volatile("s_waitcnt lgkmcnt(" #N ")" : __VA_ARGS__::"memory"); SB
#define HSTAGE2(PX, PW, QX, QW, NX)                                                                \
  BAR                                                                                              \
  WT(7, "+v"(PW[0]), "+v"(PW[1]), "+v"(PX[1]))                                                     \
  MM(0, 1, PX, PW) RW(QW[0], aw0, 0)                                                               \
  WT(6, "+v"(PW[2]), "+v"(PW[3]))                                                                  \
  MM(1, 1, PX, PW) RW(QW[1], aw0, 4096)                                                            \
  WT(6, "+v"(PX[2]))                                                                               \
  MM(0, 2, PX, PW) RX(QX[1], ax, 2048) SB                                                          \
  MM(1, 2, PX, PW) RW(QW[2], aw1, 0)                                                               \
  WT(7, "+v"(PX[3]))                                                                               \
  MM(0, 3, PX, PW) RW(QW[3], aw1, 4096) SB                                                         \
  MM(1, 3, PX, PW) RX(QX[2], ax, 4096)                                                             \
  WT(6, "+v"(PX[4]), "+v"(PX[5]), "+v"(PX[0]))                                                     \
  MM(0, 4, PX, PW) RX(QX[3], ax, 6144) SB                                                          \
  MM(1, 4, PX, PW) RX(QX[4], ax, 8192) SB                                                          \
  MM(0, 5, PX, PW) RX(QX[5], ax, 10240) SB                                                         \
  MM(1, 5, PX, PW) RX(QX[0], az, 0) SB                                                             \
  MM(0, 0, PX, PW)                                                                                 \
  NX asm volatile("" : "+v"(aw0), "+v"(aw1), "+v"(ax), "+v"(az), "+s"(so), "+s"(xo)); SB           \
  MM(1, 0, PX, PW)
#define HSTAGE1(PX, PW, QX, QW, NX)                                                                \
  BAR                                                                                              \
  ARRIVED(PX, PW)                                                                                  \
  MM(0, 1, PX, PW) RW(QW[0], aw0, 0) RW(QW[1], aw0, 4096) SB                                       \
  MM(0, 2, PX, PW) RX(QX[1], ax, 2048) RX(QX[2], ax, 4096) SB                                      \
  MM(0, 3, PX, PW) RX(QX[3], ax, 6144) RX(QX[4], ax, 8192) SB                                      \
  MM(0, 4, PX, PW) RX(QX[5], ax, 10240) RX(QX[0], az, 0) SB                                        \
  MM(0, 5, PX, PW)                                                                                 \
  NX asm volatile("" : "+v"(aw0), "+v"(ax), "+v"(az), "+s"(so), "+s"(xo)); SB                      \
  MM(0, 0, PX, PW)
#define HSTAGE(...) if (NW == 2) { HSTAGE2(__VA_ARGS__) } else { HSTAGE1(__VA_ARGS__) }
      u32 aw0 = wa[0], aw1 = wa[1], ax = xa0, az = xz0;
      asm volatile("s_waitcnt vmcnt(0)\n\ts_barrier" ::: "memory");
      LSTAMP(20)
      READS(xA, wA)
      aw0 += HSLOT; aw1 += HSLOT; ax ^= 64; az ^= 64;
      int so = 2 * HSLOT, xo = XSLOT;
#pragma unroll 1
      for (int s = 0; s < 9; ++s) {
        asm volatile(AL1 ::: "memory");
        HSTAGE(xA, wA, xB, wB, aw0 = wa[0] + so; aw1 = wa[1] + so; ax = xa0 + xo; az = xz0 + xo * m1;
               so = so >= 4 * HSLOT ? 0 : so + 2 * HSLOT; xo = xo >= 3 * XSLOT ? 0 : xo + XSLOT;)
        HSTAGE(xB, wB, xA, wA, aw0 += HSLOT; aw1 += HSLOT; ax ^= 64; az ^= 64;)
      }
      {
        HSTAGE(xA, wA, xB, wB, )
        ARRIVED(xB, wB)
        BAR
        SB
        LSTAMP(29)
      }
      char* ex = smem + EXO + cw * 6144 + lane * 8;
      if (TY == 0) {
        float st[6];
        const u32 ta = TAB_OFF + r16 * 4;
#define RT(R) asm volatile("ds_read_b32 %0, %1 offset:%2" : "=v"(st[R]) : "v"(ta), "n"(64 * (R)) : "memory");
        RT(1) RT(2) RT(3) RT(4) RT(5) RT(0)
        float cs[2][4];
#define FMS(A, M, CS) ((A) - (M) * (CS))
#define EP0(C, R)                                                                                  \
  {                                                                                                \
    const u32 a = cvt2((f32x2){FMS(acc[C][R][0], st[R], cs[C][0]), FMS(acc[C][R][1], st[R], cs[C][1])}); \
    const u32 b = cvt2((f32x2){FMS(acc[C][R][2], st[R], cs[C][2]), FMS(acc[C][R][3], st[R], cs[C][3])}); \
    *(u32x2*)(ex + (C) * 3072 + (R) * 512) = (u32x2){a, b};                                        \
    SB                                                                                             \
  }
        MM(0, 0, xB, wB) MM(1, 0, xB, wB) MM(0, 1, xB, wB) MM(1, 1, xB, wB)
#pragma unroll
        for (int i = 0; i < 4; ++i) cs[0][i] = dppf<0x150>(acc[0][0][i]);
        SB
        MM(0, 2, xB, wB)
#pragma unroll
        for (int i = 0; i < 4; ++i) cs[1][i] = dppf<0x150>(acc[1][0][i]);
        WT(0, "+v"(st[0]), "+v"(st[1]), "+v"(st[2]), "+v"(st[3]), "+v"(st[4]), "+v"(st[5]))
        MM(1, 2, xB, wB) EP0(0, 1)
        MM(0, 3, xB, wB) EP0(1, 1)
        MM(1, 3, xB, wB) EP0(0, 2)
        MM(0, 4, xB, wB) EP0(1, 2)
        MM(1, 4, xB, wB) EP0(0, 3)
        MM(0, 5, xB, wB) EP0(1, 3)
        MM(1, 5, xB, wB)
        if (cw >= 2) {
          EP0(1, 0)
          if (cw >= 3) EP0(0, 0)
        }
        EP0(0, 4) EP0(1, 4) EP0(0, 5) EP0(1, 5)
      } else {
        f32x4 mbv[6];
        const u32 ta = TAB_OFF + g * 16;
#define RV(R) RX(mbv[R], ta, 64 * (R))
        RV(1) RV(2) RV(3) RV(4) RV(5) RV(0)
        float cs[2];
#define CS1(C)                                                                                     \
  if ((C) < NW) {                                                                                  \
    const u32 v0 = __float_as_uint(acc[C][0][0]);                                                  \
    const u32 v1 = __builtin_amdgcn_permlane16_swap(v0, v0, false, false)[0];                      \
    cs[C] = __uint_as_float(__builtin_amdgcn_permlane32_swap(v1, v1, false, false)[0]);            \
    SB                                                                                             \
  }
#define EP1(C, R)                                                                                  \
  if ((C) < NW) {                                                                                  \
    const f32x4 v = (f32x4){FMS(acc[C][R][0], mbv[R][0], cs[C]), FMS(acc[C][R][1], mbv[R][1], cs[C]), FMS(acc[C][R][2], mbv[R][2], cs[C]), FMS(acc[C][R][3], mbv[R][3], cs[C])}; \
    *(u32x2*)(ex + (C) * 3072 + (R) * 512) = (u32x2){cvt2((f32x2){v[0], v[1]}), cvt2((f32x2){v[2], v[3]})}; \
    SB                                                                                             \
  }
        MM(0, 0, xB, wB) MM(1, 0, xB, wB) MM(0, 1, xB, wB) MM(1, 1, xB, wB)
        CS1(0)
        MM(0, 2, xB, wB)
        CS1(1)
        WT(0, "+v"(mbv[0]), "+v"(mbv[1]), "+v"(mbv[2]), "+v"(mbv[3]), "+v"(mbv[4]), "+v"(mbv[5]))
        MM(1, 2, xB, wB) EP1(0, 1)
        MM(0, 3, xB, wB) EP1(1, 1)
        MM(1, 3, xB, wB) EP1(0, 2)
        MM(0, 4, xB, wB) EP1(1, 2)
        MM(1, 4, xB, wB) EP1(0, 3)
        MM(0, 5, xB, wB) EP1(1, 3)
        MM(1, 5, xB, wB)
        EP1(0, 0) EP1(1, 0) EP1(0, 4) EP1(1, 4) EP1(0, 5) EP1(1, 5)
      }
    };
    if (cw < 5) run(icst<0>{});
    else if (cw < 7) run(icst<1>{});
    else run(icst<2>{});
  }
  __builtin_amdgcn_s_setprio(0);
  const bool live = r0 + rt * 16 < SEQ;
  if (w >= 2) { LSTAMP(2) }
  if (!live) {
    if (w >= 2) LDS_BARRIER();
    if (w != 7) {
      asm volatile("s_waitcnt vmcnt(0)" ::: "memory");
      FLUSH_STAMPS
      return;
    }
  } else if (w >= 2)
    asm volatile(AL2 ATT_ASM
                 :: [kh] "s"(kh), [l16] "v"(l16), [os] "s"(os), [os4] "s"(os4), [b0] "v"(bias[0]), [b1] "v"(bias[1]),
                    [s0] "v"(sel[0]), [s1] "v"(sel[1]), [s2] "v"(sel[2]), [s3] "v"(sel[3]), [e4] "v"(e4), [eh] "v"(eh), [aq] "v"(aq),
                    [aka] "v"(aka), [akb] "v"(akb), [arec] "v"(arec), [vpp] "v"(vpp), [sst] "s"(sst), [w] "s"(w)
                 : "memory", "s41", "s46", "s47", "s48", "s49", "s50", "s51", "scc", "v24", "v25", "v26", "v27", "v28", "v29", "v30", "v31", "v32", "v33", "v34", "v35", "v36", "v37", "v38", "v39", "v40", "v41", "v42", "v43", "v44", "v45", "v46", "v47", "v48", "v49", "v50", "v51", "v52", "v53", "v54", "v55", "v56", "v57", "v58", "v59", "v60", "v61", "v62", "v63", "v64", "v65", "v66", "v67", "v68", "v69", "v70", "v71", "v72", "v73", "v74", "v75", "v76", "v77", "v78", "v79", "v80", "v81", "v82", "v83", "v84", "v85", "v86", "v87");
  if (w >= 2) { LSTAMP(4) }
  u32x4 hwa, hwb;
  u32x2 hwc;
#define STP(I)
#define CNT0
#define CNT1
#define STPOUT
#define STPOUT2
#define STPEND
  const int grp = rt < 3 ? 0 : 1;
  if (w < 8) {
    const int rmax = mt == 25 ? 2 : 4;
    const int rk = rt < rmax ? rt : rmax, rd = (rt ^ 1) < rmax ? (rt ^ 1) : rmax;
    u32 ta, tk;
    if (w == 0) asm volatile("s_waitcnt vmcnt(0)" ::: "memory");
    if (w == 2 || w == 3) asm volatile("s_waitcnt vmcnt(3)" ::: "memory");
    asm volatile(AL3 "s_waitcnt lgkmcnt(0)\n\ts_barrier\n\t" OP_ASM STPEND
                 : "=&{v[84:87]}"(hwa), "=&{v[88:91]}"(hwb), "=&{v[92:93]}"(hwc), [ta] "=&v"(ta), [tk] "=&v"(tk) STPOUT
                 : [w] "s"(w), [l16] "v"(l16), [oa0] "s"(oa[0]), [so0] "s"(so_[0]), [oa1] "s"(oa[1]), [so1] "s"(so_[1]), [oa2] "s"(oa[2]), [so2] "s"(so_[2]), [oa3] "s"(oa[3]), [so3] "s"(so_[3]), [oa4] "s"(oa[4]), [so4] "s"(so_[4]), [oa5] "s"(oa[5]), [so5] "s"(so_[5]), [oa6] "s"(oa[6]), [so6] "s"(so_[6]), [oa7] "s"(oa[7]), [so7] "s"(so_[7]), [oa8] "s"(oa[8]), [so8] "s"(so_[8]), [oa9] "s"(oa[9]), [so9] "s"(so_[9]),
                   [pm] "v"(pm), [pf] "v"(pf), [ra] "v"(w < 3 ? (u32)(CTL_OFF + 72) : hi), [e4] "v"(e4),
                   [aw] "v"((lane & 1) ? (u32)(WR_OFF + g * 40) : (u32)ONES_OFF),
                   [vk] "v"(l16 + rk * 20480), [vd] "v"(l16 + rd * 20480), [sk] "s"((w & 1) * 6144),
                   [sd] "s"(EXO + (w < 4 ? 4 + w : rt ^ 1) * 5120), [sr] "s"(EXO + rt * 5120),
                   [sc] "s"(CTL_OFF + 48 + (w < 4 ? 4 : rt ^ 1) * 4), [sp] "s"(PST_OFF + rt * 256), [sa] "s"(CTL_OFF + 8 + grp * 4),
                   [sst] "s"(sst), [cp] "s"(ws + WS_PC + (2 * mt + grp) * 1280 + hg * 160), [oback] "n"(OBACK), [cap] "n"(SPIN_CAP)
                 : "memory", "vcc", "scc", "s43", "v24", "v25", "v26", "v27", "v28", "v29", "v30", "v31", "v32", "v33", "v34", "v35", "v36", "v37", "v38", "v39", "v40", "v41", "v42", "v43", "v44", "v45", "v46", "v47", "v48", "v49", "v50", "v51", "v52", "v53", "v54", "v55", "v56", "v57", "v58", "v59", "v60", "v61", "v62", "v63", "v64", "v65", "v66", "v67", "v68", "v69", "v70", "v71", "v72", "v73", "v74", "v75", "v76", "v77", "v78", "v79", "v80", "v81", "v82", "v83", "v94", "v95", "v96", "v97", "v98", "v99", "v100", "v101", "v102", "v103", "v104", "v105", "v106", "v107", "v108", "v109", "v110", "v111", "v112", "v113", "v114", "v115", "v116", "v117", "v118", "v119", "v120", "v121", "v122", "v123", "v124", "v125", "v126", "v127", "v128", "v129", "v130", "v131", "v132", "v133", "v134", "v135", "v136", "v137", "v138", "v139", "v140", "v141", "v142", "v143", "v144", "v145", "v146", "v147", "v148", "v149", "v150", "v151", "v152", "v153", "v154", "v155", "v156", "v157", "v158", "v159");
    if (!live || w < 3) {
      FLUSH_STAMPS
      return;
    }
  } else {
    if (w == 8 || w == 11) {
      const int cg = w == 11, gid = 2 * mt + cg;
      const char* sp = sst - hg * 1024;
      const char* sp2 = sp + (cg ? 0 : 7936);
      const u32 vq = (lane >> 3) * 1024 + (lane & 7) * 16 + (cg ? 640 : 768);
      u32x4 pq[3];
      u64 tb;
#define PLOADS "global_load_dwordx4 %0, %4, %5 sc1\n\tglobal_load_dwordx4 %1, %4, %5 offset:128 sc1\n\tglobal_load_dwordx4 %2, %4, %6 sc1\n\t"
      asm volatile(B2A "s_sleep %7\n\t" PLOADS "s_memrealtime %3\n\ts_setprio 1"
                   : "=&v"(pq[0]), "=&v"(pq[1]), "=&v"(pq[2]), "=&s"(tb) : "v"(vq), "s"(sp), "s"(sp2), "n"(PDEL1) : "memory");
      int lane2 = lane;
      asm volatile("" : "+v"(lane2));
      const int n16 = lane2 & 15;
      u32x4 sl;
      u32x2 km;
#pragma unroll
      for (int e = 0; e < 4; ++e) sl[e] = n16 == 2 * e ? 0x3F80u : (n16 == 2 * e + 1 ? 0x3F800000u : 0u);
      u32 rid = (u32)((((lane2 >> 4) & 1) * 2 + (lane2 >> 5)) * 4 + (lane2 & 3)) << 10;
      u32 rid1 = rid + (16 << 10), rid2 = rid + (cg ? 0 : 32 << 10), msk = n16 < 4 ? 0xFFFF0000u : 0u;
      u32 ah = HIM + cg * 7680 + (lane2 < 10 ? lane2 : 9) * 16;
      km[1] = *(const u32*)(smem + CTL_OFF);
      const char* kp = ws + WS_PK + gid * 6144 + hg * 768;
      const char* cp = ws + WS_PC + gid * 1280 + hg * 160;
      asm volatile("s_waitcnt lgkmcnt(0)" : "+s"(tb), "+s"(kp), "+s"(cp)::"memory");
      km[0] = ((u32)tb >> 1) & 1023u;
      asm volatile("s_setprio " PPRIO "\n\ts_waitcnt vmcnt(0)" : "+v"(pq[0]), "+v"(pq[1]), "+v"(pq[2]), "+v"(sl), "+v"(rid), "+v"(rid1), "+v"(rid2), "+v"(msk), "+v"(ah), "+v"(km)::"memory");
      LSTAMP(44)
#define PMISS __any(max(max(max(pq[0][0], pq[0][2]), max(pq[1][0], pq[1][2])), max(pq[2][0], pq[2][2])) == SENT)
      if (__builtin_expect(PMISS, 0)) {
#pragma nounroll
        for (int it = 0; it < SPIN_CAP; ++it) {
          u64 t2;
          asm volatile(PLOADS "s_waitcnt vmcnt(0)" : "=&v"(pq[0]), "=&v"(pq[1]), "=&v"(pq[2]), "=&s"(t2) : "v"(vq), "s"(sp), "s"(sp2) : "memory");
          if (!PMISS) break;
          __builtin_amdgcn_s_sleep(2);
        }
        LSTAMP(41)
      }
      u32 pkey = 0;
#pragma unroll
      for (int t = 0; t < 3; ++t) {
        const f32x4 ys = MFMA(__builtin_bit_cast(bf16x8, pq[t]), __builtin_bit_cast(bf16x8, sl), ((f32x4){0.f, 0.f, 0.f, 0.f}));
        const float y = tr_sum(ys);
        pkey = max(pkey, (mono(y * __builtin_amdgcn_rcpf(dppf<0x104>(y))) & msk) | (t == 0 ? rid : (t == 1 ? rid1 : rid2)));
      }
      pkey = max(pkey, dppu<0xB1>(pkey));
      pkey = max(pkey, dppu<0x4E>(pkey));
      pkey = max(max((u32)__builtin_amdgcn_readlane(pkey, 0), (u32)__builtin_amdgcn_readlane(pkey, 16)),
                 max((u32)__builtin_amdgcn_readlane(pkey, 32), (u32)__builtin_amdgcn_readlane(pkey, 48)));
      km[0] |= pkey;
      asm volatile("s_mov_b32 exec_lo, 0x01010101\n\ts_mov_b32 exec_hi, 0x0101\n\tglobal_store_dwordx2 %0, %1, %2 sc1\n\ts_mov_b64 exec, -1\n\ts_setprio 2" ::"v"(l16), "v"(km), "s"(kp) : "memory");
      LSTAMP(42)
      ah += ((pkey >> 10) & 63u) * 160;
      if (lane == 0) *(u32*)(smem + CTL_OFF + 24 + cg * 4) = ah;
      __builtin_amdgcn_s_setprio(0);
#pragma nounroll
      for (int it = 0; it < 64; ++it) {
        if ((int)((u32)__builtin_amdgcn_s_memrealtime() - (u32)tb) >= PSL) break;
        __builtin_amdgcn_s_sleep(2);
      }
      asm volatile("s_mov_b32 s40, %[cap]\n"
                   "1:\n\tds_read_b32 v100, %[ac]\n\tds_read_b32 v101, %[ac] offset:24\n\tds_read_b128 v[96:99], %[ah]\n\ts_sub_u32 s40, s40, 1\n\ts_cbranch_scc1 2f\n\ts_waitcnt lgkmcnt(0)\n\t"
                   "v_cmp_gt_u32 vcc, %[nf], v100\n\ts_cbranch_vccz 2f\n\ts_sleep 2\n\ts_branch 1b\n"
                   "2:\n\ts_waitcnt lgkmcnt(0)\n\tv_cmp_eq_u32 vcc, 0, v101\n\ts_cbranch_vccz 3f\n\t"
                   "s_mov_b64 exec, 0x3ff\n\tglobal_store_dwordx4 %[l16], v[96:99], %[cp] sc1\n\ts_mov_b64 exec, -1\n3:"
                   ::[cap] "n"(SPIN_CAP * 8), [ac] "v"((u32)(CTL_OFF + 8 + cg * 4)), [ah] "v"(ah), [nf] "s"(3 - cg), [l16] "v"(l16), [cp] "s"(cp)
                   : "memory", "vcc", "scc", "s40", "v96", "v97", "v98", "v99", "v100", "v101");
      LSTAMP(43)
      FLUSH_STAMPS
      return;
    }
    LDS_BARRIER();
    if (w == 10 && mt == 25) {
      FLUSH_STAMPS
      return;
    }
    const int cg = w - 9, gid = 2 * mt + cg;
    const int nr = cg ? 31 : 47;
    const int lr = lane < nr ? lane : nr;
    const int lrow = cg * 48 + lr;
    const int li = (lane & 31) < 20 ? (lane & 31) : 19;
    asm volatile(COL_ASM STPEND
                 : STPOUT2
                 : [lq] "v"((u32)(lr * 16 + hg * 1024)), [ac] "v"((u32)(CTL_OFF + 8 + cg * 4)), [nf] "s"(3 - cg),
                   [sp] "s"(ws + WS_STAT + (size_t)gid * 8192), [ao] "v"((u32)(PST_OFF + lrow * 16)), [rdm] "s"(1.0f / DM),
                   [swr] "v"(*(const float*)(smem + CTL_OFF + 40)), [msk] "s"(0xFFFFF000u), [rid] "v"(0x800u | (u32)(2047 - (r0 + lrow))),
                   [at] "v"((u32)(TAB_OFF + lrow * 8)), [zr] "v"(l16), "{v99}"(*(const u32*)(smem + CTL_OFF)),
                   [kp] "s"(ws + WS_PK + gid * 6144 + hg * 768 + 8), [ak] "v"((u32)(CTL_OFF + 16 + cg * 4)), [rb] "s"(2047 - r0 - cg * 48),
                   [ah] "v"((u32)(HIM + cg * 7680 + li * 8)), [co] "v"((u32)((lane < 32 ? 1280 : 0) + li * 8)),
                   [cp] "s"(ws + WS_CAND + gid * 2560 + hg * 160), [pdel] "n"(PDEL2), [cap] "n"(SPIN_CAP)
                 : "memory", "vcc", "scc", "s40", "s41", "s42", "s43", "s44", "s45", "s46", "s47", "v64", "v65", "v66", "v67", "v68", "v69", "v70", "v71", "v72", "v73", "v74", "v75", "v76", "v77", "v78", "v79", "v80", "v81", "v82", "v83", "v84", "v85", "v86", "v87", "v88", "v89", "v90", "v91", "v92", "v93", "v94", "v95", "v96", "v97", "v98", "v100", "v101", "v102", "v103", "v104", "v105", "v106", "v107", "v108", "v109", "v110", "v111", "v112", "v113", "v114", "v115", "v116", "v117", "v118", "v119", "v120", "v121", "v122", "v123");
    FLUSH_STAMPS
    return;
  }
  u32 vrow = ((rt * 16 + r16) * DM + 20 * g) * 2;
  const u32 hw[10] = {hwa[0], hwa[1], hwa[2], hwa[3], hwb[0], hwb[1], hwb[2], hwb[3], hwc[0], hwc[1]};
  f32x2 hf[10];
#pragma unroll
  for (int e = 0; e < 10; ++e) {
    hf[e] = unpk(hw[e]);
    asm volatile("" : "+v"(hf[e]));
  }
  const char* obase = (const char*)out + (size_t)r0 * 1280 + hg * 160;
  u32 rid = 2047 - (r0 + rt * 16 + r16), tilekey;
  asm volatile("" : "+s"(obase));
  asm volatile("" : "+v"(vrow), "+v"(rid));
  f32x2 st2;
  asm volatile("s_mov_b32 s40, %[cap]\n"
               "1:\n\ts_sleep 1\n\tds_read_b32 %[k], %[ak]\n\tds_read_b64 %[s], %[at]\n\ts_sub_u32 s40, s40, 1\n\ts_waitcnt lgkmcnt(0)\n\ts_cbranch_scc1 2f\n\t"
               "v_cmp_eq_u32 vcc, 0, %[k]\n\ts_cbranch_vccnz 1b\n"
               "2:"
               : [k] "=&v"(tilekey), [s] "=&v"(st2) : [cap] "n"(SPIN_CAP * 8), [ak] "v"((u32)(CTL_OFF + 16 + grp * 4)), [at] "v"((u32)(TAB_OFF + (rt * 16 + r16) * 8))
               : "memory", "vcc", "scc", "s40");
  LSTAMP(11)
  const bool held = (tilekey & 2047u) == rid;
  u32 ok[10];
#pragma unroll
  for (int e = 0; e < 10; ++e) ok[e] = cvt2(__builtin_elementwise_fma(hf[e] - (f32x2){st2[0], st2[0]}, (f32x2){st2[1], st2[1]}, hf[e]));
  if (OSL) __builtin_amdgcn_s_sleep(OSL);
  if (!held) {
    asm volatile("global_store_dwordx4 %0, %1, %2" OFL "\n\ts_nop 1" ::"v"(vrow), "v"((u32x4){ok[0], ok[1], ok[2], ok[3]}), "s"(obase) : "memory");
    asm volatile("global_store_dwordx4 %0, %1, %2 offset:16" OFL "\n\ts_nop 1" ::"v"(vrow), "v"((u32x4){ok[4], ok[5], ok[6], ok[7]}), "s"(obase) : "memory");
    asm volatile("global_store_dwordx2 %0, %1, %2 offset:32" OFL "\n\ts_nop 1" ::"v"(vrow), "v"((u32x2){ok[8], ok[9]}), "s"(obase) : "memory");
  }
  LSTAMP(15)
  FLUSH_STAMPS
}

extern "C" void kernel_launch(void* const* d_in, const int* in_sizes, int n_in,
                              void* d_out, int out_size, void* d_ws, size_t ws_size,
                              hipStream_t stream) {
  const u16* x = (const u16*)d_in[0];
  const u16* wqkv = (const u16*)d_in[1];
  const u16* wo = (const u16*)d_in[2];
  const u16* w_r = (const u16*)d_in[4];
  const u16* w_gate = (const u16*)d_in[5];
  const u16* w_up = (const u16*)d_in[6];
  const u16* w_down = (const u16*)d_in[7];
  (void)hipFuncSetAttribute((const void*)fused_block, hipFuncAttributeMaxDynamicSharedMemorySize, LDS_TOTAL);
  fused_block<<<256, 768, LDS_TOTAL, stream>>>(x, wqkv, wo, w_r, w_gate, w_up, w_down, (u16*)d_out, (char*)d_ws);
}
```

```cpp
#include <hip/hip_runtime.h>
#include <hip/hip_bf16.h>
#include <stdint.h>


#define SEQ 2048
#define DM 640
#define FF 1536

typedef __attribute__((ext_vector_type(8))) short bf16x8;
typedef __attribute__((ext_vector_type(4))) short s16x4;
typedef __attribute__((ext_vector_type(4))) float f32x4;
typedef __attribute__((ext_vector_type(2))) float f32x2;
typedef __attribute__((ext_vector_type(2))) __bf16 bf16x2_t;
typedef __attribute__((address_space(3))) s16x4* lds_s16x4_p;
typedef unsigned short u16;
typedef unsigned int u32;
typedef unsigned long long u64;
typedef __attribute__((ext_vector_type(4))) unsigned int u32x4;
typedef __attribute__((ext_vector_type(2))) unsigned int u32x2;

#define HK0 15
#define RGAP 6
#define CGAP 6
#define KGAP 7
#define CDEL 10
#define PDEL2 7
#define OBACK 2
#define OSL 0
#define PDELAY 220
#define FDELAY 450
#define KSTART 1050
#define PGAP 5
#define PDEL1 6
#define PSL 165
#define VDELAY 150
#define CCD 302
#define TQD 140
#define KCD 294
#define KOLD 300
#define OPP 1
#define PCD 212
#define PTHR 0.91f
#define GADD 62
#define OORD 0, 4, 5, 6, 7, 1, 2, 3
#define CODE_BYTES 35196

#define LDS_BARRIER() do { __builtin_amdgcn_sched_barrier(0); asm volatile("s_waitcnt lgkmcnt(0)\n\ts_barrier" ::: "memory"); __builtin_amdgcn_sched_barrier(0); } while (0)
__device__ __forceinline__ float bf2f(u16 v) { return __uint_as_float(((u32)v) << 16); }
__device__ __forceinline__ u16 f2bf(float f) { return __builtin_bit_cast(u16, (__bf16)f); }
__device__ __forceinline__ float rbf(float f) { return bf2f(f2bf(f)); }
__device__ __forceinline__ float lo_f(u32 w) { return __uint_as_float(w << 16); }
__device__ __forceinline__ float hi_f(u32 w) { return __uint_as_float(w & 0xFFFF0000u); }
__device__ __forceinline__ u32 cvt2(f32x2 v) { return __builtin_bit_cast(u32, __builtin_convertvector(v, bf16x2_t)); }
__device__ __forceinline__ f32x2 unpk(u32 w) { return (f32x2){lo_f(w), hi_f(w)}; }
#define NORM2(HV, MB, RD) cvt2(unpk(cvt2(unpk(HV) - (f32x2){MB, MB})) * (f32x2){RD, RD})
__device__ __forceinline__ s16x4 tr_read(const char* p) {
  return __builtin_amdgcn_ds_read_tr16_b64_v4i16((lds_s16x4_p)(p));
}
template <int V> struct icst { static constexpr int value = V; };
template <int CTRL>
__device__ __forceinline__ float dppf(float v) {
  return __builtin_bit_cast(float, __builtin_amdgcn_update_dpp(0, __builtin_bit_cast(int, v), CTRL, 0xf, 0xf, true));
}
__device__ __forceinline__ float row8_sum(float v) {
  v += dppf<0xB1>(v);
  v += dppf<0x4E>(v);
  v += dppf<0x141>(v);
  return v;
}
__device__ __forceinline__ float row16_sum(float v) {
  v = row8_sum(v);
  v += dppf<0x140>(v);
  return v;
}
__device__ __forceinline__ float xg_sum(float v) {
  u32 a = __float_as_uint(v);
  auto r = __builtin_amdgcn_permlane32_swap(a, a, false, false);
  v = __uint_as_float(r[0]) + __uint_as_float(r[1]);
  a = __float_as_uint(v);
  auto r2 = __builtin_amdgcn_permlane16_swap(a, a, false, false);
  return __uint_as_float(r2[0]) + __uint_as_float(r2[1]);
}
__device__ __forceinline__ float tr_sum(f32x4 ys) {
  auto r01 = __builtin_amdgcn_permlane32_swap(__float_as_uint(ys[0]), __float_as_uint(ys[1]), false, false);
  auto r23 = __builtin_amdgcn_permlane32_swap(__float_as_uint(ys[2]), __float_as_uint(ys[3]), false, false);
  const u32 u = __float_as_uint(__uint_as_float(r01[0]) + __uint_as_float(r01[1]));
  const u32 vv = __float_as_uint(__uint_as_float(r23[0]) + __uint_as_float(r23[1]));
  auto r2 = __builtin_amdgcn_permlane16_swap(u, vv, false, false);
  return __uint_as_float(r2[0]) + __uint_as_float(r2[1]);
}
__device__ __forceinline__ float wave_sum(float v) { return xg_sum(row16_sum(v)); }
template <int CTRL>
__device__ __forceinline__ u32 dppu(u32 v) { return (u32)__builtin_amdgcn_update_dpp(0, (int)v, CTRL, 0xf, 0xf, true); }
__device__ __forceinline__ u32 wave_max(u32 v) {
  v = max(v, dppu<0xB1>(v)); v = max(v, dppu<0x4E>(v)); v = max(v, dppu<0x141>(v)); v = max(v, dppu<0x140>(v));
  return max(max((u32)__builtin_amdgcn_readlane(v, 0), (u32)__builtin_amdgcn_readlane(v, 16)),
             max((u32)__builtin_amdgcn_readlane(v, 32), (u32)__builtin_amdgcn_readlane(v, 48)));
}
__device__ __forceinline__ u32 mono(float f) {
  const u32 b = __float_as_uint(f);
  return b ^ ((u32)((int)b >> 31) | 0x80000000u);
}
__device__ __forceinline__ float dot2b(u32 a, u32 b, float c) {
  return __builtin_amdgcn_fdot2_f32_bf16(__builtin_bit_cast(bf16x2_t, a), __builtin_bit_cast(bf16x2_t, b), c, false);
}
template <int OFF>
__device__ __forceinline__ void dma_m0(u32 voff, const char* sbase, u32 dst) {
  asm volatile("s_mov_b32 m0, %2\n\ts_nop 0\n\tglobal_load_lds_dwordx4 %0, %1 offset:%3"
               :: "v"(voff), "s"(sbase), "s"(dst), "n"(OFF) : "memory");
}
template <int OFF>
__device__ __forceinline__ void dma_nx(u32 voff, const char* sbase) {
  asm volatile("global_load_lds_dwordx4 %0, %1 offset:%2" :: "v"(voff), "s"(sbase), "n"(OFF) : "memory");
}
__device__ __forceinline__ u32 lds_ld32(u32 addr) {
  u32 v;
  asm volatile("ds_read_b32 %0, %1\n\ts_waitcnt lgkmcnt(0)" : "=v"(v) : "v"(addr) : "memory");
  return v;
}
__device__ __forceinline__ u32x4 lds_ld128(u32 addr) {
  u32x4 v;
  asm volatile("ds_read_b128 %0, %1\n\ts_waitcnt lgkmcnt(0)" : "=v"(v) : "v"(addr) : "memory");
  return v;
}
__device__ __forceinline__ bf16x8 mk8(u32 a, u32 b, u32 c, u32 d) {
  return __builtin_bit_cast(bf16x8, (u32x4){a, b, c, d});
}
__device__ __forceinline__ bf16x8 tr_frag(const char* p, int off2) {
  s16x4 b0 = tr_read(p);
  s16x4 b1 = tr_read(p + off2);
  bf16x8 r;
  r[0] = b0[0]; r[1] = b0[1]; r[2] = b0[2]; r[3] = b0[3];
  r[4] = b1[0]; r[5] = b1[1]; r[6] = b1[2]; r[7] = b1[3];
  return r;
}
#define SPIN_CAP 400000
#define MFMA(A, B, C) __builtin_amdgcn_mfma_f32_16x16x32_bf16(A, B, C, 0, 0, 0)

#define HSLOT 16384
#define XRING 98304
#define XSLOT 12288
#define LDS_TOTAL 163840
#define EXO 102400
#define HIM 148480
#define PST_OFF (EXO + 40960)
#define TAB_OFF 161280
#define ONES_OFF 162048
#define CTL_OFF 162176
#define WR_OFF 162304
#define REC_OFF 162816
#define HN_OFF 124928
#define HSL 1344
#define ASL 1056
#define TCH_OFF 162560
#define ACT_OFF 135168
#define KEY_OFF (ACT_OFF + 5120)
#define POST_OFF (KEY_OFF + 256)
#define DOWN_OFF 81920
#define WS_EPOCH 0
#define WS_HFLAG 4096
#define WS_VP 40960
#define WS_LN1 16384
#define WS_FILL 131072
#define WS_O WS_FILL
#define WS_STAT (WS_O + 2621440)
#define WS_CAND (WS_STAT + 425984)
#define WS_PART (WS_CAND + 133120)
#define WS_BXF (WS_PART + 122880)
#define WS_PC (WS_BXF + 26624)
#define WS_PART2 (WS_PC + 66560)
#define WS_PK (4u << 20)
#define SENT 0xFFFFFFFFu

#define GST4(VO, SB, O, V) asm volatile("global_store_dwordx4 %0, %1, %2 offset:%3 sc1\n\ts_nop 1" ::"v"(VO), "v"(V), "s"(SB), "n"(O) : "memory")
#define GST2(VO, SB, O, V) asm volatile("global_store_dwordx2 %0, %1, %2 offset:%3 sc1\n\ts_nop 1" ::"v"(VO), "v"(V), "s"(SB), "n"(O) : "memory")
#define GLD2(D, VO, SB, O) asm volatile("global_load_dwordx2 %0, %1, %2 offset:%3 sc1" : "=v"(D) : "v"(VO), "s"(SB), "n"(O) : "memory")

__device__ __forceinline__ void touch_line(const void* p) {
  asm volatile("s_mov_b32 m0, %1\n\ts_nop 0\n\tglobal_load_lds_dword %0, off" ::"v"(p), "s"(TCH_OFF) : "memory");
}
__device__ __forceinline__ u32 w_off(int i, int lane, int hg) {
  int cc = (lane & 31) ^ (((2 * i + (lane >> 5)) & 7) << 1);
  cc = cc > 29 ? 29 : cc;
  return (lane >> 5) * 3840 + ((cc >= 10 ? 1120 : 0) + (cc >= 20 ? 1120 : 0)) + hg * 160 + cc * 16;
}
#define W_ROW(I) (((((2 * (I)) & 0x13) | (((2 * (I)) & 4) << 1) | (((2 * (I)) & 8) >> 1))) * 3840)
__device__ __forceinline__ u32 x_off(int i, int lane, int r0) {
  const int xr = i * 8 + (lane >> 3);
  int grow = r0 - 16 + xr;
  grow = grow < 0 ? 0 : (grow > SEQ - 1 ? SEQ - 1 : grow);
  return grow * 1280 + (((lane & 7) ^ ((xr >> 1) & 7)) * 16);
}

#define ATT_ASM \
  "ds_read_b32 v28, %[aq] offset:64\n\tds_read_b32 v30, %[aka]\n\tds_read_b32 v31, %[akb]\n\tv_mov_b64 v[38:39], 0\n\tv_mov_b64 v[42:43], 0\n\t" \
  "s_lshl_b32 s41, %[kh], 5\n\ts_lshl_b64 s[46:47], 0xffff, s41\n\ts_lshl_b64 s[48:49], s[46:47], 16\n\ts_mov_b32 s50, 0xaaaaaaaa\n\t" \
  "s_mov_b32 s51, 0xaaaaaaaa\n\ts_setprio 1\n\ts_waitcnt lgkmcnt(0)\n\ts_barrier\n\tds_read_b64 v[36:37], %[eh] offset:512\n\t" \
  "ds_read_b64 v[44:45], %[eh] offset:15360\n\tds_read_b64 v[46:47], %[e4] offset:27648\n\tds_read_b64 v[48:49], %[eh] offset:15872\n\t" \
  "ds_read_b64 v[50:51], %[e4] offset:28160\n\ts_mov_b64 exec, s[46:47]\n\tds_read_b64 v[38:39], %[e4] offset:12800\n\ts_mov_b64 exec, s[48:49]\n\t" \
  "ds_read_b64 v[42:43], %[e4] offset:12800\n\ts_mov_b64 exec, -1\n\tds_read_b64 v[40:41], %[eh] offset:3584\n\t" \
  "ds_read_b64 v[52:53], %[eh] offset:18432\n\tds_read_b64 v[54:55], %[e4] offset:27648\n\tds_read_b64 v[56:57], %[eh] offset:18944\n\t" \
  "ds_read_b64 v[58:59], %[e4] offset:28160\n\tv_mul_f32 v28, 0x3ea52b5b, v28\n\tv_pk_mul_f32 v[32:33], v[28:29], v[30:31] op_sel_hi:[0,1]\n\t" \
  "s_waitcnt lgkmcnt(6)\n\tv_mfma_f32_16x16x32_bf16 v[44:47], v[44:47], v[36:39], 0\n\tds_read2st64_b64 v[60:63], %[eh] offset0:60 offset1:61\n\t" \
  "v_mfma_f32_16x16x32_bf16 v[48:51], v[48:51], v[36:39], 0\n\tds_read2st64_b64 v[64:67], %[eh] offset0:66 offset1:67\n\ts_waitcnt lgkmcnt(2)\n\t" \
  "v_mfma_f32_16x16x32_bf16 v[52:55], v[52:55], v[40:43], 0\n\tds_read2st64_b64 v[68:71], %[e4] offset0:84 offset1:85\n\t" \
  "v_mfma_f32_16x16x32_bf16 v[56:59], v[56:59], v[40:43], 0\n\ts_mov_b32 exec_lo, 0xfff0ffff\n\ts_mov_b32 exec_hi, 0xf000ff00\n\ts_nop 0\n\t" \
  "v_mov_b64 v[44:45], v[48:49]\n\ts_mov_b32 exec_lo, 0xffc0fffc\n\ts_mov_b32 exec_hi, 0xc000fc00\n\tv_mov_b64 v[46:47], v[50:51]\n\ts_nop 0\n\t" \
  "v_mov_b64 v[54:55], v[58:59]\n\ts_mov_b32 exec_lo, 0xfff0ffff\n\ts_mov_b32 exec_hi, 0xf000ff00\n\tv_mov_b64 v[52:53], v[56:57]\n\t" \
  "s_mov_b64 exec, -1\n\tv_cndmask_b32 v44, v44, v45, s[50:51]\n\tv_cndmask_b32 v46, v46, v47, s[50:51]\n\tv_cndmask_b32 v52, v52, v53, s[50:51]\n\t" \
  "v_cndmask_b32 v54, v54, v55, s[50:51]\n\tv_fma_f32 v72, v44, v32, %[b0]\n\tv_fma_f32 v73, v46, v33, %[b1]\n\tv_fma_f32 v74, v52, v32, %[b0]\n\t" \
  "v_fma_f32 v75, v54, v33, %[b1]\n\tv_max_f32 v34, v72, v73\n\tv_max_f32 v35, v74, v75\n\tds_read_b128 v[76:79], %[arec]\n\t" \
  "ds_read_b128 v[80:83], %[arec] offset:16\n\tv_permlane32_swap_b32 v34, v35\n\tv_max_f32 v34, v34, v35\n\tv_mov_b32 v35, v34\n\t" \
  "ds_read_b128 v[84:87], %[arec] offset:32\n\ts_nop 0\n\tv_permlane16_swap_b32 v34, v35\n\tv_max_f32 v34, v34, v35\n\tv_mov_b32 v35, v34\n\ts_nop 1\n\t" \
  "v_permlane32_swap_b32 v34, v35\n\tv_sub_f32 v72, v72, v34\n\tv_sub_f32 v73, v73, v34\n\tv_sub_f32 v74, v74, v35\n\tv_sub_f32 v75, v75, v35\n\t" \
  "v_exp_f32 v72, v72\n\tv_exp_f32 v73, v73\n\tv_exp_f32 v74, v74\n\tv_exp_f32 v75, v75\n\tv_add_f32 v34, v72, v73\n\tv_add_f32 v35, v74, v75\n\t" \
  "v_pk_mul_f32 v[72:73], v[72:73], v[30:31]\n\tv_pk_mul_f32 v[74:75], v[74:75], v[30:31]\n\tv_permlane32_swap_b32 v34, v35\n\t" \
  "v_add_f32 v34, v34, v35\n\tv_mov_b32 v35, v34\n\ts_nop 1\n\tv_permlane16_swap_b32 v34, v35\n\tv_add_f32 v34, v34, v35\n\tv_rcp_f32 v34, v34\n\t" \
  "s_nop 0\n\tv_mov_b32 v30, v34\n\ts_waitcnt lgkmcnt(0)\n\ts_nop 0\n\tv_permlane32_swap_b32 v34, v30\n\t" \
  "v_pk_mul_f32 v[72:73], v[34:35], v[72:73] op_sel_hi:[0,1]\n\tv_pk_mul_f32 v[74:75], v[30:31], v[74:75] op_sel_hi:[0,1]\n\t" \
  "v_cvt_pk_bf16_f32 v72, v72, v73\n\tv_cvt_pk_bf16_f32 v74, v74, v75\n\tv_perm_b32 v36, 0, v72, %[s0]\n\tv_perm_b32 v37, 0, v72, %[s1]\n\t" \
  "v_perm_b32 v38, 0, v72, %[s2]\n\tv_perm_b32 v39, 0, v72, %[s3]\n\tv_perm_b32 v40, 0, v74, %[s0]\n\tv_perm_b32 v41, 0, v74, %[s1]\n\t" \
  "v_mfma_f32_16x16x32_bf16 v[60:63], v[60:63], v[36:39], 0\n\tv_perm_b32 v42, 0, v74, %[s2]\n\tv_perm_b32 v43, 0, v74, %[s3]\n\t" \
  "v_mfma_f32_16x16x32_bf16 v[52:55], v[68:71], v[36:39], 0\n\ts_nop 0\n\tv_mfma_f32_16x16x32_bf16 v[64:67], v[64:67], v[40:43], 0\n\t" \
  "v_mfma_f32_16x16x32_bf16 v[56:59], v[68:71], v[40:43], 0\n\ts_nop 1\n\tv_cvt_pk_bf16_f32 v24, v60, v61\n\tv_cvt_pk_bf16_f32 v25, v62, v63\n\t" \
  "s_nop 0\n\tv_cvt_pk_bf16_f32 v52, v52, v53\n\tv_cvt_pk_bf16_f32 v53, v54, v55\n\tv_cvt_pk_bf16_f32 v26, v64, v65\n\t" \
  "v_cvt_pk_bf16_f32 v27, v66, v67\n\tglobal_store_dwordx4 %[l16], v[24:27], %[os] sc1\n\ts_mov_b64 exec, s[48:49]\n\tv_cvt_pk_bf16_f32 v52, v56, v57\n\t" \
  "v_cvt_pk_bf16_f32 v53, v58, v59\n\ts_or_b64 exec, s[46:47], s[48:49]\n\tglobal_store_dwordx2 %[l16], v[52:53], %[os4] sc1\n\ts_mov_b64 exec, -1\n\t" \
  "s_setprio 0\n\tv_mfma_f32_16x16x32_bf16 v[60:63], v[24:27], v[76:79], 0\n\tv_and_b32 v54, 0x7fff7fff, v52\n\tv_and_b32 v55, 0x7fff7fff, v53\n\t" \
  "v_and_b32 v64, 0x7fff7fff, v24\n\tv_and_b32 v65, 0x7fff7fff, v25\n\tv_and_b32 v66, 0x7fff7fff, v26\n\tv_and_b32 v67, 0x7fff7fff, v27\n\t" \
  "v_mfma_f32_16x16x32_bf16 v[60:63], v[52:55], v[80:83], v[60:63]\n\tds_write2st64_b64 %[eh], v[24:25], v[26:27] offset0:1 offset1:7\n\t" \
  "v_mfma_f32_16x16x32_bf16 v[60:63], v[64:67], v[84:87], v[60:63]\n\ts_mov_b32 exec_lo, 0x01010101\n\ts_mov_b32 exec_hi, 0x01010101\n\ts_nop 5\n\t" \
  "v_cvt_pk_bf16_f32 v60, v60, v61\n\tv_cvt_pk_bf16_f32 v61, v62, v63\n\tv_and_b32 v60, -2, v60\n\tglobal_store_dwordx2 %[vpp], v[60:61], %[sst] sc1\n\t" \
  "s_mov_b64 exec, -1\n\t" \
  ""
#define OP_ASM \
  "s_mov_b32 s43, %[cap]\n\ts_cmp_lt_u32 %[w], 4\n\ts_cbranch_scc1 LH_%=\n\ts_setprio 3\n\tglobal_load_dwordx4 v[124:127], %[vd], %[oa1] sc1\n\t" \
  "global_load_dwordx4 v[128:131], %[vk], %[oa1] sc1\n\tglobal_load_dwordx4 v[132:135], %[vd], %[oa2] sc1\n\t" \
  "global_load_dwordx4 v[136:139], %[vk], %[oa2] sc1\n\tv_add_u32 %[tk], %[sk], %[e4]\n\tv_add_u32 %[ta], %[so0], %[pm]\n\t" \
  "ds_read2st64_b64 v[104:107], %[tk] offset0:1 offset1:7\n\tv_xor_b32 %[tk], 512, %[tk]\n\tds_read2st64_b64 v[108:111], %[tk] offset0:1 offset1:7\n\t" \
  "ds_read_b64_tr_b16 v[64:65], %[ta] offset:0\n\tds_read_b64_tr_b16 v[66:67], %[ta] offset:3200\n\tds_read_b64_tr_b16 v[68:69], %[ta] offset:8\n\t" \
  "ds_read_b64_tr_b16 v[70:71], %[ta] offset:3208\n\tds_read_b64_tr_b16 v[72:73], %[ta] offset:16\n\tds_read_b64_tr_b16 v[74:75], %[ta] offset:3216\n\t" \
  "ds_read_b64_tr_b16 v[76:77], %[ta] offset:24\n\tds_read_b64_tr_b16 v[78:79], %[ta] offset:3224\n\tds_read_b64_tr_b16 v[80:81], %[ta] offset:32\n\t" \
  "ds_read_b64_tr_b16 v[82:83], %[ta] offset:3232\n\tds_read_b64 v[112:113], %[ra] offset:0\n\tds_read_b64 v[114:115], %[ra] offset:8\n\t" \
  "ds_read_b64 v[116:117], %[ra] offset:16\n\tds_read_b64 v[118:119], %[ra] offset:24\n\tds_read_b64 v[120:121], %[ra] offset:32\n\t" \
  "global_load_dwordx4 v[140:143], %[vd], %[oa3] sc1\n\tv_add_u32 %[ta], %[so1], %[pm]\n\ts_waitcnt lgkmcnt(5)\n\t" \
  "v_mfma_f32_16x16x32_bf16 v[44:47], v[64:67], v[108:111], 0\n\tds_read_b64_tr_b16 v[84:85], %[ta] offset:0\n\t" \
  "ds_read_b64_tr_b16 v[86:87], %[ta] offset:3200\n\tv_mfma_f32_16x16x32_bf16 v[48:51], v[68:71], v[108:111], 0\n\t" \
  "ds_read_b64_tr_b16 v[88:89], %[ta] offset:8\n\tds_read_b64_tr_b16 v[90:91], %[ta] offset:3208\n\t" \
  "v_mfma_f32_16x16x32_bf16 v[52:55], v[72:75], v[108:111], 0\n\tds_read_b64_tr_b16 v[92:93], %[ta] offset:16\n\t" \
  "ds_read_b64_tr_b16 v[94:95], %[ta] offset:3216\n\tv_mfma_f32_16x16x32_bf16 v[56:59], v[76:79], v[108:111], 0\n\t" \
  "ds_read_b64_tr_b16 v[96:97], %[ta] offset:24\n\tds_read_b64_tr_b16 v[98:99], %[ta] offset:3224\n\t" \
  "v_mfma_f32_16x16x32_bf16 v[60:63], v[80:83], v[108:111], 0\n\tds_read_b64_tr_b16 v[100:101], %[ta] offset:32\n\t" \
  "ds_read_b64_tr_b16 v[102:103], %[ta] offset:3232\n\ts_waitcnt lgkmcnt(10)\n\tv_lshlrev_b32 v24, 16, v112\n\tv_and_b32 v25, 0xffff0000, v112\n\t" \
  "v_lshlrev_b32 v26, 16, v113\n\tv_and_b32 v27, 0xffff0000, v113\n\tv_lshlrev_b32 v28, 16, v114\n\tv_and_b32 v29, 0xffff0000, v114\n\t" \
  "v_lshlrev_b32 v30, 16, v115\n\tv_and_b32 v31, 0xffff0000, v115\n\tv_mfma_f32_16x16x32_bf16 v[24:27], v[64:67], v[104:107], v[24:27]\n\t" \
  "v_lshlrev_b32 v32, 16, v116\n\tv_and_b32 v33, 0xffff0000, v116\n\tv_lshlrev_b32 v34, 16, v117\n\tv_and_b32 v35, 0xffff0000, v117\n\t" \
  "v_mfma_f32_16x16x32_bf16 v[28:31], v[68:71], v[104:107], v[28:31]\n\tv_lshlrev_b32 v36, 16, v118\n\tv_and_b32 v37, 0xffff0000, v118\n\t" \
  "v_lshlrev_b32 v38, 16, v119\n\tv_and_b32 v39, 0xffff0000, v119\n\tv_mfma_f32_16x16x32_bf16 v[32:35], v[72:75], v[104:107], v[32:35]\n\t" \
  "v_lshlrev_b32 v40, 16, v120\n\tv_and_b32 v41, 0xffff0000, v120\n\tv_lshlrev_b32 v42, 16, v121\n\tv_and_b32 v43, 0xffff0000, v121\n\t" \
  "v_mfma_f32_16x16x32_bf16 v[36:39], v[76:79], v[104:107], v[36:39]\n\ts_nop 0\n\tv_mfma_f32_16x16x32_bf16 v[40:43], v[80:83], v[104:107], v[40:43]\n\t" \
  "v_add_u32 %[ta], %[so2], %[pm]\n\tds_read_b64_tr_b16 v[104:105], %[ta] offset:0\n\tds_read_b64_tr_b16 v[106:107], %[ta] offset:3200\n\t" \
  "ds_read_b64_tr_b16 v[108:109], %[ta] offset:8\n\tds_read_b64_tr_b16 v[110:111], %[ta] offset:3208\n\t" \
  "ds_read_b64_tr_b16 v[112:113], %[ta] offset:16\n\tds_read_b64_tr_b16 v[114:115], %[ta] offset:3216\n\t" \
  "ds_read_b64_tr_b16 v[116:117], %[ta] offset:24\n\tds_read_b64_tr_b16 v[118:119], %[ta] offset:3224\n\t" \
  "ds_read_b64_tr_b16 v[120:121], %[ta] offset:32\n\tds_read_b64_tr_b16 v[122:123], %[ta] offset:3232\n\t" \
  "global_load_dwordx4 v[144:147], %[vk], %[oa3] sc1\n\tglobal_load_dwordx4 v[148:151], %[vd], %[oa4] sc1\n\ts_waitcnt vmcnt(5)\n\t" \
  "v_max_u32 %[tk], v124, v128\n\tv_cmp_eq_u32 vcc, -1, %[tk]\n\ts_cbranch_vccnz LSs1_%=\n\tLQs1_%=:\n\ts_waitcnt lgkmcnt(10)\n\t" \
  "v_mfma_f32_16x16x32_bf16 v[44:47], v[84:87], v[124:127], v[44:47]\n\tv_add_u32 %[ta], %[so3], %[pm]\n\tds_read_b64_tr_b16 v[64:65], %[ta] offset:0\n\t" \
  "v_mfma_f32_16x16x32_bf16 v[24:27], v[84:87], v[128:131], v[24:27]\n\tds_read_b64_tr_b16 v[66:67], %[ta] offset:3200\n\t" \
  "global_load_dwordx4 v[152:155], %[vk], %[oa4] sc1\n\tv_mfma_f32_16x16x32_bf16 v[48:51], v[88:91], v[124:127], v[48:51]\n\t" \
  "ds_read_b64_tr_b16 v[68:69], %[ta] offset:8\n\tv_mfma_f32_16x16x32_bf16 v[28:31], v[88:91], v[128:131], v[28:31]\n\t" \
  "ds_read_b64_tr_b16 v[70:71], %[ta] offset:3208\n\tglobal_load_dwordx4 v[156:159], %[vd], %[oa5] sc1\n\t" \
  "v_mfma_f32_16x16x32_bf16 v[52:55], v[92:95], v[124:127], v[52:55]\n\tds_read_b64_tr_b16 v[72:73], %[ta] offset:16\n\t" \
  "v_mfma_f32_16x16x32_bf16 v[32:35], v[92:95], v[128:131], v[32:35]\n\tds_read_b64_tr_b16 v[74:75], %[ta] offset:3216\n\t" \
  "v_mfma_f32_16x16x32_bf16 v[56:59], v[96:99], v[124:127], v[56:59]\n\tds_read_b64_tr_b16 v[76:77], %[ta] offset:24\n\t" \
  "v_mfma_f32_16x16x32_bf16 v[36:39], v[96:99], v[128:131], v[36:39]\n\tds_read_b64_tr_b16 v[78:79], %[ta] offset:3224\n\ts_waitcnt vmcnt(5)\n\t" \
  "v_max_u32 %[tk], v132, v136\n\tv_cmp_eq_u32 vcc, -1, %[tk]\n\tv_mfma_f32_16x16x32_bf16 v[60:63], v[100:103], v[124:127], v[60:63]\n\t" \
  "ds_read_b64_tr_b16 v[80:81], %[ta] offset:32\n\tv_mfma_f32_16x16x32_bf16 v[40:43], v[100:103], v[128:131], v[40:43]\n\t" \
  "ds_read_b64_tr_b16 v[82:83], %[ta] offset:3232\n\ts_cbranch_vccnz LSs2_%=\n\tLQs2_%=:\n\ts_waitcnt lgkmcnt(10)\n\t" \
  "v_mfma_f32_16x16x32_bf16 v[44:47], v[104:107], v[132:135], v[44:47]\n\tv_add_u32 %[ta], %[so4], %[pf]\n\t" \
  "ds_read_b64_tr_b16 v[84:85], %[ta] offset:0\n\tv_mfma_f32_16x16x32_bf16 v[24:27], v[104:107], v[136:139], v[24:27]\n\t" \
  "ds_read_b64_tr_b16 v[86:87], %[ta] offset:12800\n\tglobal_load_dwordx4 v[124:127], %[vk], %[oa5] sc1\n\t" \
  "v_mfma_f32_16x16x32_bf16 v[48:51], v[108:111], v[132:135], v[48:51]\n\tds_read_b64_tr_b16 v[88:89], %[ta] offset:8\n\t" \
  "v_mfma_f32_16x16x32_bf16 v[28:31], v[108:111], v[136:139], v[28:31]\n\tds_read_b64_tr_b16 v[90:91], %[ta] offset:12808\n\t" \
  "global_load_dwordx4 v[128:131], %[vd], %[oa6] sc1\n\tv_mfma_f32_16x16x32_bf16 v[52:55], v[112:115], v[132:135], v[52:55]\n\t" \
  "ds_read_b64_tr_b16 v[92:93], %[ta] offset:16\n\tv_mfma_f32_16x16x32_bf16 v[32:35], v[112:115], v[136:139], v[32:35]\n\t" \
  "ds_read_b64_tr_b16 v[94:95], %[ta] offset:12816\n\tv_mfma_f32_16x16x32_bf16 v[56:59], v[116:119], v[132:135], v[56:59]\n\t" \
  "ds_read_b64_tr_b16 v[96:97], %[ta] offset:24\n\tv_mfma_f32_16x16x32_bf16 v[36:39], v[116:119], v[136:139], v[36:39]\n\t" \
  "ds_read_b64_tr_b16 v[98:99], %[ta] offset:12824\n\ts_waitcnt vmcnt(5)\n\tv_max_u32 %[tk], v140, v144\n\tv_cmp_eq_u32 vcc, -1, %[tk]\n\t" \
  "v_mfma_f32_16x16x32_bf16 v[60:63], v[120:123], v[132:135], v[60:63]\n\tds_read_b64_tr_b16 v[100:101], %[ta] offset:32\n\t" \
  "v_mfma_f32_16x16x32_bf16 v[40:43], v[120:123], v[136:139], v[40:43]\n\tds_read_b64_tr_b16 v[102:103], %[ta] offset:12832\n\t" \
  "s_cbranch_vccnz LSs3_%=\n\tLQs3_%=:\n\ts_waitcnt lgkmcnt(10)\n\tv_mfma_f32_16x16x32_bf16 v[44:47], v[64:67], v[140:143], v[44:47]\n\t" \
  "v_add_u32 %[ta], %[so5], %[pm]\n\tds_read_b64_tr_b16 v[104:105], %[ta] offset:0\n\t" \
  "v_mfma_f32_16x16x32_bf16 v[24:27], v[64:67], v[144:147], v[24:27]\n\tds_read_b64_tr_b16 v[106:107], %[ta] offset:3200\n\t" \
  "global_load_dwordx4 v[132:135], %[vk], %[oa6] sc1\n\tv_mfma_f32_16x16x32_bf16 v[48:51], v[68:71], v[140:143], v[48:51]\n\t" \
  "ds_read_b64_tr_b16 v[108:109], %[ta] offset:8\n\tv_mfma_f32_16x16x32_bf16 v[28:31], v[68:71], v[144:147], v[28:31]\n\t" \
  "ds_read_b64_tr_b16 v[110:111], %[ta] offset:3208\n\tglobal_load_dwordx4 v[136:139], %[vd], %[oa7] sc1\n\t" \
  "v_mfma_f32_16x16x32_bf16 v[52:55], v[72:75], v[140:143], v[52:55]\n\tds_read_b64_tr_b16 v[112:113], %[ta] offset:16\n\t" \
  "v_mfma_f32_16x16x32_bf16 v[32:35], v[72:75], v[144:147], v[32:35]\n\tds_read_b64_tr_b16 v[114:115], %[ta] offset:3216\n\t" \
  "v_mfma_f32_16x16x32_bf16 v[56:59], v[76:79], v[140:143], v[56:59]\n\tds_read_b64_tr_b16 v[116:117], %[ta] offset:24\n\t" \
  "v_mfma_f32_16x16x32_bf16 v[36:39], v[76:79], v[144:147], v[36:39]\n\tds_read_b64_tr_b16 v[118:119], %[ta] offset:3224\n\ts_waitcnt vmcnt(5)\n\t" \
  "v_max3_u32 %[tk], v148, v150, v152\n\tv_max_u32 %[tk], %[tk], v154\n\tv_cmp_eq_u32 vcc, -1, %[tk]\n\t" \
  "v_mfma_f32_16x16x32_bf16 v[60:63], v[80:83], v[140:143], v[60:63]\n\tds_read_b64_tr_b16 v[120:121], %[ta] offset:32\n\t" \
  "v_mfma_f32_16x16x32_bf16 v[40:43], v[80:83], v[144:147], v[40:43]\n\tds_read_b64_tr_b16 v[122:123], %[ta] offset:3232\n\ts_cbranch_vccnz LSs4_%=\n\t" \
  "LQs4_%=:\n\ts_waitcnt lgkmcnt(10)\n\tv_mfma_f32_16x16x32_bf16 v[44:47], v[84:87], v[148:151], v[44:47]\n\tv_add_u32 %[ta], %[so6], %[pm]\n\t" \
  "ds_read_b64_tr_b16 v[64:65], %[ta] offset:0\n\tv_mfma_f32_16x16x32_bf16 v[24:27], v[84:87], v[152:155], v[24:27]\n\t" \
  "ds_read_b64_tr_b16 v[66:67], %[ta] offset:3200\n\tglobal_load_dwordx4 v[140:143], %[vd], %[oa8] sc1\n\t" \
  "v_mfma_f32_16x16x32_bf16 v[48:51], v[88:91], v[148:151], v[48:51]\n\tds_read_b64_tr_b16 v[68:69], %[ta] offset:8\n\t" \
  "v_mfma_f32_16x16x32_bf16 v[28:31], v[88:91], v[152:155], v[28:31]\n\tds_read_b64_tr_b16 v[70:71], %[ta] offset:3208\n\t" \
  "global_load_dwordx4 v[144:147], %[vd], %[oa9] sc1\n\tv_mfma_f32_16x16x32_bf16 v[52:55], v[92:95], v[148:151], v[52:55]\n\t" \
  "ds_read_b64_tr_b16 v[72:73], %[ta] offset:16\n\tv_mfma_f32_16x16x32_bf16 v[32:35], v[92:95], v[152:155], v[32:35]\n\t" \
  "ds_read_b64_tr_b16 v[74:75], %[ta] offset:3216\n\tv_mfma_f32_16x16x32_bf16 v[56:59], v[96:99], v[148:151], v[56:59]\n\t" \
  "ds_read_b64_tr_b16 v[76:77], %[ta] offset:24\n\tv_mfma_f32_16x16x32_bf16 v[36:39], v[96:99], v[152:155], v[36:39]\n\t" \
  "ds_read_b64_tr_b16 v[78:79], %[ta] offset:3224\n\ts_waitcnt vmcnt(5)\n\tv_max_u32 %[tk], v156, v124\n\tv_cmp_eq_u32 vcc, -1, %[tk]\n\t" \
  "v_mfma_f32_16x16x32_bf16 v[60:63], v[100:103], v[148:151], v[60:63]\n\tds_read_b64_tr_b16 v[80:81], %[ta] offset:32\n\t" \
  "v_mfma_f32_16x16x32_bf16 v[40:43], v[100:103], v[152:155], v[40:43]\n\tds_read_b64_tr_b16 v[82:83], %[ta] offset:3232\n\ts_cbranch_vccnz LSs5_%=\n\t" \
  "LQs5_%=:\n\ts_waitcnt lgkmcnt(10)\n\tv_mfma_f32_16x16x32_bf16 v[44:47], v[104:107], v[156:159], v[44:47]\n\tv_add_u32 %[ta], %[so7], %[pm]\n\t" \
  "ds_read_b64_tr_b16 v[84:85], %[ta] offset:0\n\tv_mfma_f32_16x16x32_bf16 v[24:27], v[104:107], v[124:127], v[24:27]\n\t" \
  "ds_read_b64_tr_b16 v[86:87], %[ta] offset:3200\n\tglobal_load_dwordx4 v[148:151], %[vk], %[oa7] sc1\n\t" \
  "v_mfma_f32_16x16x32_bf16 v[48:51], v[108:111], v[156:159], v[48:51]\n\tds_read_b64_tr_b16 v[88:89], %[ta] offset:8\n\t" \
  "v_mfma_f32_16x16x32_bf16 v[28:31], v[108:111], v[124:127], v[28:31]\n\tds_read_b64_tr_b16 v[90:91], %[ta] offset:3208\n\t" \
  "global_load_dwordx4 v[152:155], %[vk], %[oa8] sc1\n\tv_mfma_f32_16x16x32_bf16 v[52:55], v[112:115], v[156:159], v[52:55]\n\t" \
  "ds_read_b64_tr_b16 v[92:93], %[ta] offset:16\n\tv_mfma_f32_16x16x32_bf16 v[32:35], v[112:115], v[124:127], v[32:35]\n\t" \
  "ds_read_b64_tr_b16 v[94:95], %[ta] offset:3216\n\tv_mfma_f32_16x16x32_bf16 v[56:59], v[116:119], v[156:159], v[56:59]\n\t" \
  "ds_read_b64_tr_b16 v[96:97], %[ta] offset:24\n\tv_mfma_f32_16x16x32_bf16 v[36:39], v[116:119], v[124:127], v[36:39]\n\t" \
  "ds_read_b64_tr_b16 v[98:99], %[ta] offset:3224\n\ts_waitcnt vmcnt(5)\n\tv_max_u32 %[tk], v128, v132\n\tv_cmp_eq_u32 vcc, -1, %[tk]\n\t" \
  "v_mfma_f32_16x16x32_bf16 v[60:63], v[120:123], v[156:159], v[60:63]\n\tds_read_b64_tr_b16 v[100:101], %[ta] offset:32\n\t" \
  "v_mfma_f32_16x16x32_bf16 v[40:43], v[120:123], v[124:127], v[40:43]\n\tds_read_b64_tr_b16 v[102:103], %[ta] offset:3232\n\ts_cbranch_vccnz LSs6_%=\n\t" \
  "LQs6_%=:\n\ts_waitcnt lgkmcnt(10)\n\tv_mfma_f32_16x16x32_bf16 v[44:47], v[64:67], v[128:131], v[44:47]\n\tv_add_u32 %[ta], %[so8], %[pm]\n\t" \
  "ds_read_b64_tr_b16 v[104:105], %[ta] offset:0\n\tv_mfma_f32_16x16x32_bf16 v[24:27], v[64:67], v[132:135], v[24:27]\n\t" \
  "ds_read_b64_tr_b16 v[106:107], %[ta] offset:3200\n\tglobal_load_dwordx4 v[156:159], %[vk], %[oa9] sc1\n\t" \
  "v_mfma_f32_16x16x32_bf16 v[48:51], v[68:71], v[128:131], v[48:51]\n\tds_read_b64_tr_b16 v[108:109], %[ta] offset:8\n\t" \
  "v_mfma_f32_16x16x32_bf16 v[28:31], v[68:71], v[132:135], v[28:31]\n\tds_read_b64_tr_b16 v[110:111], %[ta] offset:3208\n\t" \
  "v_mfma_f32_16x16x32_bf16 v[52:55], v[72:75], v[128:131], v[52:55]\n\tds_read_b64_tr_b16 v[112:113], %[ta] offset:16\n\t" \
  "v_mfma_f32_16x16x32_bf16 v[32:35], v[72:75], v[132:135], v[32:35]\n\tds_read_b64_tr_b16 v[114:115], %[ta] offset:3216\n\t" \
  "v_mfma_f32_16x16x32_bf16 v[56:59], v[76:79], v[128:131], v[56:59]\n\tds_read_b64_tr_b16 v[116:117], %[ta] offset:24\n\t" \
  "v_mfma_f32_16x16x32_bf16 v[36:39], v[76:79], v[132:135], v[36:39]\n\tds_read_b64_tr_b16 v[118:119], %[ta] offset:3224\n\ts_waitcnt vmcnt(4)\n\t" \
  "v_max_u32 %[tk], v136, v140\n\tv_cmp_eq_u32 vcc, -1, %[tk]\n\tv_mfma_f32_16x16x32_bf16 v[60:63], v[80:83], v[128:131], v[60:63]\n\t" \
  "ds_read_b64_tr_b16 v[120:121], %[ta] offset:32\n\tv_mfma_f32_16x16x32_bf16 v[40:43], v[80:83], v[132:135], v[40:43]\n\t" \
  "ds_read_b64_tr_b16 v[122:123], %[ta] offset:3232\n\ts_cbranch_vccnz LSd7_%=\n\tLQd7_%=:\n\ts_waitcnt lgkmcnt(10)\n\t" \
  "v_mfma_f32_16x16x32_bf16 v[44:47], v[84:87], v[136:139], v[44:47]\n\tv_add_u32 %[ta], %[so9], %[pf]\n\tds_read_b64_tr_b16 v[64:65], %[ta] offset:0\n\t" \
  "ds_read_b64_tr_b16 v[66:67], %[ta] offset:12800\n\tv_mfma_f32_16x16x32_bf16 v[48:51], v[88:91], v[136:139], v[48:51]\n\t" \
  "ds_read_b64_tr_b16 v[68:69], %[ta] offset:8\n\tds_read_b64_tr_b16 v[70:71], %[ta] offset:12808\n\t" \
  "v_mfma_f32_16x16x32_bf16 v[52:55], v[92:95], v[136:139], v[52:55]\n\tds_read_b64_tr_b16 v[72:73], %[ta] offset:16\n\t" \
  "ds_read_b64_tr_b16 v[74:75], %[ta] offset:12816\n\tv_mfma_f32_16x16x32_bf16 v[56:59], v[96:99], v[136:139], v[56:59]\n\t" \
  "ds_read_b64_tr_b16 v[76:77], %[ta] offset:24\n\tds_read_b64_tr_b16 v[78:79], %[ta] offset:12824\n\t" \
  "v_mfma_f32_16x16x32_bf16 v[60:63], v[100:103], v[136:139], v[60:63]\n\tds_read_b64_tr_b16 v[80:81], %[ta] offset:32\n\t" \
  "ds_read_b64_tr_b16 v[82:83], %[ta] offset:12832\n\ts_waitcnt lgkmcnt(10)\n\tv_mfma_f32_16x16x32_bf16 v[44:47], v[104:107], v[140:143], v[44:47]\n\t" \
  "v_mfma_f32_16x16x32_bf16 v[48:51], v[108:111], v[140:143], v[48:51]\n\tv_mfma_f32_16x16x32_bf16 v[52:55], v[112:115], v[140:143], v[52:55]\n\t" \
  "s_waitcnt vmcnt(2)\n\tv_max3_u32 %[tk], v144, v146, v148\n\tv_cmp_eq_u32 vcc, -1, %[tk]\n\t" \
  "v_mfma_f32_16x16x32_bf16 v[56:59], v[116:119], v[140:143], v[56:59]\n\tv_mfma_f32_16x16x32_bf16 v[60:63], v[120:123], v[140:143], v[60:63]\n\t" \
  "s_cbranch_vccnz LSd9_%=\n\tLQd9_%=:\n\ts_waitcnt lgkmcnt(0)\n\tv_mfma_f32_16x16x32_bf16 v[44:47], v[64:67], v[144:147], v[44:47]\n\t" \
  "ds_read2_b64 v[124:127], %[aw] offset0:0 offset1:1\n\tds_read2_b64 v[128:131], %[aw] offset0:2 offset1:3\n\t" \
  "ds_read_b64 v[132:133], %[aw] offset:32\n\tv_mov_b64 v[134:135], 0\n\tv_mfma_f32_16x16x32_bf16 v[48:51], v[68:71], v[144:147], v[48:51]\n\t" \
  "v_mfma_f32_16x16x32_bf16 v[52:55], v[72:75], v[144:147], v[52:55]\n\tv_add_u32 %[ta], %[sd], %[l16]\n\t" \
  "v_mfma_f32_16x16x32_bf16 v[56:59], v[76:79], v[144:147], v[56:59]\n\tv_mfma_f32_16x16x32_bf16 v[60:63], v[80:83], v[144:147], v[60:63]\n\t" \
  "v_mov_b32 v137, %[sc]\n\tv_mov_b32 v136, 1\n\tv_mfma_f32_16x16x32_bf16 v[24:27], v[84:87], v[148:151], v[24:27]\n\t" \
  "ds_write_b128 %[ta], v[44:47] offset:0\n\tv_mfma_f32_16x16x32_bf16 v[28:31], v[88:91], v[148:151], v[28:31]\n\t" \
  "ds_write_b128 %[ta], v[48:51] offset:1024\n\tv_mfma_f32_16x16x32_bf16 v[32:35], v[92:95], v[148:151], v[32:35]\n\t" \
  "ds_write_b128 %[ta], v[52:55] offset:2048\n\ts_waitcnt vmcnt(0)\n\tv_max3_u32 %[tk], v152, v156, v158\n\tv_cmp_eq_u32 vcc, -1, %[tk]\n\t" \
  "v_mfma_f32_16x16x32_bf16 v[36:39], v[96:99], v[148:151], v[36:39]\n\tds_write_b128 %[ta], v[56:59] offset:3072\n\t" \
  "v_mfma_f32_16x16x32_bf16 v[40:43], v[100:103], v[148:151], v[40:43]\n\tds_write_b128 %[ta], v[60:63] offset:4096\n\ts_mov_b64 exec, 1\n\t" \
  "ds_add_u32 v137, v136\n\ts_mov_b64 exec, -1\n\ts_cbranch_vccnz LSk8_%=\n\tLQk8_%=:\n\t" \
  "v_mfma_f32_16x16x32_bf16 v[24:27], v[104:107], v[152:155], v[24:27]\n\tv_mov_b64 v[94:95], 0\n\t" \
  "v_mfma_f32_16x16x32_bf16 v[28:31], v[108:111], v[152:155], v[28:31]\n\tv_xor_b32 v96, 4, v137\n\t" \
  "v_mfma_f32_16x16x32_bf16 v[32:35], v[112:115], v[152:155], v[32:35]\n\tv_add_u32 %[tk], %[sr], %[l16]\n\t" \
  "v_mfma_f32_16x16x32_bf16 v[36:39], v[116:119], v[152:155], v[36:39]\n\tv_mfma_f32_16x16x32_bf16 v[40:43], v[120:123], v[152:155], v[40:43]\n\t" \
  "v_mfma_f32_16x16x32_bf16 v[24:27], v[64:67], v[156:159], v[24:27]\n\tds_read_b32 v97, v96\n\tds_read_b128 v[44:47], %[tk] offset:0\n\t" \
  "ds_read_b128 v[48:51], %[tk] offset:1024\n\tds_read_b128 v[52:55], %[tk] offset:2048\n\tds_read_b128 v[56:59], %[tk] offset:3072\n\t" \
  "ds_read_b128 v[60:63], %[tk] offset:4096\n\tv_mfma_f32_16x16x32_bf16 v[28:31], v[68:71], v[156:159], v[28:31]\n\t" \
  "v_mfma_f32_16x16x32_bf16 v[32:35], v[72:75], v[156:159], v[32:35]\n\tv_mfma_f32_16x16x32_bf16 v[36:39], v[76:79], v[156:159], v[36:39]\n\t" \
  "v_mfma_f32_16x16x32_bf16 v[40:43], v[80:83], v[156:159], v[40:43]\n\tLD1_%=:\n\ts_waitcnt lgkmcnt(0)\n\tv_cmp_eq_u32 vcc, 0, v97\n\t" \
  "s_cbranch_vccnz LD2_%=\n\tLT_%=:\n\tv_mov_b32 v137, %[sa]\n\tds_read_b32 v138, v137 offset:16\n\tv_add_f32 v24, v24, v44\n\t" \
  "v_add_f32 v25, v25, v45\n\tv_add_f32 v26, v26, v46\n\tv_add_f32 v27, v27, v47\n\tv_cvt_pk_bf16_f32 v84, v24, v25\n\t" \
  "v_cvt_pk_bf16_f32 v85, v26, v27\n\tv_add_f32 v28, v28, v48\n\tv_add_f32 v29, v29, v49\n\tv_add_f32 v30, v30, v50\n\tv_add_f32 v31, v31, v51\n\t" \
  "v_cvt_pk_bf16_f32 v86, v28, v29\n\tv_cvt_pk_bf16_f32 v87, v30, v31\n\tv_add_f32 v32, v32, v52\n\tv_add_f32 v33, v33, v53\n\t" \
  "v_add_f32 v34, v34, v54\n\tv_add_f32 v35, v35, v55\n\tv_cvt_pk_bf16_f32 v88, v32, v33\n\tv_cvt_pk_bf16_f32 v89, v34, v35\n\t" \
  "v_mfma_f32_16x16x32_bf16 v[100:103], v[84:87], v[84:87], 0\n\tv_mfma_f32_16x16x32_bf16 v[96:99], v[124:127], v[84:87], 0\n\t" \
  "v_add_f32 v36, v36, v56\n\tv_add_f32 v37, v37, v57\n\tv_add_f32 v38, v38, v58\n\tv_add_f32 v39, v39, v59\n\tv_cvt_pk_bf16_f32 v90, v36, v37\n\t" \
  "v_cvt_pk_bf16_f32 v91, v38, v39\n\tv_add_f32 v40, v40, v60\n\tv_add_f32 v41, v41, v61\n\tv_add_f32 v42, v42, v62\n\tv_add_f32 v43, v43, v63\n\t" \
  "v_cvt_pk_bf16_f32 v92, v40, v41\n\tv_cvt_pk_bf16_f32 v93, v42, v43\n\tv_mfma_f32_16x16x32_bf16 v[100:103], v[88:91], v[88:91], v[100:103]\n\t" \
  "v_mfma_f32_16x16x32_bf16 v[96:99], v[128:131], v[88:91], v[96:99]\n\tv_mfma_f32_16x16x32_bf16 v[100:103], v[92:95], v[92:95], v[100:103]\n\t" \
  "v_mfma_f32_16x16x32_bf16 v[96:99], v[132:135], v[92:95], v[96:99]\n\ts_waitcnt lgkmcnt(0)\n\tv_sub_u32 v138, %[ra], v138\n\t" \
  "v_cmp_gt_u32 vcc, 0xa0, v138\n\tv_and_b32 %[ta], 0xff, %[l16]\n\tv_add_u32 %[tk], %[sp], %[ta]\n\tv_mov_b32 v136, 1\n\ts_cbranch_vccz LP1_%=\n\t" \
  "s_mov_b64 exec, vcc\n\tglobal_store_dwordx4 v138, v[84:87], %[cp] sc1\n\tglobal_store_dwordx4 v138, v[88:91], %[cp] offset:16 sc1\n\t" \
  "global_store_dwordx2 v138, v[92:93], %[cp] offset:32 sc1\n\tds_write_b32 v137, v136 offset:24\n\tLP1_%=:\n\ts_mov_b32 exec_lo, 0x00200002\n\t" \
  "s_mov_b32 exec_hi, 0x20000200\n\tv_mov_b32 v100, v101\n\ts_lshl_b64 exec, exec, 1\n\tv_mov_b32 v100, v102\n\ts_lshl_b64 exec, exec, 1\n\t" \
  "v_mov_b32 v100, v103\n\ts_mov_b32 exec_lo, 0x00f0000f\n\ts_mov_b32 exec_hi, 0xf0000f00\n\tv_mov_b32 v98, v100\n\t" \
  "global_store_dwordx4 %[ta], v[96:99], %[sst] sc1\n\tds_write_b128 %[tk], v[96:99]\n\ts_mov_b64 exec, -1\n\tds_write_b64 %[ra], v[84:85] offset:0\n\t" \
  "ds_write_b64 %[ra], v[86:87] offset:8\n\tds_write_b64 %[ra], v[88:89] offset:16\n\tds_write_b64 %[ra], v[90:91] offset:24\n\t" \
  "ds_write_b64 %[ra], v[92:93] offset:32\n\ts_mov_b64 exec, 1\n\tds_add_u32 v137, v136\n\ts_mov_b64 exec, -1\n\ts_setprio 0\n\ts_branch LZ_%=\n\t" \
  "LD2_%=:\n\ts_sub_u32 s43, s43, 1\n\ts_cbranch_scc1 LT_%=\n\tds_read_b32 v97, v96\n\tds_read_b128 v[44:47], %[tk] offset:0\n\t" \
  "ds_read_b128 v[48:51], %[tk] offset:1024\n\tds_read_b128 v[52:55], %[tk] offset:2048\n\tds_read_b128 v[56:59], %[tk] offset:3072\n\t" \
  "ds_read_b128 v[60:63], %[tk] offset:4096\n\ts_branch LD1_%=\n\tLH_%=:\n\ts_sleep 3\n\tglobal_load_dwordx4 v[140:143], %[vk], %[oa1] sc1\n\t" \
  "global_load_dwordx4 v[144:147], %[vk], %[oa2] sc1\n\tglobal_load_dwordx4 v[148:151], %[vk], %[oa3] sc1\n\t" \
  "global_load_dwordx4 v[152:155], %[vk], %[oa4] sc1\n\tglobal_load_dwordx4 v[156:159], %[vk], %[oa0] sc1\n\ts_cmp_eq_u32 %[w], 3\n\t" \
  "s_cbranch_scc0 LG_%=\n\tds_read_b64 v[112:113], %[ra] offset:0\n\tds_read_b64 v[114:115], %[ra] offset:8\n\t" \
  "ds_read_b64 v[116:117], %[ra] offset:16\n\tds_read_b64 v[118:119], %[ra] offset:24\n\tds_read_b64 v[120:121], %[ra] offset:32\n\t" \
  "ds_read2_b64 v[124:127], %[aw] offset0:0 offset1:1\n\tds_read2_b64 v[128:131], %[aw] offset0:2 offset1:3\n\t" \
  "ds_read_b64 v[132:133], %[aw] offset:32\n\tv_mov_b64 v[134:135], 0\n\ts_waitcnt lgkmcnt(3)\n\tv_lshlrev_b32 v24, 16, v112\n\t" \
  "v_and_b32 v25, 0xffff0000, v112\n\tv_lshlrev_b32 v26, 16, v113\n\tv_and_b32 v27, 0xffff0000, v113\n\tv_lshlrev_b32 v28, 16, v114\n\t" \
  "v_and_b32 v29, 0xffff0000, v114\n\tv_lshlrev_b32 v30, 16, v115\n\tv_and_b32 v31, 0xffff0000, v115\n\tv_lshlrev_b32 v32, 16, v116\n\t" \
  "v_and_b32 v33, 0xffff0000, v116\n\tv_lshlrev_b32 v34, 16, v117\n\tv_and_b32 v35, 0xffff0000, v117\n\tv_lshlrev_b32 v36, 16, v118\n\t" \
  "v_and_b32 v37, 0xffff0000, v118\n\tv_lshlrev_b32 v38, 16, v119\n\tv_and_b32 v39, 0xffff0000, v119\n\tv_lshlrev_b32 v40, 16, v120\n\t" \
  "v_and_b32 v41, 0xffff0000, v120\n\tv_lshlrev_b32 v42, 16, v121\n\tv_and_b32 v43, 0xffff0000, v121\n\ts_branch LG2_%=\n\tLG_%=:\n\t" \
  "v_mov_b64 v[24:25], 0\n\tv_mov_b64 v[26:27], 0\n\tv_mov_b64 v[28:29], 0\n\tv_mov_b64 v[30:31], 0\n\tv_mov_b64 v[32:33], 0\n\tv_mov_b64 v[34:35], 0\n\t" \
  "v_mov_b64 v[36:37], 0\n\tv_mov_b64 v[38:39], 0\n\tv_mov_b64 v[40:41], 0\n\tv_mov_b64 v[42:43], 0\n\tLG2_%=:\n\tv_add_u32 %[ta], %[so1], %[pm]\n\t" \
  "ds_read_b64_tr_b16 v[64:65], %[ta] offset:0\n\tds_read_b64_tr_b16 v[66:67], %[ta] offset:3200\n\tds_read_b64_tr_b16 v[68:69], %[ta] offset:8\n\t" \
  "ds_read_b64_tr_b16 v[70:71], %[ta] offset:3208\n\tds_read_b64_tr_b16 v[72:73], %[ta] offset:16\n\tds_read_b64_tr_b16 v[74:75], %[ta] offset:3216\n\t" \
  "ds_read_b64_tr_b16 v[76:77], %[ta] offset:24\n\tds_read_b64_tr_b16 v[78:79], %[ta] offset:3224\n\tds_read_b64_tr_b16 v[80:81], %[ta] offset:32\n\t" \
  "ds_read_b64_tr_b16 v[82:83], %[ta] offset:3232\n\tv_add_u32 %[ta], %[so2], %[pm]\n\tds_read_b64_tr_b16 v[84:85], %[ta] offset:0\n\t" \
  "ds_read_b64_tr_b16 v[86:87], %[ta] offset:3200\n\tds_read_b64_tr_b16 v[88:89], %[ta] offset:8\n\tds_read_b64_tr_b16 v[90:91], %[ta] offset:3208\n\t" \
  "ds_read_b64_tr_b16 v[92:93], %[ta] offset:16\n\tds_read_b64_tr_b16 v[94:95], %[ta] offset:3216\n\tds_read_b64_tr_b16 v[96:97], %[ta] offset:24\n\t" \
  "ds_read_b64_tr_b16 v[98:99], %[ta] offset:3224\n\tds_read_b64_tr_b16 v[100:101], %[ta] offset:32\n\t" \
  "ds_read_b64_tr_b16 v[102:103], %[ta] offset:3232\n\ts_waitcnt vmcnt(4)\n\tv_cmp_eq_u32 vcc, -1, v140\n\ts_cbranch_vccnz LSh1_%=\n\tLQh1_%=:\n\t" \
  "s_waitcnt lgkmcnt(10)\n\tv_mfma_f32_16x16x32_bf16 v[24:27], v[64:67], v[140:143], v[24:27]\n\tv_add_u32 %[ta], %[so3], %[pm]\n\t" \
  "ds_read_b64_tr_b16 v[104:105], %[ta] offset:0\n\tds_read_b64_tr_b16 v[106:107], %[ta] offset:3200\n\t" \
  "v_mfma_f32_16x16x32_bf16 v[28:31], v[68:71], v[140:143], v[28:31]\n\tds_read_b64_tr_b16 v[108:109], %[ta] offset:8\n\t" \
  "ds_read_b64_tr_b16 v[110:111], %[ta] offset:3208\n\tv_mfma_f32_16x16x32_bf16 v[32:35], v[72:75], v[140:143], v[32:35]\n\t" \
  "ds_read_b64_tr_b16 v[112:113], %[ta] offset:16\n\tds_read_b64_tr_b16 v[114:115], %[ta] offset:3216\n\t" \
  "v_mfma_f32_16x16x32_bf16 v[36:39], v[76:79], v[140:143], v[36:39]\n\tds_read_b64_tr_b16 v[116:117], %[ta] offset:24\n\t" \
  "ds_read_b64_tr_b16 v[118:119], %[ta] offset:3224\n\ts_waitcnt vmcnt(3)\n\tv_cmp_eq_u32 vcc, -1, v144\n\t" \
  "v_mfma_f32_16x16x32_bf16 v[40:43], v[80:83], v[140:143], v[40:43]\n\tds_read_b64_tr_b16 v[120:121], %[ta] offset:32\n\t" \
  "ds_read_b64_tr_b16 v[122:123], %[ta] offset:3232\n\ts_cbranch_vccnz LSh2_%=\n\tLQh2_%=:\n\ts_waitcnt lgkmcnt(10)\n\t" \
  "v_mfma_f32_16x16x32_bf16 v[24:27], v[84:87], v[144:147], v[24:27]\n\tv_add_u32 %[ta], %[so4], %[pf]\n\tds_read_b64_tr_b16 v[64:65], %[ta] offset:0\n\t" \
  "ds_read_b64_tr_b16 v[66:67], %[ta] offset:12800\n\tv_mfma_f32_16x16x32_bf16 v[28:31], v[88:91], v[144:147], v[28:31]\n\t" \
  "ds_read_b64_tr_b16 v[68:69], %[ta] offset:8\n\tds_read_b64_tr_b16 v[70:71], %[ta] offset:12808\n\t" \
  "v_mfma_f32_16x16x32_bf16 v[32:35], v[92:95], v[144:147], v[32:35]\n\tds_read_b64_tr_b16 v[72:73], %[ta] offset:16\n\t" \
  "ds_read_b64_tr_b16 v[74:75], %[ta] offset:12816\n\tv_mfma_f32_16x16x32_bf16 v[36:39], v[96:99], v[144:147], v[36:39]\n\t" \
  "ds_read_b64_tr_b16 v[76:77], %[ta] offset:24\n\tds_read_b64_tr_b16 v[78:79], %[ta] offset:12824\n\ts_waitcnt vmcnt(2)\n\t" \
  "v_cmp_eq_u32 vcc, -1, v148\n\tv_mfma_f32_16x16x32_bf16 v[40:43], v[100:103], v[144:147], v[40:43]\n\tds_read_b64_tr_b16 v[80:81], %[ta] offset:32\n\t" \
  "ds_read_b64_tr_b16 v[82:83], %[ta] offset:12832\n\ts_cbranch_vccnz LSh3_%=\n\tLQh3_%=:\n\ts_waitcnt lgkmcnt(10)\n\t" \
  "v_mfma_f32_16x16x32_bf16 v[24:27], v[104:107], v[148:151], v[24:27]\n\tv_add_u32 %[ta], %[so0], %[pm]\n\t" \
  "ds_read_b64_tr_b16 v[84:85], %[ta] offset:0\n\tds_read_b64_tr_b16 v[86:87], %[ta] offset:3200\n\t" \
  "v_mfma_f32_16x16x32_bf16 v[28:31], v[108:111], v[148:151], v[28:31]\n\tds_read_b64_tr_b16 v[88:89], %[ta] offset:8\n\t" \
  "ds_read_b64_tr_b16 v[90:91], %[ta] offset:3208\n\tv_mfma_f32_16x16x32_bf16 v[32:35], v[112:115], v[148:151], v[32:35]\n\t" \
  "ds_read_b64_tr_b16 v[92:93], %[ta] offset:16\n\tds_read_b64_tr_b16 v[94:95], %[ta] offset:3216\n\t" \
  "v_mfma_f32_16x16x32_bf16 v[36:39], v[116:119], v[148:151], v[36:39]\n\tds_read_b64_tr_b16 v[96:97], %[ta] offset:24\n\t" \
  "ds_read_b64_tr_b16 v[98:99], %[ta] offset:3224\n\ts_waitcnt vmcnt(1)\n\tv_max_u32 %[tk], v152, v154\n\tv_cmp_eq_u32 vcc, -1, %[tk]\n\t" \
  "v_mfma_f32_16x16x32_bf16 v[40:43], v[120:123], v[148:151], v[40:43]\n\tds_read_b64_tr_b16 v[100:101], %[ta] offset:32\n\t" \
  "ds_read_b64_tr_b16 v[102:103], %[ta] offset:3232\n\ts_cbranch_vccnz LSh4_%=\n\tLQh4_%=:\n\ts_waitcnt lgkmcnt(10)\n\t" \
  "v_mfma_f32_16x16x32_bf16 v[24:27], v[64:67], v[152:155], v[24:27]\n\tv_mfma_f32_16x16x32_bf16 v[28:31], v[68:71], v[152:155], v[28:31]\n\t" \
  "v_mfma_f32_16x16x32_bf16 v[32:35], v[72:75], v[152:155], v[32:35]\n\tv_mfma_f32_16x16x32_bf16 v[36:39], v[76:79], v[152:155], v[36:39]\n\t" \
  "s_waitcnt vmcnt(0)\n\tv_cmp_eq_u32 vcc, -1, v156\n\tv_mfma_f32_16x16x32_bf16 v[40:43], v[80:83], v[152:155], v[40:43]\n\ts_cbranch_vccnz LSh0_%=\n\t" \
  "LQh0_%=:\n\ts_waitcnt lgkmcnt(0)\n\tv_mfma_f32_16x16x32_bf16 v[24:27], v[84:87], v[156:159], v[24:27]\n\t" \
  "v_mfma_f32_16x16x32_bf16 v[28:31], v[88:91], v[156:159], v[28:31]\n\tv_mfma_f32_16x16x32_bf16 v[32:35], v[92:95], v[156:159], v[32:35]\n\t" \
  "v_mfma_f32_16x16x32_bf16 v[36:39], v[96:99], v[156:159], v[36:39]\n\tv_mfma_f32_16x16x32_bf16 v[40:43], v[100:103], v[156:159], v[40:43]\n\t" \
  "s_cmp_eq_u32 %[w], 3\n\ts_cbranch_scc1 LF_%=\n\ts_setprio 3\n\tv_add_u32 %[ta], %[sd], %[l16]\n\tv_mov_b32 v137, %[sc]\n\tv_mov_b32 v136, 1\n\t" \
  "ds_write_b128 %[ta], v[24:27] offset:0\n\tds_write_b128 %[ta], v[28:31] offset:1024\n\tds_write_b128 %[ta], v[32:35] offset:2048\n\t" \
  "ds_write_b128 %[ta], v[36:39] offset:3072\n\tds_write_b128 %[ta], v[40:43] offset:4096\n\ts_mov_b64 exec, 1\n\tds_add_u32 v137, v136\n\t" \
  "s_mov_b64 exec, -1\n\ts_branch LZ_%=\n\tLF_%=:\n\ts_setprio 3\n\tv_mov_b64 v[94:95], 0\n\tv_mov_b32 v96, %[sc]\n\tv_add_u32 %[ta], %[sr], %[l16]\n\t" \
  "LF1_%=:\n\tds_read_b32 v97, v96\n\ts_waitcnt lgkmcnt(0)\n\tv_cmp_gt_u32 vcc, 3, v97\n\ts_cbranch_vccz LF2_%=\n\ts_sleep 1\n\ts_sub_u32 s43, s43, 1\n\t" \
  "s_cbranch_scc0 LF1_%=\n\tLF2_%=:\n\tds_read_b128 v[64:67], %[ta] offset:0\n\tds_read_b128 v[68:71], %[ta] offset:1024\n\t" \
  "ds_read_b128 v[72:75], %[ta] offset:2048\n\tds_read_b128 v[76:79], %[ta] offset:3072\n\tds_read_b128 v[80:83], %[ta] offset:4096\n\t" \
  "ds_read_b128 v[104:107], %[ta] offset:5120\n\tds_read_b128 v[108:111], %[ta] offset:6144\n\tds_read_b128 v[112:115], %[ta] offset:7168\n\t" \
  "ds_read_b128 v[116:119], %[ta] offset:8192\n\tds_read_b128 v[120:123], %[ta] offset:9216\n\tds_read_b128 v[44:47], %[ta] offset:10240\n\t" \
  "ds_read_b128 v[48:51], %[ta] offset:11264\n\tds_read_b128 v[52:55], %[ta] offset:12288\n\tds_read_b128 v[56:59], %[ta] offset:13312\n\t" \
  "ds_read_b128 v[60:63], %[ta] offset:14336\n\ts_waitcnt lgkmcnt(10)\n\tv_pk_add_f32 v[24:25], v[24:25], v[64:65]\n\t" \
  "v_pk_add_f32 v[26:27], v[26:27], v[66:67]\n\tv_pk_add_f32 v[28:29], v[28:29], v[68:69]\n\tv_pk_add_f32 v[30:31], v[30:31], v[70:71]\n\t" \
  "v_pk_add_f32 v[32:33], v[32:33], v[72:73]\n\tv_pk_add_f32 v[34:35], v[34:35], v[74:75]\n\tv_pk_add_f32 v[36:37], v[36:37], v[76:77]\n\t" \
  "v_pk_add_f32 v[38:39], v[38:39], v[78:79]\n\tv_pk_add_f32 v[40:41], v[40:41], v[80:81]\n\tv_pk_add_f32 v[42:43], v[42:43], v[82:83]\n\t" \
  "s_waitcnt lgkmcnt(5)\n\tv_pk_add_f32 v[24:25], v[24:25], v[104:105]\n\tv_pk_add_f32 v[26:27], v[26:27], v[106:107]\n\t" \
  "v_pk_add_f32 v[28:29], v[28:29], v[108:109]\n\tv_pk_add_f32 v[30:31], v[30:31], v[110:111]\n\tv_pk_add_f32 v[32:33], v[32:33], v[112:113]\n\t" \
  "v_pk_add_f32 v[34:35], v[34:35], v[114:115]\n\tv_pk_add_f32 v[36:37], v[36:37], v[116:117]\n\tv_pk_add_f32 v[38:39], v[38:39], v[118:119]\n\t" \
  "v_pk_add_f32 v[40:41], v[40:41], v[120:121]\n\tv_pk_add_f32 v[42:43], v[42:43], v[122:123]\n\ts_waitcnt lgkmcnt(0)\n\ts_branch LT_%=\n\tLSs1_%=:\n\t" \
  "s_sub_u32 s43, s43, 1\n\ts_cbranch_scc1 LQs1_%=\n\ts_sleep %[oback]\n\tglobal_load_dwordx4 v[124:127], %[vd], %[oa1] sc1\n\t" \
  "global_load_dwordx4 v[128:131], %[vk], %[oa1] sc1\n\ts_waitcnt vmcnt(0)\n\tv_max_u32 %[tk], v124, v128\n\tv_cmp_eq_u32 vcc, -1, %[tk]\n\t" \
  "s_cbranch_vccnz LSs1_%=\n\ts_branch LQs1_%=\n\tLSs2_%=:\n\ts_sub_u32 s43, s43, 1\n\ts_cbranch_scc1 LQs2_%=\n\ts_sleep %[oback]\n\t" \
  "global_load_dwordx4 v[132:135], %[vd], %[oa2] sc1\n\tglobal_load_dwordx4 v[136:139], %[vk], %[oa2] sc1\n\ts_waitcnt vmcnt(0)\n\t" \
  "v_max_u32 %[tk], v132, v136\n\tv_cmp_eq_u32 vcc, -1, %[tk]\n\ts_cbranch_vccnz LSs2_%=\n\ts_branch LQs2_%=\n\tLSs3_%=:\n\ts_sub_u32 s43, s43, 1\n\t" \
  "s_cbranch_scc1 LQs3_%=\n\ts_sleep %[oback]\n\tglobal_load_dwordx4 v[140:143], %[vd], %[oa3] sc1\n\t" \
  "global_load_dwordx4 v[144:147], %[vk], %[oa3] sc1\n\ts_waitcnt vmcnt(0)\n\tv_max_u32 %[tk], v140, v144\n\tv_cmp_eq_u32 vcc, -1, %[tk]\n\t" \
  "s_cbranch_vccnz LSs3_%=\n\ts_branch LQs3_%=\n\tLSs4_%=:\n\ts_sub_u32 s43, s43, 1\n\ts_cbranch_scc1 LQs4_%=\n\ts_sleep %[oback]\n\t" \
  "global_load_dwordx4 v[148:151], %[vd], %[oa4] sc1\n\tglobal_load_dwordx4 v[152:155], %[vk], %[oa4] sc1\n\ts_waitcnt vmcnt(0)\n\t" \
  "v_max3_u32 %[tk], v148, v150, v152\n\tv_max_u32 %[tk], %[tk], v154\n\tv_cmp_eq_u32 vcc, -1, %[tk]\n\ts_cbranch_vccnz LSs4_%=\n\ts_branch LQs4_%=\n\t" \
  "LSs5_%=:\n\ts_sub_u32 s43, s43, 1\n\ts_cbranch_scc1 LQs5_%=\n\ts_sleep %[oback]\n\tglobal_load_dwordx4 v[156:159], %[vd], %[oa5] sc1\n\t" \
  "global_load_dwordx4 v[124:127], %[vk], %[oa5] sc1\n\ts_waitcnt vmcnt(0)\n\tv_max_u32 %[tk], v156, v124\n\tv_cmp_eq_u32 vcc, -1, %[tk]\n\t" \
  "s_cbranch_vccnz LSs5_%=\n\ts_branch LQs5_%=\n\tLSs6_%=:\n\ts_sub_u32 s43, s43, 1\n\ts_cbranch_scc1 LQs6_%=\n\ts_sleep %[oback]\n\t" \
  "global_load_dwordx4 v[128:131], %[vd], %[oa6] sc1\n\tglobal_load_dwordx4 v[132:135], %[vk], %[oa6] sc1\n\ts_waitcnt vmcnt(0)\n\t" \
  "v_max_u32 %[tk], v128, v132\n\tv_cmp_eq_u32 vcc, -1, %[tk]\n\ts_cbranch_vccnz LSs6_%=\n\ts_branch LQs6_%=\n\tLSd7_%=:\n\ts_sub_u32 s43, s43, 1\n\t" \
  "s_cbranch_scc1 LQd7_%=\n\ts_sleep %[oback]\n\tglobal_load_dwordx4 v[136:139], %[vd], %[oa7] sc1\n\t" \
  "global_load_dwordx4 v[140:143], %[vd], %[oa8] sc1\n\ts_waitcnt vmcnt(0)\n\tv_max_u32 %[tk], v136, v140\n\tv_cmp_eq_u32 vcc, -1, %[tk]\n\t" \
  "s_cbranch_vccnz LSd7_%=\n\ts_branch LQd7_%=\n\tLSd9_%=:\n\ts_sub_u32 s43, s43, 1\n\ts_cbranch_scc1 LQd9_%=\n\ts_sleep %[oback]\n\t" \
  "global_load_dwordx4 v[144:147], %[vd], %[oa9] sc1\n\tglobal_load_dwordx4 v[148:151], %[vk], %[oa7] sc1\n\ts_waitcnt vmcnt(0)\n\t" \
  "v_max3_u32 %[tk], v144, v146, v148\n\tv_cmp_eq_u32 vcc, -1, %[tk]\n\ts_cbranch_vccnz LSd9_%=\n\ts_branch LQd9_%=\n\tLSk8_%=:\n\t" \
  "s_sub_u32 s43, s43, 1\n\ts_cbranch_scc1 LQk8_%=\n\ts_sleep %[oback]\n\tglobal_load_dwordx4 v[152:155], %[vk], %[oa8] sc1\n\t" \
  "global_load_dwordx4 v[156:159], %[vk], %[oa9] sc1\n\ts_waitcnt vmcnt(0)\n\tv_max3_u32 %[tk], v152, v156, v158\n\tv_cmp_eq_u32 vcc, -1, %[tk]\n\t" \
  "s_cbranch_vccnz LSk8_%=\n\ts_branch LQk8_%=\n\tLSh1_%=:\n\ts_sub_u32 s43, s43, 1\n\ts_cbranch_scc1 LQh1_%=\n\ts_sleep %[oback]\n\t" \
  "global_load_dwordx4 v[140:143], %[vk], %[oa1] sc1\n\ts_waitcnt vmcnt(0)\n\tv_cmp_eq_u32 vcc, -1, v140\n\ts_cbranch_vccnz LSh1_%=\n\t" \
  "s_branch LQh1_%=\n\tLSh2_%=:\n\ts_sub_u32 s43, s43, 1\n\ts_cbranch_scc1 LQh2_%=\n\ts_sleep %[oback]\n\t" \
  "global_load_dwordx4 v[144:147], %[vk], %[oa2] sc1\n\ts_waitcnt vmcnt(0)\n\tv_cmp_eq_u32 vcc, -1, v144\n\ts_cbranch_vccnz LSh2_%=\n\t" \
  "s_branch LQh2_%=\n\tLSh3_%=:\n\ts_sub_u32 s43, s43, 1\n\ts_cbranch_scc1 LQh3_%=\n\ts_sleep %[oback]\n\t" \
  "global_load_dwordx4 v[148:151], %[vk], %[oa3] sc1\n\ts_waitcnt vmcnt(0)\n\tv_cmp_eq_u32 vcc, -1, v148\n\ts_cbranch_vccnz LSh3_%=\n\t" \
  "s_branch LQh3_%=\n\tLSh4_%=:\n\ts_sub_u32 s43, s43, 1\n\ts_cbranch_scc1 LQh4_%=\n\ts_sleep %[oback]\n\t" \
  "global_load_dwordx4 v[152:155], %[vk], %[oa4] sc1\n\ts_waitcnt vmcnt(0)\n\tv_max_u32 %[tk], v152, v154\n\tv_cmp_eq_u32 vcc, -1, %[tk]\n\t" \
  "s_cbranch_vccnz LSh4_%=\n\ts_branch LQh4_%=\n\tLSh0_%=:\n\ts_sub_u32 s43, s43, 1\n\ts_cbranch_scc1 LQh0_%=\n\ts_sleep %[oback]\n\t" \
  "global_load_dwordx4 v[156:159], %[vk], %[oa0] sc1\n\ts_waitcnt vmcnt(0)\n\tv_cmp_eq_u32 vcc, -1, v156\n\ts_cbranch_vccnz LSh0_%=\n\t" \
  "s_branch LQh0_%=\n\tLZ_%=:\n\t" \
  ""
#define COL_ASM \
  "s_mov_b32 s43, %[cap]\n\tv_xor_b32 v101, 0x400, %[lq]\n\tv_xor_b32 v102, 0x800, %[lq]\n\tv_xor_b32 v103, 0xc00, %[lq]\n\t" \
  "v_xor_b32 v104, 0x1000, %[lq]\n\tv_xor_b32 v105, 0x1400, %[lq]\n\tv_xor_b32 v106, 0x1800, %[lq]\n\tv_xor_b32 v107, 0x1c00, %[lq]\n\tLC0_%=:\n\t" \
  "ds_read_b32 v96, %[ac]\n\ts_waitcnt lgkmcnt(0)\n\tv_cmp_gt_u32 vcc, %[nf], v96\n\ts_cbranch_vccz LC1_%=\n\ts_sleep 1\n\ts_sub_u32 s43, s43, 1\n\t" \
  "s_cbranch_scc0 LC0_%=\n\tLC1_%=:\n\ts_setprio 2\n\ts_sleep %[pdel]\n\tglobal_load_dwordx4 v[68:71], v101, %[sp] sc1\n\t" \
  "global_load_dwordx4 v[72:75], v102, %[sp] sc1\n\tglobal_load_dwordx4 v[76:79], v103, %[sp] sc1\n\tglobal_load_dwordx4 v[80:83], v104, %[sp] sc1\n\t" \
  "global_load_dwordx4 v[84:87], v105, %[sp] sc1\n\tglobal_load_dwordx4 v[88:91], v106, %[sp] sc1\n\tglobal_load_dwordx4 v[92:95], v107, %[sp] sc1\n\t" \
  "ds_read_b128 v[64:67], %[ao]\n\ts_waitcnt lgkmcnt(0)\n\tLC2_%=:\n\ts_waitcnt vmcnt(4)\n\tv_pk_add_f32 v[108:109], v[64:65], v[68:69]\n\t" \
  "v_add_f32 v110, v66, v70\n\tv_pk_add_f32 v[112:113], v[72:73], v[76:77]\n\tv_add_f32 v114, v74, v78\n\t" \
  "v_pk_add_f32 v[108:109], v[108:109], v[112:113]\n\tv_add_f32 v110, v110, v114\n\ts_waitcnt vmcnt(0)\n\tv_pk_add_f32 v[116:117], v[80:81], v[84:85]\n\t" \
  "v_add_f32 v118, v82, v86\n\tv_pk_add_f32 v[120:121], v[88:89], v[92:93]\n\tv_add_f32 v122, v90, v94\n\t" \
  "v_pk_add_f32 v[116:117], v[116:117], v[120:121]\n\tv_add_f32 v118, v118, v122\n\tv_pk_add_f32 v[108:109], v[108:109], v[116:117]\n\t" \
  "v_add_f32 v110, v110, v118\n\tv_mul_f32 v112, 0x3acccccd, v108\n\tv_add_f32 v111, v108, v109\n\tv_mul_f32 v113, v112, v112\n\t" \
  "v_cmp_u_f32 vcc, v111, v110\n\tv_fma_f32 v113, v110, %[rdm], -v113\n\ts_cbranch_vccnz LC5_%=\n\tLC3_%=:\n\tv_max_f32 v113, 0, v113\n\t" \
  "v_cvt_pk_bf16_f32 v113, 0, v113\n\tv_add_f32 v113, 0x3727c5ac, v113\n\tv_cvt_pk_bf16_f32 v113, 0, v113\n\tv_sqrt_f32 v113, v113\n\t" \
  "v_cvt_pk_bf16_f32 v112, 0, v112\n\tv_cvt_pk_bf16_f32 v113, 0, v113\n\tv_rcp_f32 v113, v113\n\tv_fma_f32 v114, -%[swr], v112, v109\n\t" \
  "v_mul_f32 v114, v114, v113\n\tv_ashrrev_i32 v115, 31, v114\n\tv_or_b32 v115, 0x80000000, v115\n\tv_xor_b32 v114, v114, v115\n\t" \
  "v_and_or_b32 v114, v114, %[msk], %[rid]\n\ts_nop 1\n\tv_max_u32_dpp v114, v114, v114 quad_perm:[1,0,3,2] row_mask:0xf bank_mask:0xf bound_ctrl:1\n\t" \
  "ds_write_b64 %[at], v[112:113]\n\ts_nop 0\n\tv_max_u32_dpp v114, v114, v114 quad_perm:[2,3,0,1] row_mask:0xf bank_mask:0xf bound_ctrl:1\n\ts_nop 1\n\t" \
  "v_max_u32_dpp v114, v114, v114 row_half_mirror row_mask:0xf bank_mask:0xf bound_ctrl:1\n\ts_nop 1\n\t" \
  "v_max_u32_dpp v114, v114, v114 row_mirror row_mask:0xf bank_mask:0xf bound_ctrl:1\n\ts_nop 0\n\tv_readlane_b32 s40, v114, 0\n\t" \
  "v_readlane_b32 s41, v114, 16\n\tv_readlane_b32 s42, v114, 32\n\ts_max_u32 s40, s40, s41\n\ts_max_u32 s40, s40, s42\n\tv_mov_b32 v98, s40\n\t" \
  "s_mov_b32 exec_lo, 0x01010101\n\ts_mov_b32 exec_hi, 0x0101\n\tglobal_store_dwordx2 %[zr], v[98:99], %[kp] sc1\n\tds_write_b32 %[ak], v98\n\t" \
  "s_mov_b64 exec, -1\n\ts_and_b32 s41, s40, 0x7ff\n\ts_sub_u32 s41, %[rb], s41\n\ts_mul_i32 s42, s41, 160\n\tv_add_u32 v115, s42, %[ah]\n\t" \
  "ds_read_b64 v[116:117], v115\n\tv_readlane_b32 s44, v112, s41\n\tv_readlane_b32 s46, v113, s41\n\ts_waitcnt lgkmcnt(0)\n\ts_mov_b32 exec_hi, 0\n\t" \
  "v_lshlrev_b32 v118, 16, v116\n\tv_and_b32 v119, 0xffff0000, v116\n\tv_lshlrev_b32 v120, 16, v117\n\tv_and_b32 v121, 0xffff0000, v117\n\t" \
  "v_pk_add_f32 v[122:123], v[118:119], s[44:45] op_sel_hi:[1,0] neg_lo:[0,1] neg_hi:[0,1]\n\t" \
  "v_pk_add_f32 v[114:115], v[120:121], s[44:45] op_sel_hi:[1,0] neg_lo:[0,1] neg_hi:[0,1]\n\t" \
  "v_pk_fma_f32 v[118:119], v[122:123], s[46:47], v[118:119] op_sel_hi:[1,0,1]\n\t" \
  "v_pk_fma_f32 v[120:121], v[114:115], s[46:47], v[120:121] op_sel_hi:[1,0,1]\n\tv_cvt_pk_bf16_f32 v116, v118, v119\n\t" \
  "v_cvt_pk_bf16_f32 v117, v120, v121\n\ts_mov_b32 exec_hi, -1\n\tglobal_store_dwordx2 %[co], v[116:117], %[cp] sc1\n\ts_branch LC9_%=\n\tLC5_%=:\n\t" \
  "s_sub_u32 s43, s43, 1\n\ts_cbranch_scc1 LC3_%=\n\ts_mov_b32 s40, 0\n\tv_max3_u32 v96, v68, v69, v70\n\tv_cmp_eq_u32 vcc, -1, v96\n\t" \
  "s_cbranch_vccz LC61_%=\n\tglobal_load_dwordx4 v[68:71], v101, %[sp] sc1\n\ts_mov_b32 s40, 1\n\tLC61_%=:\n\tv_max3_u32 v96, v72, v73, v74\n\t" \
  "v_cmp_eq_u32 vcc, -1, v96\n\ts_cbranch_vccz LC62_%=\n\tglobal_load_dwordx4 v[72:75], v102, %[sp] sc1\n\ts_mov_b32 s40, 1\n\tLC62_%=:\n\t" \
  "v_max3_u32 v96, v76, v77, v78\n\tv_cmp_eq_u32 vcc, -1, v96\n\ts_cbranch_vccz LC63_%=\n\tglobal_load_dwordx4 v[76:79], v103, %[sp] sc1\n\t" \
  "s_mov_b32 s40, 1\n\tLC63_%=:\n\tv_max3_u32 v96, v80, v81, v82\n\tv_cmp_eq_u32 vcc, -1, v96\n\ts_cbranch_vccz LC64_%=\n\t" \
  "global_load_dwordx4 v[80:83], v104, %[sp] sc1\n\ts_mov_b32 s40, 1\n\tLC64_%=:\n\tv_max3_u32 v96, v84, v85, v86\n\tv_cmp_eq_u32 vcc, -1, v96\n\t" \
  "s_cbranch_vccz LC65_%=\n\tglobal_load_dwordx4 v[84:87], v105, %[sp] sc1\n\ts_mov_b32 s40, 1\n\tLC65_%=:\n\tv_max3_u32 v96, v88, v89, v90\n\t" \
  "v_cmp_eq_u32 vcc, -1, v96\n\ts_cbranch_vccz LC66_%=\n\tglobal_load_dwordx4 v[88:91], v106, %[sp] sc1\n\ts_mov_b32 s40, 1\n\tLC66_%=:\n\t" \
  "v_max3_u32 v96, v92, v93, v94\n\tv_cmp_eq_u32 vcc, -1, v96\n\ts_cbranch_vccz LC67_%=\n\tglobal_load_dwordx4 v[92:95], v107, %[sp] sc1\n\t" \
  "s_mov_b32 s40, 1\n\tLC67_%=:\n\ts_cmp_eq_u32 s40, 0\n\ts_cbranch_scc1 LC3_%=\n\ts_waitcnt vmcnt(0)\n\ts_branch LC2_%=\n\tLC9_%=:\n\t" \
  ""
extern "C" __global__ __launch_bounds__(768, 3) void fused_block(const u16* x, const u16* wqkv, const u16* wo, const u16* w_r,
                                                   const u16* w_gate, const u16* w_up, const u16* w_down, u16* out,
                                                   char* ws) {
  extern __shared__ __attribute__((aligned(16))) char smem[];
  const int tid = threadIdx.x;
  const int lane = tid & 63;
  const int w = __builtin_amdgcn_readfirstlane(tid >> 6);
  const int g = lane >> 4, r16 = lane & 15, q4 = (lane & 15) >> 2, p4 = lane & 3;
  const int bid = blockIdx.x;
  const int xcd = bid & 7;
  const int bidx = bid >> 3;
  const u32 l16 = lane * 16;
  if (w == 11) {
    u64 pc;
    asm volatile("s_getpc_b64 %0" : "=s"(pc));
    if (lane < 10) touch_line((const void*)(pc + min((bidx * 10 + lane) * 128, CODE_BYTES)));
  }

  if (bidx >= 26) {
    const int fb = xcd * 6 + (bidx - 26);
    const u32 tstart = (u32)__builtin_amdgcn_s_memrealtime();
    const bool hurry = ((((u32)(u64)w_gate + fb * 64) >> 7) & 7u) == 3u;
#define WAIT_UNTIL(T)                                                                      \
  _Pragma("nounroll") for (int it = 0; it < 1000; ++it) {                                  \
    if ((int)((u32)__builtin_amdgcn_s_memrealtime() - tstart) >= (T)) break;               \
    __builtin_amdgcn_s_sleep(4);                                                           \
  }
    if (w < 3) {
      int li = (bidx - 26) * 160 + w * 64 + lane;
      if (w < 2 || lane < 32) touch_line((const char*)x + (size_t)(((xcd >> 2) * 13 + 6) * 80 - 16) * 1280 + li * 128);
    }
    u32x2 ep;
    u32 tag0;
    asm volatile("global_load_dwordx2 %0, %1, off sc1" : "=v"(ep) : "v"(ws + WS_EPOCH) : "memory");
    {
      int wi = (((xcd >> 1) & 1) * 6 + (bidx - 26)) * 12 + w;
      wi = wi > 129 ? 129 : wi;
      int row = (xcd >> 2) * 1040 + (xcd & 1) * 520 + wi * 4 + g;
      row = row > SEQ - 1 ? SEQ - 1 : row;
      const u32 vo = row * 1280 + r16 * 16;
      u32x4 xr[5];
#pragma unroll
      for (int i = 0; i < 5; ++i)
        asm volatile("global_load_dwordx4 %0, %1, %2 offset:%3" : "=v"(xr[i]) : "v"(vo), "s"(x), "n"(i * 256) : "memory");
      asm volatile("s_waitcnt vmcnt(0)" ::: "memory");
      asm volatile("" : "+v"(xr[0]), "+v"(xr[1]), "+v"(xr[2]), "+v"(xr[3]), "+v"(xr[4]), "+v"(ep)::"memory");
      tag0 = ep[0] + (ep[0] == SENT - 1u ? 2u : 1u);
      float sm = 0.f;
#pragma unroll
      for (int i = 0; i < 5; ++i)
#pragma unroll
        for (int e = 0; e < 4; ++e) sm = dot2b(xr[i][e], 0x3F803F80u, sm);
      const float mean = row16_sum(sm) * (1.0f / DM);
      float ss = 0.f;
#pragma unroll
      for (int i = 0; i < 5; ++i)
#pragma unroll
        for (int e = 0; e < 4; ++e) {
          const float a = lo_f(xr[i][e]) - mean, b = hi_f(xr[i][e]) - mean;
          ss = __builtin_fmaf(a, a, ss);
          ss = __builtin_fmaf(b, b, ss);
        }
      const float var = row16_sum(ss) * (1.0f / DM);
      const float den = rbf(__builtin_amdgcn_sqrtf(rbf(rbf(var) + 1e-5f)));
      if (r16 == 0) GST2((u32)(row * 8), ws + WS_LN1, 0, ((u32x2){cvt2((f32x2){mean, den}), tag0}));
    }
    u32x4 va[2][5], vb[5];
    const int vrk = (fb >> 4) * 14 + (int)(((((u32)(u64)w_gate + fb * 64) >> 6) - 8u) & 15u);
    const int vq0 = vrk * 4 + w * 2;
    u32x4 ra;
    u32 rb5;
    asm volatile("global_load_dwordx4 %0, %2, %4\n\tglobal_load_dword %1, %3, %4 offset:1024"
                   : "=&v"(ra), "=&v"(rb5) : "v"(l16), "v"((u32)(lane * 4)), "s"(w_r) : "memory");
    if (w >= 2) {
      WAIT_UNTIL(hurry ? 0 : PDELAY + 12 * (w - 2))
      const u32x4 s4 = (u32x4){SENT, SENT, SENT, SENT};
      const char* fp = ws + WS_FILL + (size_t)fb * 73728 + (w - 2) * 7168 + 4096;
      GST4(l16, fp, -4096, s4); GST4(l16, fp, -3072, s4); GST4(l16, fp, -2048, s4); GST4(l16, fp, -1024, s4);
      GST4(l16, fp, 0, s4); GST4(l16, fp, 1024, s4); GST4(l16, fp, 2048, s4);
      if (w < 4) GST4(l16, ws + WS_FILL + (size_t)fb * 73728 + 69632 + w * 1024, 0, s4);
      if (w >= 5) GST4((u32)(((w - 5) * 8 + (lane >> 3)) * 6144 + (lane & 7) * 16), ws + WS_PK + fb * 128, 0, s4);
    }
    if (w == 0) {
      const u64 e1 = (((u64)ep[1] << 32) | ep[0]) + (ep[0] == SENT - 1u ? 2ull : 1ull);
      if (lane == 0) *(u32x4*)(smem + CTL_OFF) = (u32x4){(u32)e1, (u32)(e1 >> 32), 0u, 0u};
      if (lane >= 1 && lane < 8) *(u32x4*)(smem + CTL_OFF + lane * 16) = (u32x4){0u, 0u, 0u, 0u};
      if (lane >= 32) *(u32x4*)(smem + KEY_OFF - 512 + lane * 16) = (u32x4){0u, 0u, 0u, 0u};
    }
    if (w >= 6) {
      const u32 o1 = tid >= 636 ? 0x3F803F80u : 0u;
      if (tid < 720) *(u32x4*)(smem + HN_OFF + (tid - 384) * 16) = (u32x4){o1, o1, o1, o1};
    }
    if (w < 2 && !hurry && vrk < 40) {
      WAIT_UNTIL(VDELAY)
      const u32 vo = (vq0 * 4 + g) * 1280 + r16 * 16;
#pragma unroll
      for (int i = 0; i < 5; ++i)
        asm volatile("global_load_dwordx4 %0, %3, %4 offset:%8\n\tglobal_load_dwordx4 %1, %3, %5 offset:%8\n\tglobal_load_dwordx4 %2, %6, %7 offset:%8"
                     : "=&v"(va[0][i]), "=&v"(va[1][i]), "=&v"(vb[i]) : "v"(vo), "s"(wo), "s"((const char*)wo + 5120), "v"((u32)(r16 * 16)), "s"(w_r), "n"(i * 256) : "memory");
      asm volatile("s_waitcnt vmcnt(0)" : "+v"(va[0][0]), "+v"(va[0][1]), "+v"(va[0][2]), "+v"(va[0][3]), "+v"(va[0][4]), "+v"(va[1][0]), "+v"(va[1][1]),
                   "+v"(va[1][2]), "+v"(va[1][3]), "+v"(va[1][4]), "+v"(vb[0]), "+v"(vb[1]), "+v"(vb[2]), "+v"(vb[3]), "+v"(vb[4])::"memory");
      float sb = 0.f;
#pragma unroll
      for (int i = 0; i < 20; ++i) sb = dot2b(vb[i >> 2][i & 3], 0x3F803F80u, sb);
      sb = row16_sum(sb) * (1.0f / DM);
      u32x4 pk2[2];
#pragma unroll
      for (int k = 0; k < 2; ++k) {
        float d = 0.f, sa = 0.f;
#pragma unroll
        for (int i = 0; i < 20; ++i) {
          d = dot2b(va[k][i >> 2][i & 3], vb[i >> 2][i & 3], d);
          sa = dot2b(va[k][i >> 2][i & 3], 0x3F803F80u, sa);
        }
        const u32 vp = __float_as_uint(row16_sum(d - sa * sb));
        pk2[k] = (u32x4){cvt2((f32x2){__uint_as_float(__builtin_amdgcn_readlane(vp, 0)), __uint_as_float(__builtin_amdgcn_readlane(vp, 16))}), tag0,
                          cvt2((f32x2){__uint_as_float(__builtin_amdgcn_readlane(vp, 32)), __uint_as_float(__builtin_amdgcn_readlane(vp, 48))}), tag0};
      }
      if (lane < 8) GST4(l16, ws + WS_VP + vq0 * 64, 0, ((lane & 1) ? pk2[1] : pk2[0]));
    } else
      asm volatile("s_waitcnt vmcnt(0)" ::: "memory");
    asm volatile("" : "+v"(ra), "+v"(rb5)::"memory");
    LDS_BARRIER();
    const u32x2 tg = *(const u32x2*)(smem + CTL_OFF);
    const u32 tag = tg[0];
    if (w == 2 && lane == 0) GST2(0u, ws + WS_HFLAG + fb * 128, 0, tg);
    if (w >= 3) {
      int li = (bidx - 26) * 534 + (w - 3) * 64 + lane;
      li = li > 3199 ? 3199 : li;
      const int rr = li / 5;
      if (w < 11 || lane < 22) touch_line((const char*)wo + rr * 1280 + ((xcd >> 1) & 1) * 640 + (li - rr * 5) * 128);
    }
    WAIT_UNTIL(hurry ? 0 : FDELAY)
    asm volatile("s_barrier" ::: "memory");
    if (w >= 2) {
      const int ch = (lane & 7) ^ (((lane >> 4) & 1) * 2 + ((w + (OPP ? fb : 0)) & 1) * 4);
      const u32 vg = (lane >> 3) * (FF * 2) + fb * 64 + (ch & 3) * 16;
      const u64 gsel = (ch & 4) ? (u64)w_up : (u64)w_gate;
#pragma nounroll
      for (int n0 = w - 2; n0 < 80; n0 += 10) {
        const int n = (OPP && (fb & 1)) ? 79 - n0 : n0;
        const u64 p = gsel + vg + (u32)(n * 8 * FF * 2);
        asm volatile("s_mov_b32 m0, %1\n\ts_nop 0\n\tglobal_load_lds_dwordx4 %0, off" ::"v"(p), "s"(n * 1024) : "memory");
      }
#pragma nounroll
      for (int n = 11 - w; n < 40; n += 10) {
        const u32 row = (u32)(n * 64 + lane) / 80u;
        asm volatile("s_mov_b32 m0, %2\n\ts_nop 0\n\tglobal_load_lds_dwordx4 %0, %1" " nt"
                     ::"v"(l16 ^ ((row & 3u) * 32u + (row & 8u) * 16u)), "s"((const char*)w_down + (size_t)fb * 40960 + n * 1024), "s"(DOWN_OFF + n * 1024) : "memory");
      }
      asm volatile("s_waitcnt vmcnt(0)" ::: "memory");
      if (lane == 0) asm volatile("ds_add_u32 %0, %1" ::"v"(CTL_OFF + 48), "v"(1u) : "memory");
    } else {
      if (w == 1 && lane < 40) touch_line((const char*)out + lane * 65536 + fb * 1280);
      WAIT_UNTIL(KSTART)
    }
    if (w >= 4) {
      const int v = w - 4, kq = v & 3, m = v >> 2;
#pragma nounroll
      for (int it = 0; it < SPIN_CAP; ++it) {
        if (__builtin_amdgcn_readfirstlane(lds_ld32(CTL_OFF + 48)) >= 10u) break;
        __builtin_amdgcn_s_sleep(1);
      }
      bf16x8 gfr[5], ufr[5], dfr[5];
      {
        const int gx = (160 * kq + 8 * g + q4) * 128 + ((m ^ (q4 >> 1) ^ ((g & 1) * 2)) * 32) + p4 * 8;
        const char* db = smem + DOWN_OFF + (8 * g + q4) * 1280 + p4 * 8;
#pragma unroll
        for (int j = 0; j < 5; ++j) {
          gfr[j] = tr_frag(smem + gx + j * 4096, 512);
          ufr[j] = tr_frag(smem + (gx ^ 64) + j * 4096, 512);
          dfr[j] = tr_frag(db + ((v * 160 + j * 32) ^ (q4 * 32 + (g & 1) * 128)), 5120);
        }
#pragma unroll
        for (int j = 0; j < 5; ++j) asm volatile("" : "+v"(gfr[j]), "+v"(ufr[j]), "+v"(dfr[j]));
      }
      u32 ahn = HN_OFF + p4 * HSL + kq * 320 + g * 16, asw = ACT_OFF + p4 * ASL + kq * 256 + (q4 & 1) * 128 + m * 64 + g * 16, asr = ACT_OFF + (lane & 31) * 4;
      u32 aav = ACT_OFF + 4352 + v * 64 + (lane & 31) * 2, aar = ACT_OFF + 4352 + v * 64 + g * 16, vpo = v * 320 + r16 * 64 + g * 16, up3 = lane >= 32 ? 3u : 0u;
      asm volatile("" : "+v"(up3), "+v"(aar));
      const char* pdst = ws + WS_PART + fb * 2560;
      asm volatile("" : "+v"(ahn), "+v"(asw), "+v"(asr), "+v"(aav), "+v"(vpo));
      asm volatile("" : "+s"(pdst));
      int pass = 0;
      asm volatile("" : "+s"(pass));
#pragma nounroll
      for (; pass < 2; ++pass) {
        asm volatile("s_barrier" ::: "memory");
        u32x4 bs;
#define GU(J, C0, C1) "v_mfma_f32_16x16x32_bf16 v[120:123], %[g" #J "], v[100+4*" #J ":103+4*" #J "], " C0 "\n\t" \
                      "v_mfma_f32_16x16x32_bf16 v[124:127], %[u" #J "], v[100+4*" #J ":103+4*" #J "], " C1 "\n\t"
#define GU1(J) GU(J, "v[120:123]", "v[124:127]")
        asm volatile("ds_read_b128 v[100:103], %[ah]\n\tds_read_b128 v[104:107], %[ah] offset:64\n\tds_read_b128 v[108:111], %[ah] offset:128\n\t"
                     "ds_read_b128 v[112:115], %[ah] offset:192\n\tds_read_b128 v[116:119], %[ah] offset:256\n\t"
                     "s_waitcnt lgkmcnt(4)\n\t" GU(0, "0", "0") "s_waitcnt lgkmcnt(3)\n\t" GU1(1) "s_waitcnt lgkmcnt(2)\n\t" GU1(2)
                     "s_waitcnt lgkmcnt(1)\n\t" GU1(3) "s_waitcnt lgkmcnt(0)\n\t" GU1(4)
                     "ds_read_b128 %[bs], %[ab]\n\ts_mov_b32 exec_lo, 0x00f000f0\n\ts_mov_b32 exec_hi, 0x00f000f0\n\ts_nop 4\n\t"
                     "v_mov_b64 v[120:121], v[124:125]\n\tv_mov_b64 v[122:123], v[126:127]\n\t"
                     "s_mov_b32 exec_lo, 0x00ff00ff\n\ts_mov_b32 exec_hi, 0x00ff00ff\n\tds_write_b128 %[aw], v[120:123]\n\ts_mov_b64 exec, -1\n\t"
                     "s_setprio 3\n\ts_waitcnt lgkmcnt(0)\n\tv_cmp_eq_u32 vcc, 3, v129\n\ts_barrier\n\t"
                     "s_cbranch_vccnz 1f\n\tds_read_b128 %[bs], %[ab]\n\ts_waitcnt lgkmcnt(0)\n1:"
                     : [bs] "=&{v[128:131]}"(bs)
                     : [ah] "v"(ahn), [ab] "v"((u32)(CTL_OFF + 112)), [aw] "v"(asw), [g0] "v"(gfr[0]), [g1] "v"(gfr[1]), [g2] "v"(gfr[2]),
                       [g3] "v"(gfr[3]), [g4] "v"(gfr[4]), [u0] "v"(ufr[0]), [u1] "v"(ufr[1]), [u2] "v"(ufr[2]), [u3] "v"(ufr[3]), [u4] "v"(ufr[4])
                     : "memory", "vcc", "scc", "v100", "v101", "v102", "v103", "v104", "v105", "v106", "v107", "v108", "v109", "v110", "v111", "v112", "v113",
                       "v114", "v115", "v116", "v117", "v118", "v119", "v120", "v121", "v122", "v123", "v124", "v125", "v126", "v127");
        const u32x2 best = {bs[2], bs[3]};
        f32x2 s0, s1, s2, s3;
        asm volatile("ds_read2_b32 %0, %4 offset0:0 offset1:32\n\tds_read2_b32 %1, %4 offset0:64 offset1:96\n\t"
                     "ds_read2_b32 %2, %4 offset0:128 offset1:160\n\tds_read2_b32 %3, %4 offset0:192 offset1:224"
                     : "=&v"(s0), "=&v"(s1), "=&v"(s2), "=&v"(s3) : "v"(asr + ((best[1] & 3u) | up3) * ASL) : "memory");
        float rd = __builtin_amdgcn_rcpf(hi_f(best[0])), mb = lo_f(best[0]);
        asm volatile("s_waitcnt lgkmcnt(0)" : "+v"(s0), "+v"(s1), "+v"(s2), "+v"(s3), "+v"(rd), "+v"(mb)::"memory");
        const f32x2 sm = (s0 + s1) + (s2 + s3);
        auto xg = __builtin_amdgcn_permlane32_swap(__float_as_uint(sm[0]), __float_as_uint(sm[0]), false, false);
        auto xu = __builtin_amdgcn_permlane32_swap(__float_as_uint(sm[1]), __float_as_uint(sm[1]), false, false);
        const f32x2 gu = unpk(cvt2((f32x2){(__uint_as_float(xg[0]) - mb * __uint_as_float(xg[1])) * rd,
                                          (__uint_as_float(xu[0]) - mb * __uint_as_float(xu[1])) * rd}));
        const float sg = rbf(gu[0] * __builtin_amdgcn_rcpf(1.0f + __expf(-gu[0])));
        const u32 av = f2bf(sg * gu[1]);
#define DN(J) "v_mfma_f32_16x16x32_bf16 v[104+4*" #J ":107+4*" #J "], %[d" #J "], v[100:103], 0\n\t"
#define MV(J) "s_lshl_b64 exec, exec, 1\n\tv_mov_b64 v[104:105], v[104+4*" #J ":105+4*" #J "]\n\tv_mov_b64 v[106:107], v[106+4*" #J ":107+4*" #J "]\n\t"
        asm volatile("ds_write_b16 %[aw], %[av]\n\tds_read_b128 v[100:103], %[ar]\n\ts_waitcnt lgkmcnt(0)\n\t" DN(0) DN(1) DN(2) DN(3) DN(4)
                     "s_mov_b32 exec_lo, 0x00010001\n\ts_mov_b32 exec_hi, 0x00010001\n\ts_nop 1\n\t" MV(1) MV(2) MV(3) MV(4)
                     "s_mov_b32 exec_lo, 0x001f001f\n\ts_mov_b32 exec_hi, 0x001f001f\n\tglobal_store_dwordx4 %[vp], v[104:107], %[pd] sc1\n\t"
                     "s_mov_b64 exec, -1\n\ts_setprio 0"
                     ::[aw] "v"(aav), [av] "v"(av), [ar] "v"(aar), [vp] "v"(vpo), [pd] "s"(pdst), [d0] "v"(dfr[0]), [d1] "v"(dfr[1]),
                       [d2] "v"(dfr[2]), [d3] "v"(dfr[3]), [d4] "v"(dfr[4])
                     : "memory", "scc", "v100", "v101", "v102", "v103", "v104", "v105", "v106", "v107", "v108", "v109", "v110", "v111", "v112", "v113",
                       "v114", "v115", "v116", "v117", "v118", "v119", "v120", "v121", "v122", "v123");
        if (pass) break;
        u32 fin = 0;
#pragma nounroll
        for (int it = 0; it < SPIN_CAP * 8; ++it) {
          fin = __builtin_amdgcn_readfirstlane(lds_ld32(CTL_OFF + 40));
          if (fin) break;
          __builtin_amdgcn_s_sleep(4);
        }
        if (fin != 2u) break;
        pdst += WS_PART2 - WS_PART;
      }
      return;
    }
    if (w == 0) {
      u32 pk6;
      asm volatile(
          "s_mov_b32 s41, %[cap]\n\ts_mov_b32 s40, 1\n\ts_mov_b64 s[46:47], 0\n\tv_mov_b32 v5, %[ln]\n\t"
          "s_mov_b32 s54, -1\n\ts_mov_b32 s55, 0x7ffff\n\tv_mov_b32 v12, 0\n\t"
#define PKLD(D) "s_mov_b64 exec, s[54:55]\n\tglobal_atomic_or_x2 " D ", %[vk], %[z], %[kb] sc0 sc1\n\ts_mov_b64 exec, -1\n\t"
          PKLD("v[8:9]") "s_sleep %[gap]\n\t" PKLD("v[10:11]") "s_sleep %[gap]\n\t" PKLD("v[14:15]")
#define MDPP(C) "s_nop 1\n\tv_max_u32_dpp v13, v13, v13 " C " row_mask:0xf bank_mask:0xf bound_ctrl:1\n\t"
#define PSTEP(L, D0, D1, DR)                                                                        \
          L "0:\n\t"                                                                               \
          "s_waitcnt vmcnt(2)\n\t"                                                                 \
          "v_cmp_eq_u32 s[42:43], " D1 ", %[tag]\n\t"                                              \
          "s_and_b64 exec, s[42:43], s[54:55]\n\ts_andn2_b64 s[54:55], s[54:55], s[42:43]\n\t"      \
          "v_and_or_b32 v12, " D0 ", %[msk], %[lid]\n\t"                                           \
          "v_mov_b32 v6, " D0 "\n\ts_mov_b64 exec, -1\n\t"                                         \
          "v_cmp_le_u32 vcc, s40, v12\n\t"                                                         \
          "s_cmp_eq_u64 vcc, s[46:47]\n\ts_cbranch_scc1 " L "1f\n\t"                              \
          "s_swappc_b64 s[50:51], s[48:49]\n"                                                      \
          L "1:\n\t"                                                                               \
          "s_cmp_eq_u64 s[54:55], 0\n\ts_cbranch_scc1 9f\n\t"                                     \
          PKLD(DR)
          "s_getpc_b64 s[48:49]\n\ts_add_u32 s48, s48, 12\n\ts_addc_u32 s49, s49, 0\n\ts_branch 10f\n\t"
          "v_mov_b32 v13, v12\n\t"
          MDPP("quad_perm:[1,0,3,2]") MDPP("quad_perm:[2,3,0,1]") MDPP("row_half_mirror") MDPP("row_mirror")
          "v_bfe_u32 v4, v6, 10, 6\n\tv_readlane_b32 s44, v13, 0\n\tv_readlane_b32 s45, v13, 16\n\tv_readlane_b32 s46, v13, 32\n\tv_readlane_b32 s47, v13, 48\n\t"
          "v_sub_u32 v4, %[rb], v4\n\ts_max_u32 s44, s44, s45\n\ts_max_u32 s46, s46, s47\n\ts_max_u32 s44, s44, s46\n\t"
          "s_and_b32 s45, s44, 0x7fffff\n\ts_bitset1_b32 s45, 23\n\ts_mul_hi_u32 s45, s45, %[shr]\n"
          "5:\n\ts_sub_u32 s40, s44, s45\n\ts_cselect_b32 s40, 1, s40\n\ts_max_u32 s40, s40, 1\n\tv_cmp_le_u32 s[46:47], s40, v12\n\ts_mul_i32 s45, s45, 3\n\ts_lshr_b32 s45, s45, 2\n\t"
          "s_bcnt1_i32_b64 s52, s[46:47]\n\ts_cmp_gt_u32 s52, 3\n\ts_cbranch_scc1 5b\n\t"
          "v_mbcnt_lo_u32_b32 v13, s46, 0\n\tv_mbcnt_hi_u32_b32 v13, s47, v13\n\tv_sub_u32 v7, %[ln], v13\n\tv_add_u32 v7, s52, v7\n\t"
          "v_cndmask_b32 v13, v7, v13, s[46:47]\n\tv_cndmask_b32 v7, 2, 1, s[46:47]\n\tv_cmp_gt_u32 s[44:45], 3, v13\n\t"
          "v_lshl_add_u32 v13, v13, 4, %[pa]\n\ts_mov_b64 exec, s[44:45]\n\tds_write_b128 v13, v[4:7]\n\ts_mov_b64 exec, -1\n\t"
          "s_setpc_b64 s[50:51]\n"
          PSTEP("1", "v8", "v9", "v[8:9]")
          PSTEP("2", "v10", "v11", "v[10:11]")
          PSTEP("3", "v14", "v15", "v[14:15]")
          "s_sub_u32 s41, s41, 1\n\ts_cbranch_scc0 10b\n\ts_swappc_b64 s[50:51], s[48:49]\n"
          "9:\n\t"  "v_or_b32 v7, 4, v7\n\ts_mov_b64 exec, s[44:45]\n\tds_write_b32 v13, v7 offset:12\n\ts_mov_b64 exec, -1\n\tds_write_b32 %[at], v6\n\ts_waitcnt lgkmcnt(0)\n\ts_barrier\n\ts_waitcnt vmcnt(0)"
          : "=&{v6}"(pk6) : [cap] "n"(SPIN_CAP), [gap] "n"(PGAP), [vk] "v"((u32)(lane * 6144)), [kb] "s"(ws + WS_PK + fb * 128), [tag] "v"(tag), [z] "v"(0ull), [ln] "v"(lane),
            [msk] "s"(~1023u), [lid] "v"(64u | lane), [rb] "v"(2047 - ((lane >> 1) * 80 + (lane & 1) * 48)),
            [shr] "s"((u32)((1.0 - PTHR) * 4294967296.0)), [pa] "s"(POST_OFF), [at] "v"((u32)(KEY_OFF - 256 + lane * 4))
          : "memory", "vcc", "scc", "s40", "s41", "s42", "s43", "s44", "s45", "s46", "s47", "s48", "s49", "s50", "s51", "s52", "s54", "s55",
            "v4", "v5", "v7", "v8", "v9", "v10", "v11", "v12", "v13", "v14", "v15");
      if (fb == 47 && lane < 8) GST4(l16, ws + WS_EPOCH, 0, ((u32x4){tg[0], tg[1], tg[0], tg[1]}));
      const u32 now = (u32)__builtin_amdgcn_s_memrealtime();
      const u32 nag = (pk6 - (now >> 1) - 1u) & 1023u;
      u32 kgo = __builtin_amdgcn_readfirstlane(now + KCD - 2 * (1023u - wave_max(lane < 51 ? nag : 0u)));
      u64 old = __ballot(lane < 51 && nag < 1023u - (KOLD - 8) / 2);
      asm volatile("" : "+s"(kgo), "+s"(old));
      asm volatile(
          "s_mov_b32 s41, %[cap]\n\ts_mov_b32 s54, -1\n\ts_mov_b32 s55, 0x7ffff\n\tv_mov_b32 v12, 0\n\t"
          "s_mov_b64 exec, %[old]\n\ts_cbranch_execz 3f\n\tglobal_load_dwordx2 v[14:15], %[vk], %[kb] sc1\n"
          "3:\n\ts_setprio 3\n\ts_barrier\n"
          "4:\n\ts_memrealtime s[42:43]\n\ts_waitcnt lgkmcnt(0)\n\ts_sub_u32 s42, s42, %[go]\n\ts_cmp_lt_i32 s42, 0\n\ts_cbranch_scc1 4b\n\t"
          "s_andn2_b64 exec, s[54:55], %[old]\n\ts_cbranch_execz 5f\n\tglobal_load_dwordx2 v[14:15], %[vk], %[kb] sc1\n"
          "5:\n\ts_mov_b64 exec, s[54:55]\n\tds_read_b32 v4, %[ctl] offset:92\n\ts_waitcnt lgkmcnt(0)\n\tv_and_b32 v4, 3, v4\n\t"
          "v_lshl_add_u32 v4, v4, 4, %[pa]\n\tds_read_b64 v[6:7], v4\n\ts_waitcnt lgkmcnt(0)\n\t"
          "v_readfirstlane_b32 s48, v6\n\tv_readfirstlane_b32 s49, v7\n\tv_mov_b32 v6, 1\n\ts_waitcnt vmcnt(0)\n\tv_cmp_ne_u32 s[54:55], v15, %[tag]\n\tv_mov_b32 v12, v14\n\ts_mov_b64 exec, -1\n\t"
          "s_cmp_eq_u64 s[54:55], 0\n\ts_cbranch_scc1 9f\n\t"
          PKLD("v[8:9]") "s_sleep %[gap]\n\t" PKLD("v[10:11]")
#define KSTEP(L, D0, D1, DR)                                                                        \
          L "0:\n\t"                                                                               \
          "s_waitcnt vmcnt(1)\n\t"                                                                 \
          "v_cmp_eq_u32 s[42:43], " D1 ", %[tag]\n\t"                                              \
          "s_and_b64 exec, s[42:43], s[54:55]\n\tv_mov_b32 v12, " D0 "\n\ts_mov_b64 exec, -1\n\t"   \
          "s_andn2_b64 s[54:55], s[54:55], s[42:43]\n\ts_cbranch_scc0 9f\n\t"                     \
          PKLD(DR)
          KSTEP("1", "v8", "v9", "v[8:9]")
          KSTEP("2", "v10", "v11", "v[10:11]")
          "s_sub_u32 s41, s41, 1\n\ts_cbranch_scc0 10b\n"
          "9:\n\t"
          "v_readlane_b32 s40, v12, s49\n\tds_write_b32 %[ak], v12\n\tv_mov_b32 v13, v12\n\t"
          "s_and_b32 s45, s40, 0x7ff\n\tv_cmp_lt_u32 vcc, s40, v12\n\ts_cmp_eq_u32 s45, s48\n\ts_cselect_b64 vcc, vcc, -1\n\ts_cbranch_vccz 8f\n\t"
          MDPP("quad_perm:[1,0,3,2]") MDPP("quad_perm:[2,3,0,1]") MDPP("row_half_mirror") MDPP("row_mirror")
          "s_nop 0\n\tv_readlane_b32 s44, v13, 0\n\tv_readlane_b32 s45, v13, 16\n\tv_readlane_b32 s46, v13, 32\n\tv_readlane_b32 s47, v13, 48\n\t"
          "s_max_u32 s44, s44, s45\n\ts_max_u32 s46, s46, s47\n\ts_max_u32 s40, s44, s46\n\t"
          "v_cmp_eq_u32 vcc, s40, v12\n\tv_mov_b32 v6, 2\n\ts_ff1_i32_b64 s44, vcc\n\tv_mov_b32 v7, s44\n"
          "8:\n\tv_mov_b32 v4, s40\n\ts_mov_b64 exec, 1\n\tds_write_b128 %[ctl], v[4:7]\n\ts_mov_b64 exec, -1\n\t"
          "s_waitcnt vmcnt(0) lgkmcnt(0)"
          :: [cap] "n"(SPIN_CAP), [gap] "n"(KGAP), [go] "s"(kgo), [old] "s"(old), [vk] "v"((u32)(lane * 6144)), [kb] "s"(ws + WS_PK + fb * 128 + 8), [tag] "v"(tag),
            [ctl] "v"((u32)(CTL_OFF + 32)), [ak] "v"((u32)(KEY_OFF + lane * 4)), [pa] "s"(POST_OFF), [z] "v"(0ull)
          : "memory", "vcc", "scc", "s40", "s41", "s42", "s43", "s44", "s45", "s46", "s47", "s48", "s49", "s54", "s55", "v4", "v5", "v6", "v7", "v8", "v9", "v10",
            "v11", "v12", "v13", "v14", "v15");
      return;
    }
    float swr = dot2b(rb5, 0x3F803F80u, 0.f);
#pragma unroll
    for (int e = 0; e < 4; ++e) swr = dot2b(ra[e], 0x3F803F80u, swr);
    swr = wave_sum(swr);
    u32 ah16 = HN_OFF + (w - 1) * HSL + l16, ah4 = HN_OFF + (w - 1) * HSL + 1024 + lane * 4, l4 = lane * 4;
    asm volatile("" : "+v"(swr), "+v"(ah16), "+v"(ah4), "+v"(l4));
#define DOT3(H, W) "v_dot2c_f32_bf16 v102, 0x3f803f80, " H "\n\tv_dot2c_f32_bf16 v103, " H ", " H "\n\tv_dot2c_f32_bf16 v104, " H ", " W "\n\t"
#define ADPP(C) "s_nop 1\n\tv_add_f32_dpp v102, v102, v102 " C " row_mask:0xf bank_mask:0xf bound_ctrl:1\n\t"
#define XDPP(C) "s_nop 1\n\tv_max_u32_dpp v102, v102, v102 " C " row_mask:0xf bank_mask:0xf bound_ctrl:1\n\t"
    const char* rp = (const char*)w_r;
    u32 second = 0;
#pragma nounroll
    for (;;) {
      asm volatile(
          "s_mov_b32 s47, %[cap]\n\ts_mov_b32 s43, -1\n\ts_sub_u32 s46, %[sec], 1\n"
          "7:\n\ts_mov_b64 s[48:49], %[rp]\n\ts_mov_b32 s45, 0\n\ts_branch 4f\n"
          "18:\n\ts_sleep 1\n"
          "1:\n\ts_sub_u32 s47, s47, 1\n\ts_cbranch_scc1 8f\n\ts_cmp_lg_u32 %[sec], 0\n\ts_cbranch_scc1 7b\n\t"
          "ds_read_b128 v[108:111], %[ap]\n\ts_memrealtime s[40:41]\n\ts_waitcnt lgkmcnt(0)\n\t"
          "v_readfirstlane_b32 s45, v111\n\tv_readfirstlane_b32 s46, v109\n\tv_readfirstlane_b32 s50, v110\n\ts_lshr_b32 s41, s40, 1\n\t"
          "s_cmp_eq_u32 s45, 6\n\ts_cbranch_scc1 8f\n\ts_bitcmp0_b32 s45, 0\n\ts_cbranch_scc1 18b\n\t"
          "s_cmp_eq_u32 s46, s42\n\ts_cbranch_scc1 6f\n\ts_cmp_eq_u32 s46, s43\n\ts_cbranch_scc1 2f\n\t"
          "s_sub_u32 s41, s41, s50\n\ts_and_b32 s41, s41, 0x3ff\n\ts_cmp_lt_u32 s41, %[tq]\n\ts_cbranch_scc1 18b\n\t"
          "s_mov_b32 s43, s46\n\ts_add_u32 s44, s50, 8\n\ts_cmp_ge_u32 s41, %[pc] - 20\n\ts_cbranch_scc1 1b\n\t"
          "s_mul_i32 s48, s46, 6144\n\tv_mov_b64 v[104:105], 0\n\tv_mov_b32 v101, s48\n\tv_mad_u32_u24 v101, %[l16], 48, v101\n\t"
          "s_mov_b64 exec, 0xff\n\tglobal_atomic_or_x2 v[102:103], v101, v[104:105], %[pk] sc0 sc1\n\ts_sub_u32 s50, s50, 512\n\ts_waitcnt vmcnt(0)\n\t"
          "v_cmp_eq_u32 vcc, v103, %[tag]\n\tv_subrev_u32 v102, s50, v102\n\tv_and_b32 v102, 0x3ff, v102\n\tv_cndmask_b32 v102, 0, v102, vcc\n\t"
          XDPP("quad_perm:[1,0,3,2]") XDPP("quad_perm:[2,3,0,1]") XDPP("row_half_mirror")
          "s_mov_b64 exec, -1\n\tv_readfirstlane_b32 s44, v102\n\ts_min_u32 s44, s44, 562\n\ts_add_u32 s44, s44, s50\n\ts_branch 1b\n"
          "2:\n\ts_sub_u32 s41, s41, s44\n\ts_and_b32 s41, s41, 0x3ff\n\ts_cmp_lt_u32 s41, %[pc] - 8\n\ts_cbranch_scc1 1b\n\t"
          "s_sub_u32 s41, %[pc], s41\n\ts_lshl1_add_u32 s51, s41, s40\n\ts_mul_i32 s48, s46, 1280\n\ts_add_u32 s48, s48, %[lo]\n\ts_addc_u32 s49, %[hi], 0\n\t"
          "3:\n\ts_memrealtime s[40:41]\n\ts_waitcnt lgkmcnt(0)\n\ts_sub_u32 s40, s40, s51\n\ts_cmp_lt_i32 s40, 0\n\ts_cbranch_scc1 3b\n"
          "4:\n\tglobal_load_dwordx4 v[96:99], %[l16], s[48:49] sc1\n\tglobal_load_dword v100, %[l4], s[48:49] offset:1024 sc1\n\t"
          "v_mov_b64 v[102:103], 0\n\tv_mov_b32 v104, 0\n\ts_mov_b32 s42, -2\n\ts_waitcnt vmcnt(0)\n\t"
          "ds_read_b128 v[108:111], %[ap]\n\tds_read_b32 v105, %[ab] offset:4\n\tds_write_b128 %[a16], v[96:99]\n\tds_write_b32 %[a4], v100\n\t"
          "v_max3_u32 v101, v96, v97, v98\n\tv_max3_u32 v101, v101, v99, v100\n\tv_cmp_eq_u32 vcc, -1, v101\n\t"
          DOT3("v96", "v92") DOT3("v97", "v93")
          "s_cbranch_vccnz 1b\n\ts_mov_b32 s42, s46\n\ts_waitcnt lgkmcnt(2)\n\tv_lshl_or_b32 v109, v111, 8, v109\n\tv_lshl_or_b32 v109, v105, 16, v109\n\t"
          "s_or_b32 s45, s46, 0x20500\n\tv_cmp_eq_u32 vcc, s45, v109\n\ts_cbranch_vccz 5f\n\ts_waitcnt lgkmcnt(0)\n\ts_barrier\n"
          "5:\n\t" DOT3("v98", "v94") DOT3("v99", "v95") DOT3("v100", "%[rb]")
          "s_nop 2\n\tv_mov_b32 v105, v104\n\tv_permlane32_swap_b32 v102, v103\n\tv_add_f32 v102, v102, v103\n\tv_permlane32_swap_b32 v104, v105\n\t"
          "v_add_f32 v104, v104, v105\n\ts_nop 1\n\tv_permlane16_swap_b32 v102, v104\n\tv_add_f32 v102, v102, v104\n\t"
          ADPP("quad_perm:[1,0,3,2]") ADPP("quad_perm:[2,3,0,1]") ADPP("row_half_mirror") ADPP("row_mirror")
          "v_mul_f32 v103, 0x3acccccd, v102\n\tv_readlane_b32 s41, v102, 16\n\tv_readlane_b32 s40, v103, 32\n\t"
          "s_nop 1\n\tv_fma_f32 v102, -v103, v103, s40\n\tv_max_f32 v102, 0, v102\n\tv_cvt_pk_bf16_f32 v102, 0, v102\n\t"
          "v_add_f32 v102, 0x3727c5ac, v102\n\tv_cvt_pk_bf16_f32 v102, 0, v102\n\tv_sqrt_f32 v102, v102\n\tv_cvt_pk_bf16_f32 v105, 0, v103\n\t"
          "v_fma_f32 v105, -%[swr], v105, s41\n\tv_cvt_pk_bf16_f32 v106, v103, v102\n\tv_and_b32 v102, 0xffff0000, v106\n\tv_rcp_f32 v102, v102\n\t"
          "v_mov_b32 v104, 1\n\tv_mul_f32 v102, v102, v105\n\tv_ashrrev_i32 v103, 31, v102\n\tv_or_b32 v103, 0x80000000, v103\n\t"
          "v_xor_b32 v102, v102, v103\n\tv_and_or_b32 v107, v102, -4, %[sl]\n\ts_cbranch_vccz 1b\n\t"
          "s_mov_b64 exec, 1\n\tds_max_u64 %[ab], v[106:107] offset:8\n\tds_add_u32 %[ab], v104 offset:4\n\ts_branch 9f\n"
          "6:\n\ts_bitcmp0_b32 s45, 2\n\ts_cbranch_scc1 1b\n\ts_mov_b64 exec, 1\n\tds_max_u64 %[ab], v[106:107] offset:8\n"
          "8:\n\tv_mov_b32 v104, 1\n\ts_mov_b64 exec, 1\n\tds_add_u32 %[ab], v104 offset:4\n\ts_barrier\n"
          "9:\n\ts_mov_b64 exec, -1\n\ts_waitcnt lgkmcnt(0)\n\ts_barrier"
          :
          : [cap] "n"(SPIN_CAP * 8), [rp] "s"(rp), [sec] "s"(second), [ap] "v"((u32)(POST_OFF + (w - 1) * 16)), [tq] "n"(TQD / 2), [pc] "n"(PCD / 2),
            [pk] "s"(ws + WS_PK + (bidx - 26) * 128), [tag] "v"(tag), [lo] "s"((u32)(u64)(ws + WS_PC)), [hi] "s"((u32)((u64)(ws + WS_PC) >> 32)),
            [l16] "v"(l16), [l4] "v"(l4), [a16] "v"(ah16), [a4] "v"(ah4), "{v[92:95]}"(ra), [rb] "v"(rb5), [swr] "v"(swr), [sl] "s"(w - 1),
            [ab] "v"((u32)(CTL_OFF + 112))
          : "memory", "vcc", "scc", "s40", "s41", "s42", "s43", "s44", "s45", "s46", "s47", "s48", "s49", "s50", "s51", "v96", "v97", "v98", "v99",
            "v100", "v101", "v102", "v103", "v104", "v105", "v106", "v107", "v108", "v109", "v110", "v111");
      if (w != 1 || second) break;
      u32x4 wst;
#pragma nounroll
      for (int it = 0; it < SPIN_CAP * 8; ++it) {
        wst = lds_ld128(CTL_OFF + 32);
        if (__builtin_amdgcn_readfirstlane(wst[2])) break;
        __builtin_amdgcn_s_sleep(1);
      }
      if (__builtin_amdgcn_readfirstlane(wst[2]) != 2u) break;
      if (lane < 2) *(u32x4*)(smem + (lane ? POST_OFF : CTL_OFF + 112)) = lane ? (u32x4){0u, 0u, 0u, 5u} : (u32x4){0u, 2u, 0x3F800000u, 0u};
      rp = ws + WS_CAND + __builtin_amdgcn_readfirstlane(wst[3]) * 2560;
      second = 1;
    }
    if (w == 1) {
    } else if (w == 3 || fb >= 40) {
      const int c = w == 3 ? fb : fb + 8;
      if (c < 51) {
        const u32 now = (u32)__builtin_amdgcn_s_memrealtime();
        const u32 age = ((now >> 1) - __builtin_amdgcn_readfirstlane(*(const u32*)(smem + KEY_OFF - 256 + c * 4))) & 1023u;
        asm volatile("s_mov_b32 s42, %[cap]\n"
                     "3:\n\ts_memrealtime s[40:41]\n\ts_waitcnt lgkmcnt(0)\n\ts_sub_u32 s40, s40, %[go]\n\ts_cmp_lt_i32 s40, 0\n\ts_cbranch_scc1 3b\n"
                     "4:\n\tglobal_load_dwordx4 v[96:99], %[l16], %[cp] sc1\n\tglobal_load_dword v103, %[l4], %[cp] offset:1024 sc1\n\t"
                     "s_sub_u32 s42, s42, 8\n\ts_waitcnt vmcnt(0)\n\ts_cbranch_scc1 2f\n\t"
                     "v_max3_u32 v101, v96, v98, v103\n\tv_cmp_eq_u32 vcc, -1, v101\n\ts_cbranch_vccnz 4b\n"
                     "1:\n\tds_read_b64 v[100:101], %[af]\n\tds_read_b32 v102, %[ak]\n\ts_sub_u32 s42, s42, 1\n\ts_cbranch_scc1 2f\n\ts_waitcnt lgkmcnt(0)\n\t"
                     "v_cmp_eq_u32 vcc, 0, v100\n\ts_cbranch_vccnz 1b\n\t"
                     "v_cmp_eq_u32 vcc, %[c], v101\n\tv_bfi_b32 v102, v102, 0, %[m]\n\ts_cbranch_vccnz 2f\n\t"
                     "v_mad_u32_u24 v100, v102, %[rs], %[l16]\n\tv_mad_u32_u24 v101, v102, %[rs], %[l4]\n\t"
                     "global_store_dwordx4 v100, v[96:99], %[out]" "\n\tglobal_store_dword v101, v103, %[out] offset:1024" "\n"
                     "2:\n\ts_waitcnt lgkmcnt(0)"
                     ::[cap] "n"(SPIN_CAP * 8), [go] "s"(now + CCD - 2 * age), [cp] "s"(ws + WS_CAND + c * 2560 + 1280), [af] "v"((u32)(CTL_OFF + 40)),
                       [ak] "v"((u32)(KEY_OFF + c * 4)), [c] "s"(c), [m] "s"(2047), [rs] "s"(1280), [l16] "v"(l16), [l4] "v"(l4), [out] "s"(out)
                     : "memory", "vcc", "scc", "s40", "s41", "s42", "v96", "v97", "v98", "v99", "v100", "v101", "v102", "v103");
      }
    } else {
      const char* pp = ws + WS_PART + fb * 64;
      int pass = 0;
      asm volatile("" : "+s"(pass));
#pragma nounroll
      for (; pass < 2; ++pass) {
        const u32x2 best = *(const u32x2*)(smem + CTL_OFF + 120);
        const u32 slot = best[1] & 3u;
        const u32 wkey = __builtin_amdgcn_readfirstlane(*(const u32*)(smem + (pass ? CTL_OFF + 32 : POST_OFF + slot * 16)));
        const float rr = rbf(__uint_as_float((best[1] & ~3u) ^ ((u32)((int)~best[1] >> 31) | 0x80000000u)));
        float rs = rbf(__builtin_amdgcn_rcpf(1.0f + __expf(-rr)));
        const int ccol = (g & 1) * 2 + (g >> 1);
        float hv = bf2f(*(const u16*)(smem + HN_OFF + slot * HSL + fb * 32 + p4 * 8 + ccol * 2));
        hv = __builtin_fmaf(hv - lo_f(best[0]), __builtin_amdgcn_rcpf(hi_f(best[0])), hv);
        const char* op_ = (const char*)out + (size_t)(2047 - (int)(wkey & 2047u)) * 1280 + fb * 32;
        u32 vo_ = p4 * 8 + ccol * 2;
        asm volatile("" : "+v"(vo_), "+v"(hv), "+v"(rs));
        asm volatile("" : "+s"(op_));
        const u32 vp = (lane >> 2) * 2560 + p4 * 16;
        typedef __attribute__((ext_vector_type(16))) unsigned int u32x16;
        u32x16 keepa, keepb;
        f32x4 ys;
        u32 fin;
        asm volatile(
#define PLD(B0, B1, B2)                                                                             \
            "global_load_dwordx4 " B0 ", %[vp], %[p0] sc1\n\t"                                       \
            "global_load_dwordx4 " B1 ", %[vp], %[p1] sc1\n\tglobal_load_dwordx4 " B2 ", %[vp], %[p2] sc1\n\t"
#define PCHK(A0, A1, B0, B1, C0, C1)                                                                \
            "s_waitcnt vmcnt(3)\n\t"                                                                 \
            "v_pk_add_f32 v[96:97], " A0 ", " B0 "\n\tv_pk_add_f32 v[98:99], " A1 ", " B1 "\n\t"    \
            "v_pk_add_f32 v[96:97], v[96:97], " C0 "\n\tv_pk_add_f32 v[98:99], v[98:99], " C1 "\n\t" \
            "v_pk_add_f32 v[102:103], v[96:97], v[98:99]\n\t"                                        \
            "v_cmp_u_f32 vcc, v102, v103\n\ts_cbranch_vccz 9f\n\t"
            "s_mov_b32 s42, %[cap]\n\ts_sleep %[cdel]\n\t"
            PLD("v[64:67]", "v[68:71]", "v[72:75]")
            "s_sleep %[gap]\n\t"
            PLD("v[80:83]", "v[84:87]", "v[88:91]")
            "1:\n\t"
            PCHK("v[64:65]", "v[66:67]", "v[68:69]", "v[70:71]", "v[72:73]", "v[74:75]")
            PLD("v[64:67]", "v[68:71]", "v[72:75]")
            PCHK("v[80:81]", "v[82:83]", "v[84:85]", "v[86:87]", "v[88:89]", "v[90:91]")
            PLD("v[80:83]", "v[84:87]", "v[88:91]")
            "s_sub_u32 s42, s42, 1\n\ts_cbranch_scc0 1b\n"
            "9:\n\tds_read_b32 %[fin], %[af]"
            : "=&{v[96:99]}"(ys), "=&{v[64:79]}"(keepa), "=&{v[80:95]}"(keepb), [fin] "=&v"(fin)
            : [cap] "n"(SPIN_CAP), [gap] "n"(CGAP), [cdel] "n"(CDEL), [vp] "v"(vp), [p0] "s"(pp),
              [p1] "s"(pp + 40960), [p2] "s"(pp + 81920), [af] "v"((u32)(CTL_OFF + 40))
            : "memory", "vcc", "scc", "s42", "v102", "v103");
        float y = tr_sum(ys);
        y += dppf<0x124>(y);
        y += dppf<0x128>(y);
        const u32 res = cvt2((f32x2){__builtin_fmaf(y, rs, hv), 0.f});
        asm volatile("s_mov_b32 s42, %[cap]\n"
                     "1:\n\ts_waitcnt lgkmcnt(0)\n\tv_cmp_eq_u32 vcc, %[ok], %[fin]\n\ts_cbranch_vccz 2f\n\t"
                     "s_mov_b32 exec_lo, 0x000f000f\n\ts_mov_b32 exec_hi, 0x000f000f\n\tglobal_store_short %[vo], %[res], %[op]" "\n\ts_mov_b64 exec, -1\n"
                     "2:\n\tv_cmp_eq_u32 vcc, 0, %[fin]\n\ts_cbranch_vccz 3f\n\tds_read_b32 %[fin], %[af]\n\ts_sub_u32 s42, s42, 1\n\ts_cbranch_scc0 1b\n"
                     "3:\n\ts_waitcnt lgkmcnt(0)\n\tv_cmp_lt_u32 vcc, %[ok], %[fin]\n\ts_cbranch_vccz 4f\n\ts_waitcnt vmcnt(0)\n4:"
                     : [fin] "+v"(fin) : [cap] "n"(SPIN_CAP * 8), [ok] "s"(pass + 1), [vo] "v"(vo_), [res] "v"(res), [op] "s"(op_),
                       [af] "v"((u32)(CTL_OFF + 40)), "{v[64:79]}"(keepa), "{v[80:95]}"(keepb) : "memory", "vcc", "scc", "s42");
        if (pass || __builtin_amdgcn_readfirstlane(fin) != 2u) break;
        asm volatile("s_barrier\n\ts_barrier" ::: "memory");
        pp += WS_PART2 - WS_PART;
      }
    }
    return;
  }

  const int idx2 = (xcd & 1) * 26 + bidx;
  const int hg = ((xcd >> 1) & 1) * 4 + (idx2 & 3);
  const int mt = (xcd >> 2) * 13 + (idx2 >> 2);
#define ROTF (((idx2 >> 2) * 3 / 2 + (idx2 & 3) * 3) % 10)
  const int kb0 = ROTF;
  const int r0 = mt * 80;
  if (w >= 4) {
    const int cw = w - 4;
    u32 vo = w_off(cw, lane, hg), vx = x_off(cw, lane, r0), vx2 = x_off(8 + (cw & 3), lane, r0);
    asm volatile("" : "+v"(vo), "+v"(vx), "+v"(vx2));
    __builtin_amdgcn_sched_barrier(0);
    const char* wb = (const char*)wqkv + (size_t)kb0 * 245760 + W_ROW(cw);
    dma_m0<0>(vo, wb, cw * 1024);
    dma_m0<0>(vx, (const char*)x + kb0 * 128, XRING + cw * 1024);
    dma_m0<0>(vo, wb + 61440, 8192 + cw * 1024);
    if (cw < 4) dma_m0<0>(vx2, (const char*)x + kb0 * 128, XRING + 8192 + cw * 1024);
  }
  const int rt = w >= 4 ? (w & 3) : 4, kh = w >= 4 ? (w - 4) >> 2 : (w & 1);
  float bias[2];
  u32 sel[4], e4, eh, aq, aka, akb, pm, pf, hi, arec, vpp;
  const char *os, *os4, *sst, *oa[10];
  int so_[10];
  int lane_ = lane, mt_ = mt, hg_ = hg;
  auto prepv = [&](const int lane) __attribute__((always_inline)) {
    const int g = lane >> 4, r16 = lane & 15, q4 = (lane & 15) >> 2, p4 = lane & 3;
    const int par = lane & 1;
    const bool ca = r16 >= 4 * g, cb = r16 >= 4 * g + 2;
    bias[0] = (ca || r0 + rt > 0) ? 0.f : -__builtin_inff();
    bias[1] = (cb || r0 + rt > 0) ? 0.f : -__builtin_inff();
    const u32 sa = par ? 0x01000C0Cu : 0x0C0C0100u, sb = par ? 0x03020C0Cu : 0x0C0C0302u;
    sel[0] = ca ? 0x0C0C0C0Cu : sa;
    sel[1] = cb ? 0x0C0C0C0Cu : sb;
    sel[2] = ca ? sa : 0x0C0C0C0Cu;
    sel[3] = cb ? sb : 0x0C0C0C0Cu;
    e4 = EXO + rt * 512 + lane * 8;
    eh = e4 + kh * 6144;
    aq = TAB_OFF + 384 + (rt * 16 + r16) * 4;
    aka = TAB_OFF + 384 + (rt * 16 + 4 * g + par + (ca ? 16 : 0)) * 4;
    akb = TAB_OFF + 384 + (rt * 16 + 4 * g + par + 2 + (cb ? 16 : 0)) * 4;
    pm = (4 * g + q4) * 160 + p4 * 40;
    pf = (20 * g + 16 + q4) * 160 + p4 * 40;
    hi = HIM + (rt * 16 + r16) * 160 + g * 40;
    arec = REC_OFF + (r16 == 0 ? kh * 192 + g * 48 : (r16 == 8 ? ((g >> 1) == kh ? 480 : 384) : 432));
    vpp = lane + kh * 64 + (rt == 0 ? 768 : (rt == 2 ? 8192 : (rt == 4 ? 512 : 640)));
    asm volatile("" : "+v"(arec), "+v"(vpp));
    asm volatile("" : "+v"(bias[0]), "+v"(bias[1]), "+v"(sel[0]), "+v"(sel[1]), "+v"(sel[2]), "+v"(sel[3]), "+v"(e4), "+v"(eh),
                 "+v"(aq), "+v"(aka), "+v"(akb), "+v"(pm), "+v"(pf), "+v"(hi));
  };
  auto preps = [&](const int mt, const int hg) __attribute__((always_inline)) {
    const char* ob = ws + WS_O + (size_t)mt * 102400;
    os = ob + rt * 20480 + (2 * hg + kh) * 1024;
    os4 = ob + rt * 20480 + (16 + (hg >> 1)) * 1024 + (hg & 1) * 8;
    sst = ws + WS_STAT + (size_t)(2 * mt + (rt < 3 ? 0 : 1)) * 8192 + hg * 1024 + (rt < 3 ? rt : rt - 3) * 256;
    asm volatile("" : "+s"(os), "+s"(os4), "+s"(sst));
    const int k2 = w & 1;
#pragma unroll
    for (int j = 0; j < 10; ++j) {
      const bool oth = j >= 5 || w < 2;
      const int mo[8] = {OORD};
      const int ch = j % 5 < 4 ? (2 * hg + k2) ^ (oth ? 2 * mo[4 + j % 5] : 2 * mo[j % 5]) : (hg >> 1) ^ ((oth != ((hg & 1) != 0)) ? (k2 ? 0 : 3) : 1 + k2);
      oa[j] = ob + (j % 5 < 4 ? ch : 16 + ch) * 1024;
      so_[j] = ch * (j % 5 < 4 ? 6400 : 25600);
      asm volatile("" : "+s"(oa[j]), "+s"(so_[j]));
    }
  };
#define WO_OFFS                                                                                    \
  u32 woff[5];                                                                                     \
  _Pragma("unroll") for (int i = 0; i < 5; ++i) {                                                  \
    const int c = i * 64 + lane;                                                                   \
    const int row = c / 10;                                                                        \
    woff[i] = row * 1120 + hg * 160 + c * 16 + (4 - i) * 1024;                                     \
  }
#define WO_RUN(RHO)                                                                                \
  {                                                                                                \
    const char* wq_ = (const char*)wo + (RHO) * 40960; \
    dma_m0<-4096>(woff[0], wq_, (RHO) * 5120 + 4096);                                               \
    dma_nx<-3072>(woff[1], wq_); dma_nx<-2048>(woff[2], wq_); dma_nx<-1024>(woff[3], wq_); dma_nx<0>(woff[4], wq_); \
  }

#define BAR asm volatile("s_barrier" ::: "memory");
#define PREP_V __builtin_amdgcn_sched_barrier(0); asm volatile("" : "+v"(lane_)); prepv(lane_); __builtin_amdgcn_sched_barrier(0);
#define PREP_S __builtin_amdgcn_sched_barrier(0); asm volatile("" : "+s"(mt_), "+s"(hg_)); preps(mt_, hg_); __builtin_amdgcn_sched_barrier(0);
  if (w < 2) {
    const int l = w;
    __builtin_amdgcn_s_setprio(3);
    u32 vw[4];
#pragma unroll
    for (int i = 0; i < 4; ++i) vw[i] = w_off(i, lane, hg) + 4096;
    u32 go = (u32)kb0 * 245760u + 122880u + l * 61440u;
    u32 so = HSLOT + l * 8192 + 4096;
#define W1(I) dma_nx<((I) - 4) * 1024>(vw[(I) & 3], wb + (W_ROW(I) - (I) * 1024));
#define ISSUE_W                                                                                    \
  {                                                                                                \
    const char* wb = (const char*)wqkv + go;                                                       \
    dma_m0<-4096>(vw[0], wb, so);                                                                  \
    W1(1) W1(2) W1(3) W1(4) W1(5) W1(6) W1(7)                                                      \
    go += 122880u;                                                                                 \
    if (go >= 2457600u) go -= 2457600u;                                                            \
    so = so >= 5 * HSLOT ? so - 5 * HSLOT : so + HSLOT;                                            \
  }
    __builtin_amdgcn_s_sleep(2);
    ISSUE_W
    __builtin_amdgcn_sched_barrier(0);
    WO_OFFS
    asm volatile("" : "+v"(woff[0]), "+v"(woff[1]), "+v"(woff[2]), "+v"(woff[3]), "+v"(woff[4]));
    BAR
    ISSUE_W
    ISSUE_W
    asm volatile("s_waitcnt vmcnt(16)\n\ts_barrier" ::: "memory");
    asm volatile("s_waitcnt vmcnt(8)\n\ts_barrier" ::: "memory");
#pragma unroll 1
    for (int h = 1; h < 17; ++h) {
      ISSUE_W
      asm volatile("s_waitcnt vmcnt(8)\n\ts_barrier" ::: "memory");
    }
    asm volatile("s_waitcnt vmcnt(0)\n\ts_barrier" ::: "memory");
    WO_RUN(12 + l)
    const u32 tagw = lds_ld32(CTL_OFF);
    const char* bxf = ws + WS_BXF + mt * 1024;
    BAR
    const u32 town = (u32)__builtin_amdgcn_s_memrealtime();
    if (l == 0) {
      if (lane == 0) GST2((u32)(hg * 128), bxf, 0, ((u32x2){town, tagw}));
    }
    WO_RUN(18 + l)
    WO_RUN(8 + l)
    WO_RUN(l)
    const u32 vfl = (((lane & 7) == hg ? hg ^ 1 : (lane & 7))) * 128;
    u32x2 f;
    asm volatile("" : "=v"(f));
    BAR
    int gate = (int)((u32)__builtin_amdgcn_s_memrealtime() - town) + GADD;
    gate = gate > 200 ? 200 : gate;
    WO_RUN(2 + l)
    WO_RUN(4 + l)
    WO_RUN(6 + l)
    if (l == 1) asm volatile("global_load_dwordx2 %0, %1, %2 sc1" : "+v"(f) : "v"(vfl), "s"(bxf) : "memory");
    __builtin_amdgcn_s_setprio(0);
    pm = (4 * g + q4) * 160 + p4 * 40;
    pf = (20 * g + 16 + q4) * 160 + p4 * 40;
    asm volatile("" : "=v"(bias[0]), "=v"(bias[1]), "=v"(sel[0]), "=v"(sel[1]), "=v"(sel[2]), "=v"(sel[3]), "=v"(e4), "=v"(eh),
                 "=v"(aq), "=v"(aka), "=v"(akb), "+v"(pm), "+v"(pf), "=v"(hi), "=v"(arec), "=v"(vpp));
    PREP_S
    if (l == 1) {
      asm volatile("s_waitcnt vmcnt(0)" : "+v"(f)::"memory");
#pragma nounroll
      for (int it = 0; it < SPIN_CAP; ++it) {
        if (__all(f[1] == tagw)) break;
        asm volatile("global_load_dwordx2 %0, %1, %2 sc1\n\ts_waitcnt vmcnt(0)" : "=&v"(f) : "v"(vfl), "s"(bxf) : "memory");
      }
#pragma nounroll
      for (int it = 0; it < 64; ++it) {
        if (__all((int)((u32)__builtin_amdgcn_s_memrealtime() - f[0]) >= gate)) break;
      }
    }
    __builtin_amdgcn_s_setprio(0);
  } else if (w < 4) {
    __builtin_amdgcn_s_setprio(3);
    if (w == 2) {
      u32 vx[12];
#pragma unroll
      for (int i = 0; i < 12; ++i) vx[i] = x_off(i, lane, r0) + (4 - (i % 6)) * 1024;
      int kb = kb0 == 9 ? 0 : kb0 + 1;
      u32 xo = XRING + XSLOT + 4096;
#define ISSUE_X0                                                                                   \
  {                                                                                                \
    const char* xb = (const char*)x + kb * 128;                                                    \
    dma_m0<-4096>(vx[0], xb, xo);                                                                  \
    dma_nx<-3072>(vx[1], xb); dma_nx<-2048>(vx[2], xb); dma_nx<-1024>(vx[3], xb); dma_nx<0>(vx[4], xb); dma_nx<1024>(vx[5], xb); \
  }
#define ISSUE_X1                                                                                   \
  {                                                                                                \
    const char* xb = (const char*)x + kb * 128;                                                    \
    dma_m0<-4096>(vx[6], xb, xo + 6144);                                                           \
    dma_nx<-3072>(vx[7], xb); dma_nx<-2048>(vx[8], xb); dma_nx<-1024>(vx[9], xb); dma_nx<0>(vx[10], xb); dma_nx<1024>(vx[11], xb); \
    kb = kb == 9 ? 0 : kb + 1;                                                                     \
    xo = xo >= XRING + 3 * XSLOT ? xo - 3 * XSLOT : xo + XSLOT;                                    \
  }
      ISSUE_X0
      __builtin_amdgcn_sched_barrier(0);
      WO_OFFS
      asm volatile("" : "+v"(woff[0]), "+v"(woff[1]), "+v"(woff[2]), "+v"(woff[3]), "+v"(woff[4]));
      BAR
      ISSUE_X1
      ISSUE_X0
      BAR
      ISSUE_X1
      asm volatile("s_waitcnt vmcnt(12)\n\ts_barrier" ::: "memory");
#pragma unroll 1
      for (int j = 3; j < 10; ++j) {
        ISSUE_X0
        BAR
        ISSUE_X1
        asm volatile("s_waitcnt vmcnt(12)\n\ts_barrier" ::: "memory");
      }
      const char* xq = (const char*)x + (size_t)r0 * 1280;
      const int nv = mt == 25 ? 480 : 800;
#define RES1(I) if ((I) < 7 || (I) * 64 + lane < nv) dma_m0<(((I) % 5) - 4) * 1024>(woff[(I) % 5], xq + ((I) / 5) * 40960, HIM + ((I) / 5) * 5120 + 4096);
      PREP_V
      BAR
      PREP_S
      asm volatile("s_waitcnt vmcnt(0)\n\ts_barrier" ::: "memory");
      RES1(0) RES1(1) RES1(2) RES1(3) RES1(4) RES1(5) RES1(6)
      BAR
      RES1(7) RES1(8) RES1(9) RES1(10) RES1(11) RES1(12)
      if (lane < 10) dma_m0<0>(l16, (const char*)w_r + hg * 160, WR_OFF);
      BAR
    } else {
      u32x2 ep;
      u32x4 ra;
      u32 rb5;
      if (lane < 8) {
        *(u32x4*)(smem + ONES_OFF + lane * 16) = (u32x4){0x3F803F80u, 0x3F803F80u, 0x3F803F80u, 0x3F803F80u};
        *(u32x4*)(smem + CTL_OFF + lane * 16) = (u32x4){0u, 0u, 0u, 0u};
      }
      {
        const u32 o1 = lane == 26 || lane == 32 ? 0x3F803F80u : 0u;
        const u32 o2 = lane == 31 ? 0x3F803F80u : o1;
        if (lane < 33) *(u32x4*)(smem + REC_OFF + lane * 16) = (u32x4){o1, o1, o2, o2};
      }
      WO_OFFS
      asm volatile("" : "+v"(woff[0]), "+v"(woff[1]), "+v"(woff[2]), "+v"(woff[3]), "+v"(woff[4]));
      LDS_BARRIER();
      BAR BAR
      if (lane < 3) touch_line(lane == 2 ? ws + (4u << 20) - 128 * (bid + 1) : (const char*)out + (size_t)(r0 + lane * 47) * 1280 + hg * 160);
      asm volatile("global_load_dwordx2 %0, %1, off sc1" : "=v"(ep) : "v"(ws + WS_EPOCH) : "memory");
      asm volatile("global_load_dwordx4 %0, %2, %4\n\tglobal_load_dword %1, %3, %4 offset:1024"
                   : "=&v"(ra), "=&v"(rb5) : "v"(l16), "v"((u32)(lane * 4)), "s"(w_r) : "memory");
      BAR BAR BAR
      PREP_V
      BAR
      PREP_S
      BAR
      asm volatile("s_waitcnt vmcnt(0)" : "+v"(ep), "+v"(ra), "+v"(rb5)::"memory");
      const u64 e1 = (((u64)ep[1] << 32) | ep[0]) + (ep[0] == SENT - 1u ? 2ull : 1ull);
      float swr = lo_f(rb5) + hi_f(rb5);
#pragma unroll
      for (int e = 0; e < 4; ++e) swr += lo_f(ra[e]) + hi_f(ra[e]);
      swr = wave_sum(swr);
      if (lane == 0) {
        *(u32x2*)(smem + CTL_OFF) = (u32x2){(u32)e1, (u32)(e1 >> 32)};
        *(float*)(smem + CTL_OFF + 40) = swr;
      }
      int ra0 = r0 - 16 + lane, ra1 = r0 + 48 + (lane & 31);
      ra0 = ra0 < 0 ? 0 : (ra0 > SEQ - 1 ? SEQ - 1 : ra0);
      ra1 = ra1 > SEQ - 1 ? SEQ - 1 : ra1;
      const int hd = lane / 5, pt = lane - hd * 5;
      u32 avp = REC_OFF + (hd >> 1) * 192 + (pt < 4 ? pt * 48 + (hd & 1) * 8 : hd * 48 + 16), vvp = lane < 20 ? (lane >> 1) * 128 + (lane & 1) * 16 : 0;
      u32 vf = (lane < 48 ? lane : 0) * 128, va0 = ra0 * 8, va1 = ra1 * 8, vt0 = TAB_OFF + lane * 4, vt1 = TAB_OFF + 256 + (lane & 31) * 4;
      asm volatile("" : "+v"(avp), "+v"(vvp), "+v"(vf), "+v"(va0), "+v"(va1), "+v"(vt0), "+v"(vt1));
#pragma nounroll
      for (int h = 7; h <= HK0; ++h) BAR
      u32x2 f, t0, t1;
      u32x4 vp4;
      GLD2(f, vf, ws + WS_HFLAG, 0);
      GLD2(t0, va0, ws + WS_LN1, 0);
      GLD2(t1, va1, ws + WS_LN1, 0);
      asm volatile("global_load_dwordx4 %0, %1, %2 sc1" : "=v"(vp4) : "v"(vvp), "s"(ws + WS_VP + hg * 1280) : "memory");
#pragma unroll
      for (int h = HK0 + 1; h <= 17; ++h) BAR
      WO_RUN(10)
      WO_RUN(11)
      BAR
      asm volatile("s_waitcnt vmcnt(10)" : "+v"(t0), "+v"(t1), "+v"(f), "+v"(vp4)::"memory");
#pragma nounroll
      for (int it = 0; it < SPIN_CAP; ++it) {
        if (__all(t0[1] == (u32)e1 && t1[1] == (u32)e1 && f[0] == (u32)e1 && f[1] == (u32)(e1 >> 32) && vp4[1] == (u32)e1 && vp4[3] == (u32)e1)) break;
        asm volatile("global_load_dwordx2 %2, %6, %9 sc1\n\tglobal_load_dwordx2 %0, %4, %8 sc1\n\tglobal_load_dwordx2 %1, %5, %8 sc1\n\t"
                     "global_load_dwordx4 %3, %7, %10 sc1\n\ts_waitcnt vmcnt(0)"
                     : "=&v"(t0), "=&v"(t1), "=&v"(f), "=&v"(vp4)
                     : "v"(va0), "v"(va1), "v"(vf), "v"(vvp), "s"(ws + WS_LN1), "s"(ws + WS_HFLAG), "s"(ws + WS_VP + hg * 1280) : "memory");
      }
      *(u32*)(smem + vt0) = t0[0] << 16;
      *(u32*)(smem + vt1) = t1[0] << 16;
      *(float*)(smem + vt0 + 384) = __builtin_amdgcn_rcpf(hi_f(t0[0]));
      *(float*)(smem + vt1 + 384) = __builtin_amdgcn_rcpf(hi_f(t1[0]));
      if (lane < 20) *(u32x2*)(smem + avp) = (u32x2){vp4[0], vp4[2]};
      asm volatile("" ::: "memory");
      WO_RUN(14)
      WO_RUN(15)
      LDS_BARRIER();
      WO_RUN(16)
      WO_RUN(17)
    }
    __builtin_amdgcn_s_setprio(0);
  } else {
    const int cw = w - 4;
    prepv(lane);
    preps(mt, hg);
    if (cw >= 4) __builtin_amdgcn_s_setprio(1);
    const int xa0 = XRING + r16 * 128 + ((g ^ ((r16 >> 1) & 7)) * 16);
    const u32 m1 = r16 == 0 ? 0u : 1u;
    const u32 xz0 = r16 == 0 ? (u32)ONES_OFF : (u32)xa0;
    int wa[2];
#pragma unroll
    for (int c = 0; c < 2; ++c) {
      const int ct = cw < 7 ? 2 * cw + c : 14;
      const int seg = ct / 5, tt = ct - seg * 5;
      const int G = tt < 4 ? 5 * tt + p4 : 5 * p4 + 4;
      wa[c] = (16 * (g >> 1) + 4 * (g & 1) + q4) * 512 + ((seg * 160 + G * 8) ^ ((4 * (g & 1) + q4) << 5));
    }
    auto run = [&](auto tyc) __attribute__((always_inline)) {
      constexpr int TY = decltype(tyc)::value;
      constexpr int NW = TY == 2 ? 1 : 2;
      f32x4 acc[2][6];
#pragma unroll
      for (int c = 0; c < 2; ++c)
#pragma unroll
        for (int r = 0; r < 6; ++r) acc[c][r] = (f32x4){0.f, 0.f, 0.f, 0.f};
      u32x4 xA[6], xB[6];
      u32x2 wA[4], wB[4];
#define RX(D, A, O) asm volatile("ds_read_b128 %0, %1 offset:%2" : "=v"(D) : "v"(A), "n"(O) : "memory");
#define RW(D, A, O) asm volatile("ds_read_b64_tr_b16 %0, %1 offset:%2" : "=v"(D) : "v"(A), "n"(O) : "memory");
#define MM(C, R, XF, WF)                                                                           \
  if ((C) < NW) {                                                                                    \
    const bf16x8 wf_ = mk8(WF[2 * (C)][0], WF[2 * (C)][1], WF[2 * (C) + 1][0], WF[2 * (C) + 1][1]); \
    const bf16x8 xf_ = __builtin_bit_cast(bf16x8, XF[R]);                                          \
    acc[C][R] = TY == 0 ? MFMA(wf_, xf_, acc[C][R]) : MFMA(xf_, wf_, acc[C][R]);                   \
    __builtin_amdgcn_sched_barrier(0);                                                             \
  }
#define READS(QX, QW)                                                                              \
  RW(QW[0], aw0, 0) RW(QW[1], aw0, 4096) RX(QX[1], ax, 2048)                                       \
  if (NW == 2) { RW(QW[2], aw1, 0) RW(QW[3], aw1, 4096) }                                          \
  RX(QX[2], ax, 4096) RX(QX[3], ax, 6144) RX(QX[4], ax, 8192) RX(QX[5], ax, 10240) RX(QX[0], az, 0)
#define ARRIVED(PX, PW)                                                                            \
  if (NW == 2)                                                                                     \
    asm volatile("s_waitcnt lgkmcnt(0)" : "+v"(PX[0]), "+v"(PX[1]), "+v"(PX[2]), "+v"(PX[3]), "+v"(PX[4]), "+v"(PX[5]), \
                 "+v"(PW[0]), "+v"(PW[1]), "+v"(PW[2]), "+v"(PW[3])::"memory");                    \
  else                                                                                             \
    asm volatile("s_waitcnt lgkmcnt(0)" : "+v"(PX[0]), "+v"(PX[1]), "+v"(PX[2]), "+v"(PX[3]), "+v"(PX[4]), "+v"(PX[5]), \
                 "+v"(PW[0]), "+v"(PW[1])::"memory");                                              \
  __builtin_amdgcn_sched_barrier(0);
#define SB __builtin_amdgcn_sched_barrier(0);
#define WT(N, ...) asm volatile("s_waitcnt lgkmcnt(" #N ")" : __VA_ARGS__::"memory"); SB
#define HSTAGE2(PX, PW, QX, QW, NX)                                                                \
  BAR                                                                                              \
  WT(7, "+v"(PW[0]), "+v"(PW[1]), "+v"(PX[1]))                                                     \
  MM(0, 1, PX, PW) RW(QW[0], aw0, 0)                                                               \
  WT(6, "+v"(PW[2]), "+v"(PW[3]))                                                                  \
  MM(1, 1, PX, PW) RW(QW[1], aw0, 4096)                                                            \
  WT(6, "+v"(PX[2]))                                                                               \
  MM(0, 2, PX, PW) RX(QX[1], ax, 2048) SB                                                          \
  MM(1, 2, PX, PW) RW(QW[2], aw1, 0)                                                               \
  WT(7, "+v"(PX[3]))                                                                               \
  MM(0, 3, PX, PW) RW(QW[3], aw1, 4096) SB                                                         \
  MM(1, 3, PX, PW) RX(QX[2], ax, 4096)                                                             \
  WT(6, "+v"(PX[4]), "+v"(PX[5]), "+v"(PX[0]))                                                     \
  MM(0, 4, PX, PW) RX(QX[3], ax, 6144) SB                                                          \
  MM(1, 4, PX, PW) RX(QX[4], ax, 8192) SB                                                          \
  MM(0, 5, PX, PW) RX(QX[5], ax, 10240) SB                                                         \
  MM(1, 5, PX, PW) RX(QX[0], az, 0) SB                                                             \
  MM(0, 0, PX, PW)                                                                                 \
  NX asm volatile("" : "+v"(aw0), "+v"(aw1), "+v"(ax), "+v"(az), "+s"(so), "+s"(xo)); SB           \
  MM(1, 0, PX, PW)
#define HSTAGE1(PX, PW, QX, QW, NX)                                                                \
  BAR                                                                                              \
  ARRIVED(PX, PW)                                                                                  \
  MM(0, 1, PX, PW) RW(QW[0], aw0, 0) RW(QW[1], aw0, 4096) SB                                       \
  MM(0, 2, PX, PW) RX(QX[1], ax, 2048) RX(QX[2], ax, 4096) SB                                      \
  MM(0, 3, PX, PW) RX(QX[3], ax, 6144) RX(QX[4], ax, 8192) SB                                      \
  MM(0, 4, PX, PW) RX(QX[5], ax, 10240) RX(QX[0], az, 0) SB                                        \
  MM(0, 5, PX, PW)                                                                                 \
  NX asm volatile("" : "+v"(aw0), "+v"(ax), "+v"(az), "+s"(so), "+s"(xo)); SB                      \
  MM(0, 0, PX, PW)
#define HSTAGE(...) if (NW == 2) { HSTAGE2(__VA_ARGS__) } else { HSTAGE1(__VA_ARGS__) }
      u32 aw0 = wa[0], aw1 = wa[1], ax = xa0, az = xz0;
      asm volatile("s_waitcnt vmcnt(0)\n\ts_barrier" ::: "memory");
      READS(xA, wA)
      aw0 += HSLOT; aw1 += HSLOT; ax ^= 64; az ^= 64;
      int so = 2 * HSLOT, xo = XSLOT;
#pragma unroll 1
      for (int s = 0; s < 9; ++s) {
        HSTAGE(xA, wA, xB, wB, aw0 = wa[0] + so; aw1 = wa[1] + so; ax = xa0 + xo; az = xz0 + xo * m1;
               so = so >= 4 * HSLOT ? 0 : so + 2 * HSLOT; xo = xo >= 3 * XSLOT ? 0 : xo + XSLOT;)
        HSTAGE(xB, wB, xA, wA, aw0 += HSLOT; aw1 += HSLOT; ax ^= 64; az ^= 64;)
      }
      {
        HSTAGE(xA, wA, xB, wB, )
        ARRIVED(xB, wB)
        BAR
        SB
      }
      char* ex = smem + EXO + cw * 6144 + lane * 8;
      if (TY == 0) {
        float st[6];
        const u32 ta = TAB_OFF + r16 * 4;
#define RT(R) asm volatile("ds_read_b32 %0, %1 offset:%2" : "=v"(st[R]) : "v"(ta), "n"(64 * (R)) : "memory");
        RT(1) RT(2) RT(3) RT(4) RT(5) RT(0)
        float cs[2][4];
#define FMS(A, M, CS) ((A) - (M) * (CS))
#define EP0(C, R)                                                                                  \
  {                                                                                                \
    const u32 a = cvt2((f32x2){FMS(acc[C][R][0], st[R], cs[C][0]), FMS(acc[C][R][1], st[R], cs[C][1])}); \
    const u32 b = cvt2((f32x2){FMS(acc[C][R][2], st[R], cs[C][2]), FMS(acc[C][R][3], st[R], cs[C][3])}); \
    *(u32x2*)(ex + (C) * 3072 + (R) * 512) = (u32x2){a, b};                                        \
    SB                                                                                             \
  }
        MM(0, 0, xB, wB) MM(1, 0, xB, wB) MM(0, 1, xB, wB) MM(1, 1, xB, wB)
#pragma unroll
        for (int i = 0; i < 4; ++i) cs[0][i] = dppf<0x150>(acc[0][0][i]);
        SB
        MM(0, 2, xB, wB)
#pragma unroll
        for (int i = 0; i < 4; ++i) cs[1][i] = dppf<0x150>(acc[1][0][i]);
        WT(0, "+v"(st[0]), "+v"(st[1]), "+v"(st[2]), "+v"(st[3]), "+v"(st[4]), "+v"(st[5]))
        MM(1, 2, xB, wB) EP0(0, 1)
        MM(0, 3, xB, wB) EP0(1, 1)
        MM(1, 3, xB, wB) EP0(0, 2)
        MM(0, 4, xB, wB) EP0(1, 2)
        MM(1, 4, xB, wB) EP0(0, 3)
        MM(0, 5, xB, wB) EP0(1, 3)
        MM(1, 5, xB, wB)
        if (cw >= 2) {
          EP0(1, 0)
          if (cw >= 3) EP0(0, 0)
        }
        EP0(0, 4) EP0(1, 4) EP0(0, 5) EP0(1, 5)
      } else {
        f32x4 mbv[6];
        const u32 ta = TAB_OFF + g * 16;
#define RV(R) RX(mbv[R], ta, 64 * (R))
        RV(1) RV(2) RV(3) RV(4) RV(5) RV(0)
        float cs[2];
#define CS1(C)                                                                                     \
  if ((C) < NW) {                                                                                  \
    const u32 v0 = __float_as_uint(acc[C][0][0]);                                                  \
    const u32 v1 = __builtin_amdgcn_permlane16_swap(v0, v0, false, false)[0];                      \
    cs[C] = __uint_as_float(__builtin_amdgcn_permlane32_swap(v1, v1, false, false)[0]);            \
    SB                                                                                             \
  }
#define EP1(C, R)                                                                                  \
  if ((C) < NW) {                                                                                  \
    const f32x4 v = (f32x4){FMS(acc[C][R][0], mbv[R][0], cs[C]), FMS(acc[C][R][1], mbv[R][1], cs[C]), FMS(acc[C][R][2], mbv[R][2], cs[C]), FMS(acc[C][R][3], mbv[R][3], cs[C])}; \
    *(u32x2*)(ex + (C) * 3072 + (R) * 512) = (u32x2){cvt2((f32x2){v[0], v[1]}), cvt2((f32x2){v[2], v[3]})}; \
    SB                                                                                             \
  }
        MM(0, 0, xB, wB) MM(1, 0, xB, wB) MM(0, 1, xB, wB) MM(1, 1, xB, wB)
        CS1(0)
        MM(0, 2, xB, wB)
        CS1(1)
        WT(0, "+v"(mbv[0]), "+v"(mbv[1]), "+v"(mbv[2]), "+v"(mbv[3]), "+v"(mbv[4]), "+v"(mbv[5]))
        MM(1, 2, xB, wB) EP1(0, 1)
        MM(0, 3, xB, wB) EP1(1, 1)
        MM(1, 3, xB, wB) EP1(0, 2)
        MM(0, 4, xB, wB) EP1(1, 2)
        MM(1, 4, xB, wB) EP1(0, 3)
        MM(0, 5, xB, wB) EP1(1, 3)
        MM(1, 5, xB, wB)
        EP1(0, 0) EP1(1, 0) EP1(0, 4) EP1(1, 4) EP1(0, 5) EP1(1, 5)
      }
    };
    if (cw < 5) run(icst<0>{});
    else if (cw < 7) run(icst<1>{});
    else run(icst<2>{});
  }
  __builtin_amdgcn_s_setprio(0);
  const bool live = r0 + rt * 16 < SEQ;
  if (!live) {
    if (w >= 2) LDS_BARRIER();
    if (w != 7) {
      asm volatile("s_waitcnt vmcnt(0)" ::: "memory");
      return;
    }
  } else if (w >= 2)
    asm volatile(ATT_ASM
                 :: [kh] "s"(kh), [l16] "v"(l16), [os] "s"(os), [os4] "s"(os4), [b0] "v"(bias[0]), [b1] "v"(bias[1]),
                    [s0] "v"(sel[0]), [s1] "v"(sel[1]), [s2] "v"(sel[2]), [s3] "v"(sel[3]), [e4] "v"(e4), [eh] "v"(eh), [aq] "v"(aq),
                    [aka] "v"(aka), [akb] "v"(akb), [arec] "v"(arec), [vpp] "v"(vpp), [sst] "s"(sst), [w] "s"(w)
                 : "memory", "s41", "s46", "s47", "s48", "s49", "s50", "s51", "scc", "v24", "v25", "v26", "v27", "v28", "v29", "v30", "v31", "v32", "v33", "v34", "v35", "v36", "v37", "v38", "v39", "v40", "v41", "v42", "v43", "v44", "v45", "v46", "v47", "v48", "v49", "v50", "v51", "v52", "v53", "v54", "v55", "v56", "v57", "v58", "v59", "v60", "v61", "v62", "v63", "v64", "v65", "v66", "v67", "v68", "v69", "v70", "v71", "v72", "v73", "v74", "v75", "v76", "v77", "v78", "v79", "v80", "v81", "v82", "v83", "v84", "v85", "v86", "v87");
  u32x4 hwa, hwb;
  u32x2 hwc;
  const int grp = rt < 3 ? 0 : 1;
  if (w < 8) {
    const int rmax = mt == 25 ? 2 : 4;
    const int rk = rt < rmax ? rt : rmax, rd = (rt ^ 1) < rmax ? (rt ^ 1) : rmax;
    u32 ta, tk;
    if (w == 0) asm volatile("s_waitcnt vmcnt(0)" ::: "memory");
    if (w == 2 || w == 3) asm volatile("s_waitcnt vmcnt(3)" ::: "memory");
    asm volatile("s_waitcnt lgkmcnt(0)\n\ts_barrier\n\t" OP_ASM
                 : "=&{v[84:87]}"(hwa), "=&{v[88:91]}"(hwb), "=&{v[92:93]}"(hwc), [ta] "=&v"(ta), [tk] "=&v"(tk)
                 : [w] "s"(w), [l16] "v"(l16), [oa0] "s"(oa[0]), [so0] "s"(so_[0]), [oa1] "s"(oa[1]), [so1] "s"(so_[1]), [oa2] "s"(oa[2]), [so2] "s"(so_[2]), [oa3] "s"(oa[3]), [so3] "s"(so_[3]), [oa4] "s"(oa[4]), [so4] "s"(so_[4]), [oa5] "s"(oa[5]), [so5] "s"(so_[5]), [oa6] "s"(oa[6]), [so6] "s"(so_[6]), [oa7] "s"(oa[7]), [so7] "s"(so_[7]), [oa8] "s"(oa[8]), [so8] "s"(so_[8]), [oa9] "s"(oa[9]), [so9] "s"(so_[9]),
                   [pm] "v"(pm), [pf] "v"(pf), [ra] "v"(w < 3 ? (u32)(CTL_OFF + 72) : hi), [e4] "v"(e4),
                   [aw] "v"((lane & 1) ? (u32)(WR_OFF + g * 40) : (u32)ONES_OFF),
                   [vk] "v"(l16 + rk * 20480), [vd] "v"(l16 + rd * 20480), [sk] "s"((w & 1) * 6144),
                   [sd] "s"(EXO + (w < 4 ? 4 + w : rt ^ 1) * 5120), [sr] "s"(EXO + rt * 5120),
                   [sc] "s"(CTL_OFF + 48 + (w < 4 ? 4 : rt ^ 1) * 4), [sp] "s"(PST_OFF + rt * 256), [sa] "s"(CTL_OFF + 8 + grp * 4),
                   [sst] "s"(sst), [cp] "s"(ws + WS_PC + (2 * mt + grp) * 1280 + hg * 160), [oback] "n"(OBACK), [cap] "n"(SPIN_CAP)
                 : "memory", "vcc", "scc", "s43", "v24", "v25", "v26", "v27", "v28", "v29", "v30", "v31", "v32", "v33", "v34", "v35", "v36", "v37", "v38", "v39", "v40", "v41", "v42", "v43", "v44", "v45", "v46", "v47", "v48", "v49", "v50", "v51", "v52", "v53", "v54", "v55", "v56", "v57", "v58", "v59", "v60", "v61", "v62", "v63", "v64", "v65", "v66", "v67", "v68", "v69", "v70", "v71", "v72", "v73", "v74", "v75", "v76", "v77", "v78", "v79", "v80", "v81", "v82", "v83", "v94", "v95", "v96", "v97", "v98", "v99", "v100", "v101", "v102", "v103", "v104", "v105", "v106", "v107", "v108", "v109", "v110", "v111", "v112", "v113", "v114", "v115", "v116", "v117", "v118", "v119", "v120", "v121", "v122", "v123", "v124", "v125", "v126", "v127", "v128", "v129", "v130", "v131", "v132", "v133", "v134", "v135", "v136", "v137", "v138", "v139", "v140", "v141", "v142", "v143", "v144", "v145", "v146", "v147", "v148", "v149", "v150", "v151", "v152", "v153", "v154", "v155", "v156", "v157", "v158", "v159");
    if (!live || w < 3) return;
  } else {
    if (w == 8 || w == 11) {
      const int cg = w == 11, gid = 2 * mt + cg;
      const char* sp = sst - hg * 1024;
      const char* sp2 = sp + (cg ? 0 : 7936);
      const u32 vq = (lane >> 3) * 1024 + (lane & 7) * 16 + (cg ? 640 : 768);
      u32x4 pq[3];
      u64 tb;
#define PLOADS "global_load_dwordx4 %0, %4, %5 sc1\n\tglobal_load_dwordx4 %1, %4, %5 offset:128 sc1\n\tglobal_load_dwordx4 %2, %4, %6 sc1\n\t"
      asm volatile("s_setprio 3\n\ts_waitcnt lgkmcnt(0)\n\ts_barrier\n\t" "s_sleep %7\n\t" PLOADS "s_memrealtime %3\n\ts_setprio 1"
                   : "=&v"(pq[0]), "=&v"(pq[1]), "=&v"(pq[2]), "=&s"(tb) : "v"(vq), "s"(sp), "s"(sp2), "n"(PDEL1) : "memory");
      int lane2 = lane;
      asm volatile("" : "+v"(lane2));
      const int n16 = lane2 & 15;
      u32x4 sl;
      u32x2 km;
#pragma unroll
      for (int e = 0; e < 4; ++e) sl[e] = n16 == 2 * e ? 0x3F80u : (n16 == 2 * e + 1 ? 0x3F800000u : 0u);
      u32 rid = (u32)((((lane2 >> 4) & 1) * 2 + (lane2 >> 5)) * 4 + (lane2 & 3)) << 10;
      u32 rid1 = rid + (16 << 10), rid2 = rid + (cg ? 0 : 32 << 10), msk = n16 < 4 ? 0xFFFF0000u : 0u;
      u32 ah = HIM + cg * 7680 + (lane2 < 10 ? lane2 : 9) * 16;
      km[1] = *(const u32*)(smem + CTL_OFF);
      const char* kp = ws + WS_PK + gid * 6144 + hg * 768;
      const char* cp = ws + WS_PC + gid * 1280 + hg * 160;
      asm volatile("s_waitcnt lgkmcnt(0)" : "+s"(tb), "+s"(kp), "+s"(cp)::"memory");
      km[0] = ((u32)tb >> 1) & 1023u;
      asm volatile("s_setprio " "3" "\n\ts_waitcnt vmcnt(0)" : "+v"(pq[0]), "+v"(pq[1]), "+v"(pq[2]), "+v"(sl), "+v"(rid), "+v"(rid1), "+v"(rid2), "+v"(msk), "+v"(ah), "+v"(km)::"memory");
#define PMISS __any(max(max(max(pq[0][0], pq[0][2]), max(pq[1][0], pq[1][2])), max(pq[2][0], pq[2][2])) == SENT)
      if (__builtin_expect(PMISS, 0)) {
#pragma nounroll
        for (int it = 0; it < SPIN_CAP; ++it) {
          u64 t2;
          asm volatile(PLOADS "s_waitcnt vmcnt(0)" : "=&v"(pq[0]), "=&v"(pq[1]), "=&v"(pq[2]), "=&s"(t2) : "v"(vq), "s"(sp), "s"(sp2) : "memory");
          if (!PMISS) break;
          __builtin_amdgcn_s_sleep(2);
        }
      }
      u32 pkey = 0;
#pragma unroll
      for (int t = 0; t < 3; ++t) {
        const f32x4 ys = MFMA(__builtin_bit_cast(bf16x8, pq[t]), __builtin_bit_cast(bf16x8, sl), ((f32x4){0.f, 0.f, 0.f, 0.f}));
        const float y = tr_sum(ys);
        pkey = max(pkey, (mono(y * __builtin_amdgcn_rcpf(dppf<0x104>(y))) & msk) | (t == 0 ? rid : (t == 1 ? rid1 : rid2)));
      }
      pkey = max(pkey, dppu<0xB1>(pkey));
      pkey = max(pkey, dppu<0x4E>(pkey));
      pkey = max(max((u32)__builtin_amdgcn_readlane(pkey, 0), (u32)__builtin_amdgcn_readlane(pkey, 16)),
                 max((u32)__builtin_amdgcn_readlane(pkey, 32), (u32)__builtin_amdgcn_readlane(pkey, 48)));
      km[0] |= pkey;
      asm volatile("s_mov_b32 exec_lo, 0x01010101\n\ts_mov_b32 exec_hi, 0x0101\n\tglobal_store_dwordx2 %0, %1, %2 sc1\n\ts_mov_b64 exec, -1\n\ts_setprio 2" ::"v"(l16), "v"(km), "s"(kp) : "memory");
      ah += ((pkey >> 10) & 63u) * 160;
      if (lane == 0) *(u32*)(smem + CTL_OFF + 24 + cg * 4) = ah;
      __builtin_amdgcn_s_setprio(0);
#pragma nounroll
      for (int it = 0; it < 32; ++it) {
        if ((int)((u32)__builtin_amdgcn_s_memrealtime() - (u32)tb) >= PSL) break;
        __builtin_amdgcn_s_sleep(6);
      }
      __builtin_amdgcn_s_setprio(2);
      asm volatile("s_mov_b32 s40, %[cap]\n"
                   "1:\n\tds_read_b32 v100, %[ac]\n\tds_read_b32 v101, %[ac] offset:24\n\tds_read_b128 v[96:99], %[ah]\n\ts_sub_u32 s40, s40, 1\n\ts_cbranch_scc1 2f\n\ts_waitcnt lgkmcnt(0)\n\t"
                   "v_cmp_gt_u32 vcc, %[nf], v100\n\ts_cbranch_vccz 2f\n\ts_sleep 2\n\ts_branch 1b\n"
                   "2:\n\ts_waitcnt lgkmcnt(0)\n\tv_cmp_eq_u32 vcc, 0, v101\n\ts_cbranch_vccz 3f\n\t"
                   "s_mov_b64 exec, 0x3ff\n\tglobal_store_dwordx4 %[l16], v[96:99], %[cp] sc1\n\ts_mov_b64 exec, -1\n3:"
                   ::[cap] "n"(SPIN_CAP * 8), [ac] "v"((u32)(CTL_OFF + 8 + cg * 4)), [ah] "v"(ah), [nf] "s"(3 - cg), [l16] "v"(l16), [cp] "s"(cp)
                   : "memory", "vcc", "scc", "s40", "v96", "v97", "v98", "v99", "v100", "v101");
      return;
    }
    LDS_BARRIER();
    if (w == 10 && mt == 25) return;
    const int cg = w - 9, gid = 2 * mt + cg;
    const int nr = cg ? 31 : 47;
    const int lr = lane < nr ? lane : nr;
    const int lrow = cg * 48 + lr;
    const int li = (lane & 31) < 20 ? (lane & 31) : 19;
    asm volatile(COL_ASM
                 :
                 : [lq] "v"((u32)(lr * 16 + hg * 1024)), [ac] "v"((u32)(CTL_OFF + 8 + cg * 4)), [nf] "s"(3 - cg),
                   [sp] "s"(ws + WS_STAT + (size_t)gid * 8192), [ao] "v"((u32)(PST_OFF + lrow * 16)), [rdm] "s"(1.0f / DM),
                   [swr] "v"(*(const float*)(smem + CTL_OFF + 40)), [msk] "s"(0xFFFFF000u), [rid] "v"(0x800u | (u32)(2047 - (r0 + lrow))),
                   [at] "v"((u32)(TAB_OFF + lrow * 8)), [zr] "v"(l16), "{v99}"(*(const u32*)(smem + CTL_OFF)),
                   [kp] "s"(ws + WS_PK + gid * 6144 + hg * 768 + 8), [ak] "v"((u32)(CTL_OFF + 16 + cg * 4)), [rb] "s"(2047 - r0 - cg * 48),
                   [ah] "v"((u32)(HIM + cg * 7680 + li * 8)), [co] "v"((u32)((lane < 32 ? 1280 : 0) + li * 8)),
                   [cp] "s"(ws + WS_CAND + gid * 2560 + hg * 160), [pdel] "n"(PDEL2), [cap] "n"(SPIN_CAP)
                 : "memory", "vcc", "scc", "s40", "s41", "s42", "s43", "s44", "s45", "s46", "s47", "v64", "v65", "v66", "v67", "v68", "v69", "v70", "v71", "v72", "v73", "v74", "v75", "v76", "v77", "v78", "v79", "v80", "v81", "v82", "v83", "v84", "v85", "v86", "v87", "v88", "v89", "v90", "v91", "v92", "v93", "v94", "v95", "v96", "v97", "v98", "v100", "v101", "v102", "v103", "v104", "v105", "v106", "v107", "v108", "v109", "v110", "v111", "v112", "v113", "v114", "v115", "v116", "v117", "v118", "v119", "v120", "v121", "v122", "v123");
    return;
  }
  const u32 hw[10] = {hwa[0], hwa[1], hwa[2], hwa[3], hwb[0], hwb[1], hwb[2], hwb[3], hwc[0], hwc[1]};
  f32x2 hf[10];
#pragma unroll
  for (int e = 0; e < 10; ++e) {
    hf[e] = unpk(hw[e]);
    asm volatile("" : "+v"(hf[e]));
  }
  const char* obase = (const char*)out + (size_t)r0 * 1280 + hg * 160;
  u32 tilekey;
  asm volatile("" : "+s"(obase));
  u32 aow = EXO + rt * 5120 + r16 * 160 + g * 40, aor = EXO + rt * 5120 + l16, vo3[3], rr3[3];
#pragma unroll
  for (int i = 0; i < 3; ++i) {
    const u32 c = i * 64 + lane, r = c / 10u;
    vo3[i] = (rt * 16 + r) * 1120 + rt * 2560 + c * 16;
    rr3[i] = c < 160u ? 2047 - (r0 + rt * 16 + r) : 4096u;
    asm volatile("" : "+v"(vo3[i]), "+v"(rr3[i]));
  }
  asm volatile("" : "+v"(aow), "+v"(aor));
  f32x2 st2;
  asm volatile("s_mov_b32 s40, %[cap]\n"
               "1:\n\ts_sleep 1\n\tds_read_b32 %[k], %[ak]\n\tds_read_b64 %[s], %[at]\n\ts_sub_u32 s40, s40, 1\n\ts_waitcnt lgkmcnt(0)\n\ts_cbranch_scc1 2f\n\t"
               "v_cmp_eq_u32 vcc, 0, %[k]\n\ts_cbranch_vccnz 1b\n"
               "2:"
               : [k] "=&v"(tilekey), [s] "=&v"(st2) : [cap] "n"(SPIN_CAP * 8), [ak] "v"((u32)(CTL_OFF + 16 + grp * 4)), [at] "v"((u32)(TAB_OFF + (rt * 16 + r16) * 8))
               : "memory", "vcc", "scc", "s40");
  u32 ok[10];
#pragma unroll
  for (int e = 0; e < 10; ++e) ok[e] = cvt2(__builtin_elementwise_fma(hf[e] - (f32x2){st2[0], st2[0]}, (f32x2){st2[1], st2[1]}, hf[e]));
  {
#pragma unroll
    for (int e = 0; e < 5; ++e) asm volatile("ds_write_b64 %0, %1 offset:%2" ::"v"(aow), "v"((u32x2){ok[2 * e], ok[2 * e + 1]}), "n"(e * 8) : "memory");
    u32x4 o3[3];
    asm volatile("ds_read_b128 %0, %3\n\tds_read_b128 %1, %3 offset:1024\n\tds_read_b128 %2, %3 offset:2048\n\ts_waitcnt lgkmcnt(0)"
                 : "=&v"(o3[0]), "=&v"(o3[1]), "=&v"(o3[2]) : "v"(aor) : "memory");
    if (OSL) __builtin_amdgcn_s_sleep(OSL);
#pragma unroll
    for (int i = 0; i < 3; ++i)
      if (rr3[i] != (tilekey & 2047u) && rr3[i] != 4096u)
        asm volatile("global_store_dwordx4 %0, %1, %2" " sc1" "\n\ts_nop 1" ::"v"(vo3[i]), "v"(o3[i]), "s"(obase) : "memory");
  }
}

extern "C" void kernel_launch(void* const* d_in, const int* in_sizes, int n_in,
                              void* d_out, int out_size, void* d_ws, size_t ws_size,
                              hipStream_t stream) {
  const u16* x = (const u16*)d_in[0];
  const u16* wqkv = (const u16*)d_in[1];
  const u16* wo = (const u16*)d_in[2];
  const u16* w_r = (const u16*)d_in[4];
  const u16* w_gate = (const u16*)d_in[5];
  const u16* w_up = (const u16*)d_in[6];
  const u16* w_down = (const u16*)d_in[7];
  (void)hipFuncSetAttribute((const void*)fused_block, hipFuncAttributeMaxDynamicSharedMemorySize, LDS_TOTAL);
  fused_block<<<256, 768, LDS_TOTAL, stream>>>(x, wqkv, wo, w_r, w_gate, w_up, w_down, (u16*)d_out, (char*)d_ws);
}
```

```cpp
#include <hip/hip_runtime.h>
#include <hip/hip_bf16.h>
#include <stdint.h>


#define SEQ 2048
#define DM 640
#define FF 1536

typedef __attribute__((ext_vector_type(8))) short bf16x8;
typedef __attribute__((ext_vector_type(4))) short s16x4;
typedef __attribute__((ext_vector_type(4))) float f32x4;
typedef __attribute__((ext_vector_type(2))) float f32x2;
typedef __attribute__((ext_vector_type(2))) __bf16 bf16x2_t;
typedef __attribute__((address_space(3))) s16x4* lds_s16x4_p;
typedef unsigned short u16;
typedef unsigned int u32;
typedef unsigned long long u64;
typedef __attribute__((ext_vector_type(4))) unsigned int u32x4;
typedef __attribute__((ext_vector_type(2))) unsigned int u32x2;

#define HK0 15
#define RGAP 6
#define CGAP 6
#define KGAP 7
#define CDEL 10
#define PDEL2 7
#define OBACK 2
#define OSL 0
#define PDELAY 220
#define FDELAY 450
#define KSTART 1050
#define PGAP 5
#define PDT 18
#define PSL 185
#define VDELAY 150
#define CCD 322
#define TQD 160
#define KCD 314
#define KOLD 322
#define OPP 1
#define TWS 12
#define TWN 5
#define PCD 230
#define PTHR 0.91f
#define GADD 61
#define OORD 0, 4, 5, 6, 7, 1, 2, 3
#define CODE_BYTES 35272

#define LDS_BARRIER() do { __builtin_amdgcn_sched_barrier(0); asm volatile("s_waitcnt lgkmcnt(0)\n\ts_barrier" ::: "memory"); __builtin_amdgcn_sched_barrier(0); } while (0)
__device__ __forceinline__ float bf2f(u16 v) { return __uint_as_float(((u32)v) << 16); }
__device__ __forceinline__ u16 f2bf(float f) { return __builtin_bit_cast(u16, (__bf16)f); }
__device__ __forceinline__ float rbf(float f) { return bf2f(f2bf(f)); }
__device__ __forceinline__ float lo_f(u32 w) { return __uint_as_float(w << 16); }
__device__ __forceinline__ float hi_f(u32 w) { return __uint_as_float(w & 0xFFFF0000u); }
__device__ __forceinline__ u32 cvt2(f32x2 v) { return __builtin_bit_cast(u32, __builtin_convertvector(v, bf16x2_t)); }
__device__ __forceinline__ f32x2 unpk(u32 w) { return (f32x2){lo_f(w), hi_f(w)}; }
#define NORM2(HV, MB, RD) cvt2(unpk(cvt2(unpk(HV) - (f32x2){MB, MB})) * (f32x2){RD, RD})
__device__ __forceinline__ s16x4 tr_read(const char* p) {
  return __builtin_amdgcn_ds_read_tr16_b64_v4i16((lds_s16x4_p)(p));
}
template <int V> struct icst { static constexpr int value = V; };
template <int CTRL>
__device__ __forceinline__ float dppf(float v) {
  return __builtin_bit_cast(float, __builtin_amdgcn_update_dpp(0, __builtin_bit_cast(int, v), CTRL, 0xf, 0xf, true));
}
__device__ __forceinline__ float row8_sum(float v) {
  v += dppf<0xB1>(v);
  v += dppf<0x4E>(v);
  v += dppf<0x141>(v);
  return v;
}
__device__ __forceinline__ float row16_sum(float v) {
  v = row8_sum(v);
  v += dppf<0x140>(v);
  return v;
}
__device__ __forceinline__ float xg_sum(float v) {
  u32 a = __float_as_uint(v);
  auto r = __builtin_amdgcn_permlane32_swap(a, a, false, false);
  v = __uint_as_float(r[0]) + __uint_as_float(r[1]);
  a = __float_as_uint(v);
  auto r2 = __builtin_amdgcn_permlane16_swap(a, a, false, false);
  return __uint_as_float(r2[0]) + __uint_as_float(r2[1]);
}
__device__ __forceinline__ float tr_sum(f32x4 ys) {
  auto r01 = __builtin_amdgcn_permlane32_swap(__float_as_uint(ys[0]), __float_as_uint(ys[1]), false, false);
  auto r23 = __builtin_amdgcn_permlane32_swap(__float_as_uint(ys[2]), __float_as_uint(ys[3]), false, false);
  const u32 u = __float_as_uint(__uint_as_float(r01[0]) + __uint_as_float(r01[1]));
  const u32 vv = __float_as_uint(__uint_as_float(r23[0]) + __uint_as_float(r23[1]));
  auto r2 = __builtin_amdgcn_permlane16_swap(u, vv, false, false);
  return __uint_as_float(r2[0]) + __uint_as_float(r2[1]);
}
__device__ __forceinline__ float wave_sum(float v) { return xg_sum(row16_sum(v)); }
template <int CTRL>
__device__ __forceinline__ u32 dppu(u32 v) { return (u32)__builtin_amdgcn_update_dpp(0, (int)v, CTRL, 0xf, 0xf, true); }
__device__ __forceinline__ u32 wave_max(u32 v) {
  v = max(v, dppu<0xB1>(v)); v = max(v, dppu<0x4E>(v)); v = max(v, dppu<0x141>(v)); v = max(v, dppu<0x140>(v));
  return max(max((u32)__builtin_amdgcn_readlane(v, 0), (u32)__builtin_amdgcn_readlane(v, 16)),
             max((u32)__builtin_amdgcn_readlane(v, 32), (u32)__builtin_amdgcn_readlane(v, 48)));
}
__device__ __forceinline__ u32 mono(float f) {
  const u32 b = __float_as_uint(f);
  return b ^ ((u32)((int)b >> 31) | 0x80000000u);
}
__device__ __forceinline__ float dot2b(u32 a, u32 b, float c) {
  return __builtin_amdgcn_fdot2_f32_bf16(__builtin_bit_cast(bf16x2_t, a), __builtin_bit_cast(bf16x2_t, b), c, false);
}
template <int OFF>
__device__ __forceinline__ void dma_m0(u32 voff, const char* sbase, u32 dst) {
  asm volatile("s_mov_b32 m0, %2\n\ts_nop 0\n\tglobal_load_lds_dwordx4 %0, %1 offset:%3"
               :: "v"(voff), "s"(sbase), "s"(dst), "n"(OFF) : "memory");
}
template <int OFF>
__device__ __forceinline__ void dma_nx(u32 voff, const char* sbase) {
  asm volatile("global_load_lds_dwordx4 %0, %1 offset:%2" :: "v"(voff), "s"(sbase), "n"(OFF) : "memory");
}
__device__ __forceinline__ u32 lds_ld32(u32 addr) {
  u32 v;
  asm volatile("ds_read_b32 %0, %1\n\ts_waitcnt lgkmcnt(0)" : "=v"(v) : "v"(addr) : "memory");
  return v;
}
__device__ __forceinline__ u32x4 lds_ld128(u32 addr) {
  u32x4 v;
  asm volatile("ds_read_b128 %0, %1\n\ts_waitcnt lgkmcnt(0)" : "=v"(v) : "v"(addr) : "memory");
  return v;
}
__device__ __forceinline__ bf16x8 mk8(u32 a, u32 b, u32 c, u32 d) {
  return __builtin_bit_cast(bf16x8, (u32x4){a, b, c, d});
}
__device__ __forceinline__ bf16x8 tr_frag(const char* p, int off2) {
  s16x4 b0 = tr_read(p);
  s16x4 b1 = tr_read(p + off2);
  bf16x8 r;
  r[0] = b0[0]; r[1] = b0[1]; r[2] = b0[2]; r[3] = b0[3];
  r[4] = b1[0]; r[5] = b1[1]; r[6] = b1[2]; r[7] = b1[3];
  return r;
}
#define SPIN_CAP 400000
#define MFMA(A, B, C) __builtin_amdgcn_mfma_f32_16x16x32_bf16(A, B, C, 0, 0, 0)

#define HSLOT 16384
#define XRING 98304
#define XSLOT 12288
#define LDS_TOTAL 163840
#define EXO 102400
#define HIM 148480
#define PST_OFF (EXO + 40960)
#define TAB_OFF 161280
#define ONES_OFF 162048
#define CTL_OFF 162176
#define WR_OFF 162304
#define REC_OFF 162816
#define HN_OFF 124928
#define HSL 1344
#define ASL 1056
#define TCH_OFF 162560
#define ACT_OFF 135168
#define KEY_OFF (ACT_OFF + 5120)
#define POST_OFF (KEY_OFF + 256)
#define DOWN_OFF 81920
#define WS_EPOCH 0
#define WS_HFLAG 4096
#define WS_VP 40960
#define WS_LN1 16384
#define WS_FILL 131072
#define WS_O WS_FILL
#define WS_STAT (WS_O + 2621440)
#define WS_CAND (WS_STAT + 425984)
#define WS_PART (WS_CAND + 133120)
#define WS_BXF (WS_PART + 122880)
#define WS_PC (WS_BXF + 26624)
#define WS_PART2 (WS_PC + 66560)
#define WS_PK (4u << 20)
#define SENT 0xFFFFFFFFu

#define GST4(VO, SB, O, V) asm volatile("global_store_dwordx4 %0, %1, %2 offset:%3 sc1\n\ts_nop 1" ::"v"(VO), "v"(V), "s"(SB), "n"(O) : "memory")
#define GST2(VO, SB, O, V) asm volatile("global_store_dwordx2 %0, %1, %2 offset:%3 sc1\n\ts_nop 1" ::"v"(VO), "v"(V), "s"(SB), "n"(O) : "memory")
#define GLD2(D, VO, SB, O) asm volatile("global_load_dwordx2 %0, %1, %2 offset:%3 sc1" : "=v"(D) : "v"(VO), "s"(SB), "n"(O) : "memory")

__device__ __forceinline__ void touch_line(const void* p) {
  asm volatile("s_mov_b32 m0, %1\n\ts_nop 0\n\tglobal_load_lds_dword %0, off" ::"v"(p), "s"(TCH_OFF) : "memory");
}
__device__ __forceinline__ u32 w_off(int i, int lane, int hg) {
  int cc = (lane & 31) ^ (((2 * i + (lane >> 5)) & 7) << 1);
  cc = cc > 29 ? 29 : cc;
  return (lane >> 5) * 3840 + ((cc >= 10 ? 1120 : 0) + (cc >= 20 ? 1120 : 0)) + hg * 160 + cc * 16;
}
#define W_ROW(I) (((((2 * (I)) & 0x13) | (((2 * (I)) & 4) << 1) | (((2 * (I)) & 8) >> 1))) * 3840)
__device__ __forceinline__ u32 x_off(int i, int lane, int r0) {
  const int xr = i * 8 + (lane >> 3);
  int grow = r0 - 16 + xr;
  grow = grow < 0 ? 0 : (grow > SEQ - 1 ? SEQ - 1 : grow);
  return grow * 1280 + (((lane & 7) ^ ((xr >> 1) & 7)) * 16);
}

#define ATT_ASM \
  "ds_read_b32 v28, %[aq] offset:64\n\tds_read_b32 v30, %[aka]\n\tds_read_b32 v31, %[akb]\n\tv_mov_b64 v[38:39], 0\n\tv_mov_b64 v[42:43], 0\n\t" \
  "s_lshl_b32 s41, %[kh], 5\n\ts_lshl_b64 s[46:47], 0xffff, s41\n\ts_lshl_b64 s[48:49], s[46:47], 16\n\ts_mov_b32 s50, 0xaaaaaaaa\n\t" \
  "s_mov_b32 s51, 0xaaaaaaaa\n\ts_setprio 1\n\ts_waitcnt lgkmcnt(0)\n\ts_barrier\n\tds_read_b64 v[36:37], %[eh] offset:512\n\t" \
  "ds_read_b64 v[44:45], %[eh] offset:15360\n\tds_read_b64 v[46:47], %[e4] offset:27648\n\tds_read_b64 v[48:49], %[eh] offset:15872\n\t" \
  "ds_read_b64 v[50:51], %[e4] offset:28160\n\ts_mov_b64 exec, s[46:47]\n\tds_read_b64 v[38:39], %[e4] offset:12800\n\ts_mov_b64 exec, s[48:49]\n\t" \
  "ds_read_b64 v[42:43], %[e4] offset:12800\n\ts_mov_b64 exec, -1\n\tds_read_b64 v[40:41], %[eh] offset:3584\n\t" \
  "ds_read_b64 v[52:53], %[eh] offset:18432\n\tds_read_b64 v[54:55], %[e4] offset:27648\n\tds_read_b64 v[56:57], %[eh] offset:18944\n\t" \
  "ds_read_b64 v[58:59], %[e4] offset:28160\n\tv_mul_f32 v28, 0x3ea52b5b, v28\n\tv_pk_mul_f32 v[32:33], v[28:29], v[30:31] op_sel_hi:[0,1]\n\t" \
  "s_waitcnt lgkmcnt(6)\n\tv_mfma_f32_16x16x32_bf16 v[44:47], v[44:47], v[36:39], 0\n\tds_read2st64_b64 v[60:63], %[eh] offset0:60 offset1:61\n\t" \
  "v_mfma_f32_16x16x32_bf16 v[48:51], v[48:51], v[36:39], 0\n\tds_read2st64_b64 v[64:67], %[eh] offset0:66 offset1:67\n\ts_waitcnt lgkmcnt(2)\n\t" \
  "v_mfma_f32_16x16x32_bf16 v[52:55], v[52:55], v[40:43], 0\n\tds_read2st64_b64 v[68:71], %[e4] offset0:84 offset1:85\n\t" \
  "v_mfma_f32_16x16x32_bf16 v[56:59], v[56:59], v[40:43], 0\n\ts_mov_b32 exec_lo, 0xfff0ffff\n\ts_mov_b32 exec_hi, 0xf000ff00\n\ts_nop 0\n\t" \
  "v_mov_b64 v[44:45], v[48:49]\n\ts_mov_b32 exec_lo, 0xffc0fffc\n\ts_mov_b32 exec_hi, 0xc000fc00\n\tv_mov_b64 v[46:47], v[50:51]\n\ts_nop 0\n\t" \
  "v_mov_b64 v[54:55], v[58:59]\n\ts_mov_b32 exec_lo, 0xfff0ffff\n\ts_mov_b32 exec_hi, 0xf000ff00\n\tv_mov_b64 v[52:53], v[56:57]\n\t" \
  "s_mov_b64 exec, -1\n\tv_cndmask_b32 v44, v44, v45, s[50:51]\n\tv_cndmask_b32 v46, v46, v47, s[50:51]\n\tv_cndmask_b32 v52, v52, v53, s[50:51]\n\t" \
  "v_cndmask_b32 v54, v54, v55, s[50:51]\n\tv_fma_f32 v72, v44, v32, %[b0]\n\tv_fma_f32 v73, v46, v33, %[b1]\n\tv_fma_f32 v74, v52, v32, %[b0]\n\t" \
  "v_fma_f32 v75, v54, v33, %[b1]\n\tv_max_f32 v34, v72, v73\n\tv_max_f32 v35, v74, v75\n\tds_read_b128 v[76:79], %[arec]\n\t" \
  "ds_read_b128 v[80:83], %[arec] offset:16\n\tv_permlane32_swap_b32 v34, v35\n\tv_max_f32 v34, v34, v35\n\tv_mov_b32 v35, v34\n\t" \
  "ds_read_b128 v[84:87], %[arec] offset:32\n\ts_nop 0\n\tv_permlane16_swap_b32 v34, v35\n\tv_max_f32 v34, v34, v35\n\tv_mov_b32 v35, v34\n\ts_nop 1\n\t" \
  "v_permlane32_swap_b32 v34, v35\n\tv_sub_f32 v72, v72, v34\n\tv_sub_f32 v73, v73, v34\n\tv_sub_f32 v74, v74, v35\n\tv_sub_f32 v75, v75, v35\n\t" \
  "v_exp_f32 v72, v72\n\tv_exp_f32 v73, v73\n\tv_exp_f32 v74, v74\n\tv_exp_f32 v75, v75\n\tv_add_f32 v34, v72, v73\n\tv_add_f32 v35, v74, v75\n\t" \
  "v_pk_mul_f32 v[72:73], v[72:73], v[30:31]\n\tv_pk_mul_f32 v[74:75], v[74:75], v[30:31]\n\tv_permlane32_swap_b32 v34, v35\n\t" \
  "v_add_f32 v34, v34, v35\n\tv_mov_b32 v35, v34\n\ts_nop 1\n\tv_permlane16_swap_b32 v34, v35\n\tv_add_f32 v34, v34, v35\n\tv_rcp_f32 v34, v34\n\t" \
  "s_nop 0\n\tv_mov_b32 v30, v34\n\ts_waitcnt lgkmcnt(0)\n\ts_nop 0\n\tv_permlane32_swap_b32 v34, v30\n\t" \
  "v_pk_mul_f32 v[72:73], v[34:35], v[72:73] op_sel_hi:[0,1]\n\tv_pk_mul_f32 v[74:75], v[30:31], v[74:75] op_sel_hi:[0,1]\n\t" \
  "v_cvt_pk_bf16_f32 v72, v72, v73\n\tv_cvt_pk_bf16_f32 v74, v74, v75\n\tv_perm_b32 v36, 0, v72, %[s0]\n\tv_perm_b32 v37, 0, v72, %[s1]\n\t" \
  "v_perm_b32 v38, 0, v72, %[s2]\n\tv_perm_b32 v39, 0, v72, %[s3]\n\tv_perm_b32 v40, 0, v74, %[s0]\n\tv_perm_b32 v41, 0, v74, %[s1]\n\t" \
  "v_mfma_f32_16x16x32_bf16 v[60:63], v[60:63], v[36:39], 0\n\tv_perm_b32 v42, 0, v74, %[s2]\n\tv_perm_b32 v43, 0, v74, %[s3]\n\t" \
  "v_mfma_f32_16x16x32_bf16 v[52:55], v[68:71], v[36:39], 0\n\ts_nop 0\n\tv_mfma_f32_16x16x32_bf16 v[64:67], v[64:67], v[40:43], 0\n\t" \
  "v_mfma_f32_16x16x32_bf16 v[56:59], v[68:71], v[40:43], 0\n\ts_nop 1\n\tv_cvt_pk_bf16_f32 v24, v60, v61\n\tv_cvt_pk_bf16_f32 v25, v62, v63\n\t" \
  "s_nop 0\n\tv_cvt_pk_bf16_f32 v52, v52, v53\n\tv_cvt_pk_bf16_f32 v53, v54, v55\n\tv_cvt_pk_bf16_f32 v26, v64, v65\n\t" \
  "v_cvt_pk_bf16_f32 v27, v66, v67\n\tglobal_store_dwordx4 %[l16], v[24:27], %[os] sc1\n\ts_mov_b64 exec, s[48:49]\n\tv_cvt_pk_bf16_f32 v52, v56, v57\n\t" \
  "v_cvt_pk_bf16_f32 v53, v58, v59\n\ts_or_b64 exec, s[46:47], s[48:49]\n\tglobal_store_dwordx2 %[l16], v[52:53], %[os4] sc1\n\ts_mov_b64 exec, -1\n\t" \
  "s_setprio 0\n\tv_mfma_f32_16x16x32_bf16 v[60:63], v[24:27], v[76:79], 0\n\tv_and_b32 v54, 0x7fff7fff, v52\n\tv_and_b32 v55, 0x7fff7fff, v53\n\t" \
  "v_and_b32 v64, 0x7fff7fff, v24\n\tv_and_b32 v65, 0x7fff7fff, v25\n\tv_and_b32 v66, 0x7fff7fff, v26\n\tv_and_b32 v67, 0x7fff7fff, v27\n\t" \
  "v_mfma_f32_16x16x32_bf16 v[60:63], v[52:55], v[80:83], v[60:63]\n\tds_write2st64_b64 %[eh], v[24:25], v[26:27] offset0:1 offset1:7\n\t" \
  "v_mfma_f32_16x16x32_bf16 v[60:63], v[64:67], v[84:87], v[60:63]\n\ts_mov_b32 exec_lo, 0x01010101\n\ts_mov_b32 exec_hi, 0x01010101\n\ts_nop 5\n\t" \
  "v_cvt_pk_bf16_f32 v60, v60, v61\n\tv_cvt_pk_bf16_f32 v61, v62, v63\n\tv_and_b32 v60, -2, v60\n\tglobal_store_dwordx2 %[vpp], v[60:61], %[sst] sc1\n\t" \
  "s_mov_b64 exec, -1\n\t" \
  ""
#define OP_ASM \
  "s_mov_b32 s43, %[cap]\n\ts_cmp_lt_u32 %[w], 4\n\ts_cbranch_scc1 LH_%=\n\ts_setprio 3\n\tglobal_load_dwordx4 v[124:127], %[vd], %[oa1] sc1\n\t" \
  "global_load_dwordx4 v[128:131], %[vk], %[oa1] sc1\n\tglobal_load_dwordx4 v[132:135], %[vd], %[oa2] sc1\n\t" \
  "global_load_dwordx4 v[136:139], %[vk], %[oa2] sc1\n\tv_add_u32 %[tk], %[sk], %[e4]\n\tv_add_u32 %[ta], %[so0], %[pm]\n\t" \
  "ds_read2st64_b64 v[104:107], %[tk] offset0:1 offset1:7\n\tv_xor_b32 %[tk], 512, %[tk]\n\tds_read2st64_b64 v[108:111], %[tk] offset0:1 offset1:7\n\t" \
  "ds_read_b64_tr_b16 v[64:65], %[ta] offset:0\n\tds_read_b64_tr_b16 v[66:67], %[ta] offset:3200\n\tds_read_b64_tr_b16 v[68:69], %[ta] offset:8\n\t" \
  "ds_read_b64_tr_b16 v[70:71], %[ta] offset:3208\n\tds_read_b64_tr_b16 v[72:73], %[ta] offset:16\n\tds_read_b64_tr_b16 v[74:75], %[ta] offset:3216\n\t" \
  "ds_read_b64_tr_b16 v[76:77], %[ta] offset:24\n\tds_read_b64_tr_b16 v[78:79], %[ta] offset:3224\n\tds_read_b64_tr_b16 v[80:81], %[ta] offset:32\n\t" \
  "ds_read_b64_tr_b16 v[82:83], %[ta] offset:3232\n\tds_read_b64 v[112:113], %[ra] offset:0\n\tds_read_b64 v[114:115], %[ra] offset:8\n\t" \
  "ds_read_b64 v[116:117], %[ra] offset:16\n\tds_read_b64 v[118:119], %[ra] offset:24\n\tds_read_b64 v[120:121], %[ra] offset:32\n\t" \
  "global_load_dwordx4 v[140:143], %[vd], %[oa3] sc1\n\tv_add_u32 %[ta], %[so1], %[pm]\n\ts_waitcnt lgkmcnt(5)\n\t" \
  "v_mfma_f32_16x16x32_bf16 v[44:47], v[64:67], v[108:111], 0\n\tds_read_b64_tr_b16 v[84:85], %[ta] offset:0\n\t" \
  "ds_read_b64_tr_b16 v[86:87], %[ta] offset:3200\n\tv_mfma_f32_16x16x32_bf16 v[48:51], v[68:71], v[108:111], 0\n\t" \
  "ds_read_b64_tr_b16 v[88:89], %[ta] offset:8\n\tds_read_b64_tr_b16 v[90:91], %[ta] offset:3208\n\t" \
  "v_mfma_f32_16x16x32_bf16 v[52:55], v[72:75], v[108:111], 0\n\tds_read_b64_tr_b16 v[92:93], %[ta] offset:16\n\t" \
  "ds_read_b64_tr_b16 v[94:95], %[ta] offset:3216\n\tv_mfma_f32_16x16x32_bf16 v[56:59], v[76:79], v[108:111], 0\n\t" \
  "ds_read_b64_tr_b16 v[96:97], %[ta] offset:24\n\tds_read_b64_tr_b16 v[98:99], %[ta] offset:3224\n\t" \
  "v_mfma_f32_16x16x32_bf16 v[60:63], v[80:83], v[108:111], 0\n\tds_read_b64_tr_b16 v[100:101], %[ta] offset:32\n\t" \
  "ds_read_b64_tr_b16 v[102:103], %[ta] offset:3232\n\ts_waitcnt lgkmcnt(10)\n\tv_lshlrev_b32 v24, 16, v112\n\tv_and_b32 v25, 0xffff0000, v112\n\t" \
  "v_lshlrev_b32 v26, 16, v113\n\tv_and_b32 v27, 0xffff0000, v113\n\tv_lshlrev_b32 v28, 16, v114\n\tv_and_b32 v29, 0xffff0000, v114\n\t" \
  "v_lshlrev_b32 v30, 16, v115\n\tv_and_b32 v31, 0xffff0000, v115\n\tv_mfma_f32_16x16x32_bf16 v[24:27], v[64:67], v[104:107], v[24:27]\n\t" \
  "v_lshlrev_b32 v32, 16, v116\n\tv_and_b32 v33, 0xffff0000, v116\n\tv_lshlrev_b32 v34, 16, v117\n\tv_and_b32 v35, 0xffff0000, v117\n\t" \
  "v_mfma_f32_16x16x32_bf16 v[28:31], v[68:71], v[104:107], v[28:31]\n\tv_lshlrev_b32 v36, 16, v118\n\tv_and_b32 v37, 0xffff0000, v118\n\t" \
  "v_lshlrev_b32 v38, 16, v119\n\tv_and_b32 v39, 0xffff0000, v119\n\tv_mfma_f32_16x16x32_bf16 v[32:35], v[72:75], v[104:107], v[32:35]\n\t" \
  "v_lshlrev_b32 v40, 16, v120\n\tv_and_b32 v41, 0xffff0000, v120\n\tv_lshlrev_b32 v42, 16, v121\n\tv_and_b32 v43, 0xffff0000, v121\n\t" \
  "v_mfma_f32_16x16x32_bf16 v[36:39], v[76:79], v[104:107], v[36:39]\n\ts_nop 0\n\tv_mfma_f32_16x16x32_bf16 v[40:43], v[80:83], v[104:107], v[40:43]\n\t" \
  "v_add_u32 %[ta], %[so2], %[pm]\n\tds_read_b64_tr_b16 v[104:105], %[ta] offset:0\n\tds_read_b64_tr_b16 v[106:107], %[ta] offset:3200\n\t" \
  "ds_read_b64_tr_b16 v[108:109], %[ta] offset:8\n\tds_read_b64_tr_b16 v[110:111], %[ta] offset:3208\n\t" \
  "ds_read_b64_tr_b16 v[112:113], %[ta] offset:16\n\tds_read_b64_tr_b16 v[114:115], %[ta] offset:3216\n\t" \
  "ds_read_b64_tr_b16 v[116:117], %[ta] offset:24\n\tds_read_b64_tr_b16 v[118:119], %[ta] offset:3224\n\t" \
  "ds_read_b64_tr_b16 v[120:121], %[ta] offset:32\n\tds_read_b64_tr_b16 v[122:123], %[ta] offset:3232\n\t" \
  "global_load_dwordx4 v[144:147], %[vk], %[oa3] sc1\n\tglobal_load_dwordx4 v[148:151], %[vd], %[oa4] sc1\n\ts_waitcnt vmcnt(5)\n\t" \
  "v_max_u32 %[tk], v124, v128\n\tv_cmp_eq_u32 vcc, -1, %[tk]\n\ts_cbranch_vccnz LSs1_%=\n\tLQs1_%=:\n\ts_waitcnt lgkmcnt(10)\n\t" \
  "v_mfma_f32_16x16x32_bf16 v[44:47], v[84:87], v[124:127], v[44:47]\n\tv_add_u32 %[ta], %[so3], %[pm]\n\tds_read_b64_tr_b16 v[64:65], %[ta] offset:0\n\t" \
  "v_mfma_f32_16x16x32_bf16 v[24:27], v[84:87], v[128:131], v[24:27]\n\tds_read_b64_tr_b16 v[66:67], %[ta] offset:3200\n\t" \
  "global_load_dwordx4 v[152:155], %[vk], %[oa4] sc1\n\tv_mfma_f32_16x16x32_bf16 v[48:51], v[88:91], v[124:127], v[48:51]\n\t" \
  "ds_read_b64_tr_b16 v[68:69], %[ta] offset:8\n\tv_mfma_f32_16x16x32_bf16 v[28:31], v[88:91], v[128:131], v[28:31]\n\t" \
  "ds_read_b64_tr_b16 v[70:71], %[ta] offset:3208\n\tglobal_load_dwordx4 v[156:159], %[vd], %[oa5] sc1\n\t" \
  "v_mfma_f32_16x16x32_bf16 v[52:55], v[92:95], v[124:127], v[52:55]\n\tds_read_b64_tr_b16 v[72:73], %[ta] offset:16\n\t" \
  "v_mfma_f32_16x16x32_bf16 v[32:35], v[92:95], v[128:131], v[32:35]\n\tds_read_b64_tr_b16 v[74:75], %[ta] offset:3216\n\t" \
  "v_mfma_f32_16x16x32_bf16 v[56:59], v[96:99], v[124:127], v[56:59]\n\tds_read_b64_tr_b16 v[76:77], %[ta] offset:24\n\t" \
  "v_mfma_f32_16x16x32_bf16 v[36:39], v[96:99], v[128:131], v[36:39]\n\tds_read_b64_tr_b16 v[78:79], %[ta] offset:3224\n\ts_waitcnt vmcnt(5)\n\t" \
  "v_max_u32 %[tk], v132, v136\n\tv_cmp_eq_u32 vcc, -1, %[tk]\n\tv_mfma_f32_16x16x32_bf16 v[60:63], v[100:103], v[124:127], v[60:63]\n\t" \
  "ds_read_b64_tr_b16 v[80:81], %[ta] offset:32\n\tv_mfma_f32_16x16x32_bf16 v[40:43], v[100:103], v[128:131], v[40:43]\n\t" \
  "ds_read_b64_tr_b16 v[82:83], %[ta] offset:3232\n\ts_cbranch_vccnz LSs2_%=\n\tLQs2_%=:\n\ts_waitcnt lgkmcnt(10)\n\t" \
  "v_mfma_f32_16x16x32_bf16 v[44:47], v[104:107], v[132:135], v[44:47]\n\tv_add_u32 %[ta], %[so4], %[pf]\n\t" \
  "ds_read_b64_tr_b16 v[84:85], %[ta] offset:0\n\tv_mfma_f32_16x16x32_bf16 v[24:27], v[104:107], v[136:139], v[24:27]\n\t" \
  "ds_read_b64_tr_b16 v[86:87], %[ta] offset:12800\n\tglobal_load_dwordx4 v[124:127], %[vk], %[oa5] sc1\n\t" \
  "v_mfma_f32_16x16x32_bf16 v[48:51], v[108:111], v[132:135], v[48:51]\n\tds_read_b64_tr_b16 v[88:89], %[ta] offset:8\n\t" \
  "v_mfma_f32_16x16x32_bf16 v[28:31], v[108:111], v[136:139], v[28:31]\n\tds_read_b64_tr_b16 v[90:91], %[ta] offset:12808\n\t" \
  "global_load_dwordx4 v[128:131], %[vd], %[oa6] sc1\n\tv_mfma_f32_16x16x32_bf16 v[52:55], v[112:115], v[132:135], v[52:55]\n\t" \
  "ds_read_b64_tr_b16 v[92:93], %[ta] offset:16\n\tv_mfma_f32_16x16x32_bf16 v[32:35], v[112:115], v[136:139], v[32:35]\n\t" \
  "ds_read_b64_tr_b16 v[94:95], %[ta] offset:12816\n\tv_mfma_f32_16x16x32_bf16 v[56:59], v[116:119], v[132:135], v[56:59]\n\t" \
  "ds_read_b64_tr_b16 v[96:97], %[ta] offset:24\n\tv_mfma_f32_16x16x32_bf16 v[36:39], v[116:119], v[136:139], v[36:39]\n\t" \
  "ds_read_b64_tr_b16 v[98:99], %[ta] offset:12824\n\ts_waitcnt vmcnt(5)\n\tv_max_u32 %[tk], v140, v144\n\tv_cmp_eq_u32 vcc, -1, %[tk]\n\t" \
  "v_mfma_f32_16x16x32_bf16 v[60:63], v[120:123], v[132:135], v[60:63]\n\tds_read_b64_tr_b16 v[100:101], %[ta] offset:32\n\t" \
  "v_mfma_f32_16x16x32_bf16 v[40:43], v[120:123], v[136:139], v[40:43]\n\tds_read_b64_tr_b16 v[102:103], %[ta] offset:12832\n\t" \
  "s_cbranch_vccnz LSs3_%=\n\tLQs3_%=:\n\ts_waitcnt lgkmcnt(10)\n\tv_mfma_f32_16x16x32_bf16 v[44:47], v[64:67], v[140:143], v[44:47]\n\t" \
  "v_add_u32 %[ta], %[so5], %[pm]\n\tds_read_b64_tr_b16 v[104:105], %[ta] offset:0\n\t" \
  "v_mfma_f32_16x16x32_bf16 v[24:27], v[64:67], v[144:147], v[24:27]\n\tds_read_b64_tr_b16 v[106:107], %[ta] offset:3200\n\t" \
  "global_load_dwordx4 v[132:135], %[vk], %[oa6] sc1\n\tv_mfma_f32_16x16x32_bf16 v[48:51], v[68:71], v[140:143], v[48:51]\n\t" \
  "ds_read_b64_tr_b16 v[108:109], %[ta] offset:8\n\tv_mfma_f32_16x16x32_bf16 v[28:31], v[68:71], v[144:147], v[28:31]\n\t" \
  "ds_read_b64_tr_b16 v[110:111], %[ta] offset:3208\n\tglobal_load_dwordx4 v[136:139], %[vd], %[oa7] sc1\n\t" \
  "v_mfma_f32_16x16x32_bf16 v[52:55], v[72:75], v[140:143], v[52:55]\n\tds_read_b64_tr_b16 v[112:113], %[ta] offset:16\n\t" \
  "v_mfma_f32_16x16x32_bf16 v[32:35], v[72:75], v[144:147], v[32:35]\n\tds_read_b64_tr_b16 v[114:115], %[ta] offset:3216\n\t" \
  "v_mfma_f32_16x16x32_bf16 v[56:59], v[76:79], v[140:143], v[56:59]\n\tds_read_b64_tr_b16 v[116:117], %[ta] offset:24\n\t" \
  "v_mfma_f32_16x16x32_bf16 v[36:39], v[76:79], v[144:147], v[36:39]\n\tds_read_b64_tr_b16 v[118:119], %[ta] offset:3224\n\ts_waitcnt vmcnt(5)\n\t" \
  "v_max3_u32 %[tk], v148, v150, v152\n\tv_max_u32 %[tk], %[tk], v154\n\tv_cmp_eq_u32 vcc, -1, %[tk]\n\t" \
  "v_mfma_f32_16x16x32_bf16 v[60:63], v[80:83], v[140:143], v[60:63]\n\tds_read_b64_tr_b16 v[120:121], %[ta] offset:32\n\t" \
  "v_mfma_f32_16x16x32_bf16 v[40:43], v[80:83], v[144:147], v[40:43]\n\tds_read_b64_tr_b16 v[122:123], %[ta] offset:3232\n\ts_cbranch_vccnz LSs4_%=\n\t" \
  "LQs4_%=:\n\ts_waitcnt lgkmcnt(10)\n\tv_mfma_f32_16x16x32_bf16 v[44:47], v[84:87], v[148:151], v[44:47]\n\tv_add_u32 %[ta], %[so6], %[pm]\n\t" \
  "ds_read_b64_tr_b16 v[64:65], %[ta] offset:0\n\tv_mfma_f32_16x16x32_bf16 v[24:27], v[84:87], v[152:155], v[24:27]\n\t" \
  "ds_read_b64_tr_b16 v[66:67], %[ta] offset:3200\n\tglobal_load_dwordx4 v[140:143], %[vd], %[oa8] sc1\n\t" \
  "v_mfma_f32_16x16x32_bf16 v[48:51], v[88:91], v[148:151], v[48:51]\n\tds_read_b64_tr_b16 v[68:69], %[ta] offset:8\n\t" \
  "v_mfma_f32_16x16x32_bf16 v[28:31], v[88:91], v[152:155], v[28:31]\n\tds_read_b64_tr_b16 v[70:71], %[ta] offset:3208\n\t" \
  "global_load_dwordx4 v[144:147], %[vd], %[oa9] sc1\n\tv_mfma_f32_16x16x32_bf16 v[52:55], v[92:95], v[148:151], v[52:55]\n\t" \
  "ds_read_b64_tr_b16 v[72:73], %[ta] offset:16\n\tv_mfma_f32_16x16x32_bf16 v[32:35], v[92:95], v[152:155], v[32:35]\n\t" \
  "ds_read_b64_tr_b16 v[74:75], %[ta] offset:3216\n\tv_mfma_f32_16x16x32_bf16 v[56:59], v[96:99], v[148:151], v[56:59]\n\t" \
  "ds_read_b64_tr_b16 v[76:77], %[ta] offset:24\n\tv_mfma_f32_16x16x32_bf16 v[36:39], v[96:99], v[152:155], v[36:39]\n\t" \
  "ds_read_b64_tr_b16 v[78:79], %[ta] offset:3224\n\ts_waitcnt vmcnt(5)\n\tv_max_u32 %[tk], v156, v124\n\tv_cmp_eq_u32 vcc, -1, %[tk]\n\t" \
  "v_mfma_f32_16x16x32_bf16 v[60:63], v[100:103], v[148:151], v[60:63]\n\tds_read_b64_tr_b16 v[80:81], %[ta] offset:32\n\t" \
  "v_mfma_f32_16x16x32_bf16 v[40:43], v[100:103], v[152:155], v[40:43]\n\tds_read_b64_tr_b16 v[82:83], %[ta] offset:3232\n\ts_cbranch_vccnz LSs5_%=\n\t" \
  "LQs5_%=:\n\ts_waitcnt lgkmcnt(10)\n\tv_mfma_f32_16x16x32_bf16 v[44:47], v[104:107], v[156:159], v[44:47]\n\tv_add_u32 %[ta], %[so7], %[pm]\n\t" \
  "ds_read_b64_tr_b16 v[84:85], %[ta] offset:0\n\tv_mfma_f32_16x16x32_bf16 v[24:27], v[104:107], v[124:127], v[24:27]\n\t" \
  "ds_read_b64_tr_b16 v[86:87], %[ta] offset:3200\n\tglobal_load_dwordx4 v[148:151], %[vk], %[oa7] sc1\n\t" \
  "v_mfma_f32_16x16x32_bf16 v[48:51], v[108:111], v[156:159], v[48:51]\n\tds_read_b64_tr_b16 v[88:89], %[ta] offset:8\n\t" \
  "v_mfma_f32_16x16x32_bf16 v[28:31], v[108:111], v[124:127], v[28:31]\n\tds_read_b64_tr_b16 v[90:91], %[ta] offset:3208\n\t" \
  "global_load_dwordx4 v[152:155], %[vk], %[oa8] sc1\n\tv_mfma_f32_16x16x32_bf16 v[52:55], v[112:115], v[156:159], v[52:55]\n\t" \
  "ds_read_b64_tr_b16 v[92:93], %[ta] offset:16\n\tv_mfma_f32_16x16x32_bf16 v[32:35], v[112:115], v[124:127], v[32:35]\n\t" \
  "ds_read_b64_tr_b16 v[94:95], %[ta] offset:3216\n\tv_mfma_f32_16x16x32_bf16 v[56:59], v[116:119], v[156:159], v[56:59]\n\t" \
  "ds_read_b64_tr_b16 v[96:97], %[ta] offset:24\n\tv_mfma_f32_16x16x32_bf16 v[36:39], v[116:119], v[124:127], v[36:39]\n\t" \
  "ds_read_b64_tr_b16 v[98:99], %[ta] offset:3224\n\ts_waitcnt vmcnt(5)\n\tv_max_u32 %[tk], v128, v132\n\tv_cmp_eq_u32 vcc, -1, %[tk]\n\t" \
  "v_mfma_f32_16x16x32_bf16 v[60:63], v[120:123], v[156:159], v[60:63]\n\tds_read_b64_tr_b16 v[100:101], %[ta] offset:32\n\t" \
  "v_mfma_f32_16x16x32_bf16 v[40:43], v[120:123], v[124:127], v[40:43]\n\tds_read_b64_tr_b16 v[102:103], %[ta] offset:3232\n\ts_cbranch_vccnz LSs6_%=\n\t" \
  "LQs6_%=:\n\ts_waitcnt lgkmcnt(10)\n\tv_mfma_f32_16x16x32_bf16 v[44:47], v[64:67], v[128:131], v[44:47]\n\tv_add_u32 %[ta], %[so8], %[pm]\n\t" \
  "ds_read_b64_tr_b16 v[104:105], %[ta] offset:0\n\tv_mfma_f32_16x16x32_bf16 v[24:27], v[64:67], v[132:135], v[24:27]\n\t" \
  "ds_read_b64_tr_b16 v[106:107], %[ta] offset:3200\n\tglobal_load_dwordx4 v[156:159], %[vk], %[oa9] sc1\n\t" \
  "v_mfma_f32_16x16x32_bf16 v[48:51], v[68:71], v[128:131], v[48:51]\n\tds_read_b64_tr_b16 v[108:109], %[ta] offset:8\n\t" \
  "v_mfma_f32_16x16x32_bf16 v[28:31], v[68:71], v[132:135], v[28:31]\n\tds_read_b64_tr_b16 v[110:111], %[ta] offset:3208\n\t" \
  "v_mfma_f32_16x16x32_bf16 v[52:55], v[72:75], v[128:131], v[52:55]\n\tds_read_b64_tr_b16 v[112:113], %[ta] offset:16\n\t" \
  "v_mfma_f32_16x16x32_bf16 v[32:35], v[72:75], v[132:135], v[32:35]\n\tds_read_b64_tr_b16 v[114:115], %[ta] offset:3216\n\t" \
  "v_mfma_f32_16x16x32_bf16 v[56:59], v[76:79], v[128:131], v[56:59]\n\tds_read_b64_tr_b16 v[116:117], %[ta] offset:24\n\t" \
  "v_mfma_f32_16x16x32_bf16 v[36:39], v[76:79], v[132:135], v[36:39]\n\tds_read_b64_tr_b16 v[118:119], %[ta] offset:3224\n\ts_waitcnt vmcnt(4)\n\t" \
  "v_max_u32 %[tk], v136, v140\n\tv_cmp_eq_u32 vcc, -1, %[tk]\n\tv_mfma_f32_16x16x32_bf16 v[60:63], v[80:83], v[128:131], v[60:63]\n\t" \
  "ds_read_b64_tr_b16 v[120:121], %[ta] offset:32\n\tv_mfma_f32_16x16x32_bf16 v[40:43], v[80:83], v[132:135], v[40:43]\n\t" \
  "ds_read_b64_tr_b16 v[122:123], %[ta] offset:3232\n\ts_cbranch_vccnz LSd7_%=\n\tLQd7_%=:\n\ts_waitcnt lgkmcnt(10)\n\t" \
  "v_mfma_f32_16x16x32_bf16 v[44:47], v[84:87], v[136:139], v[44:47]\n\tv_add_u32 %[ta], %[so9], %[pf]\n\tds_read_b64_tr_b16 v[64:65], %[ta] offset:0\n\t" \
  "ds_read_b64_tr_b16 v[66:67], %[ta] offset:12800\n\tv_mfma_f32_16x16x32_bf16 v[48:51], v[88:91], v[136:139], v[48:51]\n\t" \
  "ds_read_b64_tr_b16 v[68:69], %[ta] offset:8\n\tds_read_b64_tr_b16 v[70:71], %[ta] offset:12808\n\t" \
  "v_mfma_f32_16x16x32_bf16 v[52:55], v[92:95], v[136:139], v[52:55]\n\tds_read_b64_tr_b16 v[72:73], %[ta] offset:16\n\t" \
  "ds_read_b64_tr_b16 v[74:75], %[ta] offset:12816\n\tv_mfma_f32_16x16x32_bf16 v[56:59], v[96:99], v[136:139], v[56:59]\n\t" \
  "ds_read_b64_tr_b16 v[76:77], %[ta] offset:24\n\tds_read_b64_tr_b16 v[78:79], %[ta] offset:12824\n\t" \
  "v_mfma_f32_16x16x32_bf16 v[60:63], v[100:103], v[136:139], v[60:63]\n\tds_read_b64_tr_b16 v[80:81], %[ta] offset:32\n\t" \
  "ds_read_b64_tr_b16 v[82:83], %[ta] offset:12832\n\ts_waitcnt lgkmcnt(10)\n\tv_mfma_f32_16x16x32_bf16 v[44:47], v[104:107], v[140:143], v[44:47]\n\t" \
  "v_mfma_f32_16x16x32_bf16 v[48:51], v[108:111], v[140:143], v[48:51]\n\tv_mfma_f32_16x16x32_bf16 v[52:55], v[112:115], v[140:143], v[52:55]\n\t" \
  "s_waitcnt vmcnt(2)\n\tv_max3_u32 %[tk], v144, v146, v148\n\tv_cmp_eq_u32 vcc, -1, %[tk]\n\t" \
  "v_mfma_f32_16x16x32_bf16 v[56:59], v[116:119], v[140:143], v[56:59]\n\tv_mfma_f32_16x16x32_bf16 v[60:63], v[120:123], v[140:143], v[60:63]\n\t" \
  "s_cbranch_vccnz LSd9_%=\n\tLQd9_%=:\n\ts_waitcnt lgkmcnt(0)\n\tv_mfma_f32_16x16x32_bf16 v[44:47], v[64:67], v[144:147], v[44:47]\n\t" \
  "ds_read2_b64 v[124:127], %[aw] offset0:0 offset1:1\n\tds_read2_b64 v[128:131], %[aw] offset0:2 offset1:3\n\t" \
  "ds_read_b64 v[132:133], %[aw] offset:32\n\tv_mov_b64 v[134:135], 0\n\tv_mfma_f32_16x16x32_bf16 v[48:51], v[68:71], v[144:147], v[48:51]\n\t" \
  "v_mfma_f32_16x16x32_bf16 v[52:55], v[72:75], v[144:147], v[52:55]\n\tv_add_u32 %[ta], %[sd], %[l16]\n\t" \
  "v_mfma_f32_16x16x32_bf16 v[56:59], v[76:79], v[144:147], v[56:59]\n\tv_mfma_f32_16x16x32_bf16 v[60:63], v[80:83], v[144:147], v[60:63]\n\t" \
  "v_mov_b32 v137, %[sc]\n\tv_mov_b32 v136, 1\n\tv_mfma_f32_16x16x32_bf16 v[24:27], v[84:87], v[148:151], v[24:27]\n\t" \
  "ds_write_b128 %[ta], v[44:47] offset:0\n\tv_mfma_f32_16x16x32_bf16 v[28:31], v[88:91], v[148:151], v[28:31]\n\t" \
  "ds_write_b128 %[ta], v[48:51] offset:1024\n\tv_mfma_f32_16x16x32_bf16 v[32:35], v[92:95], v[148:151], v[32:35]\n\t" \
  "ds_write_b128 %[ta], v[52:55] offset:2048\n\ts_waitcnt vmcnt(0)\n\tv_max3_u32 %[tk], v152, v156, v158\n\tv_cmp_eq_u32 vcc, -1, %[tk]\n\t" \
  "v_mfma_f32_16x16x32_bf16 v[36:39], v[96:99], v[148:151], v[36:39]\n\tds_write_b128 %[ta], v[56:59] offset:3072\n\t" \
  "v_mfma_f32_16x16x32_bf16 v[40:43], v[100:103], v[148:151], v[40:43]\n\tds_write_b128 %[ta], v[60:63] offset:4096\n\ts_mov_b64 exec, 1\n\t" \
  "ds_add_u32 v137, v136\n\ts_mov_b64 exec, -1\n\ts_cbranch_vccnz LSk8_%=\n\tLQk8_%=:\n\t" \
  "v_mfma_f32_16x16x32_bf16 v[24:27], v[104:107], v[152:155], v[24:27]\n\tv_mov_b64 v[94:95], 0\n\t" \
  "v_mfma_f32_16x16x32_bf16 v[28:31], v[108:111], v[152:155], v[28:31]\n\tv_xor_b32 v96, 4, v137\n\t" \
  "v_mfma_f32_16x16x32_bf16 v[32:35], v[112:115], v[152:155], v[32:35]\n\tv_add_u32 %[tk], %[sr], %[l16]\n\t" \
  "v_mfma_f32_16x16x32_bf16 v[36:39], v[116:119], v[152:155], v[36:39]\n\tv_mfma_f32_16x16x32_bf16 v[40:43], v[120:123], v[152:155], v[40:43]\n\t" \
  "v_mfma_f32_16x16x32_bf16 v[24:27], v[64:67], v[156:159], v[24:27]\n\tds_read_b32 v97, v96\n\tds_read_b128 v[44:47], %[tk] offset:0\n\t" \
  "ds_read_b128 v[48:51], %[tk] offset:1024\n\tds_read_b128 v[52:55], %[tk] offset:2048\n\tds_read_b128 v[56:59], %[tk] offset:3072\n\t" \
  "ds_read_b128 v[60:63], %[tk] offset:4096\n\tv_mfma_f32_16x16x32_bf16 v[28:31], v[68:71], v[156:159], v[28:31]\n\t" \
  "v_mfma_f32_16x16x32_bf16 v[32:35], v[72:75], v[156:159], v[32:35]\n\tv_mfma_f32_16x16x32_bf16 v[36:39], v[76:79], v[156:159], v[36:39]\n\t" \
  "v_mfma_f32_16x16x32_bf16 v[40:43], v[80:83], v[156:159], v[40:43]\n\tLD1_%=:\n\ts_waitcnt lgkmcnt(0)\n\tv_cmp_eq_u32 vcc, 0, v97\n\t" \
  "s_cbranch_vccnz LD2_%=\n\tLT_%=:\n\tv_mov_b32 v137, %[sa]\n\tds_read_b32 v138, v137 offset:16\n\tv_add_f32 v24, v24, v44\n\t" \
  "v_add_f32 v25, v25, v45\n\tv_add_f32 v26, v26, v46\n\tv_add_f32 v27, v27, v47\n\tv_cvt_pk_bf16_f32 v84, v24, v25\n\t" \
  "v_cvt_pk_bf16_f32 v85, v26, v27\n\tv_add_f32 v28, v28, v48\n\tv_add_f32 v29, v29, v49\n\tv_add_f32 v30, v30, v50\n\tv_add_f32 v31, v31, v51\n\t" \
  "v_cvt_pk_bf16_f32 v86, v28, v29\n\tv_cvt_pk_bf16_f32 v87, v30, v31\n\tv_add_f32 v32, v32, v52\n\tv_add_f32 v33, v33, v53\n\t" \
  "v_add_f32 v34, v34, v54\n\tv_add_f32 v35, v35, v55\n\tv_cvt_pk_bf16_f32 v88, v32, v33\n\tv_cvt_pk_bf16_f32 v89, v34, v35\n\t" \
  "v_mfma_f32_16x16x32_bf16 v[100:103], v[84:87], v[84:87], 0\n\tv_mfma_f32_16x16x32_bf16 v[96:99], v[124:127], v[84:87], 0\n\t" \
  "v_add_f32 v36, v36, v56\n\tv_add_f32 v37, v37, v57\n\tv_add_f32 v38, v38, v58\n\tv_add_f32 v39, v39, v59\n\tv_cvt_pk_bf16_f32 v90, v36, v37\n\t" \
  "v_cvt_pk_bf16_f32 v91, v38, v39\n\tv_add_f32 v40, v40, v60\n\tv_add_f32 v41, v41, v61\n\tv_add_f32 v42, v42, v62\n\tv_add_f32 v43, v43, v63\n\t" \
  "v_cvt_pk_bf16_f32 v92, v40, v41\n\tv_cvt_pk_bf16_f32 v93, v42, v43\n\tv_mfma_f32_16x16x32_bf16 v[100:103], v[88:91], v[88:91], v[100:103]\n\t" \
  "v_mfma_f32_16x16x32_bf16 v[96:99], v[128:131], v[88:91], v[96:99]\n\tv_mfma_f32_16x16x32_bf16 v[100:103], v[92:95], v[92:95], v[100:103]\n\t" \
  "v_mfma_f32_16x16x32_bf16 v[96:99], v[132:135], v[92:95], v[96:99]\n\ts_waitcnt lgkmcnt(0)\n\tv_sub_u32 v138, %[ra], v138\n\t" \
  "v_cmp_gt_u32 vcc, 0xa0, v138\n\tv_and_b32 %[ta], 0xff, %[l16]\n\tv_add_u32 %[tk], %[sp], %[ta]\n\tv_mov_b32 v136, 1\n\ts_cbranch_vccz LP1_%=\n\t" \
  "s_mov_b64 exec, vcc\n\tglobal_store_dwordx4 v138, v[84:87], %[cp] sc1\n\tglobal_store_dwordx4 v138, v[88:91], %[cp] offset:16 sc1\n\t" \
  "global_store_dwordx2 v138, v[92:93], %[cp] offset:32 sc1\n\tds_write_b32 v137, v136 offset:24\n\tLP1_%=:\n\ts_mov_b32 exec_lo, 0x00200002\n\t" \
  "s_mov_b32 exec_hi, 0x20000200\n\tv_mov_b32 v100, v101\n\ts_lshl_b64 exec, exec, 1\n\tv_mov_b32 v100, v102\n\ts_lshl_b64 exec, exec, 1\n\t" \
  "v_mov_b32 v100, v103\n\ts_mov_b32 exec_lo, 0x00f0000f\n\ts_mov_b32 exec_hi, 0xf0000f00\n\tv_mov_b32 v98, v100\n\t" \
  "global_store_dwordx4 %[ta], v[96:99], %[sst] sc1\n\tds_write_b128 %[tk], v[96:99]\n\ts_mov_b64 exec, -1\n\tds_write_b64 %[ra], v[84:85] offset:0\n\t" \
  "ds_write_b64 %[ra], v[86:87] offset:8\n\tds_write_b64 %[ra], v[88:89] offset:16\n\tds_write_b64 %[ra], v[90:91] offset:24\n\t" \
  "ds_write_b64 %[ra], v[92:93] offset:32\n\ts_mov_b64 exec, 1\n\tds_add_u32 v137, v136\n\ts_mov_b64 exec, -1\n\ts_setprio 0\n\ts_branch LZ_%=\n\t" \
  "LD2_%=:\n\ts_sub_u32 s43, s43, 1\n\ts_cbranch_scc1 LT_%=\n\tds_read_b32 v97, v96\n\tds_read_b128 v[44:47], %[tk] offset:0\n\t" \
  "ds_read_b128 v[48:51], %[tk] offset:1024\n\tds_read_b128 v[52:55], %[tk] offset:2048\n\tds_read_b128 v[56:59], %[tk] offset:3072\n\t" \
  "ds_read_b128 v[60:63], %[tk] offset:4096\n\ts_branch LD1_%=\n\tLH_%=:\n\ts_sleep 3\n\tglobal_load_dwordx4 v[140:143], %[vk], %[oa1] sc1\n\t" \
  "global_load_dwordx4 v[144:147], %[vk], %[oa2] sc1\n\tglobal_load_dwordx4 v[148:151], %[vk], %[oa3] sc1\n\t" \
  "global_load_dwordx4 v[152:155], %[vk], %[oa4] sc1\n\tglobal_load_dwordx4 v[156:159], %[vk], %[oa0] sc1\n\ts_cmp_eq_u32 %[w], 3\n\t" \
  "s_cbranch_scc0 LG_%=\n\tds_read_b64 v[112:113], %[ra] offset:0\n\tds_read_b64 v[114:115], %[ra] offset:8\n\t" \
  "ds_read_b64 v[116:117], %[ra] offset:16\n\tds_read_b64 v[118:119], %[ra] offset:24\n\tds_read_b64 v[120:121], %[ra] offset:32\n\t" \
  "ds_read2_b64 v[124:127], %[aw] offset0:0 offset1:1\n\tds_read2_b64 v[128:131], %[aw] offset0:2 offset1:3\n\t" \
  "ds_read_b64 v[132:133], %[aw] offset:32\n\tv_mov_b64 v[134:135], 0\n\ts_waitcnt lgkmcnt(3)\n\tv_lshlrev_b32 v24, 16, v112\n\t" \
  "v_and_b32 v25, 0xffff0000, v112\n\tv_lshlrev_b32 v26, 16, v113\n\tv_and_b32 v27, 0xffff0000, v113\n\tv_lshlrev_b32 v28, 16, v114\n\t" \
  "v_and_b32 v29, 0xffff0000, v114\n\tv_lshlrev_b32 v30, 16, v115\n\tv_and_b32 v31, 0xffff0000, v115\n\tv_lshlrev_b32 v32, 16, v116\n\t" \
  "v_and_b32 v33, 0xffff0000, v116\n\tv_lshlrev_b32 v34, 16, v117\n\tv_and_b32 v35, 0xffff0000, v117\n\tv_lshlrev_b32 v36, 16, v118\n\t" \
  "v_and_b32 v37, 0xffff0000, v118\n\tv_lshlrev_b32 v38, 16, v119\n\tv_and_b32 v39, 0xffff0000, v119\n\tv_lshlrev_b32 v40, 16, v120\n\t" \
  "v_and_b32 v41, 0xffff0000, v120\n\tv_lshlrev_b32 v42, 16, v121\n\tv_and_b32 v43, 0xffff0000, v121\n\ts_branch LG2_%=\n\tLG_%=:\n\t" \
  "v_mov_b64 v[24:25], 0\n\tv_mov_b64 v[26:27], 0\n\tv_mov_b64 v[28:29], 0\n\tv_mov_b64 v[30:31], 0\n\tv_mov_b64 v[32:33], 0\n\tv_mov_b64 v[34:35], 0\n\t" \
  "v_mov_b64 v[36:37], 0\n\tv_mov_b64 v[38:39], 0\n\tv_mov_b64 v[40:41], 0\n\tv_mov_b64 v[42:43], 0\n\tLG2_%=:\n\tv_add_u32 %[ta], %[so1], %[pm]\n\t" \
  "ds_read_b64_tr_b16 v[64:65], %[ta] offset:0\n\tds_read_b64_tr_b16 v[66:67], %[ta] offset:3200\n\tds_read_b64_tr_b16 v[68:69], %[ta] offset:8\n\t" \
  "ds_read_b64_tr_b16 v[70:71], %[ta] offset:3208\n\tds_read_b64_tr_b16 v[72:73], %[ta] offset:16\n\tds_read_b64_tr_b16 v[74:75], %[ta] offset:3216\n\t" \
  "ds_read_b64_tr_b16 v[76:77], %[ta] offset:24\n\tds_read_b64_tr_b16 v[78:79], %[ta] offset:3224\n\tds_read_b64_tr_b16 v[80:81], %[ta] offset:32\n\t" \
  "ds_read_b64_tr_b16 v[82:83], %[ta] offset:3232\n\tv_add_u32 %[ta], %[so2], %[pm]\n\tds_read_b64_tr_b16 v[84:85], %[ta] offset:0\n\t" \
  "ds_read_b64_tr_b16 v[86:87], %[ta] offset:3200\n\tds_read_b64_tr_b16 v[88:89], %[ta] offset:8\n\tds_read_b64_tr_b16 v[90:91], %[ta] offset:3208\n\t" \
  "ds_read_b64_tr_b16 v[92:93], %[ta] offset:16\n\tds_read_b64_tr_b16 v[94:95], %[ta] offset:3216\n\tds_read_b64_tr_b16 v[96:97], %[ta] offset:24\n\t" \
  "ds_read_b64_tr_b16 v[98:99], %[ta] offset:3224\n\tds_read_b64_tr_b16 v[100:101], %[ta] offset:32\n\t" \
  "ds_read_b64_tr_b16 v[102:103], %[ta] offset:3232\n\ts_waitcnt vmcnt(4)\n\tv_cmp_eq_u32 vcc, -1, v140\n\ts_cbranch_vccnz LSh1_%=\n\tLQh1_%=:\n\t" \
  "s_waitcnt lgkmcnt(10)\n\tv_mfma_f32_16x16x32_bf16 v[24:27], v[64:67], v[140:143], v[24:27]\n\tv_add_u32 %[ta], %[so3], %[pm]\n\t" \
  "ds_read_b64_tr_b16 v[104:105], %[ta] offset:0\n\tds_read_b64_tr_b16 v[106:107], %[ta] offset:3200\n\t" \
  "v_mfma_f32_16x16x32_bf16 v[28:31], v[68:71], v[140:143], v[28:31]\n\tds_read_b64_tr_b16 v[108:109], %[ta] offset:8\n\t" \
  "ds_read_b64_tr_b16 v[110:111], %[ta] offset:3208\n\tv_mfma_f32_16x16x32_bf16 v[32:35], v[72:75], v[140:143], v[32:35]\n\t" \
  "ds_read_b64_tr_b16 v[112:113], %[ta] offset:16\n\tds_read_b64_tr_b16 v[114:115], %[ta] offset:3216\n\t" \
  "v_mfma_f32_16x16x32_bf16 v[36:39], v[76:79], v[140:143], v[36:39]\n\tds_read_b64_tr_b16 v[116:117], %[ta] offset:24\n\t" \
  "ds_read_b64_tr_b16 v[118:119], %[ta] offset:3224\n\ts_waitcnt vmcnt(3)\n\tv_cmp_eq_u32 vcc, -1, v144\n\t" \
  "v_mfma_f32_16x16x32_bf16 v[40:43], v[80:83], v[140:143], v[40:43]\n\tds_read_b64_tr_b16 v[120:121], %[ta] offset:32\n\t" \
  "ds_read_b64_tr_b16 v[122:123], %[ta] offset:3232\n\ts_cbranch_vccnz LSh2_%=\n\tLQh2_%=:\n\ts_waitcnt lgkmcnt(10)\n\t" \
  "v_mfma_f32_16x16x32_bf16 v[24:27], v[84:87], v[144:147], v[24:27]\n\tv_add_u32 %[ta], %[so4], %[pf]\n\tds_read_b64_tr_b16 v[64:65], %[ta] offset:0\n\t" \
  "ds_read_b64_tr_b16 v[66:67], %[ta] offset:12800\n\tv_mfma_f32_16x16x32_bf16 v[28:31], v[88:91], v[144:147], v[28:31]\n\t" \
  "ds_read_b64_tr_b16 v[68:69], %[ta] offset:8\n\tds_read_b64_tr_b16 v[70:71], %[ta] offset:12808\n\t" \
  "v_mfma_f32_16x16x32_bf16 v[32:35], v[92:95], v[144:147], v[32:35]\n\tds_read_b64_tr_b16 v[72:73], %[ta] offset:16\n\t" \
  "ds_read_b64_tr_b16 v[74:75], %[ta] offset:12816\n\tv_mfma_f32_16x16x32_bf16 v[36:39], v[96:99], v[144:147], v[36:39]\n\t" \
  "ds_read_b64_tr_b16 v[76:77], %[ta] offset:24\n\tds_read_b64_tr_b16 v[78:79], %[ta] offset:12824\n\ts_waitcnt vmcnt(2)\n\t" \
  "v_cmp_eq_u32 vcc, -1, v148\n\tv_mfma_f32_16x16x32_bf16 v[40:43], v[100:103], v[144:147], v[40:43]\n\tds_read_b64_tr_b16 v[80:81], %[ta] offset:32\n\t" \
  "ds_read_b64_tr_b16 v[82:83], %[ta] offset:12832\n\ts_cbranch_vccnz LSh3_%=\n\tLQh3_%=:\n\ts_waitcnt lgkmcnt(10)\n\t" \
  "v_mfma_f32_16x16x32_bf16 v[24:27], v[104:107], v[148:151], v[24:27]\n\tv_add_u32 %[ta], %[so0], %[pm]\n\t" \
  "ds_read_b64_tr_b16 v[84:85], %[ta] offset:0\n\tds_read_b64_tr_b16 v[86:87], %[ta] offset:3200\n\t" \
  "v_mfma_f32_16x16x32_bf16 v[28:31], v[108:111], v[148:151], v[28:31]\n\tds_read_b64_tr_b16 v[88:89], %[ta] offset:8\n\t" \
  "ds_read_b64_tr_b16 v[90:91], %[ta] offset:3208\n\tv_mfma_f32_16x16x32_bf16 v[32:35], v[112:115], v[148:151], v[32:35]\n\t" \
  "ds_read_b64_tr_b16 v[92:93], %[ta] offset:16\n\tds_read_b64_tr_b16 v[94:95], %[ta] offset:3216\n\t" \
  "v_mfma_f32_16x16x32_bf16 v[36:39], v[116:119], v[148:151], v[36:39]\n\tds_read_b64_tr_b16 v[96:97], %[ta] offset:24\n\t" \
  "ds_read_b64_tr_b16 v[98:99], %[ta] offset:3224\n\ts_waitcnt vmcnt(1)\n\tv_max_u32 %[tk], v152, v154\n\tv_cmp_eq_u32 vcc, -1, %[tk]\n\t" \
  "v_mfma_f32_16x16x32_bf16 v[40:43], v[120:123], v[148:151], v[40:43]\n\tds_read_b64_tr_b16 v[100:101], %[ta] offset:32\n\t" \
  "ds_read_b64_tr_b16 v[102:103], %[ta] offset:3232\n\ts_cbranch_vccnz LSh4_%=\n\tLQh4_%=:\n\ts_waitcnt lgkmcnt(10)\n\t" \
  "v_mfma_f32_16x16x32_bf16 v[24:27], v[64:67], v[152:155], v[24:27]\n\tv_mfma_f32_16x16x32_bf16 v[28:31], v[68:71], v[152:155], v[28:31]\n\t" \
  "v_mfma_f32_16x16x32_bf16 v[32:35], v[72:75], v[152:155], v[32:35]\n\tv_mfma_f32_16x16x32_bf16 v[36:39], v[76:79], v[152:155], v[36:39]\n\t" \
  "s_waitcnt vmcnt(0)\n\tv_cmp_eq_u32 vcc, -1, v156\n\tv_mfma_f32_16x16x32_bf16 v[40:43], v[80:83], v[152:155], v[40:43]\n\ts_cbranch_vccnz LSh0_%=\n\t" \
  "LQh0_%=:\n\ts_waitcnt lgkmcnt(0)\n\tv_mfma_f32_16x16x32_bf16 v[24:27], v[84:87], v[156:159], v[24:27]\n\t" \
  "v_mfma_f32_16x16x32_bf16 v[28:31], v[88:91], v[156:159], v[28:31]\n\tv_mfma_f32_16x16x32_bf16 v[32:35], v[92:95], v[156:159], v[32:35]\n\t" \
  "v_mfma_f32_16x16x32_bf16 v[36:39], v[96:99], v[156:159], v[36:39]\n\tv_mfma_f32_16x16x32_bf16 v[40:43], v[100:103], v[156:159], v[40:43]\n\t" \
  "s_cmp_eq_u32 %[w], 3\n\ts_cbranch_scc1 LF_%=\n\ts_setprio 3\n\tv_add_u32 %[ta], %[sd], %[l16]\n\tv_mov_b32 v137, %[sc]\n\tv_mov_b32 v136, 1\n\t" \
  "ds_write_b128 %[ta], v[24:27] offset:0\n\tds_write_b128 %[ta], v[28:31] offset:1024\n\tds_write_b128 %[ta], v[32:35] offset:2048\n\t" \
  "ds_write_b128 %[ta], v[36:39] offset:3072\n\tds_write_b128 %[ta], v[40:43] offset:4096\n\ts_mov_b64 exec, 1\n\tds_add_u32 v137, v136\n\t" \
  "s_mov_b64 exec, -1\n\ts_branch LZ_%=\n\tLF_%=:\n\ts_setprio 3\n\tv_mov_b64 v[94:95], 0\n\tv_mov_b32 v96, %[sc]\n\tv_add_u32 %[ta], %[sr], %[l16]\n\t" \
  "LF1_%=:\n\tds_read_b32 v97, v96\n\ts_waitcnt lgkmcnt(0)\n\tv_cmp_gt_u32 vcc, 3, v97\n\ts_cbranch_vccz LF2_%=\n\ts_sleep 1\n\ts_sub_u32 s43, s43, 1\n\t" \
  "s_cbranch_scc0 LF1_%=\n\tLF2_%=:\n\tds_read_b128 v[64:67], %[ta] offset:0\n\tds_read_b128 v[68:71], %[ta] offset:1024\n\t" \
  "ds_read_b128 v[72:75], %[ta] offset:2048\n\tds_read_b128 v[76:79], %[ta] offset:3072\n\tds_read_b128 v[80:83], %[ta] offset:4096\n\t" \
  "ds_read_b128 v[104:107], %[ta] offset:5120\n\tds_read_b128 v[108:111], %[ta] offset:6144\n\tds_read_b128 v[112:115], %[ta] offset:7168\n\t" \
  "ds_read_b128 v[116:119], %[ta] offset:8192\n\tds_read_b128 v[120:123], %[ta] offset:9216\n\tds_read_b128 v[44:47], %[ta] offset:10240\n\t" \
  "ds_read_b128 v[48:51], %[ta] offset:11264\n\tds_read_b128 v[52:55], %[ta] offset:12288\n\tds_read_b128 v[56:59], %[ta] offset:13312\n\t" \
  "ds_read_b128 v[60:63], %[ta] offset:14336\n\ts_waitcnt lgkmcnt(10)\n\tv_pk_add_f32 v[24:25], v[24:25], v[64:65]\n\t" \
  "v_pk_add_f32 v[26:27], v[26:27], v[66:67]\n\tv_pk_add_f32 v[28:29], v[28:29], v[68:69]\n\tv_pk_add_f32 v[30:31], v[30:31], v[70:71]\n\t" \
  "v_pk_add_f32 v[32:33], v[32:33], v[72:73]\n\tv_pk_add_f32 v[34:35], v[34:35], v[74:75]\n\tv_pk_add_f32 v[36:37], v[36:37], v[76:77]\n\t" \
  "v_pk_add_f32 v[38:39], v[38:39], v[78:79]\n\tv_pk_add_f32 v[40:41], v[40:41], v[80:81]\n\tv_pk_add_f32 v[42:43], v[42:43], v[82:83]\n\t" \
  "s_waitcnt lgkmcnt(5)\n\tv_pk_add_f32 v[24:25], v[24:25], v[104:105]\n\tv_pk_add_f32 v[26:27], v[26:27], v[106:107]\n\t" \
  "v_pk_add_f32 v[28:29], v[28:29], v[108:109]\n\tv_pk_add_f32 v[30:31], v[30:31], v[110:111]\n\tv_pk_add_f32 v[32:33], v[32:33], v[112:113]\n\t" \
  "v_pk_add_f32 v[34:35], v[34:35], v[114:115]\n\tv_pk_add_f32 v[36:37], v[36:37], v[116:117]\n\tv_pk_add_f32 v[38:39], v[38:39], v[118:119]\n\t" \
  "v_pk_add_f32 v[40:41], v[40:41], v[120:121]\n\tv_pk_add_f32 v[42:43], v[42:43], v[122:123]\n\ts_waitcnt lgkmcnt(0)\n\ts_branch LT_%=\n\tLSs1_%=:\n\t" \
  "s_sub_u32 s43, s43, 1\n\ts_cbranch_scc1 LQs1_%=\n\ts_sleep %[oback]\n\tglobal_load_dwordx4 v[124:127], %[vd], %[oa1] sc1\n\t" \
  "global_load_dwordx4 v[128:131], %[vk], %[oa1] sc1\n\ts_waitcnt vmcnt(0)\n\tv_max_u32 %[tk], v124, v128\n\tv_cmp_eq_u32 vcc, -1, %[tk]\n\t" \
  "s_cbranch_vccnz LSs1_%=\n\ts_branch LQs1_%=\n\tLSs2_%=:\n\ts_sub_u32 s43, s43, 1\n\ts_cbranch_scc1 LQs2_%=\n\ts_sleep %[oback]\n\t" \
  "global_load_dwordx4 v[132:135], %[vd], %[oa2] sc1\n\tglobal_load_dwordx4 v[136:139], %[vk], %[oa2] sc1\n\ts_waitcnt vmcnt(0)\n\t" \
  "v_max_u32 %[tk], v132, v136\n\tv_cmp_eq_u32 vcc, -1, %[tk]\n\ts_cbranch_vccnz LSs2_%=\n\ts_branch LQs2_%=\n\tLSs3_%=:\n\ts_sub_u32 s43, s43, 1\n\t" \
  "s_cbranch_scc1 LQs3_%=\n\ts_sleep %[oback]\n\tglobal_load_dwordx4 v[140:143], %[vd], %[oa3] sc1\n\t" \
  "global_load_dwordx4 v[144:147], %[vk], %[oa3] sc1\n\ts_waitcnt vmcnt(0)\n\tv_max_u32 %[tk], v140, v144\n\tv_cmp_eq_u32 vcc, -1, %[tk]\n\t" \
  "s_cbranch_vccnz LSs3_%=\n\ts_branch LQs3_%=\n\tLSs4_%=:\n\ts_sub_u32 s43, s43, 1\n\ts_cbranch_scc1 LQs4_%=\n\ts_sleep %[oback]\n\t" \
  "global_load_dwordx4 v[148:151], %[vd], %[oa4] sc1\n\tglobal_load_dwordx4 v[152:155], %[vk], %[oa4] sc1\n\ts_waitcnt vmcnt(0)\n\t" \
  "v_max3_u32 %[tk], v148, v150, v152\n\tv_max_u32 %[tk], %[tk], v154\n\tv_cmp_eq_u32 vcc, -1, %[tk]\n\ts_cbranch_vccnz LSs4_%=\n\ts_branch LQs4_%=\n\t" \
  "LSs5_%=:\n\ts_sub_u32 s43, s43, 1\n\ts_cbranch_scc1 LQs5_%=\n\ts_sleep %[oback]\n\tglobal_load_dwordx4 v[156:159], %[vd], %[oa5] sc1\n\t" \
  "global_load_dwordx4 v[124:127], %[vk], %[oa5] sc1\n\ts_waitcnt vmcnt(0)\n\tv_max_u32 %[tk], v156, v124\n\tv_cmp_eq_u32 vcc, -1, %[tk]\n\t" \
  "s_cbranch_vccnz LSs5_%=\n\ts_branch LQs5_%=\n\tLSs6_%=:\n\ts_sub_u32 s43, s43, 1\n\ts_cbranch_scc1 LQs6_%=\n\ts_sleep %[oback]\n\t" \
  "global_load_dwordx4 v[128:131], %[vd], %[oa6] sc1\n\tglobal_load_dwordx4 v[132:135], %[vk], %[oa6] sc1\n\ts_waitcnt vmcnt(0)\n\t" \
  "v_max_u32 %[tk], v128, v132\n\tv_cmp_eq_u32 vcc, -1, %[tk]\n\ts_cbranch_vccnz LSs6_%=\n\ts_branch LQs6_%=\n\tLSd7_%=:\n\ts_sub_u32 s43, s43, 1\n\t" \
  "s_cbranch_scc1 LQd7_%=\n\ts_sleep %[oback]\n\tglobal_load_dwordx4 v[136:139], %[vd], %[oa7] sc1\n\t" \
  "global_load_dwordx4 v[140:143], %[vd], %[oa8] sc1\n\ts_waitcnt vmcnt(0)\n\tv_max_u32 %[tk], v136, v140\n\tv_cmp_eq_u32 vcc, -1, %[tk]\n\t" \
  "s_cbranch_vccnz LSd7_%=\n\ts_branch LQd7_%=\n\tLSd9_%=:\n\ts_sub_u32 s43, s43, 1\n\ts_cbranch_scc1 LQd9_%=\n\ts_sleep %[oback]\n\t" \
  "global_load_dwordx4 v[144:147], %[vd], %[oa9] sc1\n\tglobal_load_dwordx4 v[148:151], %[vk], %[oa7] sc1\n\ts_waitcnt vmcnt(0)\n\t" \
  "v_max3_u32 %[tk], v144, v146, v148\n\tv_cmp_eq_u32 vcc, -1, %[tk]\n\ts_cbranch_vccnz LSd9_%=\n\ts_branch LQd9_%=\n\tLSk8_%=:\n\t" \
  "s_sub_u32 s43, s43, 1\n\ts_cbranch_scc1 LQk8_%=\n\ts_sleep %[oback]\n\tglobal_load_dwordx4 v[152:155], %[vk], %[oa8] sc1\n\t" \
  "global_load_dwordx4 v[156:159], %[vk], %[oa9] sc1\n\ts_waitcnt vmcnt(0)\n\tv_max3_u32 %[tk], v152, v156, v158\n\tv_cmp_eq_u32 vcc, -1, %[tk]\n\t" \
  "s_cbranch_vccnz LSk8_%=\n\ts_branch LQk8_%=\n\tLSh1_%=:\n\ts_sub_u32 s43, s43, 1\n\ts_cbranch_scc1 LQh1_%=\n\ts_sleep %[oback]\n\t" \
  "global_load_dwordx4 v[140:143], %[vk], %[oa1] sc1\n\ts_waitcnt vmcnt(0)\n\tv_cmp_eq_u32 vcc, -1, v140\n\ts_cbranch_vccnz LSh1_%=\n\t" \
  "s_branch LQh1_%=\n\tLSh2_%=:\n\ts_sub_u32 s43, s43, 1\n\ts_cbranch_scc1 LQh2_%=\n\ts_sleep %[oback]\n\t" \
  "global_load_dwordx4 v[144:147], %[vk], %[oa2] sc1\n\ts_waitcnt vmcnt(0)\n\tv_cmp_eq_u32 vcc, -1, v144\n\ts_cbranch_vccnz LSh2_%=\n\t" \
  "s_branch LQh2_%=\n\tLSh3_%=:\n\ts_sub_u32 s43, s43, 1\n\ts_cbranch_scc1 LQh3_%=\n\ts_sleep %[oback]\n\t" \
  "global_load_dwordx4 v[148:151], %[vk], %[oa3] sc1\n\ts_waitcnt vmcnt(0)\n\tv_cmp_eq_u32 vcc, -1, v148\n\ts_cbranch_vccnz LSh3_%=\n\t" \
  "s_branch LQh3_%=\n\tLSh4_%=:\n\ts_sub_u32 s43, s43, 1\n\ts_cbranch_scc1 LQh4_%=\n\ts_sleep %[oback]\n\t" \
  "global_load_dwordx4 v[152:155], %[vk], %[oa4] sc1\n\ts_waitcnt vmcnt(0)\n\tv_max_u32 %[tk], v152, v154\n\tv_cmp_eq_u32 vcc, -1, %[tk]\n\t" \
  "s_cbranch_vccnz LSh4_%=\n\ts_branch LQh4_%=\n\tLSh0_%=:\n\ts_sub_u32 s43, s43, 1\n\ts_cbranch_scc1 LQh0_%=\n\ts_sleep %[oback]\n\t" \
  "global_load_dwordx4 v[156:159], %[vk], %[oa0] sc1\n\ts_waitcnt vmcnt(0)\n\tv_cmp_eq_u32 vcc, -1, v156\n\ts_cbranch_vccnz LSh0_%=\n\t" \
  "s_branch LQh0_%=\n\tLZ_%=:\n\t" \
  ""
#define COL_ASM \
  "s_mov_b32 s43, %[cap]\n\tv_xor_b32 v101, 0x400, %[lq]\n\tv_xor_b32 v102, 0x800, %[lq]\n\tv_xor_b32 v103, 0xc00, %[lq]\n\t" \
  "v_xor_b32 v104, 0x1000, %[lq]\n\tv_xor_b32 v105, 0x1400, %[lq]\n\tv_xor_b32 v106, 0x1800, %[lq]\n\tv_xor_b32 v107, 0x1c00, %[lq]\n\tLC0_%=:\n\t" \
  "ds_read_b32 v96, %[ac]\n\ts_waitcnt lgkmcnt(0)\n\tv_cmp_gt_u32 vcc, %[nf], v96\n\ts_cbranch_vccz LC1_%=\n\ts_sleep 1\n\ts_sub_u32 s43, s43, 1\n\t" \
  "s_cbranch_scc0 LC0_%=\n\tLC1_%=:\n\ts_setprio 2\n\ts_sleep %[pdel]\n\tglobal_load_dwordx4 v[68:71], v101, %[sp] sc1\n\t" \
  "global_load_dwordx4 v[72:75], v102, %[sp] sc1\n\tglobal_load_dwordx4 v[76:79], v103, %[sp] sc1\n\tglobal_load_dwordx4 v[80:83], v104, %[sp] sc1\n\t" \
  "global_load_dwordx4 v[84:87], v105, %[sp] sc1\n\tglobal_load_dwordx4 v[88:91], v106, %[sp] sc1\n\tglobal_load_dwordx4 v[92:95], v107, %[sp] sc1\n\t" \
  "ds_read_b128 v[64:67], %[ao]\n\ts_waitcnt lgkmcnt(0)\n\tLC2_%=:\n\ts_waitcnt vmcnt(4)\n\tv_pk_add_f32 v[108:109], v[64:65], v[68:69]\n\t" \
  "v_add_f32 v110, v66, v70\n\tv_pk_add_f32 v[112:113], v[72:73], v[76:77]\n\tv_add_f32 v114, v74, v78\n\t" \
  "v_pk_add_f32 v[108:109], v[108:109], v[112:113]\n\tv_add_f32 v110, v110, v114\n\ts_waitcnt vmcnt(0)\n\tv_pk_add_f32 v[116:117], v[80:81], v[84:85]\n\t" \
  "v_add_f32 v118, v82, v86\n\tv_pk_add_f32 v[120:121], v[88:89], v[92:93]\n\tv_add_f32 v122, v90, v94\n\t" \
  "v_pk_add_f32 v[116:117], v[116:117], v[120:121]\n\tv_add_f32 v118, v118, v122\n\tv_pk_add_f32 v[108:109], v[108:109], v[116:117]\n\t" \
  "v_add_f32 v110, v110, v118\n\tv_mul_f32 v112, 0x3acccccd, v108\n\tv_add_f32 v111, v108, v109\n\tv_mul_f32 v113, v112, v112\n\t" \
  "v_cmp_u_f32 vcc, v111, v110\n\tv_fma_f32 v113, v110, %[rdm], -v113\n\ts_cbranch_vccnz LC5_%=\n\tLC3_%=:\n\tv_max_f32 v113, 0, v113\n\t" \
  "v_cvt_pk_bf16_f32 v113, 0, v113\n\tv_add_f32 v113, 0x3727c5ac, v113\n\tv_cvt_pk_bf16_f32 v113, 0, v113\n\tv_sqrt_f32 v113, v113\n\t" \
  "v_cvt_pk_bf16_f32 v112, 0, v112\n\tv_cvt_pk_bf16_f32 v113, 0, v113\n\tv_rcp_f32 v113, v113\n\tv_fma_f32 v114, -%[swr], v112, v109\n\t" \
  "v_mul_f32 v114, v114, v113\n\tv_ashrrev_i32 v115, 31, v114\n\tv_or_b32 v115, 0x80000000, v115\n\tv_xor_b32 v114, v114, v115\n\t" \
  "v_and_or_b32 v114, v114, %[msk], %[rid]\n\ts_nop 1\n\tv_max_u32_dpp v114, v114, v114 quad_perm:[1,0,3,2] row_mask:0xf bank_mask:0xf bound_ctrl:1\n\t" \
  "ds_write_b64 %[at], v[112:113]\n\ts_nop 0\n\tv_max_u32_dpp v114, v114, v114 quad_perm:[2,3,0,1] row_mask:0xf bank_mask:0xf bound_ctrl:1\n\ts_nop 1\n\t" \
  "v_max_u32_dpp v114, v114, v114 row_half_mirror row_mask:0xf bank_mask:0xf bound_ctrl:1\n\ts_nop 1\n\t" \
  "v_max_u32_dpp v114, v114, v114 row_mirror row_mask:0xf bank_mask:0xf bound_ctrl:1\n\ts_nop 0\n\tv_readlane_b32 s40, v114, 0\n\t" \
  "v_readlane_b32 s41, v114, 16\n\tv_readlane_b32 s42, v114, 32\n\ts_max_u32 s40, s40, s41\n\ts_max_u32 s40, s40, s42\n\tv_mov_b32 v98, s40\n\t" \
  "s_mov_b32 exec_lo, 0x01010101\n\ts_mov_b32 exec_hi, 0x0101\n\tglobal_store_dwordx2 %[zr], v[98:99], %[kp] sc1\n\tds_write_b32 %[ak], v98\n\t" \
  "s_mov_b64 exec, -1\n\ts_and_b32 s41, s40, 0x7ff\n\ts_sub_u32 s41, %[rb], s41\n\ts_mul_i32 s42, s41, 160\n\tv_add_u32 v115, s42, %[ah]\n\t" \
  "ds_read_b64 v[116:117], v115\n\tv_readlane_b32 s44, v112, s41\n\tv_readlane_b32 s46, v113, s41\n\ts_waitcnt lgkmcnt(0)\n\ts_mov_b32 exec_hi, 0\n\t" \
  "v_lshlrev_b32 v118, 16, v116\n\tv_and_b32 v119, 0xffff0000, v116\n\tv_lshlrev_b32 v120, 16, v117\n\tv_and_b32 v121, 0xffff0000, v117\n\t" \
  "v_pk_add_f32 v[122:123], v[118:119], s[44:45] op_sel_hi:[1,0] neg_lo:[0,1] neg_hi:[0,1]\n\t" \
  "v_pk_add_f32 v[114:115], v[120:121], s[44:45] op_sel_hi:[1,0] neg_lo:[0,1] neg_hi:[0,1]\n\t" \
  "v_pk_fma_f32 v[118:119], v[122:123], s[46:47], v[118:119] op_sel_hi:[1,0,1]\n\t" \
  "v_pk_fma_f32 v[120:121], v[114:115], s[46:47], v[120:121] op_sel_hi:[1,0,1]\n\tv_cvt_pk_bf16_f32 v116, v118, v119\n\t" \
  "v_cvt_pk_bf16_f32 v117, v120, v121\n\ts_mov_b32 exec_hi, -1\n\tglobal_store_dwordx2 %[co], v[116:117], %[cp] sc1\n\ts_branch LC9_%=\n\tLC5_%=:\n\t" \
  "s_sub_u32 s43, s43, 1\n\ts_cbranch_scc1 LC3_%=\n\ts_mov_b32 s40, 0\n\tv_max3_u32 v96, v68, v69, v70\n\tv_cmp_eq_u32 vcc, -1, v96\n\t" \
  "s_cbranch_vccz LC61_%=\n\tglobal_load_dwordx4 v[68:71], v101, %[sp] sc1\n\ts_mov_b32 s40, 1\n\tLC61_%=:\n\tv_max3_u32 v96, v72, v73, v74\n\t" \
  "v_cmp_eq_u32 vcc, -1, v96\n\ts_cbranch_vccz LC62_%=\n\tglobal_load_dwordx4 v[72:75], v102, %[sp] sc1\n\ts_mov_b32 s40, 1\n\tLC62_%=:\n\t" \
  "v_max3_u32 v96, v76, v77, v78\n\tv_cmp_eq_u32 vcc, -1, v96\n\ts_cbranch_vccz LC63_%=\n\tglobal_load_dwordx4 v[76:79], v103, %[sp] sc1\n\t" \
  "s_mov_b32 s40, 1\n\tLC63_%=:\n\tv_max3_u32 v96, v80, v81, v82\n\tv_cmp_eq_u32 vcc, -1, v96\n\ts_cbranch_vccz LC64_%=\n\t" \
  "global_load_dwordx4 v[80:83], v104, %[sp] sc1\n\ts_mov_b32 s40, 1\n\tLC64_%=:\n\tv_max3_u32 v96, v84, v85, v86\n\tv_cmp_eq_u32 vcc, -1, v96\n\t" \
  "s_cbranch_vccz LC65_%=\n\tglobal_load_dwordx4 v[84:87], v105, %[sp] sc1\n\ts_mov_b32 s40, 1\n\tLC65_%=:\n\tv_max3_u32 v96, v88, v89, v90\n\t" \
  "v_cmp_eq_u32 vcc, -1, v96\n\ts_cbranch_vccz LC66_%=\n\tglobal_load_dwordx4 v[88:91], v106, %[sp] sc1\n\ts_mov_b32 s40, 1\n\tLC66_%=:\n\t" \
  "v_max3_u32 v96, v92, v93, v94\n\tv_cmp_eq_u32 vcc, -1, v96\n\ts_cbranch_vccz LC67_%=\n\tglobal_load_dwordx4 v[92:95], v107, %[sp] sc1\n\t" \
  "s_mov_b32 s40, 1\n\tLC67_%=:\n\ts_cmp_eq_u32 s40, 0\n\ts_cbranch_scc1 LC3_%=\n\ts_waitcnt vmcnt(0)\n\ts_branch LC2_%=\n\tLC9_%=:\n\t" \
  ""
extern "C" __global__ __launch_bounds__(768, 3) void fused_block(const u16* x, const u16* wqkv, const u16* wo, const u16* w_r,
                                                   const u16* w_gate, const u16* w_up, const u16* w_down, u16* out,
                                                   char* ws) {
  extern __shared__ __attribute__((aligned(16))) char smem[];
  const int tid = threadIdx.x;
  const int lane = tid & 63;
  const int w = __builtin_amdgcn_readfirstlane(tid >> 6);
  const int g = lane >> 4, r16 = lane & 15, q4 = (lane & 15) >> 2, p4 = lane & 3;
  const int bid = blockIdx.x;
  const int xcd = bid & 7;
  const int bidx = bid >> 3;
  const u32 l16 = lane * 16;
  if (w == 11) {
    u64 pc;
    asm volatile("s_getpc_b64 %0" : "=s"(pc));
    if (lane < 10) touch_line((const void*)(pc + min((bidx * 10 + lane) * 128, CODE_BYTES)));
  }

  if (bidx >= 26) {
    const int fb = xcd * 6 + (bidx - 26);
    const u32 tstart = (u32)__builtin_amdgcn_s_memrealtime();
    const bool hurry = ((((u32)(u64)w_gate + fb * 64) >> 7) & 7u) == 3u;
#define WAIT_UNTIL(T)                                                                      \
  _Pragma("nounroll") for (int it = 0; it < 1000; ++it) {                                  \
    if ((int)((u32)__builtin_amdgcn_s_memrealtime() - tstart) >= (T)) break;               \
    __builtin_amdgcn_s_sleep(4);                                                           \
  }
    if (w < 3) {
      int li = (bidx - 26) * 160 + w * 64 + lane;
      if (w < 2 || lane < 32) touch_line((const char*)x + (size_t)(((xcd >> 2) * 13 + 6) * 80 - 16) * 1280 + li * 128);
    }
    u32x2 ep;
    u32 tag0;
    asm volatile("global_load_dwordx2 %0, %1, off sc1" : "=v"(ep) : "v"(ws + WS_EPOCH) : "memory");
    {
      int wi = (((xcd >> 1) & 1) * 6 + (bidx - 26)) * 12 + w;
      wi = wi > 129 ? 129 : wi;
      int row = (xcd >> 2) * 1040 + (xcd & 1) * 520 + wi * 4 + g;
      row = row > SEQ - 1 ? SEQ - 1 : row;
      const u32 vo = row * 1280 + r16 * 16;
      u32x4 xr[5];
#pragma unroll
      for (int i = 0; i < 5; ++i)
        asm volatile("global_load_dwordx4 %0, %1, %2 offset:%3" : "=v"(xr[i]) : "v"(vo), "s"(x), "n"(i * 256) : "memory");
      asm volatile("s_waitcnt vmcnt(0)" ::: "memory");
      asm volatile("" : "+v"(xr[0]), "+v"(xr[1]), "+v"(xr[2]), "+v"(xr[3]), "+v"(xr[4]), "+v"(ep)::"memory");
      tag0 = ep[0] + (ep[0] == SENT - 1u ? 2u : 1u);
      float sm = 0.f;
#pragma unroll
      for (int i = 0; i < 5; ++i)
#pragma unroll
        for (int e = 0; e < 4; ++e) sm = dot2b(xr[i][e], 0x3F803F80u, sm);
      const float mean = row16_sum(sm) * (1.0f / DM);
      float ss = 0.f;
#pragma unroll
      for (int i = 0; i < 5; ++i)
#pragma unroll
        for (int e = 0; e < 4; ++e) {
          const float a = lo_f(xr[i][e]) - mean, b = hi_f(xr[i][e]) - mean;
          ss = __builtin_fmaf(a, a, ss);
          ss = __builtin_fmaf(b, b, ss);
        }
      const float var = row16_sum(ss) * (1.0f / DM);
      const float den = rbf(__builtin_amdgcn_sqrtf(rbf(rbf(var) + 1e-5f)));
      if (r16 == 0) GST2((u32)(row * 8), ws + WS_LN1, 0, ((u32x2){cvt2((f32x2){mean, den}), tag0}));
    }
    u32x4 va[2][5], vb[5];
    const int vrk = (fb >> 4) * 14 + (int)(((((u32)(u64)w_gate + fb * 64) >> 6) - 8u) & 15u);
    const int vq0 = vrk * 4 + w * 2;
    u32x4 ra;
    u32 rb5;
    asm volatile("global_load_dwordx4 %0, %2, %4\n\tglobal_load_dword %1, %3, %4 offset:1024"
                   : "=&v"(ra), "=&v"(rb5) : "v"(l16), "v"((u32)(lane * 4)), "s"(w_r) : "memory");
    if (w >= 2) {
      WAIT_UNTIL(hurry ? 0 : PDELAY + 12 * (w - 2))
      const u32x4 s4 = (u32x4){SENT, SENT, SENT, SENT};
      const char* fp = ws + WS_FILL + (size_t)fb * 73728 + (w - 2) * 7168 + 4096;
      GST4(l16, fp, -4096, s4); GST4(l16, fp, -3072, s4); GST4(l16, fp, -2048, s4); GST4(l16, fp, -1024, s4);
      GST4(l16, fp, 0, s4); GST4(l16, fp, 1024, s4); GST4(l16, fp, 2048, s4);
      if (w < 4) GST4(l16, ws + WS_FILL + (size_t)fb * 73728 + 69632 + w * 1024, 0, s4);
      if (w >= 5) GST4((u32)(((w - 5) * 8 + (lane >> 3)) * 6144 + (lane & 7) * 16), ws + WS_PK + fb * 128, 0, s4);
    }
    if (w == 0) {
      const u64 e1 = (((u64)ep[1] << 32) | ep[0]) + (ep[0] == SENT - 1u ? 2ull : 1ull);
      if (lane == 0) *(u32x4*)(smem + CTL_OFF) = (u32x4){(u32)e1, (u32)(e1 >> 32), 0u, 0u};
      if (lane >= 1 && lane < 8) *(u32x4*)(smem + CTL_OFF + lane * 16) = (u32x4){0u, 0u, 0u, 0u};
      if (lane >= 32) *(u32x4*)(smem + KEY_OFF - 512 + lane * 16) = (u32x4){0u, 0u, 0u, 0u};
    }
    if (w >= 6) {
      const u32 o1 = tid >= 636 ? 0x3F803F80u : 0u;
      if (tid < 720) *(u32x4*)(smem + HN_OFF + (tid - 384) * 16) = (u32x4){o1, o1, o1, o1};
    }
    if (w < 2 && !hurry && vrk < 40) {
      WAIT_UNTIL(VDELAY)
      const u32 vo = (vq0 * 4 + g) * 1280 + r16 * 16;
#pragma unroll
      for (int i = 0; i < 5; ++i)
        asm volatile("global_load_dwordx4 %0, %3, %4 offset:%8\n\tglobal_load_dwordx4 %1, %3, %5 offset:%8\n\tglobal_load_dwordx4 %2, %6, %7 offset:%8"
                     : "=&v"(va[0][i]), "=&v"(va[1][i]), "=&v"(vb[i]) : "v"(vo), "s"(wo), "s"((const char*)wo + 5120), "v"((u32)(r16 * 16)), "s"(w_r), "n"(i * 256) : "memory");
      asm volatile("s_waitcnt vmcnt(0)" : "+v"(va[0][0]), "+v"(va[0][1]), "+v"(va[0][2]), "+v"(va[0][3]), "+v"(va[0][4]), "+v"(va[1][0]), "+v"(va[1][1]),
                   "+v"(va[1][2]), "+v"(va[1][3]), "+v"(va[1][4]), "+v"(vb[0]), "+v"(vb[1]), "+v"(vb[2]), "+v"(vb[3]), "+v"(vb[4])::"memory");
      float sb = 0.f;
#pragma unroll
      for (int i = 0; i < 20; ++i) sb = dot2b(vb[i >> 2][i & 3], 0x3F803F80u, sb);
      sb = row16_sum(sb) * (1.0f / DM);
      u32x4 pk2[2];
#pragma unroll
      for (int k = 0; k < 2; ++k) {
        float d = 0.f, sa = 0.f;
#pragma unroll
        for (int i = 0; i < 20; ++i) {
          d = dot2b(va[k][i >> 2][i & 3], vb[i >> 2][i & 3], d);
          sa = dot2b(va[k][i >> 2][i & 3], 0x3F803F80u, sa);
        }
        const u32 vp = __float_as_uint(row16_sum(d - sa * sb));
        pk2[k] = (u32x4){cvt2((f32x2){__uint_as_float(__builtin_amdgcn_readlane(vp, 0)), __uint_as_float(__builtin_amdgcn_readlane(vp, 16))}), tag0,
                          cvt2((f32x2){__uint_as_float(__builtin_amdgcn_readlane(vp, 32)), __uint_as_float(__builtin_amdgcn_readlane(vp, 48))}), tag0};
      }
      if (lane < 8) GST4(l16, ws + WS_VP + vq0 * 64, 0, ((lane & 1) ? pk2[1] : pk2[0]));
    } else
      asm volatile("s_waitcnt vmcnt(0)" ::: "memory");
    asm volatile("" : "+v"(ra), "+v"(rb5)::"memory");
    LDS_BARRIER();
    const u32x2 tg = *(const u32x2*)(smem + CTL_OFF);
    const u32 tag = tg[0];
    if (w == 2 && lane == 0) GST2(0u, ws + WS_HFLAG + fb * 128, 0, tg);
    if (w >= 3) {
      int li = (bidx - 26) * 534 + (w - 3) * 64 + lane;
      li = li > 3199 ? 3199 : li;
      const int rr = li / 5;
      if (w < 11 || lane < 22) touch_line((const char*)wo + rr * 1280 + ((xcd >> 1) & 1) * 640 + (li - rr * 5) * 128);
    }
    WAIT_UNTIL(hurry ? 0 : FDELAY)
    asm volatile("s_barrier" ::: "memory");
    if (w >= 2) {
      const int ch = (lane & 7) ^ (((lane >> 4) & 1) * 2 + ((w + (OPP ? fb : 0)) & 1) * 4);
      const u32 vg = (lane >> 3) * (FF * 2) + fb * 64 + (ch & 3) * 16;
      const u64 gsel = (ch & 4) ? (u64)w_up : (u64)w_gate;
#pragma nounroll
      for (int n0 = w - 2; n0 < 80; n0 += 10) {
        const int n = (OPP && (fb & 1)) ? 79 - n0 : n0;
        const u64 p = gsel + vg + (u32)(n * 8 * FF * 2);
        asm volatile("s_mov_b32 m0, %1\n\ts_nop 0\n\tglobal_load_lds_dwordx4 %0, off" ::"v"(p), "s"(n * 1024) : "memory");
      }
#pragma nounroll
      for (int n = 11 - w; n < 40; n += 10) {
        const u32 row = (u32)(n * 64 + lane) / 80u;
        asm volatile("s_mov_b32 m0, %2\n\ts_nop 0\n\tglobal_load_lds_dwordx4 %0, %1" " nt"
                     ::"v"(l16 ^ ((row & 3u) * 32u + (row & 8u) * 16u)), "s"((const char*)w_down + (size_t)fb * 40960 + n * 1024), "s"(DOWN_OFF + n * 1024) : "memory");
      }
      asm volatile("s_waitcnt vmcnt(0)" ::: "memory");
      if (lane == 0) asm volatile("ds_add_u32 %0, %1" ::"v"(CTL_OFF + 48), "v"(1u) : "memory");
    } else {
      if (w == 1 && lane < 40) touch_line((const char*)out + lane * 65536 + fb * 1280);
      WAIT_UNTIL(KSTART)
    }
    if (w >= 4) {
      const int v = w - 4, kq = v & 3, m = v >> 2;
#pragma nounroll
      for (int it = 0; it < SPIN_CAP; ++it) {
        if (__builtin_amdgcn_readfirstlane(lds_ld32(CTL_OFF + 48)) >= 10u) break;
        __builtin_amdgcn_s_sleep(1);
      }
      bf16x8 gfr[5], ufr[5], dfr[5];
      {
        const int gx = (160 * kq + 8 * g + q4) * 128 + ((m ^ (q4 >> 1) ^ ((g & 1) * 2)) * 32) + p4 * 8;
        const char* db = smem + DOWN_OFF + (8 * g + q4) * 1280 + p4 * 8;
#pragma unroll
        for (int j = 0; j < 5; ++j) {
          gfr[j] = tr_frag(smem + gx + j * 4096, 512);
          ufr[j] = tr_frag(smem + (gx ^ 64) + j * 4096, 512);
          dfr[j] = tr_frag(db + ((v * 160 + j * 32) ^ (q4 * 32 + (g & 1) * 128)), 5120);
        }
#pragma unroll
        for (int j = 0; j < 5; ++j) asm volatile("" : "+v"(gfr[j]), "+v"(ufr[j]), "+v"(dfr[j]));
      }
      u32 ahn = HN_OFF + p4 * HSL + kq * 320 + g * 16, asw = ACT_OFF + p4 * ASL + kq * 256 + (q4 & 1) * 128 + m * 64 + g * 16, asr = ACT_OFF + (lane & 31) * 4;
      u32 aav = ACT_OFF + 4352 + v * 64 + (lane & 31) * 2, aar = ACT_OFF + 4352 + v * 64 + g * 16, vpo = v * 320 + r16 * 64 + g * 16, up3 = lane >= 32 ? 3u : 0u;
      asm volatile("" : "+v"(up3), "+v"(aar));
      const char* pdst = ws + WS_PART + fb * 2560;
      asm volatile("" : "+v"(ahn), "+v"(asw), "+v"(asr), "+v"(aav), "+v"(vpo));
      asm volatile("" : "+s"(pdst));
      int pass = 0;
      asm volatile("" : "+s"(pass));
#pragma nounroll
      for (; pass < 2; ++pass) {
        asm volatile("s_barrier" ::: "memory");
        u32x4 bs;
#define GU(J, C0, C1) "v_mfma_f32_16x16x32_bf16 v[120:123], %[g" #J "], v[100+4*" #J ":103+4*" #J "], " C0 "\n\t" \
                      "v_mfma_f32_16x16x32_bf16 v[124:127], %[u" #J "], v[100+4*" #J ":103+4*" #J "], " C1 "\n\t"
#define GU1(J) GU(J, "v[120:123]", "v[124:127]")
        asm volatile("ds_read_b128 v[100:103], %[ah]\n\tds_read_b128 v[104:107], %[ah] offset:64\n\tds_read_b128 v[108:111], %[ah] offset:128\n\t"
                     "ds_read_b128 v[112:115], %[ah] offset:192\n\tds_read_b128 v[116:119], %[ah] offset:256\n\t"
                     "s_waitcnt lgkmcnt(4)\n\t" GU(0, "0", "0") "s_waitcnt lgkmcnt(3)\n\t" GU1(1) "s_waitcnt lgkmcnt(2)\n\t" GU1(2)
                     "s_waitcnt lgkmcnt(1)\n\t" GU1(3) "s_waitcnt lgkmcnt(0)\n\t" GU1(4)
                     "ds_read_b128 %[bs], %[ab]\n\ts_mov_b32 exec_lo, 0x00f000f0\n\ts_mov_b32 exec_hi, 0x00f000f0\n\ts_nop 4\n\t"
                     "v_mov_b64 v[120:121], v[124:125]\n\tv_mov_b64 v[122:123], v[126:127]\n\t"
                     "s_mov_b32 exec_lo, 0x00ff00ff\n\ts_mov_b32 exec_hi, 0x00ff00ff\n\tds_write_b128 %[aw], v[120:123]\n\ts_mov_b64 exec, -1\n\t"
                     "s_setprio 3\n\ts_waitcnt lgkmcnt(0)\n\tv_cmp_eq_u32 vcc, 3, v129\n\ts_barrier\n\t"
                     "s_cbranch_vccnz 1f\n\tds_read_b128 %[bs], %[ab]\n\ts_waitcnt lgkmcnt(0)\n1:"
                     : [bs] "=&{v[128:131]}"(bs)
                     : [ah] "v"(ahn), [ab] "v"((u32)(CTL_OFF + 112)), [aw] "v"(asw), [g0] "v"(gfr[0]), [g1] "v"(gfr[1]), [g2] "v"(gfr[2]),
                       [g3] "v"(gfr[3]), [g4] "v"(gfr[4]), [u0] "v"(ufr[0]), [u1] "v"(ufr[1]), [u2] "v"(ufr[2]), [u3] "v"(ufr[3]), [u4] "v"(ufr[4])
                     : "memory", "vcc", "scc", "v100", "v101", "v102", "v103", "v104", "v105", "v106", "v107", "v108", "v109", "v110", "v111", "v112", "v113",
                       "v114", "v115", "v116", "v117", "v118", "v119", "v120", "v121", "v122", "v123", "v124", "v125", "v126", "v127");
        const u32x2 best = {bs[2], bs[3]};
        f32x2 s0, s1, s2, s3;
        asm volatile("ds_read2_b32 %0, %4 offset0:0 offset1:32\n\tds_read2_b32 %1, %4 offset0:64 offset1:96\n\t"
                     "ds_read2_b32 %2, %4 offset0:128 offset1:160\n\tds_read2_b32 %3, %4 offset0:192 offset1:224"
                     : "=&v"(s0), "=&v"(s1), "=&v"(s2), "=&v"(s3) : "v"(asr + ((best[1] & 3u) | up3) * ASL) : "memory");
        float rd = __builtin_amdgcn_rcpf(hi_f(best[0])), mb = lo_f(best[0]);
        asm volatile("s_waitcnt lgkmcnt(0)" : "+v"(s0), "+v"(s1), "+v"(s2), "+v"(s3), "+v"(rd), "+v"(mb)::"memory");
        const f32x2 sm = (s0 + s1) + (s2 + s3);
        auto xg = __builtin_amdgcn_permlane32_swap(__float_as_uint(sm[0]), __float_as_uint(sm[0]), false, false);
        auto xu = __builtin_amdgcn_permlane32_swap(__float_as_uint(sm[1]), __float_as_uint(sm[1]), false, false);
        const f32x2 gu = unpk(cvt2((f32x2){(__uint_as_float(xg[0]) - mb * __uint_as_float(xg[1])) * rd,
                                          (__uint_as_float(xu[0]) - mb * __uint_as_float(xu[1])) * rd}));
        const float sg = rbf(gu[0] * __builtin_amdgcn_rcpf(1.0f + __expf(-gu[0])));
        const u32 av = f2bf(sg * gu[1]);
#define DN(J) "v_mfma_f32_16x16x32_bf16 v[104+4*" #J ":107+4*" #J "], %[d" #J "], v[100:103], 0\n\t"
#define MV(J) "s_lshl_b64 exec, exec, 1\n\tv_mov_b64 v[104:105], v[104+4*" #J ":105+4*" #J "]\n\tv_mov_b64 v[106:107], v[106+4*" #J ":107+4*" #J "]\n\t"
        asm volatile("ds_write_b16 %[aw], %[av]\n\tds_read_b128 v[100:103], %[ar]\n\ts_waitcnt lgkmcnt(0)\n\t" DN(0) DN(1) DN(2) DN(3) DN(4)
                     "s_mov_b32 exec_lo, 0x00010001\n\ts_mov_b32 exec_hi, 0x00010001\n\ts_nop 1\n\t" MV(1) MV(2) MV(3) MV(4)
                     "s_mov_b32 exec_lo, 0x001f001f\n\ts_mov_b32 exec_hi, 0x001f001f\n\tglobal_store_dwordx4 %[vp], v[104:107], %[pd] sc1\n\t"
                     "s_mov_b64 exec, -1\n\ts_setprio 0"
                     ::[aw] "v"(aav), [av] "v"(av), [ar] "v"(aar), [vp] "v"(vpo), [pd] "s"(pdst), [d0] "v"(dfr[0]), [d1] "v"(dfr[1]),
                       [d2] "v"(dfr[2]), [d3] "v"(dfr[3]), [d4] "v"(dfr[4])
                     : "memory", "scc", "v100", "v101", "v102", "v103", "v104", "v105", "v106", "v107", "v108", "v109", "v110", "v111", "v112", "v113",
                       "v114", "v115", "v116", "v117", "v118", "v119", "v120", "v121", "v122", "v123");
        if (pass) break;
        u32 fin = 0;
#pragma nounroll
        for (int it = 0; it < SPIN_CAP * 8; ++it) {
          fin = __builtin_amdgcn_readfirstlane(lds_ld32(CTL_OFF + 40));
          if (fin) break;
          __builtin_amdgcn_s_sleep(4);
        }
        if (fin != 2u) break;
        pdst += WS_PART2 - WS_PART;
      }
      return;
    }
    if (w == 0) {
      u32 pk6;
      asm volatile(
          "s_mov_b32 s41, %[cap]\n\ts_mov_b32 s40, 1\n\ts_mov_b64 s[46:47], 0\n\tv_mov_b32 v5, %[ln]\n\t"
          "s_mov_b32 s54, -1\n\ts_mov_b32 s55, 0x7ffff\n\tv_mov_b32 v12, 0\n\t"
#define PKLD(D) "s_mov_b64 exec, s[54:55]\n\tglobal_atomic_or_x2 " D ", %[vk], %[z], %[kb] sc0 sc1\n\ts_mov_b64 exec, -1\n\t"
          PKLD("v[8:9]") "s_sleep %[gap]\n\t" PKLD("v[10:11]") "s_sleep %[gap]\n\t" PKLD("v[14:15]")
#define MDPP(C) "s_nop 1\n\tv_max_u32_dpp v13, v13, v13 " C " row_mask:0xf bank_mask:0xf bound_ctrl:1\n\t"
#define PSTEP(L, D0, D1, DR)                                                                        \
          L "0:\n\t"                                                                               \
          "s_waitcnt vmcnt(2)\n\t"                                                                 \
          "v_cmp_eq_u32 s[42:43], " D1 ", %[tag]\n\t"                                              \
          "s_and_b64 exec, s[42:43], s[54:55]\n\ts_andn2_b64 s[54:55], s[54:55], s[42:43]\n\t"      \
          "v_and_or_b32 v12, " D0 ", %[msk], %[lid]\n\t"                                           \
          "v_mov_b32 v6, " D0 "\n\ts_mov_b64 exec, -1\n\t"                                         \
          "v_cmp_le_u32 vcc, s40, v12\n\t"                                                         \
          "s_cmp_eq_u64 vcc, s[46:47]\n\ts_cbranch_scc1 " L "1f\n\t"                              \
          "s_swappc_b64 s[50:51], s[48:49]\n"                                                      \
          L "1:\n\t"                                                                               \
          "s_cmp_eq_u64 s[54:55], 0\n\ts_cbranch_scc1 9f\n\t"                                     \
          PKLD(DR)
          "s_getpc_b64 s[48:49]\n\ts_add_u32 s48, s48, 12\n\ts_addc_u32 s49, s49, 0\n\ts_branch 10f\n\t"
          "v_mov_b32 v13, v12\n\t"
          MDPP("quad_perm:[1,0,3,2]") MDPP("quad_perm:[2,3,0,1]") MDPP("row_half_mirror") MDPP("row_mirror")
          "v_bfe_u32 v4, v6, 10, 6\n\tv_readlane_b32 s44, v13, 0\n\tv_readlane_b32 s45, v13, 16\n\tv_readlane_b32 s46, v13, 32\n\tv_readlane_b32 s47, v13, 48\n\t"
          "v_sub_u32 v4, %[rb], v4\n\ts_max_u32 s44, s44, s45\n\ts_max_u32 s46, s46, s47\n\ts_max_u32 s44, s44, s46\n\t"
          "s_and_b32 s45, s44, 0x7fffff\n\ts_bitset1_b32 s45, 23\n\ts_mul_hi_u32 s45, s45, %[shr]\n"
          "5:\n\ts_sub_u32 s40, s44, s45\n\ts_cselect_b32 s40, 1, s40\n\ts_max_u32 s40, s40, 1\n\tv_cmp_le_u32 s[46:47], s40, v12\n\ts_mul_i32 s45, s45, 3\n\ts_lshr_b32 s45, s45, 2\n\t"
          "s_bcnt1_i32_b64 s52, s[46:47]\n\ts_cmp_gt_u32 s52, 3\n\ts_cbranch_scc1 5b\n\t"
          "v_mbcnt_lo_u32_b32 v13, s46, 0\n\tv_mbcnt_hi_u32_b32 v13, s47, v13\n\tv_sub_u32 v7, %[ln], v13\n\tv_add_u32 v7, s52, v7\n\t"
          "v_cndmask_b32 v13, v7, v13, s[46:47]\n\tv_cndmask_b32 v7, 2, 1, s[46:47]\n\tv_cmp_gt_u32 s[44:45], 3, v13\n\t"
          "v_lshl_add_u32 v13, v13, 4, %[pa]\n\ts_mov_b64 exec, s[44:45]\n\tds_write_b128 v13, v[4:7]\n\ts_mov_b64 exec, -1\n\t"
          "s_setpc_b64 s[50:51]\n"
          PSTEP("1", "v8", "v9", "v[8:9]")
          PSTEP("2", "v10", "v11", "v[10:11]")
          PSTEP("3", "v14", "v15", "v[14:15]")
          "s_sub_u32 s41, s41, 1\n\ts_cbranch_scc0 10b\n\ts_swappc_b64 s[50:51], s[48:49]\n"
          "9:\n\t"  "v_or_b32 v7, 4, v7\n\ts_mov_b64 exec, s[44:45]\n\tds_write_b32 v13, v7 offset:12\n\ts_mov_b64 exec, -1\n\tds_write_b32 %[at], v6\n\ts_waitcnt lgkmcnt(0)\n\ts_barrier\n\ts_waitcnt vmcnt(0)"
          : "=&{v6}"(pk6) : [cap] "n"(SPIN_CAP), [gap] "n"(PGAP), [vk] "v"((u32)(lane * 6144)), [kb] "s"(ws + WS_PK + fb * 128), [tag] "v"(tag), [z] "v"(0ull), [ln] "v"(lane),
            [msk] "s"(~1023u), [lid] "v"(64u | lane), [rb] "v"(2047 - ((lane >> 1) * 80 + (lane & 1) * 48)),
            [shr] "s"((u32)((1.0 - PTHR) * 4294967296.0)), [pa] "s"(POST_OFF), [at] "v"((u32)(KEY_OFF - 256 + lane * 4))
          : "memory", "vcc", "scc", "s40", "s41", "s42", "s43", "s44", "s45", "s46", "s47", "s48", "s49", "s50", "s51", "s52", "s54", "s55",
            "v4", "v5", "v7", "v8", "v9", "v10", "v11", "v12", "v13", "v14", "v15");
      if (fb == 47 && lane < 8) GST4(l16, ws + WS_EPOCH, 0, ((u32x4){tg[0], tg[1], tg[0], tg[1]}));
      const u32 now = (u32)__builtin_amdgcn_s_memrealtime();
      const u32 nag = (pk6 - (now >> 1) - 1u) & 1023u;
      u32 kgo = __builtin_amdgcn_readfirstlane(now + KCD - 2 * (1023u - wave_max(lane < 51 ? nag : 0u)));
      u64 old = __ballot(lane < 51 && nag < 1023u - (KOLD - 8) / 2);
      asm volatile("" : "+s"(kgo), "+s"(old));
      asm volatile(
          "s_mov_b32 s41, %[cap]\n\ts_mov_b32 s54, -1\n\ts_mov_b32 s55, 0x7ffff\n\tv_mov_b32 v12, 0\n\t"
          "s_mov_b64 exec, %[old]\n\ts_cbranch_execz 3f\n\tglobal_load_dwordx2 v[14:15], %[vk], %[kb] sc1\n"
          "3:\n\ts_setprio 3\n\ts_barrier\n"
          "4:\n\ts_memrealtime s[42:43]\n\ts_waitcnt lgkmcnt(0)\n\ts_sub_u32 s42, s42, %[go]\n\ts_cmp_lt_i32 s42, 0\n\ts_cbranch_scc1 4b\n\t"
          "s_andn2_b64 exec, s[54:55], %[old]\n\ts_cbranch_execz 5f\n\tglobal_load_dwordx2 v[14:15], %[vk], %[kb] sc1\n"
          "5:\n\ts_mov_b64 exec, s[54:55]\n\tds_read_b32 v4, %[ctl] offset:92\n\ts_waitcnt lgkmcnt(0)\n\tv_and_b32 v4, 3, v4\n\t"
          "v_lshl_add_u32 v4, v4, 4, %[pa]\n\tds_read_b64 v[6:7], v4\n\ts_waitcnt lgkmcnt(0)\n\t"
          "v_readfirstlane_b32 s48, v6\n\tv_readfirstlane_b32 s49, v7\n\tv_mov_b32 v6, 1\n\ts_waitcnt vmcnt(0)\n\tv_cmp_ne_u32 s[54:55], v15, %[tag]\n\tv_mov_b32 v12, v14\n\ts_mov_b64 exec, -1\n\t"
          "s_cmp_eq_u64 s[54:55], 0\n\ts_cbranch_scc1 9f\n\t"
          PKLD("v[8:9]") "s_sleep %[gap]\n\t" PKLD("v[10:11]")
#define KSTEP(L, D0, D1, DR)                                                                        \
          L "0:\n\t"                                                                               \
          "s_waitcnt vmcnt(1)\n\t"                                                                 \
          "v_cmp_eq_u32 s[42:43], " D1 ", %[tag]\n\t"                                              \
          "s_and_b64 exec, s[42:43], s[54:55]\n\tv_mov_b32 v12, " D0 "\n\ts_mov_b64 exec, -1\n\t"   \
          "s_andn2_b64 s[54:55], s[54:55], s[42:43]\n\ts_cbranch_scc0 9f\n\t"                     \
          PKLD(DR)
          KSTEP("1", "v8", "v9", "v[8:9]")
          KSTEP("2", "v10", "v11", "v[10:11]")
          "s_sub_u32 s41, s41, 1\n\ts_cbranch_scc0 10b\n"
          "9:\n\t"
          "v_readlane_b32 s40, v12, s49\n\tds_write_b32 %[ak], v12\n\tv_mov_b32 v13, v12\n\t"
          "s_and_b32 s45, s40, 0x7ff\n\tv_cmp_lt_u32 vcc, s40, v12\n\ts_cmp_eq_u32 s45, s48\n\ts_cselect_b64 vcc, vcc, -1\n\ts_cbranch_vccz 8f\n\t"
          MDPP("quad_perm:[1,0,3,2]") MDPP("quad_perm:[2,3,0,1]") MDPP("row_half_mirror") MDPP("row_mirror")
          "s_nop 0\n\tv_readlane_b32 s44, v13, 0\n\tv_readlane_b32 s45, v13, 16\n\tv_readlane_b32 s46, v13, 32\n\tv_readlane_b32 s47, v13, 48\n\t"
          "s_max_u32 s44, s44, s45\n\ts_max_u32 s46, s46, s47\n\ts_max_u32 s40, s44, s46\n\t"
          "v_cmp_eq_u32 vcc, s40, v12\n\tv_mov_b32 v6, 2\n\ts_ff1_i32_b64 s44, vcc\n\tv_mov_b32 v7, s44\n"
          "8:\n\tv_mov_b32 v4, s40\n\ts_mov_b64 exec, 1\n\tds_write_b128 %[ctl], v[4:7]\n\ts_mov_b64 exec, -1\n\t"
          "s_waitcnt vmcnt(0) lgkmcnt(0)"
          :: [cap] "n"(SPIN_CAP), [gap] "n"(KGAP), [go] "s"(kgo), [old] "s"(old), [vk] "v"((u32)(lane * 6144)), [kb] "s"(ws + WS_PK + fb * 128 + 8), [tag] "v"(tag),
            [ctl] "v"((u32)(CTL_OFF + 32)), [ak] "v"((u32)(KEY_OFF + lane * 4)), [pa] "s"(POST_OFF), [z] "v"(0ull)
          : "memory", "vcc", "scc", "s40", "s41", "s42", "s43", "s44", "s45", "s46", "s47", "s48", "s49", "s54", "s55", "v4", "v5", "v6", "v7", "v8", "v9", "v10",
            "v11", "v12", "v13", "v14", "v15");
      return;
    }
    float swr = dot2b(rb5, 0x3F803F80u, 0.f);
#pragma unroll
    for (int e = 0; e < 4; ++e) swr = dot2b(ra[e], 0x3F803F80u, swr);
    swr = wave_sum(swr);
    u32 ah16 = HN_OFF + (w - 1) * HSL + l16, ah4 = HN_OFF + (w - 1) * HSL + 1024 + lane * 4, l4 = lane * 4;
    asm volatile("" : "+v"(swr), "+v"(ah16), "+v"(ah4), "+v"(l4));
#define DOT3(H, W) "v_dot2c_f32_bf16 v102, 0x3f803f80, " H "\n\tv_dot2c_f32_bf16 v103, " H ", " H "\n\tv_dot2c_f32_bf16 v104, " H ", " W "\n\t"
#define ADPP(C) "s_nop 1\n\tv_add_f32_dpp v102, v102, v102 " C " row_mask:0xf bank_mask:0xf bound_ctrl:1\n\t"
#define XDPP(C) "s_nop 1\n\tv_max_u32_dpp v102, v102, v102 " C " row_mask:0xf bank_mask:0xf bound_ctrl:1\n\t"
    const char* rp = (const char*)w_r;
    u32 second = 0;
#pragma nounroll
    for (;;) {
      asm volatile(
          "s_mov_b32 s47, %[cap]\n\ts_mov_b32 s43, -1\n\ts_sub_u32 s46, %[sec], 1\n"
          "7:\n\ts_mov_b64 s[48:49], %[rp]\n\ts_mov_b32 s45, 0\n\ts_branch 4f\n"
          "18:\n\ts_sleep 1\n"
          "1:\n\ts_sub_u32 s47, s47, 1\n\ts_cbranch_scc1 8f\n\ts_cmp_lg_u32 %[sec], 0\n\ts_cbranch_scc1 7b\n\t"
          "ds_read_b128 v[108:111], %[ap]\n\ts_memrealtime s[40:41]\n\ts_waitcnt lgkmcnt(0)\n\t"
          "v_readfirstlane_b32 s45, v111\n\tv_readfirstlane_b32 s46, v109\n\tv_readfirstlane_b32 s50, v110\n\ts_lshr_b32 s41, s40, 1\n\t"
          "s_cmp_eq_u32 s45, 6\n\ts_cbranch_scc1 8f\n\ts_bitcmp0_b32 s45, 0\n\ts_cbranch_scc1 18b\n\t"
          "s_cmp_eq_u32 s46, s42\n\ts_cbranch_scc1 6f\n\ts_cmp_eq_u32 s46, s43\n\ts_cbranch_scc1 2f\n\t"
          "s_sub_u32 s41, s41, s50\n\ts_and_b32 s41, s41, 0x3ff\n\ts_cmp_lt_u32 s41, %[tq]\n\ts_cbranch_scc1 18b\n\t"
          "s_mov_b32 s43, s46\n\ts_add_u32 s44, s50, 8\n\ts_cmp_ge_u32 s41, %[pc] - 20\n\ts_cbranch_scc1 1b\n\t"
          "s_mul_i32 s48, s46, 6144\n\tv_mov_b64 v[104:105], 0\n\tv_mov_b32 v101, s48\n\tv_mad_u32_u24 v101, %[l16], 48, v101\n\t"
          "s_mov_b64 exec, 0xff\n\tglobal_atomic_or_x2 v[102:103], v101, v[104:105], %[pk] sc0 sc1\n\ts_sub_u32 s50, s50, 512\n\ts_waitcnt vmcnt(0)\n\t"
          "v_cmp_eq_u32 vcc, v103, %[tag]\n\tv_subrev_u32 v102, s50, v102\n\tv_and_b32 v102, 0x3ff, v102\n\tv_cndmask_b32 v102, 0, v102, vcc\n\t"
          XDPP("quad_perm:[1,0,3,2]") XDPP("quad_perm:[2,3,0,1]") XDPP("row_half_mirror")
          "s_mov_b64 exec, -1\n\tv_readfirstlane_b32 s44, v102\n\ts_min_u32 s44, s44, 562\n\ts_add_u32 s44, s44, s50\n\ts_branch 1b\n"
          "2:\n\ts_sub_u32 s41, s41, s44\n\ts_and_b32 s41, s41, 0x3ff\n\ts_cmp_lt_u32 s41, %[pc] - 8\n\ts_cbranch_scc1 1b\n\t"
          "s_sub_u32 s41, %[pc], s41\n\ts_lshl1_add_u32 s51, s41, s40\n\ts_mul_i32 s48, s46, 1280\n\ts_add_u32 s48, s48, %[lo]\n\ts_addc_u32 s49, %[hi], 0\n\t"
          "3:\n\ts_memrealtime s[40:41]\n\ts_waitcnt lgkmcnt(0)\n\ts_sub_u32 s40, s40, s51\n\ts_cmp_lt_i32 s40, 0\n\ts_cbranch_scc1 3b\n"
          "4:\n\tglobal_load_dwordx4 v[96:99], %[l16], s[48:49] sc1\n\tglobal_load_dword v100, %[l4], s[48:49] offset:1024 sc1\n\t"
          "v_mov_b64 v[102:103], 0\n\tv_mov_b32 v104, 0\n\ts_mov_b32 s42, -2\n\ts_waitcnt vmcnt(0)\n\t"
          "ds_read_b128 v[108:111], %[ap]\n\tds_read_b32 v105, %[ab] offset:4\n\tds_write_b128 %[a16], v[96:99]\n\tds_write_b32 %[a4], v100\n\t"
          "v_max3_u32 v101, v96, v97, v98\n\tv_max3_u32 v101, v101, v99, v100\n\tv_cmp_eq_u32 vcc, -1, v101\n\t"
          DOT3("v96", "v92") DOT3("v97", "v93")
          "s_cbranch_vccnz 1b\n\ts_mov_b32 s42, s46\n\ts_waitcnt lgkmcnt(2)\n\tv_lshl_or_b32 v109, v111, 8, v109\n\tv_lshl_or_b32 v109, v105, 16, v109\n\t"
          "s_or_b32 s45, s46, 0x20500\n\tv_cmp_eq_u32 vcc, s45, v109\n\ts_cbranch_vccz 5f\n\ts_waitcnt lgkmcnt(0)\n\ts_barrier\n"
          "5:\n\t" DOT3("v98", "v94") DOT3("v99", "v95") DOT3("v100", "%[rb]")
          "s_nop 2\n\tv_mov_b32 v105, v104\n\tv_permlane32_swap_b32 v102, v103\n\tv_add_f32 v102, v102, v103\n\tv_permlane32_swap_b32 v104, v105\n\t"
          "v_add_f32 v104, v104, v105\n\ts_nop 1\n\tv_permlane16_swap_b32 v102, v104\n\tv_add_f32 v102, v102, v104\n\t"
          ADPP("quad_perm:[1,0,3,2]") ADPP("quad_perm:[2,3,0,1]") ADPP("row_half_mirror") ADPP("row_mirror")
          "v_mul_f32 v103, 0x3acccccd, v102\n\tv_readlane_b32 s41, v102, 16\n\tv_readlane_b32 s40, v103, 32\n\t"
          "s_nop 1\n\tv_fma_f32 v102, -v103, v103, s40\n\tv_max_f32 v102, 0, v102\n\tv_cvt_pk_bf16_f32 v102, 0, v102\n\t"
          "v_add_f32 v102, 0x3727c5ac, v102\n\tv_cvt_pk_bf16_f32 v102, 0, v102\n\tv_sqrt_f32 v102, v102\n\tv_cvt_pk_bf16_f32 v105, 0, v103\n\t"
          "v_fma_f32 v105, -%[swr], v105, s41\n\tv_cvt_pk_bf16_f32 v106, v103, v102\n\tv_and_b32 v102, 0xffff0000, v106\n\tv_rcp_f32 v102, v102\n\t"
          "v_mov_b32 v104, 1\n\tv_mul_f32 v102, v102, v105\n\tv_ashrrev_i32 v103, 31, v102\n\tv_or_b32 v103, 0x80000000, v103\n\t"
          "v_xor_b32 v102, v102, v103\n\tv_and_or_b32 v107, v102, -4, %[sl]\n\ts_cbranch_vccz 1b\n\t"
          "s_mov_b64 exec, 1\n\tds_max_u64 %[ab], v[106:107] offset:8\n\tds_add_u32 %[ab], v104 offset:4\n\ts_branch 9f\n"
          "6:\n\ts_bitcmp0_b32 s45, 2\n\ts_cbranch_scc1 1b\n\ts_mov_b64 exec, 1\n\tds_max_u64 %[ab], v[106:107] offset:8\n"
          "8:\n\tv_mov_b32 v104, 1\n\ts_mov_b64 exec, 1\n\tds_add_u32 %[ab], v104 offset:4\n\ts_barrier\n"
          "9:\n\ts_mov_b64 exec, -1\n\ts_waitcnt lgkmcnt(0)\n\ts_barrier"
          :
          : [cap] "n"(SPIN_CAP * 8), [rp] "s"(rp), [sec] "s"(second), [ap] "v"((u32)(POST_OFF + (w - 1) * 16)), [tq] "n"(TQD / 2), [pc] "n"(PCD / 2),
            [pk] "s"(ws + WS_PK + (bidx - 26) * 128), [tag] "v"(tag), [lo] "s"((u32)(u64)(ws + WS_PC)), [hi] "s"((u32)((u64)(ws + WS_PC) >> 32)),
            [l16] "v"(l16), [l4] "v"(l4), [a16] "v"(ah16), [a4] "v"(ah4), "{v[92:95]}"(ra), [rb] "v"(rb5), [swr] "v"(swr), [sl] "s"(w - 1),
            [ab] "v"((u32)(CTL_OFF + 112))
          : "memory", "vcc", "scc", "s40", "s41", "s42", "s43", "s44", "s45", "s46", "s47", "s48", "s49", "s50", "s51", "v96", "v97", "v98", "v99",
            "v100", "v101", "v102", "v103", "v104", "v105", "v106", "v107", "v108", "v109", "v110", "v111");
      if (w != 1 || second) break;
      u32x4 wst;
#pragma nounroll
      for (int it = 0; it < SPIN_CAP * 8; ++it) {
        wst = lds_ld128(CTL_OFF + 32);
        if (__builtin_amdgcn_readfirstlane(wst[2])) break;
        __builtin_amdgcn_s_sleep(1);
      }
      if (__builtin_amdgcn_readfirstlane(wst[2]) != 2u) break;
      if (lane < 2) *(u32x4*)(smem + (lane ? POST_OFF : CTL_OFF + 112)) = lane ? (u32x4){0u, 0u, 0u, 5u} : (u32x4){0u, 2u, 0x3F800000u, 0u};
      rp = ws + WS_CAND + __builtin_amdgcn_readfirstlane(wst[3]) * 2560;
      second = 1;
    }
    if (w == 1) {
    } else if (w == 3 || fb >= 40) {
      const int c = w == 3 ? fb : fb + 8;
      if (c < 51) {
        const u32 now = (u32)__builtin_amdgcn_s_memrealtime();
        const u32 age = ((now >> 1) - __builtin_amdgcn_readfirstlane(*(const u32*)(smem + KEY_OFF - 256 + c * 4))) & 1023u;
        asm volatile("s_mov_b32 s42, %[cap]\n"
                     "3:\n\ts_memrealtime s[40:41]\n\ts_waitcnt lgkmcnt(0)\n\ts_sub_u32 s40, s40, %[go]\n\ts_cmp_lt_i32 s40, 0\n\ts_cbranch_scc1 3b\n"
                     "4:\n\tglobal_load_dwordx4 v[96:99], %[l16], %[cp] sc1\n\tglobal_load_dword v103, %[l4], %[cp] offset:1024 sc1\n\t"
                     "s_sub_u32 s42, s42, 8\n\ts_waitcnt vmcnt(0)\n\ts_cbranch_scc1 2f\n\t"
                     "v_max3_u32 v101, v96, v98, v103\n\tv_cmp_eq_u32 vcc, -1, v101\n\ts_cbranch_vccnz 4b\n"
                     "1:\n\tds_read_b64 v[100:101], %[af]\n\tds_read_b32 v102, %[ak]\n\ts_sub_u32 s42, s42, 1\n\ts_cbranch_scc1 2f\n\ts_waitcnt lgkmcnt(0)\n\t"
                     "v_cmp_eq_u32 vcc, 0, v100\n\ts_cbranch_vccnz 1b\n\t"
                     "v_cmp_eq_u32 vcc, %[c], v101\n\tv_bfi_b32 v102, v102, 0, %[m]\n\ts_cbranch_vccnz 2f\n\t"
                     "v_mad_u32_u24 v100, v102, %[rs], %[l16]\n\tv_mad_u32_u24 v101, v102, %[rs], %[l4]\n\t"
                     "global_store_dwordx4 v100, v[96:99], %[out]" "\n\tglobal_store_dword v101, v103, %[out] offset:1024" "\n"
                     "2:\n\ts_waitcnt lgkmcnt(0)"
                     ::[cap] "n"(SPIN_CAP * 8), [go] "s"(now + CCD - 2 * age), [cp] "s"(ws + WS_CAND + c * 2560 + 1280), [af] "v"((u32)(CTL_OFF + 40)),
                       [ak] "v"((u32)(KEY_OFF + c * 4)), [c] "s"(c), [m] "s"(2047), [rs] "s"(1280), [l16] "v"(l16), [l4] "v"(l4), [out] "s"(out)
                     : "memory", "vcc", "scc", "s40", "s41", "s42", "v96", "v97", "v98", "v99", "v100", "v101", "v102", "v103");
      }
    } else {
      const char* pp = ws + WS_PART + fb * 64;
      int pass = 0;
      asm volatile("" : "+s"(pass));
#pragma nounroll
      for (; pass < 2; ++pass) {
        const u32x2 best = *(const u32x2*)(smem + CTL_OFF + 120);
        const u32 slot = best[1] & 3u;
        const u32 wkey = __builtin_amdgcn_readfirstlane(*(const u32*)(smem + (pass ? CTL_OFF + 32 : POST_OFF + slot * 16)));
        const float rr = rbf(__uint_as_float((best[1] & ~3u) ^ ((u32)((int)~best[1] >> 31) | 0x80000000u)));
        float rs = rbf(__builtin_amdgcn_rcpf(1.0f + __expf(-rr)));
        const int ccol = (g & 1) * 2 + (g >> 1);
        float hv = bf2f(*(const u16*)(smem + HN_OFF + slot * HSL + fb * 32 + p4 * 8 + ccol * 2));
        hv = __builtin_fmaf(hv - lo_f(best[0]), __builtin_amdgcn_rcpf(hi_f(best[0])), hv);
        const char* op_ = (const char*)out + (size_t)(2047 - (int)(wkey & 2047u)) * 1280 + fb * 32;
        u32 vo_ = p4 * 8 + ccol * 2;
        asm volatile("" : "+v"(vo_), "+v"(hv), "+v"(rs));
        asm volatile("" : "+s"(op_));
        const u32 vp = (lane >> 2) * 2560 + p4 * 16;
        typedef __attribute__((ext_vector_type(16))) unsigned int u32x16;
        u32x16 keepa, keepb;
        f32x4 ys;
        u32 fin;
        asm volatile(
#define PLD(B0, B1, B2)                                                                             \
            "global_load_dwordx4 " B0 ", %[vp], %[p0] sc1\n\t"                                       \
            "global_load_dwordx4 " B1 ", %[vp], %[p1] sc1\n\tglobal_load_dwordx4 " B2 ", %[vp], %[p2] sc1\n\t"
#define PCHK(A0, A1, B0, B1, C0, C1)                                                                \
            "s_waitcnt vmcnt(3)\n\t"                                                                 \
            "v_pk_add_f32 v[96:97], " A0 ", " B0 "\n\tv_pk_add_f32 v[98:99], " A1 ", " B1 "\n\t"    \
            "v_pk_add_f32 v[96:97], v[96:97], " C0 "\n\tv_pk_add_f32 v[98:99], v[98:99], " C1 "\n\t" \
            "v_pk_add_f32 v[102:103], v[96:97], v[98:99]\n\t"                                        \
            "v_cmp_u_f32 vcc, v102, v103\n\ts_cbranch_vccz 9f\n\t"
            "s_mov_b32 s42, %[cap]\n\ts_sleep %[cdel]\n\t"
            PLD("v[64:67]", "v[68:71]", "v[72:75]")
            "s_sleep %[gap]\n\t"
            PLD("v[80:83]", "v[84:87]", "v[88:91]")
            "1:\n\t"
            PCHK("v[64:65]", "v[66:67]", "v[68:69]", "v[70:71]", "v[72:73]", "v[74:75]")
            PLD("v[64:67]", "v[68:71]", "v[72:75]")
            PCHK("v[80:81]", "v[82:83]", "v[84:85]", "v[86:87]", "v[88:89]", "v[90:91]")
            PLD("v[80:83]", "v[84:87]", "v[88:91]")
            "s_sub_u32 s42, s42, 1\n\ts_cbranch_scc0 1b\n"
            "9:\n\tds_read_b32 %[fin], %[af]"
            : "=&{v[96:99]}"(ys), "=&{v[64:79]}"(keepa), "=&{v[80:95]}"(keepb), [fin] "=&v"(fin)
            : [cap] "n"(SPIN_CAP), [gap] "n"(CGAP), [cdel] "n"(CDEL), [vp] "v"(vp), [p0] "s"(pp),
              [p1] "s"(pp + 40960), [p2] "s"(pp + 81920), [af] "v"((u32)(CTL_OFF + 40))
            : "memory", "vcc", "scc", "s42", "v102", "v103");
        float y = tr_sum(ys);
        y += dppf<0x124>(y);
        y += dppf<0x128>(y);
        const u32 res = cvt2((f32x2){__builtin_fmaf(y, rs, hv), 0.f});
        asm volatile("s_mov_b32 s42, %[cap]\n"
                     "1:\n\ts_waitcnt lgkmcnt(0)\n\tv_cmp_eq_u32 vcc, %[ok], %[fin]\n\ts_cbranch_vccz 2f\n\t"
                     "s_mov_b32 exec_lo, 0x000f000f\n\ts_mov_b32 exec_hi, 0x000f000f\n\tglobal_store_short %[vo], %[res], %[op]" "\n\ts_mov_b64 exec, -1\n"
                     "2:\n\tv_cmp_eq_u32 vcc, 0, %[fin]\n\ts_cbranch_vccz 3f\n\tds_read_b32 %[fin], %[af]\n\ts_sub_u32 s42, s42, 1\n\ts_cbranch_scc0 1b\n"
                     "3:\n\ts_waitcnt lgkmcnt(0)\n\tv_cmp_lt_u32 vcc, %[ok], %[fin]\n\ts_cbranch_vccz 4f\n\ts_waitcnt vmcnt(0)\n4:"
                     : [fin] "+v"(fin) : [cap] "n"(SPIN_CAP * 8), [ok] "s"(pass + 1), [vo] "v"(vo_), [res] "v"(res), [op] "s"(op_),
                       [af] "v"((u32)(CTL_OFF + 40)), "{v[64:79]}"(keepa), "{v[80:95]}"(keepb) : "memory", "vcc", "scc", "s42");
        if (pass || __builtin_amdgcn_readfirstlane(fin) != 2u) break;
        asm volatile("s_barrier\n\ts_barrier" ::: "memory");
        pp += WS_PART2 - WS_PART;
      }
    }
    return;
  }

  const int idx2 = (xcd & 1) * 26 + bidx;
  const int hg = ((xcd >> 1) & 1) * 4 + (idx2 & 3);
  const int mt = (xcd >> 2) * 13 + (idx2 >> 2);
#define ROTF (((idx2 >> 2) * 3 / 2 + (idx2 & 3) * 3) % 10)
  const int kb0 = ROTF;
  const int r0 = mt * 80;
  if (w >= 4) {
    const int cw = w - 4;
    u32 vo = w_off(cw, lane, hg), vx = x_off(cw, lane, r0), vx2 = x_off(8 + (cw & 3), lane, r0);
    asm volatile("" : "+v"(vo), "+v"(vx), "+v"(vx2));
    __builtin_amdgcn_sched_barrier(0);
    const char* wb = (const char*)wqkv + (size_t)kb0 * 245760 + W_ROW(cw);
    dma_m0<0>(vo, wb, cw * 1024);
    dma_m0<0>(vx, (const char*)x + kb0 * 128, XRING + cw * 1024);
    dma_m0<0>(vo, wb + 61440, 8192 + cw * 1024);
    if (cw < 4) dma_m0<0>(vx2, (const char*)x + kb0 * 128, XRING + 8192 + cw * 1024);
  }
  const int rt = w >= 4 ? (w & 3) : 4, kh = w >= 4 ? (w - 4) >> 2 : (w & 1);
  float bias[2];
  u32 sel[4], e4, eh, aq, aka, akb, pm, pf, hi, arec, vpp;
  const char *os, *os4, *sst, *oa[10];
  int so_[10];
  int lane_ = lane, mt_ = mt, hg_ = hg;
  auto prepv = [&](const int lane) __attribute__((always_inline)) {
    const int g = lane >> 4, r16 = lane & 15, q4 = (lane & 15) >> 2, p4 = lane & 3;
    const int par = lane & 1;
    const bool ca = r16 >= 4 * g, cb = r16 >= 4 * g + 2;
    bias[0] = (ca || r0 + rt > 0) ? 0.f : -__builtin_inff();
    bias[1] = (cb || r0 + rt > 0) ? 0.f : -__builtin_inff();
    const u32 sa = par ? 0x01000C0Cu : 0x0C0C0100u, sb = par ? 0x03020C0Cu : 0x0C0C0302u;
    sel[0] = ca ? 0x0C0C0C0Cu : sa;
    sel[1] = cb ? 0x0C0C0C0Cu : sb;
    sel[2] = ca ? sa : 0x0C0C0C0Cu;
    sel[3] = cb ? sb : 0x0C0C0C0Cu;
    e4 = EXO + rt * 512 + lane * 8;
    eh = e4 + kh * 6144;
    aq = TAB_OFF + 384 + (rt * 16 + r16) * 4;
    aka = TAB_OFF + 384 + (rt * 16 + 4 * g + par + (ca ? 16 : 0)) * 4;
    akb = TAB_OFF + 384 + (rt * 16 + 4 * g + par + 2 + (cb ? 16 : 0)) * 4;
    pm = (4 * g + q4) * 160 + p4 * 40;
    pf = (20 * g + 16 + q4) * 160 + p4 * 40;
    hi = HIM + (rt * 16 + r16) * 160 + g * 40;
    arec = REC_OFF + (r16 == 0 ? kh * 192 + g * 48 : (r16 == 8 ? ((g >> 1) == kh ? 480 : 384) : 432));
    vpp = lane + kh * 64 + (rt == 0 ? 768 : (rt == 2 ? 8192 : (rt == 4 ? 512 : 640)));
    asm volatile("" : "+v"(arec), "+v"(vpp));
    asm volatile("" : "+v"(bias[0]), "+v"(bias[1]), "+v"(sel[0]), "+v"(sel[1]), "+v"(sel[2]), "+v"(sel[3]), "+v"(e4), "+v"(eh),
                 "+v"(aq), "+v"(aka), "+v"(akb), "+v"(pm), "+v"(pf), "+v"(hi));
  };
  auto preps = [&](const int mt, const int hg) __attribute__((always_inline)) {
    const char* ob = ws + WS_O + (size_t)mt * 102400;
    os = ob + rt * 20480 + (2 * hg + kh) * 1024;
    os4 = ob + rt * 20480 + (16 + (hg >> 1)) * 1024 + (hg & 1) * 8;
    sst = ws + WS_STAT + (size_t)(2 * mt + (rt < 3 ? 0 : 1)) * 8192 + hg * 1024 + (rt < 3 ? rt : rt - 3) * 256;
    asm volatile("" : "+s"(os), "+s"(os4), "+s"(sst));
    const int k2 = w & 1;
#pragma unroll
    for (int j = 0; j < 10; ++j) {
      const bool oth = j >= 5 || w < 2;
      const int mo[8] = {OORD};
      const int ch = j % 5 < 4 ? (2 * hg + k2) ^ (oth ? 2 * mo[4 + j % 5] : 2 * mo[j % 5]) : (hg >> 1) ^ ((oth != ((hg & 1) != 0)) ? (k2 ? 0 : 3) : 1 + k2);
      oa[j] = ob + (j % 5 < 4 ? ch : 16 + ch) * 1024;
      so_[j] = ch * (j % 5 < 4 ? 6400 : 25600);
      asm volatile("" : "+s"(oa[j]), "+s"(so_[j]));
    }
  };
#define WO_OFFS                                                                                    \
  u32 woff[5];                                                                                     \
  _Pragma("unroll") for (int i = 0; i < 5; ++i) {                                                  \
    const int c = i * 64 + lane;                                                                   \
    const int row = c / 10;                                                                        \
    woff[i] = row * 1120 + hg * 160 + c * 16 + (4 - i) * 1024;                                     \
  }
#define WO_RUN(RHO)                                                                                \
  {                                                                                                \
    const char* wq_ = (const char*)wo + (RHO) * 40960; \
    dma_m0<-4096>(woff[0], wq_, (RHO) * 5120 + 4096);                                               \
    dma_nx<-3072>(woff[1], wq_); dma_nx<-2048>(woff[2], wq_); dma_nx<-1024>(woff[3], wq_); dma_nx<0>(woff[4], wq_); \
  }

#define BAR asm volatile("s_barrier" ::: "memory");
#define PREP_V __builtin_amdgcn_sched_barrier(0); asm volatile("" : "+v"(lane_)); prepv(lane_); __builtin_amdgcn_sched_barrier(0);
#define PREP_S __builtin_amdgcn_sched_barrier(0); asm volatile("" : "+s"(mt_), "+s"(hg_)); preps(mt_, hg_); __builtin_amdgcn_sched_barrier(0);
  if (w < 2) {
    const int l = w;
    __builtin_amdgcn_s_setprio(3);
    u32 vw[4];
#pragma unroll
    for (int i = 0; i < 4; ++i) vw[i] = w_off(i, lane, hg) + 4096;
    u32 go = (u32)kb0 * 245760u + 122880u + l * 61440u;
    u32 so = HSLOT + l * 8192 + 4096;
#define W1(I) dma_nx<((I) - 4) * 1024>(vw[(I) & 3], wb + (W_ROW(I) - (I) * 1024));
#define ISSUE_W                                                                                    \
  {                                                                                                \
    const char* wb = (const char*)wqkv + go;                                                       \
    dma_m0<-4096>(vw[0], wb, so);                                                                  \
    W1(1) W1(2) W1(3) W1(4) W1(5) W1(6) W1(7)                                                      \
    go += 122880u;                                                                                 \
    if (go >= 2457600u) go -= 2457600u;                                                            \
    so = so >= 5 * HSLOT ? so - 5 * HSLOT : so + HSLOT;                                            \
  }
    __builtin_amdgcn_s_sleep(2);
    ISSUE_W
    __builtin_amdgcn_sched_barrier(0);
    WO_OFFS
    asm volatile("" : "+v"(woff[0]), "+v"(woff[1]), "+v"(woff[2]), "+v"(woff[3]), "+v"(woff[4]));
    BAR
    ISSUE_W
    ISSUE_W
    asm volatile("s_waitcnt vmcnt(16)\n\ts_barrier" ::: "memory");
    asm volatile("s_waitcnt vmcnt(8)\n\ts_barrier" ::: "memory");
#pragma unroll 1
    for (int h = 1; h < 17; ++h) {
      ISSUE_W
      asm volatile("s_waitcnt vmcnt(8)\n\ts_barrier" ::: "memory");
    }
    asm volatile("s_waitcnt vmcnt(0)\n\ts_barrier" ::: "memory");
    WO_RUN(12 + l)
    const u32 tagw = lds_ld32(CTL_OFF);
    const char* bxf = ws + WS_BXF + mt * 1024;
    BAR
    const u32 town = (u32)__builtin_amdgcn_s_memrealtime();
    if (l == 0) {
      if (lane == 0) GST2((u32)(hg * 128), bxf, 0, ((u32x2){town, tagw}));
    }
    WO_RUN(18 + l)
    WO_RUN(8 + l)
    WO_RUN(l)
    const u32 vfl = (((lane & 7) == hg ? hg ^ 1 : (lane & 7))) * 128;
    u32x2 f;
    asm volatile("" : "=v"(f));
    BAR
    int gate = (int)((u32)__builtin_amdgcn_s_memrealtime() - town) + GADD;
    gate = gate > 200 ? 200 : gate;
    WO_RUN(2 + l)
    WO_RUN(4 + l)
    WO_RUN(6 + l)
    if (l == 1) asm volatile("global_load_dwordx2 %0, %1, %2 sc1" : "+v"(f) : "v"(vfl), "s"(bxf) : "memory");
    __builtin_amdgcn_s_setprio(0);
    pm = (4 * g + q4) * 160 + p4 * 40;
    pf = (20 * g + 16 + q4) * 160 + p4 * 40;
    asm volatile("" : "=v"(bias[0]), "=v"(bias[1]), "=v"(sel[0]), "=v"(sel[1]), "=v"(sel[2]), "=v"(sel[3]), "=v"(e4), "=v"(eh),
                 "=v"(aq), "=v"(aka), "=v"(akb), "+v"(pm), "+v"(pf), "=v"(hi), "=v"(arec), "=v"(vpp));
    PREP_S
    if (l == 1) {
      asm volatile("s_waitcnt vmcnt(0)" : "+v"(f)::"memory");
#pragma nounroll
      for (int it = 0; it < SPIN_CAP; ++it) {
        if (__all(f[1] == tagw)) break;
        asm volatile("global_load_dwordx2 %0, %1, %2 sc1\n\ts_waitcnt vmcnt(0)" : "=&v"(f) : "v"(vfl), "s"(bxf) : "memory");
      }
#pragma nounroll
      for (int it = 0; it < 64; ++it) {
        if (__all((int)((u32)__builtin_amdgcn_s_memrealtime() - f[0]) >= gate)) break;
      }
    }
    __builtin_amdgcn_s_setprio(0);
  } else if (w < 4) {
    __builtin_amdgcn_s_setprio(3);
    if (w == 2) {
      u32 vx[12];
#pragma unroll
      for (int i = 0; i < 12; ++i) vx[i] = x_off(i, lane, r0) + (4 - (i % 6)) * 1024;
      int kb = kb0 == 9 ? 0 : kb0 + 1;
      u32 xo = XRING + XSLOT + 4096;
#define ISSUE_X0                                                                                   \
  {                                                                                                \
    const char* xb = (const char*)x + kb * 128;                                                    \
    dma_m0<-4096>(vx[0], xb, xo);                                                                  \
    dma_nx<-3072>(vx[1], xb); dma_nx<-2048>(vx[2], xb); dma_nx<-1024>(vx[3], xb); dma_nx<0>(vx[4], xb); dma_nx<1024>(vx[5], xb); \
  }
#define ISSUE_X1                                                                                   \
  {                                                                                                \
    const char* xb = (const char*)x + kb * 128;                                                    \
    dma_m0<-4096>(vx[6], xb, xo + 6144);                                                           \
    dma_nx<-3072>(vx[7], xb); dma_nx<-2048>(vx[8], xb); dma_nx<-1024>(vx[9], xb); dma_nx<0>(vx[10], xb); dma_nx<1024>(vx[11], xb); \
    kb = kb == 9 ? 0 : kb + 1;                                                                     \
    xo = xo >= XRING + 3 * XSLOT ? xo - 3 * XSLOT : xo + XSLOT;                                    \
  }
      ISSUE_X0
      __builtin_amdgcn_sched_barrier(0);
      WO_OFFS
      asm volatile("" : "+v"(woff[0]), "+v"(woff[1]), "+v"(woff[2]), "+v"(woff[3]), "+v"(woff[4]));
      BAR
      ISSUE_X1
      ISSUE_X0
      BAR
      ISSUE_X1
      asm volatile("s_waitcnt vmcnt(12)\n\ts_barrier" ::: "memory");
#pragma unroll 1
      for (int j = 3; j < 10; ++j) {
        ISSUE_X0
        BAR
        ISSUE_X1
        asm volatile("s_waitcnt vmcnt(12)\n\ts_barrier" ::: "memory");
      }
      const char* xq = (const char*)x + (size_t)r0 * 1280;
      const int nv = mt == 25 ? 480 : 800;
#define RES1(I) if ((I) < 7 || (I) * 64 + lane < nv) dma_m0<(((I) % 5) - 4) * 1024>(woff[(I) % 5], xq + ((I) / 5) * 40960, HIM + ((I) / 5) * 5120 + 4096);
      PREP_V
      BAR
      PREP_S
      asm volatile("s_waitcnt vmcnt(0)\n\ts_barrier" ::: "memory");
      RES1(0) RES1(1) RES1(2) RES1(3) RES1(4) RES1(5) RES1(6)
      BAR
      RES1(7) RES1(8) RES1(9) RES1(10) RES1(11) RES1(12)
      if (lane < 10) dma_m0<0>(l16, (const char*)w_r + hg * 160, WR_OFF);
      BAR
    } else {
      u32x2 ep;
      u32x4 ra;
      u32 rb5;
      if (lane < 8) {
        *(u32x4*)(smem + ONES_OFF + lane * 16) = (u32x4){0x3F803F80u, 0x3F803F80u, 0x3F803F80u, 0x3F803F80u};
        *(u32x4*)(smem + CTL_OFF + lane * 16) = (u32x4){0u, 0u, 0u, 0u};
      }
      {
        const u32 o1 = lane == 26 || lane == 32 ? 0x3F803F80u : 0u;
        const u32 o2 = lane == 31 ? 0x3F803F80u : o1;
        if (lane < 33) *(u32x4*)(smem + REC_OFF + lane * 16) = (u32x4){o1, o1, o2, o2};
      }
      WO_OFFS
      asm volatile("" : "+v"(woff[0]), "+v"(woff[1]), "+v"(woff[2]), "+v"(woff[3]), "+v"(woff[4]));
      __builtin_amdgcn_s_sleep(TWS);
      if (lane < TWN)
        touch_line(lane == 4 ? (const char*)wo + hg * 160
                             : (lane >= 2 ? ws + WS_PK - 65536 + (lane - 2) * 524288 + bid * 128
                                          : (const char*)out + (size_t)(r0 + lane * 47) * 1280 + hg * 160));
      LDS_BARRIER();
      BAR BAR
      asm volatile("global_load_dwordx2 %0, %1, off sc1" : "=v"(ep) : "v"(ws + WS_EPOCH) : "memory");
      asm volatile("global_load_dwordx4 %0, %2, %4\n\tglobal_load_dword %1, %3, %4 offset:1024"
                   : "=&v"(ra), "=&v"(rb5) : "v"(l16), "v"((u32)(lane * 4)), "s"(w_r) : "memory");
      BAR BAR BAR
      PREP_V
      BAR
      PREP_S
      BAR
      asm volatile("s_waitcnt vmcnt(0)" : "+v"(ep), "+v"(ra), "+v"(rb5)::"memory");
      const u64 e1 = (((u64)ep[1] << 32) | ep[0]) + (ep[0] == SENT - 1u ? 2ull : 1ull);
      float swr = lo_f(rb5) + hi_f(rb5);
#pragma unroll
      for (int e = 0; e < 4; ++e) swr += lo_f(ra[e]) + hi_f(ra[e]);
      swr = wave_sum(swr);
      if (lane == 0) {
        *(u32x2*)(smem + CTL_OFF) = (u32x2){(u32)e1, (u32)(e1 >> 32)};
        *(float*)(smem + CTL_OFF + 40) = swr;
      }
      int ra0 = r0 - 16 + lane, ra1 = r0 + 48 + (lane & 31);
      ra0 = ra0 < 0 ? 0 : (ra0 > SEQ - 1 ? SEQ - 1 : ra0);
      ra1 = ra1 > SEQ - 1 ? SEQ - 1 : ra1;
      const int hd = lane / 5, pt = lane - hd * 5;
      u32 avp = REC_OFF + (hd >> 1) * 192 + (pt < 4 ? pt * 48 + (hd & 1) * 8 : hd * 48 + 16), vvp = lane < 20 ? (lane >> 1) * 128 + (lane & 1) * 16 : 0;
      u32 vf = (lane < 48 ? lane : 0) * 128, va0 = ra0 * 8, va1 = ra1 * 8, vt0 = TAB_OFF + lane * 4, vt1 = TAB_OFF + 256 + (lane & 31) * 4;
      asm volatile("" : "+v"(avp), "+v"(vvp), "+v"(vf), "+v"(va0), "+v"(va1), "+v"(vt0), "+v"(vt1));
#pragma nounroll
      for (int h = 7; h <= HK0; ++h) BAR
      u32x2 f, t0, t1;
      u32x4 vp4;
      GLD2(f, vf, ws + WS_HFLAG, 0);
      GLD2(t0, va0, ws + WS_LN1, 0);
      GLD2(t1, va1, ws + WS_LN1, 0);
      asm volatile("global_load_dwordx4 %0, %1, %2 sc1" : "=v"(vp4) : "v"(vvp), "s"(ws + WS_VP + hg * 1280) : "memory");
#pragma unroll
      for (int h = HK0 + 1; h <= 17; ++h) BAR
      WO_RUN(10)
      WO_RUN(11)
      BAR
      asm volatile("s_waitcnt vmcnt(10)" : "+v"(t0), "+v"(t1), "+v"(f), "+v"(vp4)::"memory");
#pragma nounroll
      for (int it = 0; it < SPIN_CAP; ++it) {
        if (__all(t0[1] == (u32)e1 && t1[1] == (u32)e1 && f[0] == (u32)e1 && f[1] == (u32)(e1 >> 32) && vp4[1] == (u32)e1 && vp4[3] == (u32)e1)) break;
        asm volatile("global_load_dwordx2 %2, %6, %9 sc1\n\tglobal_load_dwordx2 %0, %4, %8 sc1\n\tglobal_load_dwordx2 %1, %5, %8 sc1\n\t"
                     "global_load_dwordx4 %3, %7, %10 sc1\n\ts_waitcnt vmcnt(0)"
                     : "=&v"(t0), "=&v"(t1), "=&v"(f), "=&v"(vp4)
                     : "v"(va0), "v"(va1), "v"(vf), "v"(vvp), "s"(ws + WS_LN1), "s"(ws + WS_HFLAG), "s"(ws + WS_VP + hg * 1280) : "memory");
      }
      *(u32*)(smem + vt0) = t0[0] << 16;
      *(u32*)(smem + vt1) = t1[0] << 16;
      *(float*)(smem + vt0 + 384) = __builtin_amdgcn_rcpf(hi_f(t0[0]));
      *(float*)(smem + vt1 + 384) = __builtin_amdgcn_rcpf(hi_f(t1[0]));
      if (lane < 20) *(u32x2*)(smem + avp) = (u32x2){vp4[0], vp4[2]};
      asm volatile("" ::: "memory");
      WO_RUN(14)
      WO_RUN(15)
      LDS_BARRIER();
      WO_RUN(16)
      WO_RUN(17)
    }
    __builtin_amdgcn_s_setprio(0);
  } else {
    const int cw = w - 4;
    prepv(lane);
    preps(mt, hg);
    if (cw >= 4) __builtin_amdgcn_s_setprio(1);
    const int xa0 = XRING + r16 * 128 + ((g ^ ((r16 >> 1) & 7)) * 16);
    const u32 m1 = r16 == 0 ? 0u : 1u;
    const u32 xz0 = r16 == 0 ? (u32)ONES_OFF : (u32)xa0;
    int wa[2];
#pragma unroll
    for (int c = 0; c < 2; ++c) {
      const int ct = cw < 7 ? 2 * cw + c : 14;
      const int seg = ct / 5, tt = ct - seg * 5;
      const int G = tt < 4 ? 5 * tt + p4 : 5 * p4 + 4;
      wa[c] = (16 * (g >> 1) + 4 * (g & 1) + q4) * 512 + ((seg * 160 + G * 8) ^ ((4 * (g & 1) + q4) << 5));
    }
    auto run = [&](auto tyc) __attribute__((always_inline)) {
      constexpr int TY = decltype(tyc)::value;
      constexpr int NW = TY == 2 ? 1 : 2;
      f32x4 acc[2][6];
#pragma unroll
      for (int c = 0; c < 2; ++c)
#pragma unroll
        for (int r = 0; r < 6; ++r) acc[c][r] = (f32x4){0.f, 0.f, 0.f, 0.f};
      u32x4 xA[6], xB[6];
      u32x2 wA[4], wB[4];
#define RX(D, A, O) asm volatile("ds_read_b128 %0, %1 offset:%2" : "=v"(D) : "v"(A), "n"(O) : "memory");
#define RW(D, A, O) asm volatile("ds_read_b64_tr_b16 %0, %1 offset:%2" : "=v"(D) : "v"(A), "n"(O) : "memory");
#define MM(C, R, XF, WF)                                                                           \
  if ((C) < NW) {                                                                                    \
    const bf16x8 wf_ = mk8(WF[2 * (C)][0], WF[2 * (C)][1], WF[2 * (C) + 1][0], WF[2 * (C) + 1][1]); \
    const bf16x8 xf_ = __builtin_bit_cast(bf16x8, XF[R]);                                          \
    acc[C][R] = TY == 0 ? MFMA(wf_, xf_, acc[C][R]) : MFMA(xf_, wf_, acc[C][R]);                   \
    __builtin_amdgcn_sched_barrier(0);                                                             \
  }
#define READS(QX, QW)                                                                              \
  RW(QW[0], aw0, 0) RW(QW[1], aw0, 4096) RX(QX[1], ax, 2048)                                       \
  if (NW == 2) { RW(QW[2], aw1, 0) RW(QW[3], aw1, 4096) }                                          \
  RX(QX[2], ax, 4096) RX(QX[3], ax, 6144) RX(QX[4], ax, 8192) RX(QX[5], ax, 10240) RX(QX[0], az, 0)
#define ARRIVED(PX, PW)                                                                            \
  if (NW == 2)                                                                                     \
    asm volatile("s_waitcnt lgkmcnt(0)" : "+v"(PX[0]), "+v"(PX[1]), "+v"(PX[2]), "+v"(PX[3]), "+v"(PX[4]), "+v"(PX[5]), \
                 "+v"(PW[0]), "+v"(PW[1]), "+v"(PW[2]), "+v"(PW[3])::"memory");                    \
  else                                                                                             \
    asm volatile("s_waitcnt lgkmcnt(0)" : "+v"(PX[0]), "+v"(PX[1]), "+v"(PX[2]), "+v"(PX[3]), "+v"(PX[4]), "+v"(PX[5]), \
                 "+v"(PW[0]), "+v"(PW[1])::"memory");                                              \
  __builtin_amdgcn_sched_barrier(0);
#define SB __builtin_amdgcn_sched_barrier(0);
#define WT(N, ...) asm volatile("s_waitcnt lgkmcnt(" #N ")" : __VA_ARGS__::"memory"); SB
#define HSTAGE2(PX, PW, QX, QW, NX)                                                                \
  BAR                                                                                              \
  WT(7, "+v"(PW[0]), "+v"(PW[1]), "+v"(PX[1]))                                                     \
  MM(0, 1, PX, PW) RW(QW[0], aw0, 0)                                                               \
  WT(6, "+v"(PW[2]), "+v"(PW[3]))                                                                  \
  MM(1, 1, PX, PW) RW(QW[1], aw0, 4096)                                                            \
  WT(6, "+v"(PX[2]))                                                                               \
  MM(0, 2, PX, PW) RX(QX[1], ax, 2048) SB                                                          \
  MM(1, 2, PX, PW) RW(QW[2], aw1, 0)                                                               \
  WT(7, "+v"(PX[3]))                                                                               \
  MM(0, 3, PX, PW) RW(QW[3], aw1, 4096) SB                                                         \
  MM(1, 3, PX, PW) RX(QX[2], ax, 4096)                                                             \
  WT(6, "+v"(PX[4]), "+v"(PX[5]), "+v"(PX[0]))                                                     \
  MM(0, 4, PX, PW) RX(QX[3], ax, 6144) SB                                                          \
  MM(1, 4, PX, PW) RX(QX[4], ax, 8192) SB                                                          \
  MM(0, 5, PX, PW) RX(QX[5], ax, 10240) SB                                                         \
  MM(1, 5, PX, PW) RX(QX[0], az, 0) SB                                                             \
  MM(0, 0, PX, PW)                                                                                 \
  NX asm volatile("" : "+v"(aw0), "+v"(aw1), "+v"(ax), "+v"(az), "+s"(so), "+s"(xo)); SB           \
  MM(1, 0, PX, PW)
#define HSTAGE1(PX, PW, QX, QW, NX)                                                                \
  BAR                                                                                              \
  ARRIVED(PX, PW)                                                                                  \
  MM(0, 1, PX, PW) RW(QW[0], aw0, 0) RW(QW[1], aw0, 4096) SB                                       \
  MM(0, 2, PX, PW) RX(QX[1], ax, 2048) RX(QX[2], ax, 4096) SB                                      \
  MM(0, 3, PX, PW) RX(QX[3], ax, 6144) RX(QX[4], ax, 8192) SB                                      \
  MM(0, 4, PX, PW) RX(QX[5], ax, 10240) RX(QX[0], az, 0) SB                                        \
  MM(0, 5, PX, PW)                                                                                 \
  NX asm volatile("" : "+v"(aw0), "+v"(ax), "+v"(az), "+s"(so), "+s"(xo)); SB                      \
  MM(0, 0, PX, PW)
#define HSTAGE(...) if (NW == 2) { HSTAGE2(__VA_ARGS__) } else { HSTAGE1(__VA_ARGS__) }
      u32 aw0 = wa[0], aw1 = wa[1], ax = xa0, az = xz0;
      asm volatile("s_waitcnt vmcnt(0)\n\ts_barrier" ::: "memory");
      READS(xA, wA)
      aw0 += HSLOT; aw1 += HSLOT; ax ^= 64; az ^= 64;
      int so = 2 * HSLOT, xo = XSLOT;
#pragma unroll 1
      for (int s = 0; s < 9; ++s) {
        HSTAGE(xA, wA, xB, wB, aw0 = wa[0] + so; aw1 = wa[1] + so; ax = xa0 + xo; az = xz0 + xo * m1;
               so = so >= 4 * HSLOT ? 0 : so + 2 * HSLOT; xo = xo >= 3 * XSLOT ? 0 : xo + XSLOT;)
        HSTAGE(xB, wB, xA, wA, aw0 += HSLOT; aw1 += HSLOT; ax ^= 64; az ^= 64;)
      }
      {
        HSTAGE(xA, wA, xB, wB, )
        ARRIVED(xB, wB)
        BAR
        SB
      }
      char* ex = smem + EXO + cw * 6144 + lane * 8;
      if (TY == 0) {
        float st[6];
        const u32 ta = TAB_OFF + r16 * 4;
#define RT(R) asm volatile("ds_read_b32 %0, %1 offset:%2" : "=v"(st[R]) : "v"(ta), "n"(64 * (R)) : "memory");
        RT(1) RT(2) RT(3) RT(4) RT(5) RT(0)
        float cs[2][4];
#define FMS(A, M, CS) ((A) - (M) * (CS))
#define EP0(C, R)                                                                                  \
  {                                                                                                \
    const u32 a = cvt2((f32x2){FMS(acc[C][R][0], st[R], cs[C][0]), FMS(acc[C][R][1], st[R], cs[C][1])}); \
    const u32 b = cvt2((f32x2){FMS(acc[C][R][2], st[R], cs[C][2]), FMS(acc[C][R][3], st[R], cs[C][3])}); \
    *(u32x2*)(ex + (C) * 3072 + (R) * 512) = (u32x2){a, b};                                        \
    SB                                                                                             \
  }
        MM(0, 0, xB, wB) MM(1, 0, xB, wB) MM(0, 1, xB, wB) MM(1, 1, xB, wB)
#pragma unroll
        for (int i = 0; i < 4; ++i) cs[0][i] = dppf<0x150>(acc[0][0][i]);
        SB
        MM(0, 2, xB, wB)
#pragma unroll
        for (int i = 0; i < 4; ++i) cs[1][i] = dppf<0x150>(acc[1][0][i]);
        WT(0, "+v"(st[0]), "+v"(st[1]), "+v"(st[2]), "+v"(st[3]), "+v"(st[4]), "+v"(st[5]))
        MM(1, 2, xB, wB) EP0(0, 1)
        MM(0, 3, xB, wB) EP0(1, 1)
        MM(1, 3, xB, wB) EP0(0, 2)
        MM(0, 4, xB, wB) EP0(1, 2)
        MM(1, 4, xB, wB) EP0(0, 3)
        MM(0, 5, xB, wB) EP0(1, 3)
        MM(1, 5, xB, wB)
        if (cw >= 2) {
          EP0(1, 0)
          if (cw >= 3) EP0(0, 0)
        }
        EP0(0, 4) EP0(1, 4) EP0(0, 5) EP0(1, 5)
      } else {
        f32x4 mbv[6];
        const u32 ta = TAB_OFF + g * 16;
#define RV(R) RX(mbv[R], ta, 64 * (R))
        RV(1) RV(2) RV(3) RV(4) RV(5) RV(0)
        float cs[2];
#define CS1(C)                                                                                     \
  if ((C) < NW) {                                                                                  \
    const u32 v0 = __float_as_uint(acc[C][0][0]);                                                  \
    const u32 v1 = __builtin_amdgcn_permlane16_swap(v0, v0, false, false)[0];                      \
    cs[C] = __uint_as_float(__builtin_amdgcn_permlane32_swap(v1, v1, false, false)[0]);            \
    SB                                                                                             \
  }
#define EP1(C, R)                                                                                  \
  if ((C) < NW) {                                                                                  \
    const f32x4 v = (f32x4){FMS(acc[C][R][0], mbv[R][0], cs[C]), FMS(acc[C][R][1], mbv[R][1], cs[C]), FMS(acc[C][R][2], mbv[R][2], cs[C]), FMS(acc[C][R][3], mbv[R][3], cs[C])}; \
    *(u32x2*)(ex + (C) * 3072 + (R) * 512) = (u32x2){cvt2((f32x2){v[0], v[1]}), cvt2((f32x2){v[2], v[3]})}; \
    SB                                                                                             \
  }
        MM(0, 0, xB, wB) MM(1, 0, xB, wB) MM(0, 1, xB, wB) MM(1, 1, xB, wB)
        CS1(0)
        MM(0, 2, xB, wB)
        CS1(1)
        WT(0, "+v"(mbv[0]), "+v"(mbv[1]), "+v"(mbv[2]), "+v"(mbv[3]), "+v"(mbv[4]), "+v"(mbv[5]))
        MM(1, 2, xB, wB) EP1(0, 1)
        MM(0, 3, xB, wB) EP1(1, 1)
        MM(1, 3, xB, wB) EP1(0, 2)
        MM(0, 4, xB, wB) EP1(1, 2)
        MM(1, 4, xB, wB) EP1(0, 3)
        MM(0, 5, xB, wB) EP1(1, 3)
        MM(1, 5, xB, wB)
        EP1(0, 0) EP1(1, 0) EP1(0, 4) EP1(1, 4) EP1(0, 5) EP1(1, 5)
      }
    };
    if (cw < 5) run(icst<0>{});
    else if (cw < 7) run(icst<1>{});
    else run(icst<2>{});
  }
  __builtin_amdgcn_s_setprio(0);
  const bool live = r0 + rt * 16 < SEQ;
  if (!live) {
    if (w >= 2) LDS_BARRIER();
    if (w != 7) {
      asm volatile("s_waitcnt vmcnt(0)" ::: "memory");
      return;
    }
  } else if (w >= 2)
    asm volatile(ATT_ASM
                 :: [kh] "s"(kh), [l16] "v"(l16), [os] "s"(os), [os4] "s"(os4), [b0] "v"(bias[0]), [b1] "v"(bias[1]),
                    [s0] "v"(sel[0]), [s1] "v"(sel[1]), [s2] "v"(sel[2]), [s3] "v"(sel[3]), [e4] "v"(e4), [eh] "v"(eh), [aq] "v"(aq),
                    [aka] "v"(aka), [akb] "v"(akb), [arec] "v"(arec), [vpp] "v"(vpp), [sst] "s"(sst), [w] "s"(w)
                 : "memory", "s41", "s46", "s47", "s48", "s49", "s50", "s51", "scc", "v24", "v25", "v26", "v27", "v28", "v29", "v30", "v31", "v32", "v33", "v34", "v35", "v36", "v37", "v38", "v39", "v40", "v41", "v42", "v43", "v44", "v45", "v46", "v47", "v48", "v49", "v50", "v51", "v52", "v53", "v54", "v55", "v56", "v57", "v58", "v59", "v60", "v61", "v62", "v63", "v64", "v65", "v66", "v67", "v68", "v69", "v70", "v71", "v72", "v73", "v74", "v75", "v76", "v77", "v78", "v79", "v80", "v81", "v82", "v83", "v84", "v85", "v86", "v87");
  u32x4 hwa, hwb;
  u32x2 hwc;
  const int grp = rt < 3 ? 0 : 1;
  if (w < 8) {
    const int rmax = mt == 25 ? 2 : 4;
    const int rk = rt < rmax ? rt : rmax, rd = (rt ^ 1) < rmax ? (rt ^ 1) : rmax;
    u32 ta, tk;
    if (w == 0) asm volatile("s_waitcnt vmcnt(0)" ::: "memory");
    if (w == 2 || w == 3) asm volatile("s_waitcnt vmcnt(3)" ::: "memory");
    asm volatile("s_waitcnt lgkmcnt(0)\n\ts_barrier\n\t" OP_ASM
                 : "=&{v[84:87]}"(hwa), "=&{v[88:91]}"(hwb), "=&{v[92:93]}"(hwc), [ta] "=&v"(ta), [tk] "=&v"(tk)
                 : [w] "s"(w), [l16] "v"(l16), [oa0] "s"(oa[0]), [so0] "s"(so_[0]), [oa1] "s"(oa[1]), [so1] "s"(so_[1]), [oa2] "s"(oa[2]), [so2] "s"(so_[2]), [oa3] "s"(oa[3]), [so3] "s"(so_[3]), [oa4] "s"(oa[4]), [so4] "s"(so_[4]), [oa5] "s"(oa[5]), [so5] "s"(so_[5]), [oa6] "s"(oa[6]), [so6] "s"(so_[6]), [oa7] "s"(oa[7]), [so7] "s"(so_[7]), [oa8] "s"(oa[8]), [so8] "s"(so_[8]), [oa9] "s"(oa[9]), [so9] "s"(so_[9]),
                   [pm] "v"(pm), [pf] "v"(pf), [ra] "v"(w < 3 ? (u32)(CTL_OFF + 72) : hi), [e4] "v"(e4),
                   [aw] "v"((lane & 1) ? (u32)(WR_OFF + g * 40) : (u32)ONES_OFF),
                   [vk] "v"(l16 + rk * 20480), [vd] "v"(l16 + rd * 20480), [sk] "s"((w & 1) * 6144),
                   [sd] "s"(EXO + (w < 4 ? 4 + w : rt ^ 1) * 5120), [sr] "s"(EXO + rt * 5120),
                   [sc] "s"(CTL_OFF + 48 + (w < 4 ? 4 : rt ^ 1) * 4), [sp] "s"(PST_OFF + rt * 256), [sa] "s"(CTL_OFF + 8 + grp * 4),
                   [sst] "s"(sst), [cp] "s"(ws + WS_PC + (2 * mt + grp) * 1280 + hg * 160), [oback] "n"(OBACK), [cap] "n"(SPIN_CAP)
                 : "memory", "vcc", "scc", "s43", "v24", "v25", "v26", "v27", "v28", "v29", "v30", "v31", "v32", "v33", "v34", "v35", "v36", "v37", "v38", "v39", "v40", "v41", "v42", "v43", "v44", "v45", "v46", "v47", "v48", "v49", "v50", "v51", "v52", "v53", "v54", "v55", "v56", "v57", "v58", "v59", "v60", "v61", "v62", "v63", "v64", "v65", "v66", "v67", "v68", "v69", "v70", "v71", "v72", "v73", "v74", "v75", "v76", "v77", "v78", "v79", "v80", "v81", "v82", "v83", "v94", "v95", "v96", "v97", "v98", "v99", "v100", "v101", "v102", "v103", "v104", "v105", "v106", "v107", "v108", "v109", "v110", "v111", "v112", "v113", "v114", "v115", "v116", "v117", "v118", "v119", "v120", "v121", "v122", "v123", "v124", "v125", "v126", "v127", "v128", "v129", "v130", "v131", "v132", "v133", "v134", "v135", "v136", "v137", "v138", "v139", "v140", "v141", "v142", "v143", "v144", "v145", "v146", "v147", "v148", "v149", "v150", "v151", "v152", "v153", "v154", "v155", "v156", "v157", "v158", "v159");
    if (!live || w < 3) return;
  } else {
    u64 tb;
    asm volatile("s_setprio 3\n\ts_waitcnt lgkmcnt(0)\n\ts_barrier\n\ts_memrealtime %0\n\ts_waitcnt lgkmcnt(0)" : "=s"(tb)::"memory");
    if (w == 8 || w == 11) {
      const int cg = w == 11, gid = 2 * mt + cg;
      const char* sp = sst - hg * 1024;
      const char* sp2 = sp + (cg ? 0 : 7936);
      const u32 vq = (lane >> 3) * 1024 + (lane & 7) * 16 + (cg ? 640 : 768);
      u32x4 pq[3];
      u64 t2;
#define PLOADS "global_load_dwordx4 %0, %4, %5 sc1\n\tglobal_load_dwordx4 %1, %4, %5 offset:128 sc1\n\tglobal_load_dwordx4 %2, %4, %6 sc1\n\t"
      asm volatile("1:\n\ts_memrealtime %3\n\ts_waitcnt lgkmcnt(0)\n\ts_sub_u32 s40, %7, s40\n\ts_cmp_gt_i32 s40, 0\n\ts_cbranch_scc1 1b\n\t" PLOADS "s_setprio 1"
                   : "=&v"(pq[0]), "=&v"(pq[1]), "=&v"(pq[2]), "=&{s[40:41]}"(t2) : "v"(vq), "s"(sp), "s"(sp2), "s"((u32)tb + PDT) : "memory", "scc");
      int lane2 = lane;
      asm volatile("" : "+v"(lane2));
      const int n16 = lane2 & 15;
      u32x4 sl;
      u32x2 km;
#pragma unroll
      for (int e = 0; e < 4; ++e) sl[e] = n16 == 2 * e ? 0x3F80u : (n16 == 2 * e + 1 ? 0x3F800000u : 0u);
      u32 rid = (u32)((((lane2 >> 4) & 1) * 2 + (lane2 >> 5)) * 4 + (lane2 & 3)) << 10;
      u32 rid1 = rid + (16 << 10), rid2 = rid + (cg ? 0 : 32 << 10), msk = n16 < 4 ? 0xFFFF0000u : 0u;
      u32 ah = HIM + cg * 7680 + (lane2 < 10 ? lane2 : 9) * 16;
      km[1] = *(const u32*)(smem + CTL_OFF);
      const char* kp = ws + WS_PK + gid * 6144 + hg * 768;
      const char* cp = ws + WS_PC + gid * 1280 + hg * 160;
      asm volatile("" : "+s"(kp), "+s"(cp));
      km[0] = ((u32)tb >> 1) & 1023u;
      asm volatile("s_setprio " "3" "\n\ts_waitcnt vmcnt(0)" : "+v"(pq[0]), "+v"(pq[1]), "+v"(pq[2]), "+v"(sl), "+v"(rid), "+v"(rid1), "+v"(rid2), "+v"(msk), "+v"(ah), "+v"(km)::"memory");
#define PMISS __any(max(max(max(pq[0][0], pq[0][2]), max(pq[1][0], pq[1][2])), max(pq[2][0], pq[2][2])) == SENT)
      if (__builtin_expect(PMISS, 0)) {
#pragma nounroll
        for (int it = 0; it < SPIN_CAP; ++it) {
          asm volatile(PLOADS "s_waitcnt vmcnt(0)" : "=&v"(pq[0]), "=&v"(pq[1]), "=&v"(pq[2]), "=&s"(t2) : "v"(vq), "s"(sp), "s"(sp2) : "memory");
          if (!PMISS) break;
          __builtin_amdgcn_s_sleep(2);
        }
      }
      u32 pkey = 0;
#pragma unroll
      for (int t = 0; t < 3; ++t) {
        const f32x4 ys = MFMA(__builtin_bit_cast(bf16x8, pq[t]), __builtin_bit_cast(bf16x8, sl), ((f32x4){0.f, 0.f, 0.f, 0.f}));
        const float y = tr_sum(ys);
        pkey = max(pkey, (mono(y * __builtin_amdgcn_rcpf(dppf<0x104>(y))) & msk) | (t == 0 ? rid : (t == 1 ? rid1 : rid2)));
      }
      pkey = max(pkey, dppu<0xB1>(pkey));
      pkey = max(pkey, dppu<0x4E>(pkey));
      pkey = max(max((u32)__builtin_amdgcn_readlane(pkey, 0), (u32)__builtin_amdgcn_readlane(pkey, 16)),
                 max((u32)__builtin_amdgcn_readlane(pkey, 32), (u32)__builtin_amdgcn_readlane(pkey, 48)));
      km[0] |= pkey;
      asm volatile("s_mov_b32 exec_lo, 0x01010101\n\ts_mov_b32 exec_hi, 0x0101\n\tglobal_store_dwordx2 %0, %1, %2 sc1\n\ts_mov_b64 exec, -1\n\ts_setprio 2" ::"v"(l16), "v"(km), "s"(kp) : "memory");
      ah += ((pkey >> 10) & 63u) * 160;
      if (lane == 0) *(u32*)(smem + CTL_OFF + 24 + cg * 4) = ah;
      __builtin_amdgcn_s_setprio(0);
#pragma nounroll
      for (int it = 0; it < 32; ++it) {
        if ((int)((u32)__builtin_amdgcn_s_memrealtime() - (u32)tb) >= PSL) break;
        __builtin_amdgcn_s_sleep(6);
      }
      __builtin_amdgcn_s_setprio(2);
      asm volatile("s_mov_b32 s40, %[cap]\n"
                   "1:\n\tds_read_b32 v100, %[ac]\n\tds_read_b32 v101, %[ac] offset:24\n\tds_read_b128 v[96:99], %[ah]\n\ts_sub_u32 s40, s40, 1\n\ts_cbranch_scc1 2f\n\ts_waitcnt lgkmcnt(0)\n\t"
                   "v_cmp_gt_u32 vcc, %[nf], v100\n\ts_cbranch_vccz 2f\n\ts_sleep 2\n\ts_branch 1b\n"
                   "2:\n\ts_waitcnt lgkmcnt(0)\n\tv_cmp_eq_u32 vcc, 0, v101\n\ts_cbranch_vccz 3f\n\t"
                   "s_mov_b64 exec, 0x3ff\n\tglobal_store_dwordx4 %[l16], v[96:99], %[cp] sc1\n\ts_mov_b64 exec, -1\n3:"
                   ::[cap] "n"(SPIN_CAP * 8), [ac] "v"((u32)(CTL_OFF + 8 + cg * 4)), [ah] "v"(ah), [nf] "s"(3 - cg), [l16] "v"(l16), [cp] "s"(cp)
                   : "memory", "vcc", "scc", "s40", "v96", "v97", "v98", "v99", "v100", "v101");
      return;
    }
    __builtin_amdgcn_s_setprio(0);
    if (w == 10 && mt == 25) return;
    const int cg = w - 9, gid = 2 * mt + cg;
    const int nr = cg ? 31 : 47;
    const int lr = lane < nr ? lane : nr;
    const int lrow = cg * 48 + lr;
    const int li = (lane & 31) < 20 ? (lane & 31) : 19;
    asm volatile(COL_ASM
                 :
                 : [lq] "v"((u32)(lr * 16 + hg * 1024)), [ac] "v"((u32)(CTL_OFF + 8 + cg * 4)), [nf] "s"(3 - cg),
                   [sp] "s"(ws + WS_STAT + (size_t)gid * 8192), [ao] "v"((u32)(PST_OFF + lrow * 16)), [rdm] "s"(1.0f / DM),
                   [swr] "v"(*(const float*)(smem + CTL_OFF + 40)), [msk] "s"(0xFFFFF000u), [rid] "v"(0x800u | (u32)(2047 - (r0 + lrow))),
                   [at] "v"((u32)(TAB_OFF + lrow * 8)), [zr] "v"(l16), "{v99}"(*(const u32*)(smem + CTL_OFF)),
                   [kp] "s"(ws + WS_PK + gid * 6144 + hg * 768 + 8), [ak] "v"((u32)(CTL_OFF + 16 + cg * 4)), [rb] "s"(2047 - r0 - cg * 48),
                   [ah] "v"((u32)(HIM + cg * 7680 + li * 8)), [co] "v"((u32)((lane < 32 ? 1280 : 0) + li * 8)),
                   [cp] "s"(ws + WS_CAND + gid * 2560 + hg * 160), [pdel] "n"(PDEL2), [cap] "n"(SPIN_CAP)
                 : "memory", "vcc", "scc", "s40", "s41", "s42", "s43", "s44", "s45", "s46", "s47", "v64", "v65", "v66", "v67", "v68", "v69", "v70", "v71", "v72", "v73", "v74", "v75", "v76", "v77", "v78", "v79", "v80", "v81", "v82", "v83", "v84", "v85", "v86", "v87", "v88", "v89", "v90", "v91", "v92", "v93", "v94", "v95", "v96", "v97", "v98", "v100", "v101", "v102", "v103", "v104", "v105", "v106", "v107", "v108", "v109", "v110", "v111", "v112", "v113", "v114", "v115", "v116", "v117", "v118", "v119", "v120", "v121", "v122", "v123");
    return;
  }
  const u32 hw[10] = {hwa[0], hwa[1], hwa[2], hwa[3], hwb[0], hwb[1], hwb[2], hwb[3], hwc[0], hwc[1]};
  f32x2 hf[10];
#pragma unroll
  for (int e = 0; e < 10; ++e) {
    hf[e] = unpk(hw[e]);
    asm volatile("" : "+v"(hf[e]));
  }
  const char* obase = (const char*)out + (size_t)r0 * 1280 + hg * 160;
  u32 tilekey;
  asm volatile("" : "+s"(obase));
  u32 aow = EXO + rt * 5120 + r16 * 160 + g * 40, aor = EXO + rt * 5120 + l16, vo3[3], rr3[3];
#pragma unroll
  for (int i = 0; i < 3; ++i) {
    const u32 c = i * 64 + lane, r = c / 10u;
    vo3[i] = (rt * 16 + r) * 1120 + rt * 2560 + c * 16;
    rr3[i] = c < 160u ? 2047 - (r0 + rt * 16 + r) : 4096u;
    asm volatile("" : "+v"(vo3[i]), "+v"(rr3[i]));
  }
  asm volatile("" : "+v"(aow), "+v"(aor));
  f32x2 st2;
  asm volatile("s_mov_b32 s40, %[cap]\n"
               "1:\n\ts_sleep 1\n\tds_read_b32 %[k], %[ak]\n\tds_read_b64 %[s], %[at]\n\ts_sub_u32 s40, s40, 1\n\ts_waitcnt lgkmcnt(0)\n\ts_cbranch_scc1 2f\n\t"
               "v_cmp_eq_u32 vcc, 0, %[k]\n\ts_cbranch_vccnz 1b\n"
               "2:"
               : [k] "=&v"(tilekey), [s] "=&v"(st2) : [cap] "n"(SPIN_CAP * 8), [ak] "v"((u32)(CTL_OFF + 16 + grp * 4)), [at] "v"((u32)(TAB_OFF + (rt * 16 + r16) * 8))
               : "memory", "vcc", "scc", "s40");
  u32 ok[10];
#pragma unroll
  for (int e = 0; e < 10; ++e) ok[e] = cvt2(__builtin_elementwise_fma(hf[e] - (f32x2){st2[0], st2[0]}, (f32x2){st2[1], st2[1]}, hf[e]));
  {
#pragma unroll
    for (int e = 0; e < 5; ++e) asm volatile("ds_write_b64 %0, %1 offset:%2" ::"v"(aow), "v"((u32x2){ok[2 * e], ok[2 * e + 1]}), "n"(e * 8) : "memory");
    u32x4 o3[3];
    asm volatile("ds_read_b128 %0, %3\n\tds_read_b128 %1, %3 offset:1024\n\tds_read_b128 %2, %3 offset:2048\n\ts_waitcnt lgkmcnt(0)"
                 : "=&v"(o3[0]), "=&v"(o3[1]), "=&v"(o3[2]) : "v"(aor) : "memory");
    if (OSL) __builtin_amdgcn_s_sleep(OSL);
#pragma unroll
    for (int i = 0; i < 3; ++i)
      if (rr3[i] != (tilekey & 2047u) && rr3[i] != 4096u)
        asm volatile("global_store_dwordx4 %0, %1, %2" " sc1" "\n\ts_nop 1" ::"v"(vo3[i]), "v"(o3[i]), "s"(obase) : "memory");
  }
}

extern "C" void kernel_launch(void* const* d_in, const int* in_sizes, int n_in,
                              void* d_out, int out_size, void* d_ws, size_t ws_size,
                              hipStream_t stream) {
  const u16* x = (const u16*)d_in[0];
  const u16* wqkv = (const u16*)d_in[1];
  const u16* wo = (const u16*)d_in[2];
  const u16* w_r = (const u16*)d_in[4];
  const u16* w_gate = (const u16*)d_in[5];
  const u16* w_up = (const u16*)d_in[6];
  const u16* w_down = (const u16*)d_in[7];
  (void)hipFuncSetAttribute((const void*)fused_block, hipFuncAttributeMaxDynamicSharedMemorySize, LDS_TOTAL);
  fused_block<<<256, 768, LDS_TOTAL, stream>>>(x, wqkv, wo, w_r, w_gate, w_up, w_down, (u16*)d_out, (char*)d_ws);
}
```

```cpp
#include <hip/hip_runtime.h>
#include <hip/hip_bf16.h>
#include <stdint.h>


#define SEQ 2048
#define DM 640
#define FF 1536

typedef __attribute__((ext_vector_type(8))) short bf16x8;
typedef __attribute__((ext_vector_type(4))) short s16x4;
typedef __attribute__((ext_vector_type(4))) float f32x4;
typedef __attribute__((ext_vector_type(2))) float f32x2;
typedef __attribute__((ext_vector_type(2))) __bf16 bf16x2_t;
typedef __attribute__((address_space(3))) s16x4* lds_s16x4_p;
typedef unsigned short u16;
typedef unsigned int u32;
typedef unsigned long long u64;
typedef __attribute__((ext_vector_type(4))) unsigned int u32x4;
typedef __attribute__((ext_vector_type(2))) unsigned int u32x2;

#define HK0 15
#define RGAP 6
#define CGAP 6
#define KGAP 7
#define CDEL 10
#define PDEL2 7
#define OBACK 2
#define OSL 0
#define PDELAY 220
#define FDELAY 450
#define KSTART 1050
#define PGAP 5
#define PDT 18
#define PSL 185
#define VDELAY 150
#define CCD 322
#define TQD 160
#define KCD 314
#define KOLD 322
#define OPP 1
#define TWS 12
#define TWN 5
#define HN 3
#define PCD 230
#define PTHR 0.91f
#define GADD 61
#define OORD 0, 4, 5, 6, 7, 1, 2, 3
#define CODE_BYTES 35328

#define LDS_BARRIER() do { __builtin_amdgcn_sched_barrier(0); asm volatile("s_waitcnt lgkmcnt(0)\n\ts_barrier" ::: "memory"); __builtin_amdgcn_sched_barrier(0); } while (0)
__device__ __forceinline__ float bf2f(u16 v) { return __uint_as_float(((u32)v) << 16); }
__device__ __forceinline__ u16 f2bf(float f) { return __builtin_bit_cast(u16, (__bf16)f); }
__device__ __forceinline__ float rbf(float f) { return bf2f(f2bf(f)); }
__device__ __forceinline__ float lo_f(u32 w) { return __uint_as_float(w << 16); }
__device__ __forceinline__ float hi_f(u32 w) { return __uint_as_float(w & 0xFFFF0000u); }
__device__ __forceinline__ u32 cvt2(f32x2 v) { return __builtin_bit_cast(u32, __builtin_convertvector(v, bf16x2_t)); }
__device__ __forceinline__ f32x2 unpk(u32 w) { return (f32x2){lo_f(w), hi_f(w)}; }
#define NORM2(HV, MB, RD) cvt2(unpk(cvt2(unpk(HV) - (f32x2){MB, MB})) * (f32x2){RD, RD})
__device__ __forceinline__ s16x4 tr_read(const char* p) {
  return __builtin_amdgcn_ds_read_tr16_b64_v4i16((lds_s16x4_p)(p));
}
template <int V> struct icst { static constexpr int value = V; };
template <int CTRL>
__device__ __forceinline__ float dppf(float v) {
  return __builtin_bit_cast(float, __builtin_amdgcn_update_dpp(0, __builtin_bit_cast(int, v), CTRL, 0xf, 0xf, true));
}
__device__ __forceinline__ float row8_sum(float v) {
  v += dppf<0xB1>(v);
  v += dppf<0x4E>(v);
  v += dppf<0x141>(v);
  return v;
}
__device__ __forceinline__ float row16_sum(float v) {
  v = row8_sum(v);
  v += dppf<0x140>(v);
  return v;
}
__device__ __forceinline__ float xg_sum(float v) {
  u32 a = __float_as_uint(v);
  auto r = __builtin_amdgcn_permlane32_swap(a, a, false, false);
  v = __uint_as_float(r[0]) + __uint_as_float(r[1]);
  a = __float_as_uint(v);
  auto r2 = __builtin_amdgcn_permlane16_swap(a, a, false, false);
  return __uint_as_float(r2[0]) + __uint_as_float(r2[1]);
}
__device__ __forceinline__ float tr_sum(f32x4 ys) {
  auto r01 = __builtin_amdgcn_permlane32_swap(__float_as_uint(ys[0]), __float_as_uint(ys[1]), false, false);
  auto r23 = __builtin_amdgcn_permlane32_swap(__float_as_uint(ys[2]), __float_as_uint(ys[3]), false, false);
  const u32 u = __float_as_uint(__uint_as_float(r01[0]) + __uint_as_float(r01[1]));
  const u32 vv = __float_as_uint(__uint_as_float(r23[0]) + __uint_as_float(r23[1]));
  auto r2 = __builtin_amdgcn_permlane16_swap(u, vv, false, false);
  return __uint_as_float(r2[0]) + __uint_as_float(r2[1]);
}
__device__ __forceinline__ float wave_sum(float v) { return xg_sum(row16_sum(v)); }
template <int CTRL>
__device__ __forceinline__ u32 dppu(u32 v) { return (u32)__builtin_amdgcn_update_dpp(0, (int)v, CTRL, 0xf, 0xf, true); }
__device__ __forceinline__ u32 wave_max(u32 v) {
  v = max(v, dppu<0xB1>(v)); v = max(v, dppu<0x4E>(v)); v = max(v, dppu<0x141>(v)); v = max(v, dppu<0x140>(v));
  return max(max((u32)__builtin_amdgcn_readlane(v, 0), (u32)__builtin_amdgcn_readlane(v, 16)),
             max((u32)__builtin_amdgcn_readlane(v, 32), (u32)__builtin_amdgcn_readlane(v, 48)));
}
__device__ __forceinline__ u32 mono(float f) {
  const u32 b = __float_as_uint(f);
  return b ^ ((u32)((int)b >> 31) | 0x80000000u);
}
__device__ __forceinline__ float dot2b(u32 a, u32 b, float c) {
  return __builtin_amdgcn_fdot2_f32_bf16(__builtin_bit_cast(bf16x2_t, a), __builtin_bit_cast(bf16x2_t, b), c, false);
}
template <int OFF>
__device__ __forceinline__ void dma_m0(u32 voff, const char* sbase, u32 dst) {
  asm volatile("s_mov_b32 m0, %2\n\ts_nop 0\n\tglobal_load_lds_dwordx4 %0, %1 offset:%3"
               :: "v"(voff), "s"(sbase), "s"(dst), "n"(OFF) : "memory");
}
template <int OFF>
__device__ __forceinline__ void dma_nx(u32 voff, const char* sbase) {
  asm volatile("global_load_lds_dwordx4 %0, %1 offset:%2" :: "v"(voff), "s"(sbase), "n"(OFF) : "memory");
}
__device__ __forceinline__ u32 lds_ld32(u32 addr) {
  u32 v;
  asm volatile("ds_read_b32 %0, %1\n\ts_waitcnt lgkmcnt(0)" : "=v"(v) : "v"(addr) : "memory");
  return v;
}
__device__ __forceinline__ u32x4 lds_ld128(u32 addr) {
  u32x4 v;
  asm volatile("ds_read_b128 %0, %1\n\ts_waitcnt lgkmcnt(0)" : "=v"(v) : "v"(addr) : "memory");
  return v;
}
__device__ __forceinline__ bf16x8 mk8(u32 a, u32 b, u32 c, u32 d) {
  return __builtin_bit_cast(bf16x8, (u32x4){a, b, c, d});
}
__device__ __forceinline__ bf16x8 tr_frag(const char* p, int off2) {
  s16x4 b0 = tr_read(p);
  s16x4 b1 = tr_read(p + off2);
  bf16x8 r;
  r[0] = b0[0]; r[1] = b0[1]; r[2] = b0[2]; r[3] = b0[3];
  r[4] = b1[0]; r[5] = b1[1]; r[6] = b1[2]; r[7] = b1[3];
  return r;
}
#define SPIN_CAP 400000
#define MFMA(A, B, C) __builtin_amdgcn_mfma_f32_16x16x32_bf16(A, B, C, 0, 0, 0)

#define HSLOT 16384
#define XRING 98304
#define XSLOT 12288
#define LDS_TOTAL 163840
#define EXO 102400
#define HIM 148480
#define PST_OFF (EXO + 40960)
#define TAB_OFF 161280
#define ONES_OFF 162048
#define CTL_OFF 162176
#define WR_OFF 162304
#define REC_OFF 162816
#define HN_OFF 124928
#define HSL 1344
#define ASL 1056
#define TCH_OFF 162560
#define ACT_OFF 135168
#define KEY_OFF (ACT_OFF + 5120)
#define POST_OFF (KEY_OFF + 256)
#define DOWN_OFF 81920
#define WS_EPOCH 0
#define WS_HFLAG 4096
#define WS_VP 40960
#define WS_LN1 16384
#define WS_FILL 131072
#define WS_O WS_FILL
#define WS_STAT (WS_O + 2621440)
#define WS_CAND (WS_STAT + 425984)
#define WS_PART (WS_CAND + 133120)
#define WS_BXF (WS_PART + 122880)
#define WS_PC (WS_BXF + 26624)
#define WS_PART2 (WS_PC + 66560)
#define WS_PK (4u << 20)
#define SENT 0xFFFFFFFFu

#define GST4(VO, SB, O, V) asm volatile("global_store_dwordx4 %0, %1, %2 offset:%3 sc1\n\ts_nop 1" ::"v"(VO), "v"(V), "s"(SB), "n"(O) : "memory")
#define GST2(VO, SB, O, V) asm volatile("global_store_dwordx2 %0, %1, %2 offset:%3 sc1\n\ts_nop 1" ::"v"(VO), "v"(V), "s"(SB), "n"(O) : "memory")
#define GLD2(D, VO, SB, O) asm volatile("global_load_dwordx2 %0, %1, %2 offset:%3 sc1" : "=v"(D) : "v"(VO), "s"(SB), "n"(O) : "memory")

__device__ __forceinline__ void touch_line(const void* p) {
  asm volatile("s_mov_b32 m0, %1\n\ts_nop 0\n\tglobal_load_lds_dword %0, off" ::"v"(p), "s"(TCH_OFF) : "memory");
}
__device__ __forceinline__ u32 w_off(int i, int lane, int hg) {
  int cc = (lane & 31) ^ (((2 * i + (lane >> 5)) & 7) << 1);
  cc = cc > 29 ? 29 : cc;
  return (lane >> 5) * 3840 + ((cc >= 10 ? 1120 : 0) + (cc >= 20 ? 1120 : 0)) + hg * 160 + cc * 16;
}
#define W_ROW(I) (((((2 * (I)) & 0x13) | (((2 * (I)) & 4) << 1) | (((2 * (I)) & 8) >> 1))) * 3840)
__device__ __forceinline__ u32 x_off(int i, int lane, int r0) {
  const int xr = i * 8 + (lane >> 3);
  int grow = r0 - 16 + xr;
  grow = grow < 0 ? 0 : (grow > SEQ - 1 ? SEQ - 1 : grow);
  return grow * 1280 + (((lane & 7) ^ ((xr >> 1) & 7)) * 16);
}

#define ATT_ASM \
  "ds_read_b32 v28, %[aq] offset:64\n\tds_read_b32 v30, %[aka]\n\tds_read_b32 v31, %[akb]\n\tv_mov_b64 v[38:39], 0\n\tv_mov_b64 v[42:43], 0\n\t" \
  "s_lshl_b32 s41, %[kh], 5\n\ts_lshl_b64 s[46:47], 0xffff, s41\n\ts_lshl_b64 s[48:49], s[46:47], 16\n\ts_mov_b32 s50, 0xaaaaaaaa\n\t" \
  "s_mov_b32 s51, 0xaaaaaaaa\n\ts_setprio 1\n\ts_waitcnt lgkmcnt(0)\n\ts_barrier\n\tds_read_b64 v[36:37], %[eh] offset:512\n\t" \
  "ds_read_b64 v[44:45], %[eh] offset:15360\n\tds_read_b64 v[46:47], %[e4] offset:27648\n\tds_read_b64 v[48:49], %[eh] offset:15872\n\t" \
  "ds_read_b64 v[50:51], %[e4] offset:28160\n\ts_mov_b64 exec, s[46:47]\n\tds_read_b64 v[38:39], %[e4] offset:12800\n\ts_mov_b64 exec, s[48:49]\n\t" \
  "ds_read_b64 v[42:43], %[e4] offset:12800\n\ts_mov_b64 exec, -1\n\tds_read_b64 v[40:41], %[eh] offset:3584\n\t" \
  "ds_read_b64 v[52:53], %[eh] offset:18432\n\tds_read_b64 v[54:55], %[e4] offset:27648\n\tds_read_b64 v[56:57], %[eh] offset:18944\n\t" \
  "ds_read_b64 v[58:59], %[e4] offset:28160\n\tv_mul_f32 v28, 0x3ea52b5b, v28\n\tv_pk_mul_f32 v[32:33], v[28:29], v[30:31] op_sel_hi:[0,1]\n\t" \
  "s_waitcnt lgkmcnt(6)\n\tv_mfma_f32_16x16x32_bf16 v[44:47], v[44:47], v[36:39], 0\n\tds_read2st64_b64 v[60:63], %[eh] offset0:60 offset1:61\n\t" \
  "v_mfma_f32_16x16x32_bf16 v[48:51], v[48:51], v[36:39], 0\n\tds_read2st64_b64 v[64:67], %[eh] offset0:66 offset1:67\n\ts_waitcnt lgkmcnt(2)\n\t" \
  "v_mfma_f32_16x16x32_bf16 v[52:55], v[52:55], v[40:43], 0\n\tds_read2st64_b64 v[68:71], %[e4] offset0:84 offset1:85\n\t" \
  "v_mfma_f32_16x16x32_bf16 v[56:59], v[56:59], v[40:43], 0\n\ts_mov_b32 exec_lo, 0xfff0ffff\n\ts_mov_b32 exec_hi, 0xf000ff00\n\ts_nop 0\n\t" \
  "v_mov_b64 v[44:45], v[48:49]\n\ts_mov_b32 exec_lo, 0xffc0fffc\n\ts_mov_b32 exec_hi, 0xc000fc00\n\tv_mov_b64 v[46:47], v[50:51]\n\ts_nop 0\n\t" \
  "v_mov_b64 v[54:55], v[58:59]\n\ts_mov_b32 exec_lo, 0xfff0ffff\n\ts_mov_b32 exec_hi, 0xf000ff00\n\tv_mov_b64 v[52:53], v[56:57]\n\t" \
  "s_mov_b64 exec, -1\n\tv_cndmask_b32 v44, v44, v45, s[50:51]\n\tv_cndmask_b32 v46, v46, v47, s[50:51]\n\tv_cndmask_b32 v52, v52, v53, s[50:51]\n\t" \
  "v_cndmask_b32 v54, v54, v55, s[50:51]\n\tv_fma_f32 v72, v44, v32, %[b0]\n\tv_fma_f32 v73, v46, v33, %[b1]\n\tv_fma_f32 v74, v52, v32, %[b0]\n\t" \
  "v_fma_f32 v75, v54, v33, %[b1]\n\tv_max_f32 v34, v72, v73\n\tv_max_f32 v35, v74, v75\n\tds_read_b128 v[76:79], %[arec]\n\t" \
  "ds_read_b128 v[80:83], %[arec] offset:16\n\tv_permlane32_swap_b32 v34, v35\n\tv_max_f32 v34, v34, v35\n\tv_mov_b32 v35, v34\n\t" \
  "ds_read_b128 v[84:87], %[arec] offset:32\n\ts_nop 0\n\tv_permlane16_swap_b32 v34, v35\n\tv_max_f32 v34, v34, v35\n\tv_mov_b32 v35, v34\n\ts_nop 1\n\t" \
  "v_permlane32_swap_b32 v34, v35\n\tv_sub_f32 v72, v72, v34\n\tv_sub_f32 v73, v73, v34\n\tv_sub_f32 v74, v74, v35\n\tv_sub_f32 v75, v75, v35\n\t" \
  "v_exp_f32 v72, v72\n\tv_exp_f32 v73, v73\n\tv_exp_f32 v74, v74\n\tv_exp_f32 v75, v75\n\tv_add_f32 v34, v72, v73\n\tv_add_f32 v35, v74, v75\n\t" \
  "v_pk_mul_f32 v[72:73], v[72:73], v[30:31]\n\tv_pk_mul_f32 v[74:75], v[74:75], v[30:31]\n\tv_permlane32_swap_b32 v34, v35\n\t" \
  "v_add_f32 v34, v34, v35\n\tv_mov_b32 v35, v34\n\ts_nop 1\n\tv_permlane16_swap_b32 v34, v35\n\tv_add_f32 v34, v34, v35\n\tv_rcp_f32 v34, v34\n\t" \
  "s_nop 0\n\tv_mov_b32 v30, v34\n\ts_waitcnt lgkmcnt(0)\n\ts_nop 0\n\tv_permlane32_swap_b32 v34, v30\n\t" \
  "v_pk_mul_f32 v[72:73], v[34:35], v[72:73] op_sel_hi:[0,1]\n\tv_pk_mul_f32 v[74:75], v[30:31], v[74:75] op_sel_hi:[0,1]\n\t" \
  "v_cvt_pk_bf16_f32 v72, v72, v73\n\tv_cvt_pk_bf16_f32 v74, v74, v75\n\tv_perm_b32 v36, 0, v72, %[s0]\n\tv_perm_b32 v37, 0, v72, %[s1]\n\t" \
  "v_perm_b32 v38, 0, v72, %[s2]\n\tv_perm_b32 v39, 0, v72, %[s3]\n\tv_perm_b32 v40, 0, v74, %[s0]\n\tv_perm_b32 v41, 0, v74, %[s1]\n\t" \
  "v_mfma_f32_16x16x32_bf16 v[60:63], v[60:63], v[36:39], 0\n\tv_perm_b32 v42, 0, v74, %[s2]\n\tv_perm_b32 v43, 0, v74, %[s3]\n\t" \
  "v_mfma_f32_16x16x32_bf16 v[52:55], v[68:71], v[36:39], 0\n\ts_nop 0\n\tv_mfma_f32_16x16x32_bf16 v[64:67], v[64:67], v[40:43], 0\n\t" \
  "v_mfma_f32_16x16x32_bf16 v[56:59], v[68:71], v[40:43], 0\n\ts_nop 1\n\tv_cvt_pk_bf16_f32 v24, v60, v61\n\tv_cvt_pk_bf16_f32 v25, v62, v63\n\t" \
  "s_nop 0\n\tv_cvt_pk_bf16_f32 v52, v52, v53\n\tv_cvt_pk_bf16_f32 v53, v54, v55\n\tv_cvt_pk_bf16_f32 v26, v64, v65\n\t" \
  "v_cvt_pk_bf16_f32 v27, v66, v67\n\tglobal_store_dwordx4 %[l16], v[24:27], %[os] sc1\n\ts_mov_b64 exec, s[48:49]\n\tv_cvt_pk_bf16_f32 v52, v56, v57\n\t" \
  "v_cvt_pk_bf16_f32 v53, v58, v59\n\ts_or_b64 exec, s[46:47], s[48:49]\n\tglobal_store_dwordx2 %[l16], v[52:53], %[os4] sc1\n\ts_mov_b64 exec, -1\n\t" \
  "s_setprio 0\n\tv_mfma_f32_16x16x32_bf16 v[60:63], v[24:27], v[76:79], 0\n\tv_and_b32 v54, 0x7fff7fff, v52\n\tv_and_b32 v55, 0x7fff7fff, v53\n\t" \
  "v_and_b32 v64, 0x7fff7fff, v24\n\tv_and_b32 v65, 0x7fff7fff, v25\n\tv_and_b32 v66, 0x7fff7fff, v26\n\tv_and_b32 v67, 0x7fff7fff, v27\n\t" \
  "v_mfma_f32_16x16x32_bf16 v[60:63], v[52:55], v[80:83], v[60:63]\n\tds_write2st64_b64 %[eh], v[24:25], v[26:27] offset0:1 offset1:7\n\t" \
  "v_mfma_f32_16x16x32_bf16 v[60:63], v[64:67], v[84:87], v[60:63]\n\ts_mov_b32 exec_lo, 0x01010101\n\ts_mov_b32 exec_hi, 0x01010101\n\ts_nop 5\n\t" \
  "v_cvt_pk_bf16_f32 v60, v60, v61\n\tv_cvt_pk_bf16_f32 v61, v62, v63\n\tv_and_b32 v60, -2, v60\n\tglobal_store_dwordx2 %[vpp], v[60:61], %[sst] sc1\n\t" \
  "s_mov_b64 exec, -1\n\t" \
  ""
#define OP_ASM \
  "s_mov_b32 s43, %[cap]\n\ts_cmp_lt_u32 %[w], 4\n\ts_cbranch_scc1 LH_%=\n\ts_setprio 3\n\tglobal_load_dwordx4 v[124:127], %[vd], %[oa1] sc1\n\t" \
  "global_load_dwordx4 v[128:131], %[vk], %[oa1] sc1\n\tglobal_load_dwordx4 v[132:135], %[vd], %[oa2] sc1\n\t" \
  "global_load_dwordx4 v[136:139], %[vk], %[oa2] sc1\n\tv_add_u32 %[tk], %[sk], %[e4]\n\tv_add_u32 %[ta], %[so0], %[pm]\n\t" \
  "ds_read2st64_b64 v[104:107], %[tk] offset0:1 offset1:7\n\tv_xor_b32 %[tk], 512, %[tk]\n\tds_read2st64_b64 v[108:111], %[tk] offset0:1 offset1:7\n\t" \
  "ds_read_b64_tr_b16 v[64:65], %[ta] offset:0\n\tds_read_b64_tr_b16 v[66:67], %[ta] offset:3200\n\tds_read_b64_tr_b16 v[68:69], %[ta] offset:8\n\t" \
  "ds_read_b64_tr_b16 v[70:71], %[ta] offset:3208\n\tds_read_b64_tr_b16 v[72:73], %[ta] offset:16\n\tds_read_b64_tr_b16 v[74:75], %[ta] offset:3216\n\t" \
  "ds_read_b64_tr_b16 v[76:77], %[ta] offset:24\n\tds_read_b64_tr_b16 v[78:79], %[ta] offset:3224\n\tds_read_b64_tr_b16 v[80:81], %[ta] offset:32\n\t" \
  "ds_read_b64_tr_b16 v[82:83], %[ta] offset:3232\n\tds_read_b64 v[112:113], %[ra] offset:0\n\tds_read_b64 v[114:115], %[ra] offset:8\n\t" \
  "ds_read_b64 v[116:117], %[ra] offset:16\n\tds_read_b64 v[118:119], %[ra] offset:24\n\tds_read_b64 v[120:121], %[ra] offset:32\n\t" \
  "global_load_dwordx4 v[140:143], %[vd], %[oa3] sc1\n\tv_add_u32 %[ta], %[so1], %[pm]\n\ts_waitcnt lgkmcnt(5)\n\t" \
  "v_mfma_f32_16x16x32_bf16 v[44:47], v[64:67], v[108:111], 0\n\tds_read_b64_tr_b16 v[84:85], %[ta] offset:0\n\t" \
  "ds_read_b64_tr_b16 v[86:87], %[ta] offset:3200\n\tv_mfma_f32_16x16x32_bf16 v[48:51], v[68:71], v[108:111], 0\n\t" \
  "ds_read_b64_tr_b16 v[88:89], %[ta] offset:8\n\tds_read_b64_tr_b16 v[90:91], %[ta] offset:3208\n\t" \
  "v_mfma_f32_16x16x32_bf16 v[52:55], v[72:75], v[108:111], 0\n\tds_read_b64_tr_b16 v[92:93], %[ta] offset:16\n\t" \
  "ds_read_b64_tr_b16 v[94:95], %[ta] offset:3216\n\tv_mfma_f32_16x16x32_bf16 v[56:59], v[76:79], v[108:111], 0\n\t" \
  "ds_read_b64_tr_b16 v[96:97], %[ta] offset:24\n\tds_read_b64_tr_b16 v[98:99], %[ta] offset:3224\n\t" \
  "v_mfma_f32_16x16x32_bf16 v[60:63], v[80:83], v[108:111], 0\n\tds_read_b64_tr_b16 v[100:101], %[ta] offset:32\n\t" \
  "ds_read_b64_tr_b16 v[102:103], %[ta] offset:3232\n\ts_waitcnt lgkmcnt(10)\n\tv_lshlrev_b32 v24, 16, v112\n\tv_and_b32 v25, 0xffff0000, v112\n\t" \
  "v_lshlrev_b32 v26, 16, v113\n\tv_and_b32 v27, 0xffff0000, v113\n\tv_lshlrev_b32 v28, 16, v114\n\tv_and_b32 v29, 0xffff0000, v114\n\t" \
  "v_lshlrev_b32 v30, 16, v115\n\tv_and_b32 v31, 0xffff0000, v115\n\tv_mfma_f32_16x16x32_bf16 v[24:27], v[64:67], v[104:107], v[24:27]\n\t" \
  "v_lshlrev_b32 v32, 16, v116\n\tv_and_b32 v33, 0xffff0000, v116\n\tv_lshlrev_b32 v34, 16, v117\n\tv_and_b32 v35, 0xffff0000, v117\n\t" \
  "v_mfma_f32_16x16x32_bf16 v[28:31], v[68:71], v[104:107], v[28:31]\n\tv_lshlrev_b32 v36, 16, v118\n\tv_and_b32 v37, 0xffff0000, v118\n\t" \
  "v_lshlrev_b32 v38, 16, v119\n\tv_and_b32 v39, 0xffff0000, v119\n\tv_mfma_f32_16x16x32_bf16 v[32:35], v[72:75], v[104:107], v[32:35]\n\t" \
  "v_lshlrev_b32 v40, 16, v120\n\tv_and_b32 v41, 0xffff0000, v120\n\tv_lshlrev_b32 v42, 16, v121\n\tv_and_b32 v43, 0xffff0000, v121\n\t" \
  "v_mfma_f32_16x16x32_bf16 v[36:39], v[76:79], v[104:107], v[36:39]\n\ts_nop 0\n\tv_mfma_f32_16x16x32_bf16 v[40:43], v[80:83], v[104:107], v[40:43]\n\t" \
  "v_add_u32 %[ta], %[so2], %[pm]\n\tds_read_b64_tr_b16 v[104:105], %[ta] offset:0\n\tds_read_b64_tr_b16 v[106:107], %[ta] offset:3200\n\t" \
  "ds_read_b64_tr_b16 v[108:109], %[ta] offset:8\n\tds_read_b64_tr_b16 v[110:111], %[ta] offset:3208\n\t" \
  "ds_read_b64_tr_b16 v[112:113], %[ta] offset:16\n\tds_read_b64_tr_b16 v[114:115], %[ta] offset:3216\n\t" \
  "ds_read_b64_tr_b16 v[116:117], %[ta] offset:24\n\tds_read_b64_tr_b16 v[118:119], %[ta] offset:3224\n\t" \
  "ds_read_b64_tr_b16 v[120:121], %[ta] offset:32\n\tds_read_b64_tr_b16 v[122:123], %[ta] offset:3232\n\t" \
  "global_load_dwordx4 v[144:147], %[vk], %[oa3] sc1\n\tglobal_load_dwordx4 v[148:151], %[vd], %[oa4] sc1\n\ts_waitcnt vmcnt(5)\n\t" \
  "v_max_u32 %[tk], v124, v128\n\tv_cmp_eq_u32 vcc, -1, %[tk]\n\ts_cbranch_vccnz LSs1_%=\n\tLQs1_%=:\n\ts_waitcnt lgkmcnt(10)\n\t" \
  "v_mfma_f32_16x16x32_bf16 v[44:47], v[84:87], v[124:127], v[44:47]\n\tv_add_u32 %[ta], %[so3], %[pm]\n\tds_read_b64_tr_b16 v[64:65], %[ta] offset:0\n\t" \
  "v_mfma_f32_16x16x32_bf16 v[24:27], v[84:87], v[128:131], v[24:27]\n\tds_read_b64_tr_b16 v[66:67], %[ta] offset:3200\n\t" \
  "global_load_dwordx4 v[152:155], %[vk], %[oa4] sc1\n\tv_mfma_f32_16x16x32_bf16 v[48:51], v[88:91], v[124:127], v[48:51]\n\t" \
  "ds_read_b64_tr_b16 v[68:69], %[ta] offset:8\n\tv_mfma_f32_16x16x32_bf16 v[28:31], v[88:91], v[128:131], v[28:31]\n\t" \
  "ds_read_b64_tr_b16 v[70:71], %[ta] offset:3208\n\tglobal_load_dwordx4 v[156:159], %[vd], %[oa5] sc1\n\t" \
  "v_mfma_f32_16x16x32_bf16 v[52:55], v[92:95], v[124:127], v[52:55]\n\tds_read_b64_tr_b16 v[72:73], %[ta] offset:16\n\t" \
  "v_mfma_f32_16x16x32_bf16 v[32:35], v[92:95], v[128:131], v[32:35]\n\tds_read_b64_tr_b16 v[74:75], %[ta] offset:3216\n\t" \
  "v_mfma_f32_16x16x32_bf16 v[56:59], v[96:99], v[124:127], v[56:59]\n\tds_read_b64_tr_b16 v[76:77], %[ta] offset:24\n\t" \
  "v_mfma_f32_16x16x32_bf16 v[36:39], v[96:99], v[128:131], v[36:39]\n\tds_read_b64_tr_b16 v[78:79], %[ta] offset:3224\n\ts_waitcnt vmcnt(5)\n\t" \
  "v_max_u32 %[tk], v132, v136\n\tv_cmp_eq_u32 vcc, -1, %[tk]\n\tv_mfma_f32_16x16x32_bf16 v[60:63], v[100:103], v[124:127], v[60:63]\n\t" \
  "ds_read_b64_tr_b16 v[80:81], %[ta] offset:32\n\tv_mfma_f32_16x16x32_bf16 v[40:43], v[100:103], v[128:131], v[40:43]\n\t" \
  "ds_read_b64_tr_b16 v[82:83], %[ta] offset:3232\n\ts_cbranch_vccnz LSs2_%=\n\tLQs2_%=:\n\ts_waitcnt lgkmcnt(10)\n\t" \
  "v_mfma_f32_16x16x32_bf16 v[44:47], v[104:107], v[132:135], v[44:47]\n\tv_add_u32 %[ta], %[so4], %[pf]\n\t" \
  "ds_read_b64_tr_b16 v[84:85], %[ta] offset:0\n\tv_mfma_f32_16x16x32_bf16 v[24:27], v[104:107], v[136:139], v[24:27]\n\t" \
  "ds_read_b64_tr_b16 v[86:87], %[ta] offset:12800\n\tglobal_load_dwordx4 v[124:127], %[vk], %[oa5] sc1\n\t" \
  "v_mfma_f32_16x16x32_bf16 v[48:51], v[108:111], v[132:135], v[48:51]\n\tds_read_b64_tr_b16 v[88:89], %[ta] offset:8\n\t" \
  "v_mfma_f32_16x16x32_bf16 v[28:31], v[108:111], v[136:139], v[28:31]\n\tds_read_b64_tr_b16 v[90:91], %[ta] offset:12808\n\t" \
  "global_load_dwordx4 v[128:131], %[vd], %[oa6] sc1\n\tv_mfma_f32_16x16x32_bf16 v[52:55], v[112:115], v[132:135], v[52:55]\n\t" \
  "ds_read_b64_tr_b16 v[92:93], %[ta] offset:16\n\tv_mfma_f32_16x16x32_bf16 v[32:35], v[112:115], v[136:139], v[32:35]\n\t" \
  "ds_read_b64_tr_b16 v[94:95], %[ta] offset:12816\n\tv_mfma_f32_16x16x32_bf16 v[56:59], v[116:119], v[132:135], v[56:59]\n\t" \
  "ds_read_b64_tr_b16 v[96:97], %[ta] offset:24\n\tv_mfma_f32_16x16x32_bf16 v[36:39], v[116:119], v[136:139], v[36:39]\n\t" \
  "ds_read_b64_tr_b16 v[98:99], %[ta] offset:12824\n\ts_waitcnt vmcnt(5)\n\tv_max_u32 %[tk], v140, v144\n\tv_cmp_eq_u32 vcc, -1, %[tk]\n\t" \
  "v_mfma_f32_16x16x32_bf16 v[60:63], v[120:123], v[132:135], v[60:63]\n\tds_read_b64_tr_b16 v[100:101], %[ta] offset:32\n\t" \
  "v_mfma_f32_16x16x32_bf16 v[40:43], v[120:123], v[136:139], v[40:43]\n\tds_read_b64_tr_b16 v[102:103], %[ta] offset:12832\n\t" \
  "s_cbranch_vccnz LSs3_%=\n\tLQs3_%=:\n\ts_waitcnt lgkmcnt(10)\n\tv_mfma_f32_16x16x32_bf16 v[44:47], v[64:67], v[140:143], v[44:47]\n\t" \
  "v_add_u32 %[ta], %[so5], %[pm]\n\tds_read_b64_tr_b16 v[104:105], %[ta] offset:0\n\t" \
  "v_mfma_f32_16x16x32_bf16 v[24:27], v[64:67], v[144:147], v[24:27]\n\tds_read_b64_tr_b16 v[106:107], %[ta] offset:3200\n\t" \
  "global_load_dwordx4 v[132:135], %[vk], %[oa6] sc1\n\tv_mfma_f32_16x16x32_bf16 v[48:51], v[68:71], v[140:143], v[48:51]\n\t" \
  "ds_read_b64_tr_b16 v[108:109], %[ta] offset:8\n\tv_mfma_f32_16x16x32_bf16 v[28:31], v[68:71], v[144:147], v[28:31]\n\t" \
  "ds_read_b64_tr_b16 v[110:111], %[ta] offset:3208\n\tglobal_load_dwordx4 v[136:139], %[vd], %[oa7] sc1\n\t" \
  "v_mfma_f32_16x16x32_bf16 v[52:55], v[72:75], v[140:143], v[52:55]\n\tds_read_b64_tr_b16 v[112:113], %[ta] offset:16\n\t" \
  "v_mfma_f32_16x16x32_bf16 v[32:35], v[72:75], v[144:147], v[32:35]\n\tds_read_b64_tr_b16 v[114:115], %[ta] offset:3216\n\t" \
  "v_mfma_f32_16x16x32_bf16 v[56:59], v[76:79], v[140:143], v[56:59]\n\tds_read_b64_tr_b16 v[116:117], %[ta] offset:24\n\t" \
  "v_mfma_f32_16x16x32_bf16 v[36:39], v[76:79], v[144:147], v[36:39]\n\tds_read_b64_tr_b16 v[118:119], %[ta] offset:3224\n\ts_waitcnt vmcnt(5)\n\t" \
  "v_max3_u32 %[tk], v148, v150, v152\n\tv_max_u32 %[tk], %[tk], v154\n\tv_cmp_eq_u32 vcc, -1, %[tk]\n\t" \
  "v_mfma_f32_16x16x32_bf16 v[60:63], v[80:83], v[140:143], v[60:63]\n\tds_read_b64_tr_b16 v[120:121], %[ta] offset:32\n\t" \
  "v_mfma_f32_16x16x32_bf16 v[40:43], v[80:83], v[144:147], v[40:43]\n\tds_read_b64_tr_b16 v[122:123], %[ta] offset:3232\n\ts_cbranch_vccnz LSs4_%=\n\t" \
  "LQs4_%=:\n\ts_waitcnt lgkmcnt(10)\n\tv_mfma_f32_16x16x32_bf16 v[44:47], v[84:87], v[148:151], v[44:47]\n\tv_add_u32 %[ta], %[so6], %[pm]\n\t" \
  "ds_read_b64_tr_b16 v[64:65], %[ta] offset:0\n\tv_mfma_f32_16x16x32_bf16 v[24:27], v[84:87], v[152:155], v[24:27]\n\t" \
  "ds_read_b64_tr_b16 v[66:67], %[ta] offset:3200\n\tglobal_load_dwordx4 v[140:143], %[vd], %[oa8] sc1\n\t" \
  "v_mfma_f32_16x16x32_bf16 v[48:51], v[88:91], v[148:151], v[48:51]\n\tds_read_b64_tr_b16 v[68:69], %[ta] offset:8\n\t" \
  "v_mfma_f32_16x16x32_bf16 v[28:31], v[88:91], v[152:155], v[28:31]\n\tds_read_b64_tr_b16 v[70:71], %[ta] offset:3208\n\t" \
  "global_load_dwordx4 v[144:147], %[vd], %[oa9] sc1\n\tv_mfma_f32_16x16x32_bf16 v[52:55], v[92:95], v[148:151], v[52:55]\n\t" \
  "ds_read_b64_tr_b16 v[72:73], %[ta] offset:16\n\tv_mfma_f32_16x16x32_bf16 v[32:35], v[92:95], v[152:155], v[32:35]\n\t" \
  "ds_read_b64_tr_b16 v[74:75], %[ta] offset:3216\n\tv_mfma_f32_16x16x32_bf16 v[56:59], v[96:99], v[148:151], v[56:59]\n\t" \
  "ds_read_b64_tr_b16 v[76:77], %[ta] offset:24\n\tv_mfma_f32_16x16x32_bf16 v[36:39], v[96:99], v[152:155], v[36:39]\n\t" \
  "ds_read_b64_tr_b16 v[78:79], %[ta] offset:3224\n\ts_waitcnt vmcnt(5)\n\tv_max_u32 %[tk], v156, v124\n\tv_cmp_eq_u32 vcc, -1, %[tk]\n\t" \
  "v_mfma_f32_16x16x32_bf16 v[60:63], v[100:103], v[148:151], v[60:63]\n\tds_read_b64_tr_b16 v[80:81], %[ta] offset:32\n\t" \
  "v_mfma_f32_16x16x32_bf16 v[40:43], v[100:103], v[152:155], v[40:43]\n\tds_read_b64_tr_b16 v[82:83], %[ta] offset:3232\n\ts_cbranch_vccnz LSs5_%=\n\t" \
  "LQs5_%=:\n\ts_waitcnt lgkmcnt(10)\n\tv_mfma_f32_16x16x32_bf16 v[44:47], v[104:107], v[156:159], v[44:47]\n\tv_add_u32 %[ta], %[so7], %[pm]\n\t" \
  "ds_read_b64_tr_b16 v[84:85], %[ta] offset:0\n\tv_mfma_f32_16x16x32_bf16 v[24:27], v[104:107], v[124:127], v[24:27]\n\t" \
  "ds_read_b64_tr_b16 v[86:87], %[ta] offset:3200\n\tglobal_load_dwordx4 v[148:151], %[vk], %[oa7] sc1\n\t" \
  "v_mfma_f32_16x16x32_bf16 v[48:51], v[108:111], v[156:159], v[48:51]\n\tds_read_b64_tr_b16 v[88:89], %[ta] offset:8\n\t" \
  "v_mfma_f32_16x16x32_bf16 v[28:31], v[108:111], v[124:127], v[28:31]\n\tds_read_b64_tr_b16 v[90:91], %[ta] offset:3208\n\t" \
  "global_load_dwordx4 v[152:155], %[vk], %[oa8] sc1\n\tv_mfma_f32_16x16x32_bf16 v[52:55], v[112:115], v[156:159], v[52:55]\n\t" \
  "ds_read_b64_tr_b16 v[92:93], %[ta] offset:16\n\tv_mfma_f32_16x16x32_bf16 v[32:35], v[112:115], v[124:127], v[32:35]\n\t" \
  "ds_read_b64_tr_b16 v[94:95], %[ta] offset:3216\n\tv_mfma_f32_16x16x32_bf16 v[56:59], v[116:119], v[156:159], v[56:59]\n\t" \
  "ds_read_b64_tr_b16 v[96:97], %[ta] offset:24\n\tv_mfma_f32_16x16x32_bf16 v[36:39], v[116:119], v[124:127], v[36:39]\n\t" \
  "ds_read_b64_tr_b16 v[98:99], %[ta] offset:3224\n\ts_waitcnt vmcnt(5)\n\tv_max_u32 %[tk], v128, v132\n\tv_cmp_eq_u32 vcc, -1, %[tk]\n\t" \
  "v_mfma_f32_16x16x32_bf16 v[60:63], v[120:123], v[156:159], v[60:63]\n\tds_read_b64_tr_b16 v[100:101], %[ta] offset:32\n\t" \
  "v_mfma_f32_16x16x32_bf16 v[40:43], v[120:123], v[124:127], v[40:43]\n\tds_read_b64_tr_b16 v[102:103], %[ta] offset:3232\n\ts_cbranch_vccnz LSs6_%=\n\t" \
  "LQs6_%=:\n\ts_waitcnt lgkmcnt(10)\n\tv_mfma_f32_16x16x32_bf16 v[44:47], v[64:67], v[128:131], v[44:47]\n\tv_add_u32 %[ta], %[so8], %[pm]\n\t" \
  "ds_read_b64_tr_b16 v[104:105], %[ta] offset:0\n\tv_mfma_f32_16x16x32_bf16 v[24:27], v[64:67], v[132:135], v[24:27]\n\t" \
  "ds_read_b64_tr_b16 v[106:107], %[ta] offset:3200\n\tglobal_load_dwordx4 v[156:159], %[vk], %[oa9] sc1\n\t" \
  "v_mfma_f32_16x16x32_bf16 v[48:51], v[68:71], v[128:131], v[48:51]\n\tds_read_b64_tr_b16 v[108:109], %[ta] offset:8\n\t" \
  "v_mfma_f32_16x16x32_bf16 v[28:31], v[68:71], v[132:135], v[28:31]\n\tds_read_b64_tr_b16 v[110:111], %[ta] offset:3208\n\t" \
  "v_mfma_f32_16x16x32_bf16 v[52:55], v[72:75], v[128:131], v[52:55]\n\tds_read_b64_tr_b16 v[112:113], %[ta] offset:16\n\t" \
  "v_mfma_f32_16x16x32_bf16 v[32:35], v[72:75], v[132:135], v[32:35]\n\tds_read_b64_tr_b16 v[114:115], %[ta] offset:3216\n\t" \
  "v_mfma_f32_16x16x32_bf16 v[56:59], v[76:79], v[128:131], v[56:59]\n\tds_read_b64_tr_b16 v[116:117], %[ta] offset:24\n\t" \
  "v_mfma_f32_16x16x32_bf16 v[36:39], v[76:79], v[132:135], v[36:39]\n\tds_read_b64_tr_b16 v[118:119], %[ta] offset:3224\n\ts_waitcnt vmcnt(4)\n\t" \
  "v_max_u32 %[tk], v136, v140\n\tv_cmp_eq_u32 vcc, -1, %[tk]\n\tv_mfma_f32_16x16x32_bf16 v[60:63], v[80:83], v[128:131], v[60:63]\n\t" \
  "ds_read_b64_tr_b16 v[120:121], %[ta] offset:32\n\tv_mfma_f32_16x16x32_bf16 v[40:43], v[80:83], v[132:135], v[40:43]\n\t" \
  "ds_read_b64_tr_b16 v[122:123], %[ta] offset:3232\n\ts_cbranch_vccnz LSd7_%=\n\tLQd7_%=:\n\ts_waitcnt lgkmcnt(10)\n\t" \
  "v_mfma_f32_16x16x32_bf16 v[44:47], v[84:87], v[136:139], v[44:47]\n\tv_add_u32 %[ta], %[so9], %[pf]\n\tds_read_b64_tr_b16 v[64:65], %[ta] offset:0\n\t" \
  "ds_read_b64_tr_b16 v[66:67], %[ta] offset:12800\n\tv_mfma_f32_16x16x32_bf16 v[48:51], v[88:91], v[136:139], v[48:51]\n\t" \
  "ds_read_b64_tr_b16 v[68:69], %[ta] offset:8\n\tds_read_b64_tr_b16 v[70:71], %[ta] offset:12808\n\t" \
  "v_mfma_f32_16x16x32_bf16 v[52:55], v[92:95], v[136:139], v[52:55]\n\tds_read_b64_tr_b16 v[72:73], %[ta] offset:16\n\t" \
  "ds_read_b64_tr_b16 v[74:75], %[ta] offset:12816\n\tv_mfma_f32_16x16x32_bf16 v[56:59], v[96:99], v[136:139], v[56:59]\n\t" \
  "ds_read_b64_tr_b16 v[76:77], %[ta] offset:24\n\tds_read_b64_tr_b16 v[78:79], %[ta] offset:12824\n\t" \
  "v_mfma_f32_16x16x32_bf16 v[60:63], v[100:103], v[136:139], v[60:63]\n\tds_read_b64_tr_b16 v[80:81], %[ta] offset:32\n\t" \
  "ds_read_b64_tr_b16 v[82:83], %[ta] offset:12832\n\ts_waitcnt lgkmcnt(10)\n\tv_mfma_f32_16x16x32_bf16 v[44:47], v[104:107], v[140:143], v[44:47]\n\t" \
  "v_mfma_f32_16x16x32_bf16 v[48:51], v[108:111], v[140:143], v[48:51]\n\tv_mfma_f32_16x16x32_bf16 v[52:55], v[112:115], v[140:143], v[52:55]\n\t" \
  "s_waitcnt vmcnt(2)\n\tv_max3_u32 %[tk], v144, v146, v148\n\tv_cmp_eq_u32 vcc, -1, %[tk]\n\t" \
  "v_mfma_f32_16x16x32_bf16 v[56:59], v[116:119], v[140:143], v[56:59]\n\tv_mfma_f32_16x16x32_bf16 v[60:63], v[120:123], v[140:143], v[60:63]\n\t" \
  "s_cbranch_vccnz LSd9_%=\n\tLQd9_%=:\n\ts_waitcnt lgkmcnt(0)\n\tv_mfma_f32_16x16x32_bf16 v[44:47], v[64:67], v[144:147], v[44:47]\n\t" \
  "ds_read2_b64 v[124:127], %[aw] offset0:0 offset1:1\n\tds_read2_b64 v[128:131], %[aw] offset0:2 offset1:3\n\t" \
  "ds_read_b64 v[132:133], %[aw] offset:32\n\tv_mov_b64 v[134:135], 0\n\tv_mfma_f32_16x16x32_bf16 v[48:51], v[68:71], v[144:147], v[48:51]\n\t" \
  "v_mfma_f32_16x16x32_bf16 v[52:55], v[72:75], v[144:147], v[52:55]\n\tv_add_u32 %[ta], %[sd], %[l16]\n\t" \
  "v_mfma_f32_16x16x32_bf16 v[56:59], v[76:79], v[144:147], v[56:59]\n\tv_mfma_f32_16x16x32_bf16 v[60:63], v[80:83], v[144:147], v[60:63]\n\t" \
  "v_mov_b32 v137, %[sc]\n\tv_mov_b32 v136, 1\n\tv_mfma_f32_16x16x32_bf16 v[24:27], v[84:87], v[148:151], v[24:27]\n\t" \
  "ds_write_b128 %[ta], v[44:47] offset:0\n\tv_mfma_f32_16x16x32_bf16 v[28:31], v[88:91], v[148:151], v[28:31]\n\t" \
  "ds_write_b128 %[ta], v[48:51] offset:1024\n\tv_mfma_f32_16x16x32_bf16 v[32:35], v[92:95], v[148:151], v[32:35]\n\t" \
  "ds_write_b128 %[ta], v[52:55] offset:2048\n\ts_waitcnt vmcnt(0)\n\tv_max3_u32 %[tk], v152, v156, v158\n\tv_cmp_eq_u32 vcc, -1, %[tk]\n\t" \
  "v_mfma_f32_16x16x32_bf16 v[36:39], v[96:99], v[148:151], v[36:39]\n\tds_write_b128 %[ta], v[56:59] offset:3072\n\t" \
  "v_mfma_f32_16x16x32_bf16 v[40:43], v[100:103], v[148:151], v[40:43]\n\tds_write_b128 %[ta], v[60:63] offset:4096\n\ts_mov_b64 exec, 1\n\t" \
  "ds_add_u32 v137, v136\n\ts_mov_b64 exec, -1\n\ts_cbranch_vccnz LSk8_%=\n\tLQk8_%=:\n\t" \
  "v_mfma_f32_16x16x32_bf16 v[24:27], v[104:107], v[152:155], v[24:27]\n\tv_mov_b64 v[94:95], 0\n\t" \
  "v_mfma_f32_16x16x32_bf16 v[28:31], v[108:111], v[152:155], v[28:31]\n\tv_xor_b32 v96, 4, v137\n\t" \
  "v_mfma_f32_16x16x32_bf16 v[32:35], v[112:115], v[152:155], v[32:35]\n\tv_add_u32 %[tk], %[sr], %[l16]\n\t" \
  "v_mfma_f32_16x16x32_bf16 v[36:39], v[116:119], v[152:155], v[36:39]\n\tv_mfma_f32_16x16x32_bf16 v[40:43], v[120:123], v[152:155], v[40:43]\n\t" \
  "v_mfma_f32_16x16x32_bf16 v[24:27], v[64:67], v[156:159], v[24:27]\n\tds_read_b32 v97, v96\n\tds_read_b128 v[44:47], %[tk] offset:0\n\t" \
  "ds_read_b128 v[48:51], %[tk] offset:1024\n\tds_read_b128 v[52:55], %[tk] offset:2048\n\tds_read_b128 v[56:59], %[tk] offset:3072\n\t" \
  "ds_read_b128 v[60:63], %[tk] offset:4096\n\tv_mfma_f32_16x16x32_bf16 v[28:31], v[68:71], v[156:159], v[28:31]\n\t" \
  "v_mfma_f32_16x16x32_bf16 v[32:35], v[72:75], v[156:159], v[32:35]\n\tv_mfma_f32_16x16x32_bf16 v[36:39], v[76:79], v[156:159], v[36:39]\n\t" \
  "v_mfma_f32_16x16x32_bf16 v[40:43], v[80:83], v[156:159], v[40:43]\n\tLD1_%=:\n\ts_waitcnt lgkmcnt(0)\n\tv_cmp_eq_u32 vcc, 0, v97\n\t" \
  "s_cbranch_vccnz LD2_%=\n\tLT_%=:\n\tv_mov_b32 v137, %[sa]\n\tds_read_b32 v138, v137 offset:16\n\tv_add_f32 v24, v24, v44\n\t" \
  "v_add_f32 v25, v25, v45\n\tv_add_f32 v26, v26, v46\n\tv_add_f32 v27, v27, v47\n\tv_cvt_pk_bf16_f32 v84, v24, v25\n\t" \
  "v_cvt_pk_bf16_f32 v85, v26, v27\n\tv_add_f32 v28, v28, v48\n\tv_add_f32 v29, v29, v49\n\tv_add_f32 v30, v30, v50\n\tv_add_f32 v31, v31, v51\n\t" \
  "v_cvt_pk_bf16_f32 v86, v28, v29\n\tv_cvt_pk_bf16_f32 v87, v30, v31\n\tv_add_f32 v32, v32, v52\n\tv_add_f32 v33, v33, v53\n\t" \
  "v_add_f32 v34, v34, v54\n\tv_add_f32 v35, v35, v55\n\tv_cvt_pk_bf16_f32 v88, v32, v33\n\tv_cvt_pk_bf16_f32 v89, v34, v35\n\t" \
  "v_mfma_f32_16x16x32_bf16 v[100:103], v[84:87], v[84:87], 0\n\tv_mfma_f32_16x16x32_bf16 v[96:99], v[124:127], v[84:87], 0\n\t" \
  "v_add_f32 v36, v36, v56\n\tv_add_f32 v37, v37, v57\n\tv_add_f32 v38, v38, v58\n\tv_add_f32 v39, v39, v59\n\tv_cvt_pk_bf16_f32 v90, v36, v37\n\t" \
  "v_cvt_pk_bf16_f32 v91, v38, v39\n\tv_add_f32 v40, v40, v60\n\tv_add_f32 v41, v41, v61\n\tv_add_f32 v42, v42, v62\n\tv_add_f32 v43, v43, v63\n\t" \
  "v_cvt_pk_bf16_f32 v92, v40, v41\n\tv_cvt_pk_bf16_f32 v93, v42, v43\n\tv_mfma_f32_16x16x32_bf16 v[100:103], v[88:91], v[88:91], v[100:103]\n\t" \
  "v_mfma_f32_16x16x32_bf16 v[96:99], v[128:131], v[88:91], v[96:99]\n\tv_mfma_f32_16x16x32_bf16 v[100:103], v[92:95], v[92:95], v[100:103]\n\t" \
  "v_mfma_f32_16x16x32_bf16 v[96:99], v[132:135], v[92:95], v[96:99]\n\ts_waitcnt lgkmcnt(0)\n\tv_sub_u32 v138, %[ra], v138\n\t" \
  "v_cmp_gt_u32 vcc, 0xa0, v138\n\tv_and_b32 %[ta], 0xff, %[l16]\n\tv_add_u32 %[tk], %[sp], %[ta]\n\tv_mov_b32 v136, 1\n\ts_cbranch_vccz LP1_%=\n\t" \
  "s_mov_b64 exec, vcc\n\tglobal_store_dwordx4 v138, v[84:87], %[cp] sc1\n\tglobal_store_dwordx4 v138, v[88:91], %[cp] offset:16 sc1\n\t" \
  "global_store_dwordx2 v138, v[92:93], %[cp] offset:32 sc1\n\tds_write_b32 v137, v136 offset:24\n\tLP1_%=:\n\ts_mov_b32 exec_lo, 0x00200002\n\t" \
  "s_mov_b32 exec_hi, 0x20000200\n\tv_mov_b32 v100, v101\n\ts_lshl_b64 exec, exec, 1\n\tv_mov_b32 v100, v102\n\ts_lshl_b64 exec, exec, 1\n\t" \
  "v_mov_b32 v100, v103\n\ts_mov_b32 exec_lo, 0x00f0000f\n\ts_mov_b32 exec_hi, 0xf0000f00\n\tv_mov_b32 v98, v100\n\t" \
  "global_store_dwordx4 %[ta], v[96:99], %[sst] sc1\n\tds_write_b128 %[tk], v[96:99]\n\ts_mov_b64 exec, -1\n\tds_write_b64 %[ra], v[84:85] offset:0\n\t" \
  "ds_write_b64 %[ra], v[86:87] offset:8\n\tds_write_b64 %[ra], v[88:89] offset:16\n\tds_write_b64 %[ra], v[90:91] offset:24\n\t" \
  "ds_write_b64 %[ra], v[92:93] offset:32\n\ts_mov_b64 exec, 1\n\tds_add_u32 v137, v136\n\ts_mov_b64 exec, -1\n\ts_setprio 0\n\ts_branch LZ_%=\n\t" \
  "LD2_%=:\n\ts_sub_u32 s43, s43, 1\n\ts_cbranch_scc1 LT_%=\n\tds_read_b32 v97, v96\n\tds_read_b128 v[44:47], %[tk] offset:0\n\t" \
  "ds_read_b128 v[48:51], %[tk] offset:1024\n\tds_read_b128 v[52:55], %[tk] offset:2048\n\tds_read_b128 v[56:59], %[tk] offset:3072\n\t" \
  "ds_read_b128 v[60:63], %[tk] offset:4096\n\ts_branch LD1_%=\n\tLH_%=:\n\ts_sleep 3\n\tglobal_load_dwordx4 v[140:143], %[vk], %[oa1] sc1\n\t" \
  "global_load_dwordx4 v[144:147], %[vk], %[oa2] sc1\n\tglobal_load_dwordx4 v[148:151], %[vk], %[oa3] sc1\n\t" \
  "global_load_dwordx4 v[152:155], %[vk], %[oa4] sc1\n\tglobal_load_dwordx4 v[156:159], %[vk], %[oa0] sc1\n\ts_cmp_eq_u32 %[w], 3\n\t" \
  "s_cbranch_scc0 LG_%=\n\tds_read_b64 v[112:113], %[ra] offset:0\n\tds_read_b64 v[114:115], %[ra] offset:8\n\t" \
  "ds_read_b64 v[116:117], %[ra] offset:16\n\tds_read_b64 v[118:119], %[ra] offset:24\n\tds_read_b64 v[120:121], %[ra] offset:32\n\t" \
  "ds_read2_b64 v[124:127], %[aw] offset0:0 offset1:1\n\tds_read2_b64 v[128:131], %[aw] offset0:2 offset1:3\n\t" \
  "ds_read_b64 v[132:133], %[aw] offset:32\n\tv_mov_b64 v[134:135], 0\n\ts_waitcnt lgkmcnt(3)\n\tv_lshlrev_b32 v24, 16, v112\n\t" \
  "v_and_b32 v25, 0xffff0000, v112\n\tv_lshlrev_b32 v26, 16, v113\n\tv_and_b32 v27, 0xffff0000, v113\n\tv_lshlrev_b32 v28, 16, v114\n\t" \
  "v_and_b32 v29, 0xffff0000, v114\n\tv_lshlrev_b32 v30, 16, v115\n\tv_and_b32 v31, 0xffff0000, v115\n\tv_lshlrev_b32 v32, 16, v116\n\t" \
  "v_and_b32 v33, 0xffff0000, v116\n\tv_lshlrev_b32 v34, 16, v117\n\tv_and_b32 v35, 0xffff0000, v117\n\tv_lshlrev_b32 v36, 16, v118\n\t" \
  "v_and_b32 v37, 0xffff0000, v118\n\tv_lshlrev_b32 v38, 16, v119\n\tv_and_b32 v39, 0xffff0000, v119\n\tv_lshlrev_b32 v40, 16, v120\n\t" \
  "v_and_b32 v41, 0xffff0000, v120\n\tv_lshlrev_b32 v42, 16, v121\n\tv_and_b32 v43, 0xffff0000, v121\n\ts_branch LG2_%=\n\tLG_%=:\n\t" \
  "v_mov_b64 v[24:25], 0\n\tv_mov_b64 v[26:27], 0\n\tv_mov_b64 v[28:29], 0\n\tv_mov_b64 v[30:31], 0\n\tv_mov_b64 v[32:33], 0\n\tv_mov_b64 v[34:35], 0\n\t" \
  "v_mov_b64 v[36:37], 0\n\tv_mov_b64 v[38:39], 0\n\tv_mov_b64 v[40:41], 0\n\tv_mov_b64 v[42:43], 0\n\tLG2_%=:\n\tv_add_u32 %[ta], %[so1], %[pm]\n\t" \
  "ds_read_b64_tr_b16 v[64:65], %[ta] offset:0\n\tds_read_b64_tr_b16 v[66:67], %[ta] offset:3200\n\tds_read_b64_tr_b16 v[68:69], %[ta] offset:8\n\t" \
  "ds_read_b64_tr_b16 v[70:71], %[ta] offset:3208\n\tds_read_b64_tr_b16 v[72:73], %[ta] offset:16\n\tds_read_b64_tr_b16 v[74:75], %[ta] offset:3216\n\t" \
  "ds_read_b64_tr_b16 v[76:77], %[ta] offset:24\n\tds_read_b64_tr_b16 v[78:79], %[ta] offset:3224\n\tds_read_b64_tr_b16 v[80:81], %[ta] offset:32\n\t" \
  "ds_read_b64_tr_b16 v[82:83], %[ta] offset:3232\n\tv_add_u32 %[ta], %[so2], %[pm]\n\tds_read_b64_tr_b16 v[84:85], %[ta] offset:0\n\t" \
  "ds_read_b64_tr_b16 v[86:87], %[ta] offset:3200\n\tds_read_b64_tr_b16 v[88:89], %[ta] offset:8\n\tds_read_b64_tr_b16 v[90:91], %[ta] offset:3208\n\t" \
  "ds_read_b64_tr_b16 v[92:93], %[ta] offset:16\n\tds_read_b64_tr_b16 v[94:95], %[ta] offset:3216\n\tds_read_b64_tr_b16 v[96:97], %[ta] offset:24\n\t" \
  "ds_read_b64_tr_b16 v[98:99], %[ta] offset:3224\n\tds_read_b64_tr_b16 v[100:101], %[ta] offset:32\n\t" \
  "ds_read_b64_tr_b16 v[102:103], %[ta] offset:3232\n\ts_waitcnt vmcnt(4)\n\tv_cmp_eq_u32 vcc, -1, v140\n\ts_cbranch_vccnz LSh1_%=\n\tLQh1_%=:\n\t" \
  "s_waitcnt lgkmcnt(10)\n\tv_mfma_f32_16x16x32_bf16 v[24:27], v[64:67], v[140:143], v[24:27]\n\tv_add_u32 %[ta], %[so3], %[pm]\n\t" \
  "ds_read_b64_tr_b16 v[104:105], %[ta] offset:0\n\tds_read_b64_tr_b16 v[106:107], %[ta] offset:3200\n\t" \
  "v_mfma_f32_16x16x32_bf16 v[28:31], v[68:71], v[140:143], v[28:31]\n\tds_read_b64_tr_b16 v[108:109], %[ta] offset:8\n\t" \
  "ds_read_b64_tr_b16 v[110:111], %[ta] offset:3208\n\tv_mfma_f32_16x16x32_bf16 v[32:35], v[72:75], v[140:143], v[32:35]\n\t" \
  "ds_read_b64_tr_b16 v[112:113], %[ta] offset:16\n\tds_read_b64_tr_b16 v[114:115], %[ta] offset:3216\n\t" \
  "v_mfma_f32_16x16x32_bf16 v[36:39], v[76:79], v[140:143], v[36:39]\n\tds_read_b64_tr_b16 v[116:117], %[ta] offset:24\n\t" \
  "ds_read_b64_tr_b16 v[118:119], %[ta] offset:3224\n\ts_waitcnt vmcnt(3)\n\tv_cmp_eq_u32 vcc, -1, v144\n\t" \
  "v_mfma_f32_16x16x32_bf16 v[40:43], v[80:83], v[140:143], v[40:43]\n\tds_read_b64_tr_b16 v[120:121], %[ta] offset:32\n\t" \
  "ds_read_b64_tr_b16 v[122:123], %[ta] offset:3232\n\ts_cbranch_vccnz LSh2_%=\n\tLQh2_%=:\n\ts_waitcnt lgkmcnt(10)\n\t" \
  "v_mfma_f32_16x16x32_bf16 v[24:27], v[84:87], v[144:147], v[24:27]\n\tv_add_u32 %[ta], %[so4], %[pf]\n\tds_read_b64_tr_b16 v[64:65], %[ta] offset:0\n\t" \
  "ds_read_b64_tr_b16 v[66:67], %[ta] offset:12800\n\tv_mfma_f32_16x16x32_bf16 v[28:31], v[88:91], v[144:147], v[28:31]\n\t" \
  "ds_read_b64_tr_b16 v[68:69], %[ta] offset:8\n\tds_read_b64_tr_b16 v[70:71], %[ta] offset:12808\n\t" \
  "v_mfma_f32_16x16x32_bf16 v[32:35], v[92:95], v[144:147], v[32:35]\n\tds_read_b64_tr_b16 v[72:73], %[ta] offset:16\n\t" \
  "ds_read_b64_tr_b16 v[74:75], %[ta] offset:12816\n\tv_mfma_f32_16x16x32_bf16 v[36:39], v[96:99], v[144:147], v[36:39]\n\t" \
  "ds_read_b64_tr_b16 v[76:77], %[ta] offset:24\n\tds_read_b64_tr_b16 v[78:79], %[ta] offset:12824\n\ts_waitcnt vmcnt(2)\n\t" \
  "v_cmp_eq_u32 vcc, -1, v148\n\tv_mfma_f32_16x16x32_bf16 v[40:43], v[100:103], v[144:147], v[40:43]\n\tds_read_b64_tr_b16 v[80:81], %[ta] offset:32\n\t" \
  "ds_read_b64_tr_b16 v[82:83], %[ta] offset:12832\n\ts_cbranch_vccnz LSh3_%=\n\tLQh3_%=:\n\ts_waitcnt lgkmcnt(10)\n\t" \
  "v_mfma_f32_16x16x32_bf16 v[24:27], v[104:107], v[148:151], v[24:27]\n\tv_add_u32 %[ta], %[so0], %[pm]\n\t" \
  "ds_read_b64_tr_b16 v[84:85], %[ta] offset:0\n\tds_read_b64_tr_b16 v[86:87], %[ta] offset:3200\n\t" \
  "v_mfma_f32_16x16x32_bf16 v[28:31], v[108:111], v[148:151], v[28:31]\n\tds_read_b64_tr_b16 v[88:89], %[ta] offset:8\n\t" \
  "ds_read_b64_tr_b16 v[90:91], %[ta] offset:3208\n\tv_mfma_f32_16x16x32_bf16 v[32:35], v[112:115], v[148:151], v[32:35]\n\t" \
  "ds_read_b64_tr_b16 v[92:93], %[ta] offset:16\n\tds_read_b64_tr_b16 v[94:95], %[ta] offset:3216\n\t" \
  "v_mfma_f32_16x16x32_bf16 v[36:39], v[116:119], v[148:151], v[36:39]\n\tds_read_b64_tr_b16 v[96:97], %[ta] offset:24\n\t" \
  "ds_read_b64_tr_b16 v[98:99], %[ta] offset:3224\n\ts_waitcnt vmcnt(1)\n\tv_max_u32 %[tk], v152, v154\n\tv_cmp_eq_u32 vcc, -1, %[tk]\n\t" \
  "v_mfma_f32_16x16x32_bf16 v[40:43], v[120:123], v[148:151], v[40:43]\n\tds_read_b64_tr_b16 v[100:101], %[ta] offset:32\n\t" \
  "ds_read_b64_tr_b16 v[102:103], %[ta] offset:3232\n\ts_cbranch_vccnz LSh4_%=\n\tLQh4_%=:\n\ts_waitcnt lgkmcnt(10)\n\t" \
  "v_mfma_f32_16x16x32_bf16 v[24:27], v[64:67], v[152:155], v[24:27]\n\tv_mfma_f32_16x16x32_bf16 v[28:31], v[68:71], v[152:155], v[28:31]\n\t" \
  "v_mfma_f32_16x16x32_bf16 v[32:35], v[72:75], v[152:155], v[32:35]\n\tv_mfma_f32_16x16x32_bf16 v[36:39], v[76:79], v[152:155], v[36:39]\n\t" \
  "s_waitcnt vmcnt(0)\n\tv_cmp_eq_u32 vcc, -1, v156\n\tv_mfma_f32_16x16x32_bf16 v[40:43], v[80:83], v[152:155], v[40:43]\n\ts_cbranch_vccnz LSh0_%=\n\t" \
  "LQh0_%=:\n\ts_waitcnt lgkmcnt(0)\n\tv_mfma_f32_16x16x32_bf16 v[24:27], v[84:87], v[156:159], v[24:27]\n\t" \
  "v_mfma_f32_16x16x32_bf16 v[28:31], v[88:91], v[156:159], v[28:31]\n\tv_mfma_f32_16x16x32_bf16 v[32:35], v[92:95], v[156:159], v[32:35]\n\t" \
  "v_mfma_f32_16x16x32_bf16 v[36:39], v[96:99], v[156:159], v[36:39]\n\tv_mfma_f32_16x16x32_bf16 v[40:43], v[100:103], v[156:159], v[40:43]\n\t" \
  "s_cmp_eq_u32 %[w], 3\n\ts_cbranch_scc1 LF_%=\n\ts_setprio 3\n\tv_add_u32 %[ta], %[sd], %[l16]\n\tv_mov_b32 v137, %[sc]\n\tv_mov_b32 v136, 1\n\t" \
  "ds_write_b128 %[ta], v[24:27] offset:0\n\tds_write_b128 %[ta], v[28:31] offset:1024\n\tds_write_b128 %[ta], v[32:35] offset:2048\n\t" \
  "ds_write_b128 %[ta], v[36:39] offset:3072\n\tds_write_b128 %[ta], v[40:43] offset:4096\n\ts_mov_b64 exec, 1\n\tds_add_u32 v137, v136\n\t" \
  "s_mov_b64 exec, -1\n\ts_branch LZ_%=\n\tLF_%=:\n\ts_setprio 3\n\tv_mov_b64 v[94:95], 0\n\tv_mov_b32 v96, %[sc]\n\tv_add_u32 %[ta], %[sr], %[l16]\n\t" \
  "LF1_%=:\n\tds_read_b32 v97, v96\n\ts_waitcnt lgkmcnt(0)\n\tv_cmp_gt_u32 vcc, 3, v97\n\ts_cbranch_vccz LF2_%=\n\ts_sleep 1\n\ts_sub_u32 s43, s43, 1\n\t" \
  "s_cbranch_scc0 LF1_%=\n\tLF2_%=:\n\tds_read_b128 v[64:67], %[ta] offset:0\n\tds_read_b128 v[68:71], %[ta] offset:1024\n\t" \
  "ds_read_b128 v[72:75], %[ta] offset:2048\n\tds_read_b128 v[76:79], %[ta] offset:3072\n\tds_read_b128 v[80:83], %[ta] offset:4096\n\t" \
  "ds_read_b128 v[104:107], %[ta] offset:5120\n\tds_read_b128 v[108:111], %[ta] offset:6144\n\tds_read_b128 v[112:115], %[ta] offset:7168\n\t" \
  "ds_read_b128 v[116:119], %[ta] offset:8192\n\tds_read_b128 v[120:123], %[ta] offset:9216\n\tds_read_b128 v[44:47], %[ta] offset:10240\n\t" \
  "ds_read_b128 v[48:51], %[ta] offset:11264\n\tds_read_b128 v[52:55], %[ta] offset:12288\n\tds_read_b128 v[56:59], %[ta] offset:13312\n\t" \
  "ds_read_b128 v[60:63], %[ta] offset:14336\n\ts_waitcnt lgkmcnt(10)\n\tv_pk_add_f32 v[24:25], v[24:25], v[64:65]\n\t" \
  "v_pk_add_f32 v[26:27], v[26:27], v[66:67]\n\tv_pk_add_f32 v[28:29], v[28:29], v[68:69]\n\tv_pk_add_f32 v[30:31], v[30:31], v[70:71]\n\t" \
  "v_pk_add_f32 v[32:33], v[32:33], v[72:73]\n\tv_pk_add_f32 v[34:35], v[34:35], v[74:75]\n\tv_pk_add_f32 v[36:37], v[36:37], v[76:77]\n\t" \
  "v_pk_add_f32 v[38:39], v[38:39], v[78:79]\n\tv_pk_add_f32 v[40:41], v[40:41], v[80:81]\n\tv_pk_add_f32 v[42:43], v[42:43], v[82:83]\n\t" \
  "s_waitcnt lgkmcnt(5)\n\tv_pk_add_f32 v[24:25], v[24:25], v[104:105]\n\tv_pk_add_f32 v[26:27], v[26:27], v[106:107]\n\t" \
  "v_pk_add_f32 v[28:29], v[28:29], v[108:109]\n\tv_pk_add_f32 v[30:31], v[30:31], v[110:111]\n\tv_pk_add_f32 v[32:33], v[32:33], v[112:113]\n\t" \
  "v_pk_add_f32 v[34:35], v[34:35], v[114:115]\n\tv_pk_add_f32 v[36:37], v[36:37], v[116:117]\n\tv_pk_add_f32 v[38:39], v[38:39], v[118:119]\n\t" \
  "v_pk_add_f32 v[40:41], v[40:41], v[120:121]\n\tv_pk_add_f32 v[42:43], v[42:43], v[122:123]\n\ts_waitcnt lgkmcnt(0)\n\ts_branch LT_%=\n\tLSs1_%=:\n\t" \
  "s_sub_u32 s43, s43, 1\n\ts_cbranch_scc1 LQs1_%=\n\ts_sleep %[oback]\n\tglobal_load_dwordx4 v[124:127], %[vd], %[oa1] sc1\n\t" \
  "global_load_dwordx4 v[128:131], %[vk], %[oa1] sc1\n\ts_waitcnt vmcnt(0)\n\tv_max_u32 %[tk], v124, v128\n\tv_cmp_eq_u32 vcc, -1, %[tk]\n\t" \
  "s_cbranch_vccnz LSs1_%=\n\ts_branch LQs1_%=\n\tLSs2_%=:\n\ts_sub_u32 s43, s43, 1\n\ts_cbranch_scc1 LQs2_%=\n\ts_sleep %[oback]\n\t" \
  "global_load_dwordx4 v[132:135], %[vd], %[oa2] sc1\n\tglobal_load_dwordx4 v[136:139], %[vk], %[oa2] sc1\n\ts_waitcnt vmcnt(0)\n\t" \
  "v_max_u32 %[tk], v132, v136\n\tv_cmp_eq_u32 vcc, -1, %[tk]\n\ts_cbranch_vccnz LSs2_%=\n\ts_branch LQs2_%=\n\tLSs3_%=:\n\ts_sub_u32 s43, s43, 1\n\t" \
  "s_cbranch_scc1 LQs3_%=\n\ts_sleep %[oback]\n\tglobal_load_dwordx4 v[140:143], %[vd], %[oa3] sc1\n\t" \
  "global_load_dwordx4 v[144:147], %[vk], %[oa3] sc1\n\ts_waitcnt vmcnt(0)\n\tv_max_u32 %[tk], v140, v144\n\tv_cmp_eq_u32 vcc, -1, %[tk]\n\t" \
  "s_cbranch_vccnz LSs3_%=\n\ts_branch LQs3_%=\n\tLSs4_%=:\n\ts_sub_u32 s43, s43, 1\n\ts_cbranch_scc1 LQs4_%=\n\ts_sleep %[oback]\n\t" \
  "global_load_dwordx4 v[148:151], %[vd], %[oa4] sc1\n\tglobal_load_dwordx4 v[152:155], %[vk], %[oa4] sc1\n\ts_waitcnt vmcnt(0)\n\t" \
  "v_max3_u32 %[tk], v148, v150, v152\n\tv_max_u32 %[tk], %[tk], v154\n\tv_cmp_eq_u32 vcc, -1, %[tk]\n\ts_cbranch_vccnz LSs4_%=\n\ts_branch LQs4_%=\n\t" \
  "LSs5_%=:\n\ts_sub_u32 s43, s43, 1\n\ts_cbranch_scc1 LQs5_%=\n\ts_sleep %[oback]\n\tglobal_load_dwordx4 v[156:159], %[vd], %[oa5] sc1\n\t" \
  "global_load_dwordx4 v[124:127], %[vk], %[oa5] sc1\n\ts_waitcnt vmcnt(0)\n\tv_max_u32 %[tk], v156, v124\n\tv_cmp_eq_u32 vcc, -1, %[tk]\n\t" \
  "s_cbranch_vccnz LSs5_%=\n\ts_branch LQs5_%=\n\tLSs6_%=:\n\ts_sub_u32 s43, s43, 1\n\ts_cbranch_scc1 LQs6_%=\n\ts_sleep %[oback]\n\t" \
  "global_load_dwordx4 v[128:131], %[vd], %[oa6] sc1\n\tglobal_load_dwordx4 v[132:135], %[vk], %[oa6] sc1\n\ts_waitcnt vmcnt(0)\n\t" \
  "v_max_u32 %[tk], v128, v132\n\tv_cmp_eq_u32 vcc, -1, %[tk]\n\ts_cbranch_vccnz LSs6_%=\n\ts_branch LQs6_%=\n\tLSd7_%=:\n\ts_sub_u32 s43, s43, 1\n\t" \
  "s_cbranch_scc1 LQd7_%=\n\ts_sleep %[oback]\n\tglobal_load_dwordx4 v[136:139], %[vd], %[oa7] sc1\n\t" \
  "global_load_dwordx4 v[140:143], %[vd], %[oa8] sc1\n\ts_waitcnt vmcnt(0)\n\tv_max_u32 %[tk], v136, v140\n\tv_cmp_eq_u32 vcc, -1, %[tk]\n\t" \
  "s_cbranch_vccnz LSd7_%=\n\ts_branch LQd7_%=\n\tLSd9_%=:\n\ts_sub_u32 s43, s43, 1\n\ts_cbranch_scc1 LQd9_%=\n\ts_sleep %[oback]\n\t" \
  "global_load_dwordx4 v[144:147], %[vd], %[oa9] sc1\n\tglobal_load_dwordx4 v[148:151], %[vk], %[oa7] sc1\n\ts_waitcnt vmcnt(0)\n\t" \
  "v_max3_u32 %[tk], v144, v146, v148\n\tv_cmp_eq_u32 vcc, -1, %[tk]\n\ts_cbranch_vccnz LSd9_%=\n\ts_branch LQd9_%=\n\tLSk8_%=:\n\t" \
  "s_sub_u32 s43, s43, 1\n\ts_cbranch_scc1 LQk8_%=\n\ts_sleep %[oback]\n\tglobal_load_dwordx4 v[152:155], %[vk], %[oa8] sc1\n\t" \
  "global_load_dwordx4 v[156:159], %[vk], %[oa9] sc1\n\ts_waitcnt vmcnt(0)\n\tv_max3_u32 %[tk], v152, v156, v158\n\tv_cmp_eq_u32 vcc, -1, %[tk]\n\t" \
  "s_cbranch_vccnz LSk8_%=\n\ts_branch LQk8_%=\n\tLSh1_%=:\n\ts_sub_u32 s43, s43, 1\n\ts_cbranch_scc1 LQh1_%=\n\ts_sleep %[oback]\n\t" \
  "global_load_dwordx4 v[140:143], %[vk], %[oa1] sc1\n\ts_waitcnt vmcnt(0)\n\tv_cmp_eq_u32 vcc, -1, v140\n\ts_cbranch_vccnz LSh1_%=\n\t" \
  "s_branch LQh1_%=\n\tLSh2_%=:\n\ts_sub_u32 s43, s43, 1\n\ts_cbranch_scc1 LQh2_%=\n\ts_sleep %[oback]\n\t" \
  "global_load_dwordx4 v[144:147], %[vk], %[oa2] sc1\n\ts_waitcnt vmcnt(0)\n\tv_cmp_eq_u32 vcc, -1, v144\n\ts_cbranch_vccnz LSh2_%=\n\t" \
  "s_branch LQh2_%=\n\tLSh3_%=:\n\ts_sub_u32 s43, s43, 1\n\ts_cbranch_scc1 LQh3_%=\n\ts_sleep %[oback]\n\t" \
  "global_load_dwordx4 v[148:151], %[vk], %[oa3] sc1\n\ts_waitcnt vmcnt(0)\n\tv_cmp_eq_u32 vcc, -1, v148\n\ts_cbranch_vccnz LSh3_%=\n\t" \
  "s_branch LQh3_%=\n\tLSh4_%=:\n\ts_sub_u32 s43, s43, 1\n\ts_cbranch_scc1 LQh4_%=\n\ts_sleep %[oback]\n\t" \
  "global_load_dwordx4 v[152:155], %[vk], %[oa4] sc1\n\ts_waitcnt vmcnt(0)\n\tv_max_u32 %[tk], v152, v154\n\tv_cmp_eq_u32 vcc, -1, %[tk]\n\t" \
  "s_cbranch_vccnz LSh4_%=\n\ts_branch LQh4_%=\n\tLSh0_%=:\n\ts_sub_u32 s43, s43, 1\n\ts_cbranch_scc1 LQh0_%=\n\ts_sleep %[oback]\n\t" \
  "global_load_dwordx4 v[156:159], %[vk], %[oa0] sc1\n\ts_waitcnt vmcnt(0)\n\tv_cmp_eq_u32 vcc, -1, v156\n\ts_cbranch_vccnz LSh0_%=\n\t" \
  "s_branch LQh0_%=\n\tLZ_%=:\n\t" \
  ""
#define COL_ASM \
  "s_mov_b32 s43, %[cap]\n\tv_xor_b32 v101, 0x400, %[lq]\n\tv_xor_b32 v102, 0x800, %[lq]\n\tv_xor_b32 v103, 0xc00, %[lq]\n\t" \
  "v_xor_b32 v104, 0x1000, %[lq]\n\tv_xor_b32 v105, 0x1400, %[lq]\n\tv_xor_b32 v106, 0x1800, %[lq]\n\tv_xor_b32 v107, 0x1c00, %[lq]\n\tLC0_%=:\n\t" \
  "ds_read_b32 v96, %[ac]\n\ts_waitcnt lgkmcnt(0)\n\tv_cmp_gt_u32 vcc, %[nf], v96\n\ts_cbranch_vccz LC1_%=\n\ts_sleep 1\n\ts_sub_u32 s43, s43, 1\n\t" \
  "s_cbranch_scc0 LC0_%=\n\tLC1_%=:\n\ts_setprio 2\n\ts_sleep %[pdel]\n\tglobal_load_dwordx4 v[68:71], v101, %[sp] sc1\n\t" \
  "global_load_dwordx4 v[72:75], v102, %[sp] sc1\n\tglobal_load_dwordx4 v[76:79], v103, %[sp] sc1\n\tglobal_load_dwordx4 v[80:83], v104, %[sp] sc1\n\t" \
  "global_load_dwordx4 v[84:87], v105, %[sp] sc1\n\tglobal_load_dwordx4 v[88:91], v106, %[sp] sc1\n\tglobal_load_dwordx4 v[92:95], v107, %[sp] sc1\n\t" \
  "ds_read_b128 v[64:67], %[ao]\n\ts_waitcnt lgkmcnt(0)\n\tLC2_%=:\n\ts_waitcnt vmcnt(4)\n\tv_pk_add_f32 v[108:109], v[64:65], v[68:69]\n\t" \
  "v_add_f32 v110, v66, v70\n\tv_pk_add_f32 v[112:113], v[72:73], v[76:77]\n\tv_add_f32 v114, v74, v78\n\t" \
  "v_pk_add_f32 v[108:109], v[108:109], v[112:113]\n\tv_add_f32 v110, v110, v114\n\ts_waitcnt vmcnt(0)\n\tv_pk_add_f32 v[116:117], v[80:81], v[84:85]\n\t" \
  "v_add_f32 v118, v82, v86\n\tv_pk_add_f32 v[120:121], v[88:89], v[92:93]\n\tv_add_f32 v122, v90, v94\n\t" \
  "v_pk_add_f32 v[116:117], v[116:117], v[120:121]\n\tv_add_f32 v118, v118, v122\n\tv_pk_add_f32 v[108:109], v[108:109], v[116:117]\n\t" \
  "v_add_f32 v110, v110, v118\n\tv_mul_f32 v112, 0x3acccccd, v108\n\tv_add_f32 v111, v108, v109\n\tv_mul_f32 v113, v112, v112\n\t" \
  "v_cmp_u_f32 vcc, v111, v110\n\tv_fma_f32 v113, v110, %[rdm], -v113\n\ts_cbranch_vccnz LC5_%=\n\tLC3_%=:\n\tv_max_f32 v113, 0, v113\n\t" \
  "v_cvt_pk_bf16_f32 v113, 0, v113\n\tv_add_f32 v113, 0x3727c5ac, v113\n\tv_cvt_pk_bf16_f32 v113, 0, v113\n\tv_sqrt_f32 v113, v113\n\t" \
  "v_cvt_pk_bf16_f32 v112, 0, v112\n\tv_cvt_pk_bf16_f32 v113, 0, v113\n\tv_rcp_f32 v113, v113\n\tv_fma_f32 v114, -%[swr], v112, v109\n\t" \
  "v_mul_f32 v114, v114, v113\n\tv_ashrrev_i32 v115, 31, v114\n\tv_or_b32 v115, 0x80000000, v115\n\tv_xor_b32 v114, v114, v115\n\t" \
  "v_and_or_b32 v114, v114, %[msk], %[rid]\n\ts_nop 1\n\tv_max_u32_dpp v114, v114, v114 quad_perm:[1,0,3,2] row_mask:0xf bank_mask:0xf bound_ctrl:1\n\t" \
  "ds_write_b64 %[at], v[112:113]\n\ts_nop 0\n\tv_max_u32_dpp v114, v114, v114 quad_perm:[2,3,0,1] row_mask:0xf bank_mask:0xf bound_ctrl:1\n\ts_nop 1\n\t" \
  "v_max_u32_dpp v114, v114, v114 row_half_mirror row_mask:0xf bank_mask:0xf bound_ctrl:1\n\ts_nop 1\n\t" \
  "v_max_u32_dpp v114, v114, v114 row_mirror row_mask:0xf bank_mask:0xf bound_ctrl:1\n\ts_nop 0\n\tv_readlane_b32 s40, v114, 0\n\t" \
  "v_readlane_b32 s41, v114, 16\n\tv_readlane_b32 s42, v114, 32\n\ts_max_u32 s40, s40, s41\n\ts_max_u32 s40, s40, s42\n\tv_mov_b32 v98, s40\n\t" \
  "s_mov_b32 exec_lo, 0x01010101\n\ts_mov_b32 exec_hi, 0x0101\n\tglobal_store_dwordx2 %[zr], v[98:99], %[kp] sc1\n\tds_write_b32 %[ak], v98\n\t" \
  "s_mov_b64 exec, -1\n\ts_and_b32 s41, s40, 0x7ff\n\ts_sub_u32 s41, %[rb], s41\n\ts_mul_i32 s42, s41, 160\n\tv_add_u32 v115, s42, %[ah]\n\t" \
  "ds_read_b64 v[116:117], v115\n\tv_readlane_b32 s44, v112, s41\n\tv_readlane_b32 s46, v113, s41\n\ts_waitcnt lgkmcnt(0)\n\ts_mov_b32 exec_hi, 0\n\t" \
  "v_lshlrev_b32 v118, 16, v116\n\tv_and_b32 v119, 0xffff0000, v116\n\tv_lshlrev_b32 v120, 16, v117\n\tv_and_b32 v121, 0xffff0000, v117\n\t" \
  "v_pk_add_f32 v[122:123], v[118:119], s[44:45] op_sel_hi:[1,0] neg_lo:[0,1] neg_hi:[0,1]\n\t" \
  "v_pk_add_f32 v[114:115], v[120:121], s[44:45] op_sel_hi:[1,0] neg_lo:[0,1] neg_hi:[0,1]\n\t" \
  "v_pk_fma_f32 v[118:119], v[122:123], s[46:47], v[118:119] op_sel_hi:[1,0,1]\n\t" \
  "v_pk_fma_f32 v[120:121], v[114:115], s[46:47], v[120:121] op_sel_hi:[1,0,1]\n\tv_cvt_pk_bf16_f32 v116, v118, v119\n\t" \
  "v_cvt_pk_bf16_f32 v117, v120, v121\n\ts_mov_b32 exec_hi, -1\n\tglobal_store_dwordx2 %[co], v[116:117], %[cp] sc1\n\ts_branch LC9_%=\n\tLC5_%=:\n\t" \
  "s_sub_u32 s43, s43, 1\n\ts_cbranch_scc1 LC3_%=\n\ts_mov_b32 s40, 0\n\tv_max3_u32 v96, v68, v69, v70\n\tv_cmp_eq_u32 vcc, -1, v96\n\t" \
  "s_cbranch_vccz LC61_%=\n\tglobal_load_dwordx4 v[68:71], v101, %[sp] sc1\n\ts_mov_b32 s40, 1\n\tLC61_%=:\n\tv_max3_u32 v96, v72, v73, v74\n\t" \
  "v_cmp_eq_u32 vcc, -1, v96\n\ts_cbranch_vccz LC62_%=\n\tglobal_load_dwordx4 v[72:75], v102, %[sp] sc1\n\ts_mov_b32 s40, 1\n\tLC62_%=:\n\t" \
  "v_max3_u32 v96, v76, v77, v78\n\tv_cmp_eq_u32 vcc, -1, v96\n\ts_cbranch_vccz LC63_%=\n\tglobal_load_dwordx4 v[76:79], v103, %[sp] sc1\n\t" \
  "s_mov_b32 s40, 1\n\tLC63_%=:\n\tv_max3_u32 v96, v80, v81, v82\n\tv_cmp_eq_u32 vcc, -1, v96\n\ts_cbranch_vccz LC64_%=\n\t" \
  "global_load_dwordx4 v[80:83], v104, %[sp] sc1\n\ts_mov_b32 s40, 1\n\tLC64_%=:\n\tv_max3_u32 v96, v84, v85, v86\n\tv_cmp_eq_u32 vcc, -1, v96\n\t" \
  "s_cbranch_vccz LC65_%=\n\tglobal_load_dwordx4 v[84:87], v105, %[sp] sc1\n\ts_mov_b32 s40, 1\n\tLC65_%=:\n\tv_max3_u32 v96, v88, v89, v90\n\t" \
  "v_cmp_eq_u32 vcc, -1, v96\n\ts_cbranch_vccz LC66_%=\n\tglobal_load_dwordx4 v[88:91], v106, %[sp] sc1\n\ts_mov_b32 s40, 1\n\tLC66_%=:\n\t" \
  "v_max3_u32 v96, v92, v93, v94\n\tv_cmp_eq_u32 vcc, -1, v96\n\ts_cbranch_vccz LC67_%=\n\tglobal_load_dwordx4 v[92:95], v107, %[sp] sc1\n\t" \
  "s_mov_b32 s40, 1\n\tLC67_%=:\n\ts_cmp_eq_u32 s40, 0\n\ts_cbranch_scc1 LC3_%=\n\ts_waitcnt vmcnt(0)\n\ts_branch LC2_%=\n\tLC9_%=:\n\t" \
  ""
extern "C" __global__ __launch_bounds__(768, 3) void fused_block(const u16* x, const u16* wqkv, const u16* wo, const u16* w_r,
                                                   const u16* w_gate, const u16* w_up, const u16* w_down, u16* out,
                                                   char* ws) {
  extern __shared__ __attribute__((aligned(16))) char smem[];
  const int tid = threadIdx.x;
  const int lane = tid & 63;
  const int w = __builtin_amdgcn_readfirstlane(tid >> 6);
  const int g = lane >> 4, r16 = lane & 15, q4 = (lane & 15) >> 2, p4 = lane & 3;
  const int bid = blockIdx.x;
  const int xcd = bid & 7;
  const int bidx = bid >> 3;
  const u32 l16 = lane * 16;
  if (w == 11) {
    u64 pc;
    asm volatile("s_getpc_b64 %0" : "=s"(pc));
    if (lane < 10) touch_line((const void*)(pc + min((bidx * 10 + lane) * 128, CODE_BYTES)));
  }

  if (bidx >= 26) {
    const int fb = xcd * 6 + (bidx - 26);
    const u32 tstart = (u32)__builtin_amdgcn_s_memrealtime();
    const bool hurry = ((((u32)(u64)w_gate + fb * 64) >> 7) & 7u) == 3u;
#define WAIT_UNTIL(T)                                                                      \
  _Pragma("nounroll") for (int it = 0; it < 1000; ++it) {                                  \
    if ((int)((u32)__builtin_amdgcn_s_memrealtime() - tstart) >= (T)) break;               \
    __builtin_amdgcn_s_sleep(4);                                                           \
  }
    if (w < 3) {
      int li = (bidx - 26) * 160 + w * 64 + lane;
      if (w < 2 || lane < 32) touch_line((const char*)x + (size_t)(((xcd >> 2) * 13 + 6) * 80 - 16) * 1280 + li * 128);
    }
    u32x2 ep;
    u32 tag0;
    asm volatile("global_load_dwordx2 %0, %1, off sc1" : "=v"(ep) : "v"(ws + WS_EPOCH) : "memory");
    {
      int wi = (((xcd >> 1) & 1) * 6 + (bidx - 26)) * 12 + w;
      wi = wi > 129 ? 129 : wi;
      int row = (xcd >> 2) * 1040 + (xcd & 1) * 520 + wi * 4 + g;
      row = row > SEQ - 1 ? SEQ - 1 : row;
      const u32 vo = row * 1280 + r16 * 16;
      u32x4 xr[5];
#pragma unroll
      for (int i = 0; i < 5; ++i)
        asm volatile("global_load_dwordx4 %0, %1, %2 offset:%3" : "=v"(xr[i]) : "v"(vo), "s"(x), "n"(i * 256) : "memory");
      asm volatile("s_waitcnt vmcnt(0)" ::: "memory");
      asm volatile("" : "+v"(xr[0]), "+v"(xr[1]), "+v"(xr[2]), "+v"(xr[3]), "+v"(xr[4]), "+v"(ep)::"memory");
      tag0 = ep[0] + (ep[0] == SENT - 1u ? 2u : 1u);
      float sm = 0.f;
#pragma unroll
      for (int i = 0; i < 5; ++i)
#pragma unroll
        for (int e = 0; e < 4; ++e) sm = dot2b(xr[i][e], 0x3F803F80u, sm);
      const float mean = row16_sum(sm) * (1.0f / DM);
      float ss = 0.f;
#pragma unroll
      for (int i = 0; i < 5; ++i)
#pragma unroll
        for (int e = 0; e < 4; ++e) {
          const float a = lo_f(xr[i][e]) - mean, b = hi_f(xr[i][e]) - mean;
          ss = __builtin_fmaf(a, a, ss);
          ss = __builtin_fmaf(b, b, ss);
        }
      const float var = row16_sum(ss) * (1.0f / DM);
      const float den = rbf(__builtin_amdgcn_sqrtf(rbf(rbf(var) + 1e-5f)));
      if (r16 == 0) GST2((u32)(row * 8), ws + WS_LN1, 0, ((u32x2){cvt2((f32x2){mean, den}), tag0}));
    }
    u32x4 va[2][5], vb[5];
    const int vrk = (fb >> 4) * 14 + (int)(((((u32)(u64)w_gate + fb * 64) >> 6) - 8u) & 15u);
    const int vq0 = vrk * 4 + w * 2;
    u32x4 ra;
    u32 rb5;
    asm volatile("global_load_dwordx4 %0, %2, %4\n\tglobal_load_dword %1, %3, %4 offset:1024"
                   : "=&v"(ra), "=&v"(rb5) : "v"(l16), "v"((u32)(lane * 4)), "s"(w_r) : "memory");
    if (w >= 2) {
      WAIT_UNTIL(hurry ? 0 : PDELAY + 12 * (w - 2))
      const u32x4 s4 = (u32x4){SENT, SENT, SENT, SENT};
      const char* fp = ws + WS_FILL + (size_t)fb * 73728 + (w - 2) * 7168 + 4096;
      GST4(l16, fp, -4096, s4); GST4(l16, fp, -3072, s4); GST4(l16, fp, -2048, s4); GST4(l16, fp, -1024, s4);
      GST4(l16, fp, 0, s4); GST4(l16, fp, 1024, s4); GST4(l16, fp, 2048, s4);
      if (w < 4) GST4(l16, ws + WS_FILL + (size_t)fb * 73728 + 69632 + w * 1024, 0, s4);
      if (w >= 5) GST4((u32)(((w - 5) * 8 + (lane >> 3)) * 6144 + (lane & 7) * 16), ws + WS_PK + fb * 128, 0, s4);
    }
    if (w == 0) {
      const u64 e1 = (((u64)ep[1] << 32) | ep[0]) + (ep[0] == SENT - 1u ? 2ull : 1ull);
      if (lane == 0) *(u32x4*)(smem + CTL_OFF) = (u32x4){(u32)e1, (u32)(e1 >> 32), 0u, 0u};
      if (lane >= 1 && lane < 8) *(u32x4*)(smem + CTL_OFF + lane * 16) = (u32x4){0u, 0u, 0u, 0u};
      if (lane >= 32) *(u32x4*)(smem + KEY_OFF - 512 + lane * 16) = (u32x4){0u, 0u, 0u, 0u};
    }
    if (w >= 6) {
      const u32 o1 = tid >= 636 ? 0x3F803F80u : 0u;
      if (tid < 720) *(u32x4*)(smem + HN_OFF + (tid - 384) * 16) = (u32x4){o1, o1, o1, o1};
    }
    if (w < 2 && !hurry && vrk < 40) {
      WAIT_UNTIL(VDELAY)
      const u32 vo = (vq0 * 4 + g) * 1280 + r16 * 16;
#pragma unroll
      for (int i = 0; i < 5; ++i)
        asm volatile("global_load_dwordx4 %0, %3, %4 offset:%8\n\tglobal_load_dwordx4 %1, %3, %5 offset:%8\n\tglobal_load_dwordx4 %2, %6, %7 offset:%8"
                     : "=&v"(va[0][i]), "=&v"(va[1][i]), "=&v"(vb[i]) : "v"(vo), "s"(wo), "s"((const char*)wo + 5120), "v"((u32)(r16 * 16)), "s"(w_r), "n"(i * 256) : "memory");
      asm volatile("s_waitcnt vmcnt(0)" : "+v"(va[0][0]), "+v"(va[0][1]), "+v"(va[0][2]), "+v"(va[0][3]), "+v"(va[0][4]), "+v"(va[1][0]), "+v"(va[1][1]),
                   "+v"(va[1][2]), "+v"(va[1][3]), "+v"(va[1][4]), "+v"(vb[0]), "+v"(vb[1]), "+v"(vb[2]), "+v"(vb[3]), "+v"(vb[4])::"memory");
      float sb = 0.f;
#pragma unroll
      for (int i = 0; i < 20; ++i) sb = dot2b(vb[i >> 2][i & 3], 0x3F803F80u, sb);
      sb = row16_sum(sb) * (1.0f / DM);
      u32x4 pk2[2];
#pragma unroll
      for (int k = 0; k < 2; ++k) {
        float d = 0.f, sa = 0.f;
#pragma unroll
        for (int i = 0; i < 20; ++i) {
          d = dot2b(va[k][i >> 2][i & 3], vb[i >> 2][i & 3], d);
          sa = dot2b(va[k][i >> 2][i & 3], 0x3F803F80u, sa);
        }
        const u32 vp = __float_as_uint(row16_sum(d - sa * sb));
        pk2[k] = (u32x4){cvt2((f32x2){__uint_as_float(__builtin_amdgcn_readlane(vp, 0)), __uint_as_float(__builtin_amdgcn_readlane(vp, 16))}), tag0,
                          cvt2((f32x2){__uint_as_float(__builtin_amdgcn_readlane(vp, 32)), __uint_as_float(__builtin_amdgcn_readlane(vp, 48))}), tag0};
      }
      if (lane < 8) GST4(l16, ws + WS_VP + vq0 * 64, 0, ((lane & 1) ? pk2[1] : pk2[0]));
    } else
      asm volatile("s_waitcnt vmcnt(0)" ::: "memory");
    asm volatile("" : "+v"(ra), "+v"(rb5)::"memory");
    LDS_BARRIER();
    const u32x2 tg = *(const u32x2*)(smem + CTL_OFF);
    const u32 tag = tg[0];
    if (w == 2 && lane == 0) GST2(0u, ws + WS_HFLAG + fb * 128, 0, tg);
    if (w >= 3) {
      int li = (bidx - 26) * 534 + (w - 3) * 64 + lane;
      li = li > 3199 ? 3199 : li;
      const int rr = li / 5;
      if (w < 11 || lane < 22) touch_line((const char*)wo + rr * 1280 + ((xcd >> 1) & 1) * 640 + (li - rr * 5) * 128);
    }
    WAIT_UNTIL(hurry ? 0 : FDELAY)
    asm volatile("s_barrier" ::: "memory");
    if (w >= 2) {
      const int ch = (lane & 7) ^ (((lane >> 4) & 1) * 2 + ((w + (OPP ? fb : 0)) & 1) * 4);
      const u32 vg = (lane >> 3) * (FF * 2) + fb * 64 + (ch & 3) * 16;
      const u64 gsel = (ch & 4) ? (u64)w_up : (u64)w_gate;
#pragma nounroll
      for (int n0 = w - 2; n0 < 80; n0 += 10) {
        const int n = (OPP && (fb & 1)) ? 79 - n0 : n0;
        const u64 p = gsel + vg + (u32)(n * 8 * FF * 2);
        asm volatile("s_mov_b32 m0, %1\n\ts_nop 0\n\tglobal_load_lds_dwordx4 %0, off" ::"v"(p), "s"(n * 1024) : "memory");
      }
#pragma nounroll
      for (int n = 11 - w; n < 40; n += 10) {
        const u32 row = (u32)(n * 64 + lane) / 80u;
        asm volatile("s_mov_b32 m0, %2\n\ts_nop 0\n\tglobal_load_lds_dwordx4 %0, %1" " nt"
                     ::"v"(l16 ^ ((row & 3u) * 32u + (row & 8u) * 16u)), "s"((const char*)w_down + (size_t)fb * 40960 + n * 1024), "s"(DOWN_OFF + n * 1024) : "memory");
      }
      asm volatile("s_waitcnt vmcnt(0)" ::: "memory");
      if (lane == 0) asm volatile("ds_add_u32 %0, %1" ::"v"(CTL_OFF + 48), "v"(1u) : "memory");
    } else {
      if (w == 1 && lane <= HN)
        touch_line(lane == HN ? ws + WS_PK - 65536 + bid * 128 : (const char*)out + lane * (2555904 / (HN - 1) & ~127) + fb * 1280);
      WAIT_UNTIL(KSTART)
    }
    if (w >= 4) {
      const int v = w - 4, kq = v & 3, m = v >> 2;
#pragma nounroll
      for (int it = 0; it < SPIN_CAP; ++it) {
        if (__builtin_amdgcn_readfirstlane(lds_ld32(CTL_OFF + 48)) >= 10u) break;
        __builtin_amdgcn_s_sleep(1);
      }
      bf16x8 gfr[5], ufr[5], dfr[5];
      {
        const int gx = (160 * kq + 8 * g + q4) * 128 + ((m ^ (q4 >> 1) ^ ((g & 1) * 2)) * 32) + p4 * 8;
        const char* db = smem + DOWN_OFF + (8 * g + q4) * 1280 + p4 * 8;
#pragma unroll
        for (int j = 0; j < 5; ++j) {
          gfr[j] = tr_frag(smem + gx + j * 4096, 512);
          ufr[j] = tr_frag(smem + (gx ^ 64) + j * 4096, 512);
          dfr[j] = tr_frag(db + ((v * 160 + j * 32) ^ (q4 * 32 + (g & 1) * 128)), 5120);
        }
#pragma unroll
        for (int j = 0; j < 5; ++j) asm volatile("" : "+v"(gfr[j]), "+v"(ufr[j]), "+v"(dfr[j]));
      }
      u32 ahn = HN_OFF + p4 * HSL + kq * 320 + g * 16, asw = ACT_OFF + p4 * ASL + kq * 256 + (q4 & 1) * 128 + m * 64 + g * 16, asr = ACT_OFF + (lane & 31) * 4;
      u32 aav = ACT_OFF + 4352 + v * 64 + (lane & 31) * 2, aar = ACT_OFF + 4352 + v * 64 + g * 16, vpo = v * 320 + r16 * 64 + g * 16, up3 = lane >= 32 ? 3u : 0u;
      asm volatile("" : "+v"(up3), "+v"(aar));
      const char* pdst = ws + WS_PART + fb * 2560;
      asm volatile("" : "+v"(ahn), "+v"(asw), "+v"(asr), "+v"(aav), "+v"(vpo));
      asm volatile("" : "+s"(pdst));
      int pass = 0;
      asm volatile("" : "+s"(pass));
#pragma nounroll
      for (; pass < 2; ++pass) {
        asm volatile("s_barrier" ::: "memory");
        u32x4 bs;
#define GU(J, C0, C1) "v_mfma_f32_16x16x32_bf16 v[120:123], %[g" #J "], v[100+4*" #J ":103+4*" #J "], " C0 "\n\t" \
                      "v_mfma_f32_16x16x32_bf16 v[124:127], %[u" #J "], v[100+4*" #J ":103+4*" #J "], " C1 "\n\t"
#define GU1(J) GU(J, "v[120:123]", "v[124:127]")
        asm volatile("ds_read_b128 v[100:103], %[ah]\n\tds_read_b128 v[104:107], %[ah] offset:64\n\tds_read_b128 v[108:111], %[ah] offset:128\n\t"
                     "ds_read_b128 v[112:115], %[ah] offset:192\n\tds_read_b128 v[116:119], %[ah] offset:256\n\t"
                     "s_waitcnt lgkmcnt(4)\n\t" GU(0, "0", "0") "s_waitcnt lgkmcnt(3)\n\t" GU1(1) "s_waitcnt lgkmcnt(2)\n\t" GU1(2)
                     "s_waitcnt lgkmcnt(1)\n\t" GU1(3) "s_waitcnt lgkmcnt(0)\n\t" GU1(4)
                     "ds_read_b128 %[bs], %[ab]\n\ts_mov_b32 exec_lo, 0x00f000f0\n\ts_mov_b32 exec_hi, 0x00f000f0\n\ts_nop 4\n\t"
                     "v_mov_b64 v[120:121], v[124:125]\n\tv_mov_b64 v[122:123], v[126:127]\n\t"
                     "s_mov_b32 exec_lo, 0x00ff00ff\n\ts_mov_b32 exec_hi, 0x00ff00ff\n\tds_write_b128 %[aw], v[120:123]\n\ts_mov_b64 exec, -1\n\t"
                     "s_setprio 3\n\ts_waitcnt lgkmcnt(0)\n\tv_cmp_eq_u32 vcc, 3, v129\n\ts_barrier\n\t"
                     "s_cbranch_vccnz 1f\n\tds_read_b128 %[bs], %[ab]\n\ts_waitcnt lgkmcnt(0)\n1:"
                     : [bs] "=&{v[128:131]}"(bs)
                     : [ah] "v"(ahn), [ab] "v"((u32)(CTL_OFF + 112)), [aw] "v"(asw), [g0] "v"(gfr[0]), [g1] "v"(gfr[1]), [g2] "v"(gfr[2]),
                       [g3] "v"(gfr[3]), [g4] "v"(gfr[4]), [u0] "v"(ufr[0]), [u1] "v"(ufr[1]), [u2] "v"(ufr[2]), [u3] "v"(ufr[3]), [u4] "v"(ufr[4])
                     : "memory", "vcc", "scc", "v100", "v101", "v102", "v103", "v104", "v105", "v106", "v107", "v108", "v109", "v110", "v111", "v112", "v113",
                       "v114", "v115", "v116", "v117", "v118", "v119", "v120", "v121", "v122", "v123", "v124", "v125", "v126", "v127");
        const u32x2 best = {bs[2], bs[3]};
        f32x2 s0, s1, s2, s3;
        asm volatile("ds_read2_b32 %0, %4 offset0:0 offset1:32\n\tds_read2_b32 %1, %4 offset0:64 offset1:96\n\t"
                     "ds_read2_b32 %2, %4 offset0:128 offset1:160\n\tds_read2_b32 %3, %4 offset0:192 offset1:224"
                     : "=&v"(s0), "=&v"(s1), "=&v"(s2), "=&v"(s3) : "v"(asr + ((best[1] & 3u) | up3) * ASL) : "memory");
        float rd = __builtin_amdgcn_rcpf(hi_f(best[0])), mb = lo_f(best[0]);
        asm volatile("s_waitcnt lgkmcnt(0)" : "+v"(s0), "+v"(s1), "+v"(s2), "+v"(s3), "+v"(rd), "+v"(mb)::"memory");
        const f32x2 sm = (s0 + s1) + (s2 + s3);
        auto xg = __builtin_amdgcn_permlane32_swap(__float_as_uint(sm[0]), __float_as_uint(sm[0]), false, false);
        auto xu = __builtin_amdgcn_permlane32_swap(__float_as_uint(sm[1]), __float_as_uint(sm[1]), false, false);
        const f32x2 gu = unpk(cvt2((f32x2){(__uint_as_float(xg[0]) - mb * __uint_as_float(xg[1])) * rd,
                                          (__uint_as_float(xu[0]) - mb * __uint_as_float(xu[1])) * rd}));
        const float sg = rbf(gu[0] * __builtin_amdgcn_rcpf(1.0f + __expf(-gu[0])));
        const u32 av = f2bf(sg * gu[1]);
#define DN(J) "v_mfma_f32_16x16x32_bf16 v[104+4*" #J ":107+4*" #J "], %[d" #J "], v[100:103], 0\n\t"
#define MV(J) "s_lshl_b64 exec, exec, 1\n\tv_mov_b64 v[104:105], v[104+4*" #J ":105+4*" #J "]\n\tv_mov_b64 v[106:107], v[106+4*" #J ":107+4*" #J "]\n\t"
        asm volatile("ds_write_b16 %[aw], %[av]\n\tds_read_b128 v[100:103], %[ar]\n\ts_waitcnt lgkmcnt(0)\n\t" DN(0) DN(1) DN(2) DN(3) DN(4)
                     "s_mov_b32 exec_lo, 0x00010001\n\ts_mov_b32 exec_hi, 0x00010001\n\ts_nop 1\n\t" MV(1) MV(2) MV(3) MV(4)
                     "s_mov_b32 exec_lo, 0x001f001f\n\ts_mov_b32 exec_hi, 0x001f001f\n\tglobal_store_dwordx4 %[vp], v[104:107], %[pd] sc1\n\t"
                     "s_mov_b64 exec, -1\n\ts_setprio 0"
                     ::[aw] "v"(aav), [av] "v"(av), [ar] "v"(aar), [vp] "v"(vpo), [pd] "s"(pdst), [d0] "v"(dfr[0]), [d1] "v"(dfr[1]),
                       [d2] "v"(dfr[2]), [d3] "v"(dfr[3]), [d4] "v"(dfr[4])
                     : "memory", "scc", "v100", "v101", "v102", "v103", "v104", "v105", "v106", "v107", "v108", "v109", "v110", "v111", "v112", "v113",
                       "v114", "v115", "v116", "v117", "v118", "v119", "v120", "v121", "v122", "v123");
        if (pass) break;
        u32 fin = 0;
#pragma nounroll
        for (int it = 0; it < SPIN_CAP * 8; ++it) {
          fin = __builtin_amdgcn_readfirstlane(lds_ld32(CTL_OFF + 40));
          if (fin) break;
          __builtin_amdgcn_s_sleep(4);
        }
        if (fin != 2u) break;
        pdst += WS_PART2 - WS_PART;
      }
      return;
    }
    if (w == 0) {
      u32 pk6;
      asm volatile(
          "s_mov_b32 s41, %[cap]\n\ts_mov_b32 s40, 1\n\ts_mov_b64 s[46:47], 0\n\tv_mov_b32 v5, %[ln]\n\t"
          "s_mov_b32 s54, -1\n\ts_mov_b32 s55, 0x7ffff\n\tv_mov_b32 v12, 0\n\t"
#define PKLD(D) "s_mov_b64 exec, s[54:55]\n\tglobal_atomic_or_x2 " D ", %[vk], %[z], %[kb] sc0 sc1\n\ts_mov_b64 exec, -1\n\t"
          PKLD("v[8:9]") "s_sleep %[gap]\n\t" PKLD("v[10:11]") "s_sleep %[gap]\n\t" PKLD("v[14:15]")
#define MDPP(C) "s_nop 1\n\tv_max_u32_dpp v13, v13, v13 " C " row_mask:0xf bank_mask:0xf bound_ctrl:1\n\t"
#define PSTEP(L, D0, D1, DR)                                                                        \
          L "0:\n\t"                                                                               \
          "s_waitcnt vmcnt(2)\n\t"                                                                 \
          "v_cmp_eq_u32 s[42:43], " D1 ", %[tag]\n\t"                                              \
          "s_and_b64 exec, s[42:43], s[54:55]\n\ts_andn2_b64 s[54:55], s[54:55], s[42:43]\n\t"      \
          "v_and_or_b32 v12, " D0 ", %[msk], %[lid]\n\t"                                           \
          "v_mov_b32 v6, " D0 "\n\ts_mov_b64 exec, -1\n\t"                                         \
          "v_cmp_le_u32 vcc, s40, v12\n\t"                                                         \
          "s_cmp_eq_u64 vcc, s[46:47]\n\ts_cbranch_scc1 " L "1f\n\t"                              \
          "s_swappc_b64 s[50:51], s[48:49]\n"                                                      \
          L "1:\n\t"                                                                               \
          "s_cmp_eq_u64 s[54:55], 0\n\ts_cbranch_scc1 9f\n\t"                                     \
          PKLD(DR)
          "s_getpc_b64 s[48:49]\n\ts_add_u32 s48, s48, 12\n\ts_addc_u32 s49, s49, 0\n\ts_branch 10f\n\t"
          "v_mov_b32 v13, v12\n\t"
          MDPP("quad_perm:[1,0,3,2]") MDPP("quad_perm:[2,3,0,1]") MDPP("row_half_mirror") MDPP("row_mirror")
          "v_bfe_u32 v4, v6, 10, 6\n\tv_readlane_b32 s44, v13, 0\n\tv_readlane_b32 s45, v13, 16\n\tv_readlane_b32 s46, v13, 32\n\tv_readlane_b32 s47, v13, 48\n\t"
          "v_sub_u32 v4, %[rb], v4\n\ts_max_u32 s44, s44, s45\n\ts_max_u32 s46, s46, s47\n\ts_max_u32 s44, s44, s46\n\t"
          "s_and_b32 s45, s44, 0x7fffff\n\ts_bitset1_b32 s45, 23\n\ts_mul_hi_u32 s45, s45, %[shr]\n"
          "5:\n\ts_sub_u32 s40, s44, s45\n\ts_cselect_b32 s40, 1, s40\n\ts_max_u32 s40, s40, 1\n\tv_cmp_le_u32 s[46:47], s40, v12\n\ts_mul_i32 s45, s45, 3\n\ts_lshr_b32 s45, s45, 2\n\t"
          "s_bcnt1_i32_b64 s52, s[46:47]\n\ts_cmp_gt_u32 s52, 3\n\ts_cbranch_scc1 5b\n\t"
          "v_mbcnt_lo_u32_b32 v13, s46, 0\n\tv_mbcnt_hi_u32_b32 v13, s47, v13\n\tv_sub_u32 v7, %[ln], v13\n\tv_add_u32 v7, s52, v7\n\t"
          "v_cndmask_b32 v13, v7, v13, s[46:47]\n\tv_cndmask_b32 v7, 2, 1, s[46:47]\n\tv_cmp_gt_u32 s[44:45], 3, v13\n\t"
          "v_lshl_add_u32 v13, v13, 4, %[pa]\n\ts_mov_b64 exec, s[44:45]\n\tds_write_b128 v13, v[4:7]\n\ts_mov_b64 exec, -1\n\t"
          "s_setpc_b64 s[50:51]\n"
          PSTEP("1", "v8", "v9", "v[8:9]")
          PSTEP("2", "v10", "v11", "v[10:11]")
          PSTEP("3", "v14", "v15", "v[14:15]")
          "s_sub_u32 s41, s41, 1\n\ts_cbranch_scc0 10b\n\ts_swappc_b64 s[50:51], s[48:49]\n"
          "9:\n\t"  "v_or_b32 v7, 4, v7\n\ts_mov_b64 exec, s[44:45]\n\tds_write_b32 v13, v7 offset:12\n\ts_mov_b64 exec, -1\n\tds_write_b32 %[at], v6\n\ts_waitcnt lgkmcnt(0)\n\ts_barrier\n\ts_waitcnt vmcnt(0)"
          : "=&{v6}"(pk6) : [cap] "n"(SPIN_CAP), [gap] "n"(PGAP), [vk] "v"((u32)(lane * 6144)), [kb] "s"(ws + WS_PK + fb * 128), [tag] "v"(tag), [z] "v"(0ull), [ln] "v"(lane),
            [msk] "s"(~1023u), [lid] "v"(64u | lane), [rb] "v"(2047 - ((lane >> 1) * 80 + (lane & 1) * 48)),
            [shr] "s"((u32)((1.0 - PTHR) * 4294967296.0)), [pa] "s"(POST_OFF), [at] "v"((u32)(KEY_OFF - 256 + lane * 4))
          : "memory", "vcc", "scc", "s40", "s41", "s42", "s43", "s44", "s45", "s46", "s47", "s48", "s49", "s50", "s51", "s52", "s54", "s55",
            "v4", "v5", "v7", "v8", "v9", "v10", "v11", "v12", "v13", "v14", "v15");
      if (fb == 47 && lane < 8) GST4(l16, ws + WS_EPOCH, 0, ((u32x4){tg[0], tg[1], tg[0], tg[1]}));
      const u32 now = (u32)__builtin_amdgcn_s_memrealtime();
      const u32 nag = (pk6 - (now >> 1) - 1u) & 1023u;
      u32 kgo = __builtin_amdgcn_readfirstlane(now + KCD - 2 * (1023u - wave_max(lane < 51 ? nag : 0u)));
      u64 old = __ballot(lane < 51 && nag < 1023u - (KOLD - 8) / 2);
      asm volatile("" : "+s"(kgo), "+s"(old));
      asm volatile(
          "s_mov_b32 s41, %[cap]\n\ts_mov_b32 s54, -1\n\ts_mov_b32 s55, 0x7ffff\n\tv_mov_b32 v12, 0\n\t"
          "s_mov_b64 exec, %[old]\n\ts_cbranch_execz 3f\n\tglobal_load_dwordx2 v[14:15], %[vk], %[kb] sc1\n"
          "3:\n\ts_setprio 3\n\ts_barrier\n"
          "4:\n\ts_memrealtime s[42:43]\n\ts_waitcnt lgkmcnt(0)\n\ts_sub_u32 s42, s42, %[go]\n\ts_cmp_lt_i32 s42, 0\n\ts_cbranch_scc1 4b\n\t"
          "s_andn2_b64 exec, s[54:55], %[old]\n\ts_cbranch_execz 5f\n\tglobal_load_dwordx2 v[14:15], %[vk], %[kb] sc1\n"
          "5:\n\ts_mov_b64 exec, s[54:55]\n\tds_read_b32 v4, %[ctl] offset:92\n\ts_waitcnt lgkmcnt(0)\n\tv_and_b32 v4, 3, v4\n\t"
          "v_lshl_add_u32 v4, v4, 4, %[pa]\n\tds_read_b64 v[6:7], v4\n\ts_waitcnt lgkmcnt(0)\n\t"
          "v_readfirstlane_b32 s48, v6\n\tv_readfirstlane_b32 s49, v7\n\tv_mov_b32 v6, 1\n\ts_waitcnt vmcnt(0)\n\tv_cmp_ne_u32 s[54:55], v15, %[tag]\n\tv_mov_b32 v12, v14\n\ts_mov_b64 exec, -1\n\t"
          "s_cmp_eq_u64 s[54:55], 0\n\ts_cbranch_scc1 9f\n\t"
          PKLD("v[8:9]") "s_sleep %[gap]\n\t" PKLD("v[10:11]")
#define KSTEP(L, D0, D1, DR)                                                                        \
          L "0:\n\t"                                                                               \
          "s_waitcnt vmcnt(1)\n\t"                                                                 \
          "v_cmp_eq_u32 s[42:43], " D1 ", %[tag]\n\t"                                              \
          "s_and_b64 exec, s[42:43], s[54:55]\n\tv_mov_b32 v12, " D0 "\n\ts_mov_b64 exec, -1\n\t"   \
          "s_andn2_b64 s[54:55], s[54:55], s[42:43]\n\ts_cbranch_scc0 9f\n\t"                     \
          PKLD(DR)
          KSTEP("1", "v8", "v9", "v[8:9]")
          KSTEP("2", "v10", "v11", "v[10:11]")
          "s_sub_u32 s41, s41, 1\n\ts_cbranch_scc0 10b\n"
          "9:\n\t"
          "v_readlane_b32 s40, v12, s49\n\tds_write_b32 %[ak], v12\n\tv_mov_b32 v13, v12\n\t"
          "s_and_b32 s45, s40, 0x7ff\n\tv_cmp_lt_u32 vcc, s40, v12\n\ts_cmp_eq_u32 s45, s48\n\ts_cselect_b64 vcc, vcc, -1\n\ts_cbranch_vccz 8f\n\t"
          MDPP("quad_perm:[1,0,3,2]") MDPP("quad_perm:[2,3,0,1]") MDPP("row_half_mirror") MDPP("row_mirror")
          "s_nop 0\n\tv_readlane_b32 s44, v13, 0\n\tv_readlane_b32 s45, v13, 16\n\tv_readlane_b32 s46, v13, 32\n\tv_readlane_b32 s47, v13, 48\n\t"
          "s_max_u32 s44, s44, s45\n\ts_max_u32 s46, s46, s47\n\ts_max_u32 s40, s44, s46\n\t"
          "v_cmp_eq_u32 vcc, s40, v12\n\tv_mov_b32 v6, 2\n\ts_ff1_i32_b64 s44, vcc\n\tv_mov_b32 v7, s44\n"
          "8:\n\tv_mov_b32 v4, s40\n\ts_mov_b64 exec, 1\n\tds_write_b128 %[ctl], v[4:7]\n\ts_mov_b64 exec, -1\n\t"
          "s_waitcnt vmcnt(0) lgkmcnt(0)"
          :: [cap] "n"(SPIN_CAP), [gap] "n"(KGAP), [go] "s"(kgo), [old] "s"(old), [vk] "v"((u32)(lane * 6144)), [kb] "s"(ws + WS_PK + fb * 128 + 8), [tag] "v"(tag),
            [ctl] "v"((u32)(CTL_OFF + 32)), [ak] "v"((u32)(KEY_OFF + lane * 4)), [pa] "s"(POST_OFF), [z] "v"(0ull)
          : "memory", "vcc", "scc", "s40", "s41", "s42", "s43", "s44", "s45", "s46", "s47", "s48", "s49", "s54", "s55", "v4", "v5", "v6", "v7", "v8", "v9", "v10",
            "v11", "v12", "v13", "v14", "v15");
      return;
    }
    float swr = dot2b(rb5, 0x3F803F80u, 0.f);
#pragma unroll
    for (int e = 0; e < 4; ++e) swr = dot2b(ra[e], 0x3F803F80u, swr);
    swr = wave_sum(swr);
    u32 ah16 = HN_OFF + (w - 1) * HSL + l16, ah4 = HN_OFF + (w - 1) * HSL + 1024 + lane * 4, l4 = lane * 4;
    asm volatile("" : "+v"(swr), "+v"(ah16), "+v"(ah4), "+v"(l4));
#define DOT3(H, W) "v_dot2c_f32_bf16 v102, 0x3f803f80, " H "\n\tv_dot2c_f32_bf16 v103, " H ", " H "\n\tv_dot2c_f32_bf16 v104, " H ", " W "\n\t"
#define ADPP(C) "s_nop 1\n\tv_add_f32_dpp v102, v102, v102 " C " row_mask:0xf bank_mask:0xf bound_ctrl:1\n\t"
#define XDPP(C) "s_nop 1\n\tv_max_u32_dpp v102, v102, v102 " C " row_mask:0xf bank_mask:0xf bound_ctrl:1\n\t"
    const char* rp = (const char*)w_r;
    u32 second = 0;
#pragma nounroll
    for (;;) {
      asm volatile(
          "s_mov_b32 s47, %[cap]\n\ts_mov_b32 s43, -1\n\ts_sub_u32 s46, %[sec], 1\n"
          "7:\n\ts_mov_b64 s[48:49], %[rp]\n\ts_mov_b32 s45, 0\n\ts_branch 4f\n"
          "18:\n\ts_sleep 1\n"
          "1:\n\ts_sub_u32 s47, s47, 1\n\ts_cbranch_scc1 8f\n\ts_cmp_lg_u32 %[sec], 0\n\ts_cbranch_scc1 7b\n\t"
          "ds_read_b128 v[108:111], %[ap]\n\ts_memrealtime s[40:41]\n\ts_waitcnt lgkmcnt(0)\n\t"
          "v_readfirstlane_b32 s45, v111\n\tv_readfirstlane_b32 s46, v109\n\tv_readfirstlane_b32 s50, v110\n\ts_lshr_b32 s41, s40, 1\n\t"
          "s_cmp_eq_u32 s45, 6\n\ts_cbranch_scc1 8f\n\ts_bitcmp0_b32 s45, 0\n\ts_cbranch_scc1 18b\n\t"
          "s_cmp_eq_u32 s46, s42\n\ts_cbranch_scc1 6f\n\ts_cmp_eq_u32 s46, s43\n\ts_cbranch_scc1 2f\n\t"
          "s_sub_u32 s41, s41, s50\n\ts_and_b32 s41, s41, 0x3ff\n\ts_cmp_lt_u32 s41, %[tq]\n\ts_cbranch_scc1 18b\n\t"
          "s_mov_b32 s43, s46\n\ts_add_u32 s44, s50, 8\n\ts_cmp_ge_u32 s41, %[pc] - 20\n\ts_cbranch_scc1 1b\n\t"
          "s_mul_i32 s48, s46, 6144\n\tv_mov_b64 v[104:105], 0\n\tv_mov_b32 v101, s48\n\tv_mad_u32_u24 v101, %[l16], 48, v101\n\t"
          "s_mov_b64 exec, 0xff\n\tglobal_atomic_or_x2 v[102:103], v101, v[104:105], %[pk] sc0 sc1\n\ts_sub_u32 s50, s50, 512\n\ts_waitcnt vmcnt(0)\n\t"
          "v_cmp_eq_u32 vcc, v103, %[tag]\n\tv_subrev_u32 v102, s50, v102\n\tv_and_b32 v102, 0x3ff, v102\n\tv_cndmask_b32 v102, 0, v102, vcc\n\t"
          XDPP("quad_perm:[1,0,3,2]") XDPP("quad_perm:[2,3,0,1]") XDPP("row_half_mirror")
          "s_mov_b64 exec, -1\n\tv_readfirstlane_b32 s44, v102\n\ts_min_u32 s44, s44, 562\n\ts_add_u32 s44, s44, s50\n\ts_branch 1b\n"
          "2:\n\ts_sub_u32 s41, s41, s44\n\ts_and_b32 s41, s41, 0x3ff\n\ts_cmp_lt_u32 s41, %[pc] - 8\n\ts_cbranch_scc1 1b\n\t"
          "s_sub_u32 s41, %[pc], s41\n\ts_lshl1_add_u32 s51, s41, s40\n\ts_mul_i32 s48, s46, 1280\n\ts_add_u32 s48, s48, %[lo]\n\ts_addc_u32 s49, %[hi], 0\n\t"
          "3:\n\ts_memrealtime s[40:41]\n\ts_waitcnt lgkmcnt(0)\n\ts_sub_u32 s40, s40, s51\n\ts_cmp_lt_i32 s40, 0\n\ts_cbranch_scc1 3b\n"
          "4:\n\tglobal_load_dwordx4 v[96:99], %[l16], s[48:49] sc1\n\tglobal_load_dword v100, %[l4], s[48:49] offset:1024 sc1\n\t"
          "v_mov_b64 v[102:103], 0\n\tv_mov_b32 v104, 0\n\ts_mov_b32 s42, -2\n\ts_waitcnt vmcnt(0)\n\t"
          "ds_read_b128 v[108:111], %[ap]\n\tds_read_b32 v105, %[ab] offset:4\n\tds_write_b128 %[a16], v[96:99]\n\tds_write_b32 %[a4], v100\n\t"
          "v_max3_u32 v101, v96, v97, v98\n\tv_max3_u32 v101, v101, v99, v100\n\tv_cmp_eq_u32 vcc, -1, v101\n\t"
          DOT3("v96", "v92") DOT3("v97", "v93")
          "s_cbranch_vccnz 1b\n\ts_mov_b32 s42, s46\n\ts_waitcnt lgkmcnt(2)\n\tv_lshl_or_b32 v109, v111, 8, v109\n\tv_lshl_or_b32 v109, v105, 16, v109\n\t"
          "s_or_b32 s45, s46, 0x20500\n\tv_cmp_eq_u32 vcc, s45, v109\n\ts_cbranch_vccz 5f\n\ts_waitcnt lgkmcnt(0)\n\ts_barrier\n"
          "5:\n\t" DOT3("v98", "v94") DOT3("v99", "v95") DOT3("v100", "%[rb]")
          "s_nop 2\n\tv_mov_b32 v105, v104\n\tv_permlane32_swap_b32 v102, v103\n\tv_add_f32 v102, v102, v103\n\tv_permlane32_swap_b32 v104, v105\n\t"
          "v_add_f32 v104, v104, v105\n\ts_nop 1\n\tv_permlane16_swap_b32 v102, v104\n\tv_add_f32 v102, v102, v104\n\t"
          ADPP("quad_perm:[1,0,3,2]") ADPP("quad_perm:[2,3,0,1]") ADPP("row_half_mirror") ADPP("row_mirror")
          "v_mul_f32 v103, 0x3acccccd, v102\n\tv_readlane_b32 s41, v102, 16\n\tv_readlane_b32 s40, v103, 32\n\t"
          "s_nop 1\n\tv_fma_f32 v102, -v103, v103, s40\n\tv_max_f32 v102, 0, v102\n\tv_cvt_pk_bf16_f32 v102, 0, v102\n\t"
          "v_add_f32 v102, 0x3727c5ac, v102\n\tv_cvt_pk_bf16_f32 v102, 0, v102\n\tv_sqrt_f32 v102, v102\n\tv_cvt_pk_bf16_f32 v105, 0, v103\n\t"
          "v_fma_f32 v105, -%[swr], v105, s41\n\tv_cvt_pk_bf16_f32 v106, v103, v102\n\tv_and_b32 v102, 0xffff0000, v106\n\tv_rcp_f32 v102, v102\n\t"
          "v_mov_b32 v104, 1\n\tv_mul_f32 v102, v102, v105\n\tv_ashrrev_i32 v103, 31, v102\n\tv_or_b32 v103, 0x80000000, v103\n\t"
          "v_xor_b32 v102, v102, v103\n\tv_and_or_b32 v107, v102, -4, %[sl]\n\ts_cbranch_vccz 1b\n\t"
          "s_mov_b64 exec, 1\n\tds_max_u64 %[ab], v[106:107] offset:8\n\tds_add_u32 %[ab], v104 offset:4\n\ts_branch 9f\n"
          "6:\n\ts_bitcmp0_b32 s45, 2\n\ts_cbranch_scc1 1b\n\ts_mov_b64 exec, 1\n\tds_max_u64 %[ab], v[106:107] offset:8\n"
          "8:\n\tv_mov_b32 v104, 1\n\ts_mov_b64 exec, 1\n\tds_add_u32 %[ab], v104 offset:4\n\ts_barrier\n"
          "9:\n\ts_mov_b64 exec, -1\n\ts_waitcnt lgkmcnt(0)\n\ts_barrier"
          :
          : [cap] "n"(SPIN_CAP * 8), [rp] "s"(rp), [sec] "s"(second), [ap] "v"((u32)(POST_OFF + (w - 1) * 16)), [tq] "n"(TQD / 2), [pc] "n"(PCD / 2),
            [pk] "s"(ws + WS_PK + (bidx - 26) * 128), [tag] "v"(tag), [lo] "s"((u32)(u64)(ws + WS_PC)), [hi] "s"((u32)((u64)(ws + WS_PC) >> 32)),
            [l16] "v"(l16), [l4] "v"(l4), [a16] "v"(ah16), [a4] "v"(ah4), "{v[92:95]}"(ra), [rb] "v"(rb5), [swr] "v"(swr), [sl] "s"(w - 1),
            [ab] "v"((u32)(CTL_OFF + 112))
          : "memory", "vcc", "scc", "s40", "s41", "s42", "s43", "s44", "s45", "s46", "s47", "s48", "s49", "s50", "s51", "v96", "v97", "v98", "v99",
            "v100", "v101", "v102", "v103", "v104", "v105", "v106", "v107", "v108", "v109", "v110", "v111");
      if (w != 1 || second) break;
      u32x4 wst;
#pragma nounroll
      for (int it = 0; it < SPIN_CAP * 8; ++it) {
        wst = lds_ld128(CTL_OFF + 32);
        if (__builtin_amdgcn_readfirstlane(wst[2])) break;
        __builtin_amdgcn_s_sleep(1);
      }
      if (__builtin_amdgcn_readfirstlane(wst[2]) != 2u) break;
      if (lane < 2) *(u32x4*)(smem + (lane ? POST_OFF : CTL_OFF + 112)) = lane ? (u32x4){0u, 0u, 0u, 5u} : (u32x4){0u, 2u, 0x3F800000u, 0u};
      rp = ws + WS_CAND + __builtin_amdgcn_readfirstlane(wst[3]) * 2560;
      second = 1;
    }
    if (w == 1) {
    } else if (w == 3 || fb >= 40) {
      const int c = w == 3 ? fb : fb + 8;
      if (c < 51) {
        const u32 now = (u32)__builtin_amdgcn_s_memrealtime();
        const u32 age = ((now >> 1) - __builtin_amdgcn_readfirstlane(*(const u32*)(smem + KEY_OFF - 256 + c * 4))) & 1023u;
        asm volatile("s_mov_b32 s42, %[cap]\n"
                     "3:\n\ts_memrealtime s[40:41]\n\ts_waitcnt lgkmcnt(0)\n\ts_sub_u32 s40, s40, %[go]\n\ts_cmp_lt_i32 s40, 0\n\ts_cbranch_scc1 3b\n"
                     "4:\n\tglobal_load_dwordx4 v[96:99], %[l16], %[cp] sc1\n\tglobal_load_dword v103, %[l4], %[cp] offset:1024 sc1\n\t"
                     "s_sub_u32 s42, s42, 8\n\ts_waitcnt vmcnt(0)\n\ts_cbranch_scc1 2f\n\t"
                     "v_max3_u32 v101, v96, v98, v103\n\tv_cmp_eq_u32 vcc, -1, v101\n\ts_cbranch_vccnz 4b\n"
                     "1:\n\tds_read_b64 v[100:101], %[af]\n\tds_read_b32 v102, %[ak]\n\ts_sub_u32 s42, s42, 1\n\ts_cbranch_scc1 2f\n\ts_waitcnt lgkmcnt(0)\n\t"
                     "v_cmp_eq_u32 vcc, 0, v100\n\ts_cbranch_vccnz 1b\n\t"
                     "v_cmp_eq_u32 vcc, %[c], v101\n\tv_bfi_b32 v102, v102, 0, %[m]\n\ts_cbranch_vccnz 2f\n\t"
                     "v_mad_u32_u24 v100, v102, %[rs], %[l16]\n\tv_mad_u32_u24 v101, v102, %[rs], %[l4]\n\t"
                     "global_store_dwordx4 v100, v[96:99], %[out]" "\n\tglobal_store_dword v101, v103, %[out] offset:1024" "\n"
                     "2:\n\ts_waitcnt lgkmcnt(0)"
                     ::[cap] "n"(SPIN_CAP * 8), [go] "s"(now + CCD - 2 * age), [cp] "s"(ws + WS_CAND + c * 2560 + 1280), [af] "v"((u32)(CTL_OFF + 40)),
                       [ak] "v"((u32)(KEY_OFF + c * 4)), [c] "s"(c), [m] "s"(2047), [rs] "s"(1280), [l16] "v"(l16), [l4] "v"(l4), [out] "s"(out)
                     : "memory", "vcc", "scc", "s40", "s41", "s42", "v96", "v97", "v98", "v99", "v100", "v101", "v102", "v103");
      }
    } else {
      const char* pp = ws + WS_PART + fb * 64;
      int pass = 0;
      asm volatile("" : "+s"(pass));
#pragma nounroll
      for (; pass < 2; ++pass) {
        const u32x2 best = *(const u32x2*)(smem + CTL_OFF + 120);
        const u32 slot = best[1] & 3u;
        const u32 wkey = __builtin_amdgcn_readfirstlane(*(const u32*)(smem + (pass ? CTL_OFF + 32 : POST_OFF + slot * 16)));
        const float rr = rbf(__uint_as_float((best[1] & ~3u) ^ ((u32)((int)~best[1] >> 31) | 0x80000000u)));
        float rs = rbf(__builtin_amdgcn_rcpf(1.0f + __expf(-rr)));
        const int ccol = (g & 1) * 2 + (g >> 1);
        float hv = bf2f(*(const u16*)(smem + HN_OFF + slot * HSL + fb * 32 + p4 * 8 + ccol * 2));
        hv = __builtin_fmaf(hv - lo_f(best[0]), __builtin_amdgcn_rcpf(hi_f(best[0])), hv);
        const char* op_ = (const char*)out + (size_t)(2047 - (int)(wkey & 2047u)) * 1280 + fb * 32;
        u32 vo_ = p4 * 8 + ccol * 2;
        asm volatile("" : "+v"(vo_), "+v"(hv), "+v"(rs));
        asm volatile("" : "+s"(op_));
        const u32 vp = (lane >> 2) * 2560 + p4 * 16;
        typedef __attribute__((ext_vector_type(16))) unsigned int u32x16;
        u32x16 keepa, keepb;
        f32x4 ys;
        u32 fin;
        asm volatile(
#define PLD(B0, B1, B2)                                                                             \
            "global_load_dwordx4 " B0 ", %[vp], %[p0] sc1\n\t"                                       \
            "global_load_dwordx4 " B1 ", %[vp], %[p1] sc1\n\tglobal_load_dwordx4 " B2 ", %[vp], %[p2] sc1\n\t"
#define PCHK(A0, A1, B0, B1, C0, C1)                                                                \
            "s_waitcnt vmcnt(3)\n\t"                                                                 \
            "v_pk_add_f32 v[96:97], " A0 ", " B0 "\n\tv_pk_add_f32 v[98:99], " A1 ", " B1 "\n\t"    \
            "v_pk_add_f32 v[96:97], v[96:97], " C0 "\n\tv_pk_add_f32 v[98:99], v[98:99], " C1 "\n\t" \
            "v_pk_add_f32 v[102:103], v[96:97], v[98:99]\n\t"                                        \
            "v_cmp_u_f32 vcc, v102, v103\n\ts_cbranch_vccz 9f\n\t"
            "s_mov_b32 s42, %[cap]\n\ts_sleep %[cdel]\n\t"
            PLD("v[64:67]", "v[68:71]", "v[72:75]")
            "s_sleep %[gap]\n\t"
            PLD("v[80:83]", "v[84:87]", "v[88:91]")
            "1:\n\t"
            PCHK("v[64:65]", "v[66:67]", "v[68:69]", "v[70:71]", "v[72:73]", "v[74:75]")
            PLD("v[64:67]", "v[68:71]", "v[72:75]")
            PCHK("v[80:81]", "v[82:83]", "v[84:85]", "v[86:87]", "v[88:89]", "v[90:91]")
            PLD("v[80:83]", "v[84:87]", "v[88:91]")
            "s_sub_u32 s42, s42, 1\n\ts_cbranch_scc0 1b\n"
            "9:\n\tds_read_b32 %[fin], %[af]"
            : "=&{v[96:99]}"(ys), "=&{v[64:79]}"(keepa), "=&{v[80:95]}"(keepb), [fin] "=&v"(fin)
            : [cap] "n"(SPIN_CAP), [gap] "n"(CGAP), [cdel] "n"(CDEL), [vp] "v"(vp), [p0] "s"(pp),
              [p1] "s"(pp + 40960), [p2] "s"(pp + 81920), [af] "v"((u32)(CTL_OFF + 40))
            : "memory", "vcc", "scc", "s42", "v102", "v103");
        float y = tr_sum(ys);
        y += dppf<0x124>(y);
        y += dppf<0x128>(y);
        const u32 res = cvt2((f32x2){__builtin_fmaf(y, rs, hv), 0.f});
        asm volatile("s_mov_b32 s42, %[cap]\n"
                     "1:\n\ts_waitcnt lgkmcnt(0)\n\tv_cmp_eq_u32 vcc, %[ok], %[fin]\n\ts_cbranch_vccz 2f\n\t"
                     "s_mov_b32 exec_lo, 0x000f000f\n\ts_mov_b32 exec_hi, 0x000f000f\n\tglobal_store_short %[vo], %[res], %[op]" "\n\ts_mov_b64 exec, -1\n"
                     "2:\n\tv_cmp_eq_u32 vcc, 0, %[fin]\n\ts_cbranch_vccz 3f\n\tds_read_b32 %[fin], %[af]\n\ts_sub_u32 s42, s42, 1\n\ts_cbranch_scc0 1b\n"
                     "3:\n\ts_waitcnt lgkmcnt(0)\n\tv_cmp_lt_u32 vcc, %[ok], %[fin]\n\ts_cbranch_vccz 4f\n\ts_waitcnt vmcnt(0)\n4:"
                     : [fin] "+v"(fin) : [cap] "n"(SPIN_CAP * 8), [ok] "s"(pass + 1), [vo] "v"(vo_), [res] "v"(res), [op] "s"(op_),
                       [af] "v"((u32)(CTL_OFF + 40)), "{v[64:79]}"(keepa), "{v[80:95]}"(keepb) : "memory", "vcc", "scc", "s42");
        if (pass || __builtin_amdgcn_readfirstlane(fin) != 2u) break;
        asm volatile("s_barrier\n\ts_barrier" ::: "memory");
        pp += WS_PART2 - WS_PART;
      }
    }
    return;
  }

  const int idx2 = (xcd & 1) * 26 + bidx;
  const int hg = ((xcd >> 1) & 1) * 4 + (idx2 & 3);
  const int mt = (xcd >> 2) * 13 + (idx2 >> 2);
#define ROTF (((idx2 >> 2) * 3 / 2 + (idx2 & 3) * 3) % 10)
  const int kb0 = ROTF;
  const int r0 = mt * 80;
  if (w >= 4) {
    const int cw = w - 4;
    u32 vo = w_off(cw, lane, hg), vx = x_off(cw, lane, r0), vx2 = x_off(8 + (cw & 3), lane, r0);
    asm volatile("" : "+v"(vo), "+v"(vx), "+v"(vx2));
    __builtin_amdgcn_sched_barrier(0);
    const char* wb = (const char*)wqkv + (size_t)kb0 * 245760 + W_ROW(cw);
    dma_m0<0>(vo, wb, cw * 1024);
    dma_m0<0>(vx, (const char*)x + kb0 * 128, XRING + cw * 1024);
    dma_m0<0>(vo, wb + 61440, 8192 + cw * 1024);
    if (cw < 4) dma_m0<0>(vx2, (const char*)x + kb0 * 128, XRING + 8192 + cw * 1024);
  }
  const int rt = w >= 4 ? (w & 3) : 4, kh = w >= 4 ? (w - 4) >> 2 : (w & 1);
  float bias[2];
  u32 sel[4], e4, eh, aq, aka, akb, pm, pf, hi, arec, vpp;
  const char *os, *os4, *sst, *oa[10];
  int so_[10];
  int lane_ = lane, mt_ = mt, hg_ = hg;
  auto prepv = [&](const int lane) __attribute__((always_inline)) {
    const int g = lane >> 4, r16 = lane & 15, q4 = (lane & 15) >> 2, p4 = lane & 3;
    const int par = lane & 1;
    const bool ca = r16 >= 4 * g, cb = r16 >= 4 * g + 2;
    bias[0] = (ca || r0 + rt > 0) ? 0.f : -__builtin_inff();
    bias[1] = (cb || r0 + rt > 0) ? 0.f : -__builtin_inff();
    const u32 sa = par ? 0x01000C0Cu : 0x0C0C0100u, sb = par ? 0x03020C0Cu : 0x0C0C0302u;
    sel[0] = ca ? 0x0C0C0C0Cu : sa;
    sel[1] = cb ? 0x0C0C0C0Cu : sb;
    sel[2] = ca ? sa : 0x0C0C0C0Cu;
    sel[3] = cb ? sb : 0x0C0C0C0Cu;
    e4 = EXO + rt * 512 + lane * 8;
    eh = e4 + kh * 6144;
    aq = TAB_OFF + 384 + (rt * 16 + r16) * 4;
    aka = TAB_OFF + 384 + (rt * 16 + 4 * g + par + (ca ? 16 : 0)) * 4;
    akb = TAB_OFF + 384 + (rt * 16 + 4 * g + par + 2 + (cb ? 16 : 0)) * 4;
    pm = (4 * g + q4) * 160 + p4 * 40;
    pf = (20 * g + 16 + q4) * 160 + p4 * 40;
    hi = HIM + (rt * 16 + r16) * 160 + g * 40;
    arec = REC_OFF + (r16 == 0 ? kh * 192 + g * 48 : (r16 == 8 ? ((g >> 1) == kh ? 480 : 384) : 432));
    vpp = lane + kh * 64 + (rt == 0 ? 768 : (rt == 2 ? 8192 : (rt == 4 ? 512 : 640)));
    asm volatile("" : "+v"(arec), "+v"(vpp));
    asm volatile("" : "+v"(bias[0]), "+v"(bias[1]), "+v"(sel[0]), "+v"(sel[1]), "+v"(sel[2]), "+v"(sel[3]), "+v"(e4), "+v"(eh),
                 "+v"(aq), "+v"(aka), "+v"(akb), "+v"(pm), "+v"(pf), "+v"(hi));
  };
  auto preps = [&](const int mt, const int hg) __attribute__((always_inline)) {
    const char* ob = ws + WS_O + (size_t)mt * 102400;
    os = ob + rt * 20480 + (2 * hg + kh) * 1024;
    os4 = ob + rt * 20480 + (16 + (hg >> 1)) * 1024 + (hg & 1) * 8;
    sst = ws + WS_STAT + (size_t)(2 * mt + (rt < 3 ? 0 : 1)) * 8192 + hg * 1024 + (rt < 3 ? rt : rt - 3) * 256;
    asm volatile("" : "+s"(os), "+s"(os4), "+s"(sst));
    const int k2 = w & 1;
#pragma unroll
    for (int j = 0; j < 10; ++j) {
      const bool oth = j >= 5 || w < 2;
      const int mo[8] = {OORD};
      const int ch = j % 5 < 4 ? (2 * hg + k2) ^ (oth ? 2 * mo[4 + j % 5] : 2 * mo[j % 5]) : (hg >> 1) ^ ((oth != ((hg & 1) != 0)) ? (k2 ? 0 : 3) : 1 + k2);
      oa[j] = ob + (j % 5 < 4 ? ch : 16 + ch) * 1024;
      so_[j] = ch * (j % 5 < 4 ? 6400 : 25600);
      asm volatile("" : "+s"(oa[j]), "+s"(so_[j]));
    }
  };
#define WO_OFFS                                                                                    \
  u32 woff[5];                                                                                     \
  _Pragma("unroll") for (int i = 0; i < 5; ++i) {                                                  \
    const int c = i * 64 + lane;                                                                   \
    const int row = c / 10;                                                                        \
    woff[i] = row * 1120 + hg * 160 + c * 16 + (4 - i) * 1024;                                     \
  }
#define WO_RUN(RHO)                                                                                \
  {                                                                                                \
    const char* wq_ = (const char*)wo + (RHO) * 40960; \
    dma_m0<-4096>(woff[0], wq_, (RHO) * 5120 + 4096);                                               \
    dma_nx<-3072>(woff[1], wq_); dma_nx<-2048>(woff[2], wq_); dma_nx<-1024>(woff[3], wq_); dma_nx<0>(woff[4], wq_); \
  }

#define BAR asm volatile("s_barrier" ::: "memory");
#define PREP_V __builtin_amdgcn_sched_barrier(0); asm volatile("" : "+v"(lane_)); prepv(lane_); __builtin_amdgcn_sched_barrier(0);
#define PREP_S __builtin_amdgcn_sched_barrier(0); asm volatile("" : "+s"(mt_), "+s"(hg_)); preps(mt_, hg_); __builtin_amdgcn_sched_barrier(0);
  if (w < 2) {
    const int l = w;
    __builtin_amdgcn_s_setprio(3);
    u32 vw[4];
#pragma unroll
    for (int i = 0; i < 4; ++i) vw[i] = w_off(i, lane, hg) + 4096;
    u32 go = (u32)kb0 * 245760u + 122880u + l * 61440u;
    u32 so = HSLOT + l * 8192 + 4096;
#define W1(I) dma_nx<((I) - 4) * 1024>(vw[(I) & 3], wb + (W_ROW(I) - (I) * 1024));
#define ISSUE_W                                                                                    \
  {                                                                                                \
    const char* wb = (const char*)wqkv + go;                                                       \
    dma_m0<-4096>(vw[0], wb, so);                                                                  \
    W1(1) W1(2) W1(3) W1(4) W1(5) W1(6) W1(7)                                                      \
    go += 122880u;                                                                                 \
    if (go >= 2457600u) go -= 2457600u;                                                            \
    so = so >= 5 * HSLOT ? so - 5 * HSLOT : so + HSLOT;                                            \
  }
    __builtin_amdgcn_s_sleep(2);
    ISSUE_W
    __builtin_amdgcn_sched_barrier(0);
    WO_OFFS
    asm volatile("" : "+v"(woff[0]), "+v"(woff[1]), "+v"(woff[2]), "+v"(woff[3]), "+v"(woff[4]));
    BAR
    ISSUE_W
    ISSUE_W
    asm volatile("s_waitcnt vmcnt(16)\n\ts_barrier" ::: "memory");
    asm volatile("s_waitcnt vmcnt(8)\n\ts_barrier" ::: "memory");
#pragma unroll 1
    for (int h = 1; h < 17; ++h) {
      ISSUE_W
      asm volatile("s_waitcnt vmcnt(8)\n\ts_barrier" ::: "memory");
    }
    asm volatile("s_waitcnt vmcnt(0)\n\ts_barrier" ::: "memory");
    WO_RUN(12 + l)
    const u32 tagw = lds_ld32(CTL_OFF);
    const char* bxf = ws + WS_BXF + mt * 1024;
    BAR
    const u32 town = (u32)__builtin_amdgcn_s_memrealtime();
    if (l == 0) {
      if (lane == 0) GST2((u32)(hg * 128), bxf, 0, ((u32x2){town, tagw}));
    }
    WO_RUN(18 + l)
    WO_RUN(8 + l)
    WO_RUN(l)
    const u32 vfl = (((lane & 7) == hg ? hg ^ 1 : (lane & 7))) * 128;
    u32x2 f;
    asm volatile("" : "=v"(f));
    BAR
    int gate = (int)((u32)__builtin_amdgcn_s_memrealtime() - town) + GADD;
    gate = gate > 200 ? 200 : gate;
    WO_RUN(2 + l)
    WO_RUN(4 + l)
    WO_RUN(6 + l)
    if (l == 1) asm volatile("global_load_dwordx2 %0, %1, %2 sc1" : "+v"(f) : "v"(vfl), "s"(bxf) : "memory");
    __builtin_amdgcn_s_setprio(0);
    pm = (4 * g + q4) * 160 + p4 * 40;
    pf = (20 * g + 16 + q4) * 160 + p4 * 40;
    asm volatile("" : "=v"(bias[0]), "=v"(bias[1]), "=v"(sel[0]), "=v"(sel[1]), "=v"(sel[2]), "=v"(sel[3]), "=v"(e4), "=v"(eh),
                 "=v"(aq), "=v"(aka), "=v"(akb), "+v"(pm), "+v"(pf), "=v"(hi), "=v"(arec), "=v"(vpp));
    PREP_S
    if (l == 1) {
      asm volatile("s_waitcnt vmcnt(0)" : "+v"(f)::"memory");
#pragma nounroll
      for (int it = 0; it < SPIN_CAP; ++it) {
        if (__all(f[1] == tagw)) break;
        asm volatile("global_load_dwordx2 %0, %1, %2 sc1\n\ts_waitcnt vmcnt(0)" : "=&v"(f) : "v"(vfl), "s"(bxf) : "memory");
      }
#pragma nounroll
      for (int it = 0; it < 64; ++it) {
        if (__all((int)((u32)__builtin_amdgcn_s_memrealtime() - f[0]) >= gate)) break;
      }
    }
    __builtin_amdgcn_s_setprio(0);
  } else if (w < 4) {
    __builtin_amdgcn_s_setprio(3);
    if (w == 2) {
      u32 vx[12];
#pragma unroll
      for (int i = 0; i < 12; ++i) vx[i] = x_off(i, lane, r0) + (4 - (i % 6)) * 1024;
      int kb = kb0 == 9 ? 0 : kb0 + 1;
      u32 xo = XRING + XSLOT + 4096;
#define ISSUE_X0                                                                                   \
  {                                                                                                \
    const char* xb = (const char*)x + kb * 128;                                                    \
    dma_m0<-4096>(vx[0], xb, xo);                                                                  \
    dma_nx<-3072>(vx[1], xb); dma_nx<-2048>(vx[2], xb); dma_nx<-1024>(vx[3], xb); dma_nx<0>(vx[4], xb); dma_nx<1024>(vx[5], xb); \
  }
#define ISSUE_X1                                                                                   \
  {                                                                                                \
    const char* xb = (const char*)x + kb * 128;                                                    \
    dma_m0<-4096>(vx[6], xb, xo + 6144);                                                           \
    dma_nx<-3072>(vx[7], xb); dma_nx<-2048>(vx[8], xb); dma_nx<-1024>(vx[9], xb); dma_nx<0>(vx[10], xb); dma_nx<1024>(vx[11], xb); \
    kb = kb == 9 ? 0 : kb + 1;                                                                     \
    xo = xo >= XRING + 3 * XSLOT ? xo - 3 * XSLOT : xo + XSLOT;                                    \
  }
      ISSUE_X0
      __builtin_amdgcn_sched_barrier(0);
      WO_OFFS
      asm volatile("" : "+v"(woff[0]), "+v"(woff[1]), "+v"(woff[2]), "+v"(woff[3]), "+v"(woff[4]));
      BAR
      ISSUE_X1
      ISSUE_X0
      BAR
      ISSUE_X1
      asm volatile("s_waitcnt vmcnt(12)\n\ts_barrier" ::: "memory");
#pragma unroll 1
      for (int j = 3; j < 10; ++j) {
        ISSUE_X0
        BAR
        ISSUE_X1
        asm volatile("s_waitcnt vmcnt(12)\n\ts_barrier" ::: "memory");
      }
      const char* xq = (const char*)x + (size_t)r0 * 1280;
      const int nv = mt == 25 ? 480 : 800;
#define RES1(I) if ((I) < 7 || (I) * 64 + lane < nv) dma_m0<(((I) % 5) - 4) * 1024>(woff[(I) % 5], xq + ((I) / 5) * 40960, HIM + ((I) / 5) * 5120 + 4096);
      PREP_V
      BAR
      PREP_S
      asm volatile("s_waitcnt vmcnt(0)\n\ts_barrier" ::: "memory");
      RES1(0) RES1(1) RES1(2) RES1(3) RES1(4) RES1(5) RES1(6)
      BAR
      RES1(7) RES1(8) RES1(9) RES1(10) RES1(11) RES1(12)
      if (lane < 10) dma_m0<0>(l16, (const char*)w_r + hg * 160, WR_OFF);
      BAR
    } else {
      u32x2 ep;
      u32x4 ra;
      u32 rb5;
      if (lane < 8) {
        *(u32x4*)(smem + ONES_OFF + lane * 16) = (u32x4){0x3F803F80u, 0x3F803F80u, 0x3F803F80u, 0x3F803F80u};
        *(u32x4*)(smem + CTL_OFF + lane * 16) = (u32x4){0u, 0u, 0u, 0u};
      }
      {
        const u32 o1 = lane == 26 || lane == 32 ? 0x3F803F80u : 0u;
        const u32 o2 = lane == 31 ? 0x3F803F80u : o1;
        if (lane < 33) *(u32x4*)(smem + REC_OFF + lane * 16) = (u32x4){o1, o1, o2, o2};
      }
      WO_OFFS
      asm volatile("" : "+v"(woff[0]), "+v"(woff[1]), "+v"(woff[2]), "+v"(woff[3]), "+v"(woff[4]));
      __builtin_amdgcn_s_sleep(TWS);
      if (lane < TWN)
        touch_line(lane == 4 ? (const char*)wo + hg * 160
                             : (lane >= 2 ? ws + WS_PK - 65536 + (lane - 2) * 524288 + bid * 128
                                          : (const char*)out + (size_t)(r0 + lane * 47) * 1280 + hg * 160));
      LDS_BARRIER();
      BAR BAR
      asm volatile("global_load_dwordx2 %0, %1, off sc1" : "=v"(ep) : "v"(ws + WS_EPOCH) : "memory");
      asm volatile("global_load_dwordx4 %0, %2, %4\n\tglobal_load_dword %1, %3, %4 offset:1024"
                   : "=&v"(ra), "=&v"(rb5) : "v"(l16), "v"((u32)(lane * 4)), "s"(w_r) : "memory");
      BAR BAR BAR
      PREP_V
      BAR
      PREP_S
      BAR
      asm volatile("s_waitcnt vmcnt(0)" : "+v"(ep), "+v"(ra), "+v"(rb5)::"memory");
      const u64 e1 = (((u64)ep[1] << 32) | ep[0]) + (ep[0] == SENT - 1u ? 2ull : 1ull);
      float swr = lo_f(rb5) + hi_f(rb5);
#pragma unroll
      for (int e = 0; e < 4; ++e) swr += lo_f(ra[e]) + hi_f(ra[e]);
      swr = wave_sum(swr);
      if (lane == 0) {
        *(u32x2*)(smem + CTL_OFF) = (u32x2){(u32)e1, (u32)(e1 >> 32)};
        *(float*)(smem + CTL_OFF + 40) = swr;
      }
      int ra0 = r0 - 16 + lane, ra1 = r0 + 48 + (lane & 31);
      ra0 = ra0 < 0 ? 0 : (ra0 > SEQ - 1 ? SEQ - 1 : ra0);
      ra1 = ra1 > SEQ - 1 ? SEQ - 1 : ra1;
      const int hd = lane / 5, pt = lane - hd * 5;
      u32 avp = REC_OFF + (hd >> 1) * 192 + (pt < 4 ? pt * 48 + (hd & 1) * 8 : hd * 48 + 16), vvp = lane < 20 ? (lane >> 1) * 128 + (lane & 1) * 16 : 0;
      u32 vf = (lane < 48 ? lane : 0) * 128, va0 = ra0 * 8, va1 = ra1 * 8, vt0 = TAB_OFF + lane * 4, vt1 = TAB_OFF + 256 + (lane & 31) * 4;
      asm volatile("" : "+v"(avp), "+v"(vvp), "+v"(vf), "+v"(va0), "+v"(va1), "+v"(vt0), "+v"(vt1));
#pragma nounroll
      for (int h = 7; h <= HK0; ++h) BAR
      u32x2 f, t0, t1;
      u32x4 vp4;
      GLD2(f, vf, ws + WS_HFLAG, 0);
      GLD2(t0, va0, ws + WS_LN1, 0);
      GLD2(t1, va1, ws + WS_LN1, 0);
      asm volatile("global_load_dwordx4 %0, %1, %2 sc1" : "=v"(vp4) : "v"(vvp), "s"(ws + WS_VP + hg * 1280) : "memory");
#pragma unroll
      for (int h = HK0 + 1; h <= 17; ++h) BAR
      WO_RUN(10)
      WO_RUN(11)
      BAR
      asm volatile("s_waitcnt vmcnt(10)" : "+v"(t0), "+v"(t1), "+v"(f), "+v"(vp4)::"memory");
#pragma nounroll
      for (int it = 0; it < SPIN_CAP; ++it) {
        if (__all(t0[1] == (u32)e1 && t1[1] == (u32)e1 && f[0] == (u32)e1 && f[1] == (u32)(e1 >> 32) && vp4[1] == (u32)e1 && vp4[3] == (u32)e1)) break;
        asm volatile("global_load_dwordx2 %2, %6, %9 sc1\n\tglobal_load_dwordx2 %0, %4, %8 sc1\n\tglobal_load_dwordx2 %1, %5, %8 sc1\n\t"
                     "global_load_dwordx4 %3, %7, %10 sc1\n\ts_waitcnt vmcnt(0)"
                     : "=&v"(t0), "=&v"(t1), "=&v"(f), "=&v"(vp4)
                     : "v"(va0), "v"(va1), "v"(vf), "v"(vvp), "s"(ws + WS_LN1), "s"(ws + WS_HFLAG), "s"(ws + WS_VP + hg * 1280) : "memory");
      }
      *(u32*)(smem + vt0) = t0[0] << 16;
      *(u32*)(smem + vt1) = t1[0] << 16;
      *(float*)(smem + vt0 + 384) = __builtin_amdgcn_rcpf(hi_f(t0[0]));
      *(float*)(smem + vt1 + 384) = __builtin_amdgcn_rcpf(hi_f(t1[0]));
      if (lane < 20) *(u32x2*)(smem + avp) = (u32x2){vp4[0], vp4[2]};
      asm volatile("" ::: "memory");
      WO_RUN(14)
      WO_RUN(15)
      LDS_BARRIER();
      WO_RUN(16)
      WO_RUN(17)
    }
    __builtin_amdgcn_s_setprio(0);
  } else {
    const int cw = w - 4;
    prepv(lane);
    preps(mt, hg);
    if (cw >= 4) __builtin_amdgcn_s_setprio(1);
    const int xa0 = XRING + r16 * 128 + ((g ^ ((r16 >> 1) & 7)) * 16);
    const u32 m1 = r16 == 0 ? 0u : 1u;
    const u32 xz0 = r16 == 0 ? (u32)ONES_OFF : (u32)xa0;
    int wa[2];
#pragma unroll
    for (int c = 0; c < 2; ++c) {
      const int ct = cw < 7 ? 2 * cw + c : 14;
      const int seg = ct / 5, tt = ct - seg * 5;
      const int G = tt < 4 ? 5 * tt + p4 : 5 * p4 + 4;
      wa[c] = (16 * (g >> 1) + 4 * (g & 1) + q4) * 512 + ((seg * 160 + G * 8) ^ ((4 * (g & 1) + q4) << 5));
    }
    auto run = [&](auto tyc) __attribute__((always_inline)) {
      constexpr int TY = decltype(tyc)::value;
      constexpr int NW = TY == 2 ? 1 : 2;
      f32x4 acc[2][6];
#pragma unroll
      for (int c = 0; c < 2; ++c)
#pragma unroll
        for (int r = 0; r < 6; ++r) acc[c][r] = (f32x4){0.f, 0.f, 0.f, 0.f};
      u32x4 xA[6], xB[6];
      u32x2 wA[4], wB[4];
#define RX(D, A, O) asm volatile("ds_read_b128 %0, %1 offset:%2" : "=v"(D) : "v"(A), "n"(O) : "memory");
#define RW(D, A, O) asm volatile("ds_read_b64_tr_b16 %0, %1 offset:%2" : "=v"(D) : "v"(A), "n"(O) : "memory");
#define MM(C, R, XF, WF)                                                                           \
  if ((C) < NW) {                                                                                    \
    const bf16x8 wf_ = mk8(WF[2 * (C)][0], WF[2 * (C)][1], WF[2 * (C) + 1][0], WF[2 * (C) + 1][1]); \
    const bf16x8 xf_ = __builtin_bit_cast(bf16x8, XF[R]);                                          \
    acc[C][R] = TY == 0 ? MFMA(wf_, xf_, acc[C][R]) : MFMA(xf_, wf_, acc[C][R]);                   \
    __builtin_amdgcn_sched_barrier(0);                                                             \
  }
#define READS(QX, QW)                                                                              \
  RW(QW[0], aw0, 0) RW(QW[1], aw0, 4096) RX(QX[1], ax, 2048)                                       \
  if (NW == 2) { RW(QW[2], aw1, 0) RW(QW[3], aw1, 4096) }                                          \
  RX(QX[2], ax, 4096) RX(QX[3], ax, 6144) RX(QX[4], ax, 8192) RX(QX[5], ax, 10240) RX(QX[0], az, 0)
#define ARRIVED(PX, PW)                                                                            \
  if (NW == 2)                                                                                     \
    asm volatile("s_waitcnt lgkmcnt(0)" : "+v"(PX[0]), "+v"(PX[1]), "+v"(PX[2]), "+v"(PX[3]), "+v"(PX[4]), "+v"(PX[5]), \
                 "+v"(PW[0]), "+v"(PW[1]), "+v"(PW[2]), "+v"(PW[3])::"memory");                    \
  else                                                                                             \
    asm volatile("s_waitcnt lgkmcnt(0)" : "+v"(PX[0]), "+v"(PX[1]), "+v"(PX[2]), "+v"(PX[3]), "+v"(PX[4]), "+v"(PX[5]), \
                 "+v"(PW[0]), "+v"(PW[1])::"memory");                                              \
  __builtin_amdgcn_sched_barrier(0);
#define SB __builtin_amdgcn_sched_barrier(0);
#define WT(N, ...) asm volatile("s_waitcnt lgkmcnt(" #N ")" : __VA_ARGS__::"memory"); SB
#define HSTAGE2(PX, PW, QX, QW, NX)                                                                \
  BAR                                                                                              \
  WT(7, "+v"(PW[0]), "+v"(PW[1]), "+v"(PX[1]))                                                     \
  MM(0, 1, PX, PW) RW(QW[0], aw0, 0)                                                               \
  WT(6, "+v"(PW[2]), "+v"(PW[3]))                                                                  \
  MM(1, 1, PX, PW) RW(QW[1], aw0, 4096)                                                            \
  WT(6, "+v"(PX[2]))                                                                               \
  MM(0, 2, PX, PW) RX(QX[1], ax, 2048) SB                                                          \
  MM(1, 2, PX, PW) RW(QW[2], aw1, 0)                                                               \
  WT(7, "+v"(PX[3]))                                                                               \
  MM(0, 3, PX, PW) RW(QW[3], aw1, 4096) SB                                                         \
  MM(1, 3, PX, PW) RX(QX[2], ax, 4096)                                                             \
  WT(6, "+v"(PX[4]), "+v"(PX[5]), "+v"(PX[0]))                                                     \
  MM(0, 4, PX, PW) RX(QX[3], ax, 6144) SB                                                          \
  MM(1, 4, PX, PW) RX(QX[4], ax, 8192) SB                                                          \
  MM(0, 5, PX, PW) RX(QX[5], ax, 10240) SB                                                         \
  MM(1, 5, PX, PW) RX(QX[0], az, 0) SB                                                             \
  MM(0, 0, PX, PW)                                                                                 \
  NX asm volatile("" : "+v"(aw0), "+v"(aw1), "+v"(ax), "+v"(az), "+s"(so), "+s"(xo)); SB           \
  MM(1, 0, PX, PW)
#define HSTAGE1(PX, PW, QX, QW, NX)                                                                \
  BAR                                                                                              \
  ARRIVED(PX, PW)                                                                                  \
  MM(0, 1, PX, PW) RW(QW[0], aw0, 0) RW(QW[1], aw0, 4096) SB                                       \
  MM(0, 2, PX, PW) RX(QX[1], ax, 2048) RX(QX[2], ax, 4096) SB                                      \
  MM(0, 3, PX, PW) RX(QX[3], ax, 6144) RX(QX[4], ax, 8192) SB                                      \
  MM(0, 4, PX, PW) RX(QX[5], ax, 10240) RX(QX[0], az, 0) SB                                        \
  MM(0, 5, PX, PW)                                                                                 \
  NX asm volatile("" : "+v"(aw0), "+v"(ax), "+v"(az), "+s"(so), "+s"(xo)); SB                      \
  MM(0, 0, PX, PW)
#define HSTAGE(...) if (NW == 2) { HSTAGE2(__VA_ARGS__) } else { HSTAGE1(__VA_ARGS__) }
      u32 aw0 = wa[0], aw1 = wa[1], ax = xa0, az = xz0;
      asm volatile("s_waitcnt vmcnt(0)\n\ts_barrier" ::: "memory");
      READS(xA, wA)
      aw0 += HSLOT; aw1 += HSLOT; ax ^= 64; az ^= 64;
      int so = 2 * HSLOT, xo = XSLOT;
#pragma unroll 1
      for (int s = 0; s < 9; ++s) {
        HSTAGE(xA, wA, xB, wB, aw0 = wa[0] + so; aw1 = wa[1] + so; ax = xa0 + xo; az = xz0 + xo * m1;
               so = so >= 4 * HSLOT ? 0 : so + 2 * HSLOT; xo = xo >= 3 * XSLOT ? 0 : xo + XSLOT;)
        HSTAGE(xB, wB, xA, wA, aw0 += HSLOT; aw1 += HSLOT; ax ^= 64; az ^= 64;)
      }
      {
        HSTAGE(xA, wA, xB, wB, )
        ARRIVED(xB, wB)
        BAR
        SB
      }
      char* ex = smem + EXO + cw * 6144 + lane * 8;
      if (TY == 0) {
        float st[6];
        const u32 ta = TAB_OFF + r16 * 4;
#define RT(R) asm volatile("ds_read_b32 %0, %1 offset:%2" : "=v"(st[R]) : "v"(ta), "n"(64 * (R)) : "memory");
        RT(1) RT(2) RT(3) RT(4) RT(5) RT(0)
        float cs[2][4];
#define FMS(A, M, CS) ((A) - (M) * (CS))
#define EP0(C, R)                                                                                  \
  {                                                                                                \
    const u32 a = cvt2((f32x2){FMS(acc[C][R][0], st[R], cs[C][0]), FMS(acc[C][R][1], st[R], cs[C][1])}); \
    const u32 b = cvt2((f32x2){FMS(acc[C][R][2], st[R], cs[C][2]), FMS(acc[C][R][3], st[R], cs[C][3])}); \
    *(u32x2*)(ex + (C) * 3072 + (R) * 512) = (u32x2){a, b};                                        \
    SB                                                                                             \
  }
        MM(0, 0, xB, wB) MM(1, 0, xB, wB) MM(0, 1, xB, wB) MM(1, 1, xB, wB)
#pragma unroll
        for (int i = 0; i < 4; ++i) cs[0][i] = dppf<0x150>(acc[0][0][i]);
        SB
        MM(0, 2, xB, wB)
#pragma unroll
        for (int i = 0; i < 4; ++i) cs[1][i] = dppf<0x150>(acc[1][0][i]);
        WT(0, "+v"(st[0]), "+v"(st[1]), "+v"(st[2]), "+v"(st[3]), "+v"(st[4]), "+v"(st[5]))
        MM(1, 2, xB, wB) EP0(0, 1)
        MM(0, 3, xB, wB) EP0(1, 1)
        MM(1, 3, xB, wB) EP0(0, 2)
        MM(0, 4, xB, wB) EP0(1, 2)
        MM(1, 4, xB, wB) EP0(0, 3)
        MM(0, 5, xB, wB) EP0(1, 3)
        MM(1, 5, xB, wB)
        if (cw >= 2) {
          EP0(1, 0)
          if (cw >= 3) EP0(0, 0)
        }
        EP0(0, 4) EP0(1, 4) EP0(0, 5) EP0(1, 5)
      } else {
        f32x4 mbv[6];
        const u32 ta = TAB_OFF + g * 16;
#define RV(R) RX(mbv[R], ta, 64 * (R))
        RV(1) RV(2) RV(3) RV(4) RV(5) RV(0)
        float cs[2];
#define CS1(C)                                                                                     \
  if ((C) < NW) {                                                                                  \
    const u32 v0 = __float_as_uint(acc[C][0][0]);                                                  \
    const u32 v1 = __builtin_amdgcn_permlane16_swap(v0, v0, false, false)[0];                      \
    cs[C] = __uint_as_float(__builtin_amdgcn_permlane32_swap(v1, v1, false, false)[0]);            \
    SB                                                                                             \
  }
#define EP1(C, R)                                                                                  \
  if ((C) < NW) {                                                                                  \
    const f32x4 v = (f32x4){FMS(acc[C][R][0], mbv[R][0], cs[C]), FMS(acc[C][R][1], mbv[R][1], cs[C]), FMS(acc[C][R][2], mbv[R][2], cs[C]), FMS(acc[C][R][3], mbv[R][3], cs[C])}; \
    *(u32x2*)(ex + (C) * 3072 + (R) * 512) = (u32x2){cvt2((f32x2){v[0], v[1]}), cvt2((f32x2){v[2], v[3]})}; \
    SB                                                                                             \
  }
        MM(0, 0, xB, wB) MM(1, 0, xB, wB) MM(0, 1, xB, wB) MM(1, 1, xB, wB)
        CS1(0)
        MM(0, 2, xB, wB)
        CS1(1)
        WT(0, "+v"(mbv[0]), "+v"(mbv[1]), "+v"(mbv[2]), "+v"(mbv[3]), "+v"(mbv[4]), "+v"(mbv[5]))
        MM(1, 2, xB, wB) EP1(0, 1)
        MM(0, 3, xB, wB) EP1(1, 1)
        MM(1, 3, xB, wB) EP1(0, 2)
        MM(0, 4, xB, wB) EP1(1, 2)
        MM(1, 4, xB, wB) EP1(0, 3)
        MM(0, 5, xB, wB) EP1(1, 3)
        MM(1, 5, xB, wB)
        EP1(0, 0) EP1(1, 0) EP1(0, 4) EP1(1, 4) EP1(0, 5) EP1(1, 5)
      }
    };
    if (cw < 5) run(icst<0>{});
    else if (cw < 7) run(icst<1>{});
    else run(icst<2>{});
  }
  __builtin_amdgcn_s_setprio(0);
  const bool live = r0 + rt * 16 < SEQ;
  if (!live) {
    if (w >= 2) LDS_BARRIER();
    if (w != 7) {
      asm volatile("s_waitcnt vmcnt(0)" ::: "memory");
      return;
    }
  } else if (w >= 2)
    asm volatile(ATT_ASM
                 :: [kh] "s"(kh), [l16] "v"(l16), [os] "s"(os), [os4] "s"(os4), [b0] "v"(bias[0]), [b1] "v"(bias[1]),
                    [s0] "v"(sel[0]), [s1] "v"(sel[1]), [s2] "v"(sel[2]), [s3] "v"(sel[3]), [e4] "v"(e4), [eh] "v"(eh), [aq] "v"(aq),
                    [aka] "v"(aka), [akb] "v"(akb), [arec] "v"(arec), [vpp] "v"(vpp), [sst] "s"(sst), [w] "s"(w)
                 : "memory", "s41", "s46", "s47", "s48", "s49", "s50", "s51", "scc", "v24", "v25", "v26", "v27", "v28", "v29", "v30", "v31", "v32", "v33", "v34", "v35", "v36", "v37", "v38", "v39", "v40", "v41", "v42", "v43", "v44", "v45", "v46", "v47", "v48", "v49", "v50", "v51", "v52", "v53", "v54", "v55", "v56", "v57", "v58", "v59", "v60", "v61", "v62", "v63", "v64", "v65", "v66", "v67", "v68", "v69", "v70", "v71", "v72", "v73", "v74", "v75", "v76", "v77", "v78", "v79", "v80", "v81", "v82", "v83", "v84", "v85", "v86", "v87");
  u32x4 hwa, hwb;
  u32x2 hwc;
  const int grp = rt < 3 ? 0 : 1;
  if (w < 8) {
    const int rmax = mt == 25 ? 2 : 4;
    const int rk = rt < rmax ? rt : rmax, rd = (rt ^ 1) < rmax ? (rt ^ 1) : rmax;
    u32 ta, tk;
    if (w == 0) asm volatile("s_waitcnt vmcnt(0)" ::: "memory");
    if (w == 2 || w == 3) asm volatile("s_waitcnt vmcnt(3)" ::: "memory");
    asm volatile("s_waitcnt lgkmcnt(0)\n\ts_barrier\n\t" OP_ASM
                 : "=&{v[84:87]}"(hwa), "=&{v[88:91]}"(hwb), "=&{v[92:93]}"(hwc), [ta] "=&v"(ta), [tk] "=&v"(tk)
                 : [w] "s"(w), [l16] "v"(l16), [oa0] "s"(oa[0]), [so0] "s"(so_[0]), [oa1] "s"(oa[1]), [so1] "s"(so_[1]), [oa2] "s"(oa[2]), [so2] "s"(so_[2]), [oa3] "s"(oa[3]), [so3] "s"(so_[3]), [oa4] "s"(oa[4]), [so4] "s"(so_[4]), [oa5] "s"(oa[5]), [so5] "s"(so_[5]), [oa6] "s"(oa[6]), [so6] "s"(so_[6]), [oa7] "s"(oa[7]), [so7] "s"(so_[7]), [oa8] "s"(oa[8]), [so8] "s"(so_[8]), [oa9] "s"(oa[9]), [so9] "s"(so_[9]),
                   [pm] "v"(pm), [pf] "v"(pf), [ra] "v"(w < 3 ? (u32)(CTL_OFF + 72) : hi), [e4] "v"(e4),
                   [aw] "v"((lane & 1) ? (u32)(WR_OFF + g * 40) : (u32)ONES_OFF),
                   [vk] "v"(l16 + rk * 20480), [vd] "v"(l16 + rd * 20480), [sk] "s"((w & 1) * 6144),
                   [sd] "s"(EXO + (w < 4 ? 4 + w : rt ^ 1) * 5120), [sr] "s"(EXO + rt * 5120),
                   [sc] "s"(CTL_OFF + 48 + (w < 4 ? 4 : rt ^ 1) * 4), [sp] "s"(PST_OFF + rt * 256), [sa] "s"(CTL_OFF + 8 + grp * 4),
                   [sst] "s"(sst), [cp] "s"(ws + WS_PC + (2 * mt + grp) * 1280 + hg * 160), [oback] "n"(OBACK), [cap] "n"(SPIN_CAP)
                 : "memory", "vcc", "scc", "s43", "v24", "v25", "v26", "v27", "v28", "v29", "v30", "v31", "v32", "v33", "v34", "v35", "v36", "v37", "v38", "v39", "v40", "v41", "v42", "v43", "v44", "v45", "v46", "v47", "v48", "v49", "v50", "v51", "v52", "v53", "v54", "v55", "v56", "v57", "v58", "v59", "v60", "v61", "v62", "v63", "v64", "v65", "v66", "v67", "v68", "v69", "v70", "v71", "v72", "v73", "v74", "v75", "v76", "v77", "v78", "v79", "v80", "v81", "v82", "v83", "v94", "v95", "v96", "v97", "v98", "v99", "v100", "v101", "v102", "v103", "v104", "v105", "v106", "v107", "v108", "v109", "v110", "v111", "v112", "v113", "v114", "v115", "v116", "v117", "v118", "v119", "v120", "v121", "v122", "v123", "v124", "v125", "v126", "v127", "v128", "v129", "v130", "v131", "v132", "v133", "v134", "v135", "v136", "v137", "v138", "v139", "v140", "v141", "v142", "v143", "v144", "v145", "v146", "v147", "v148", "v149", "v150", "v151", "v152", "v153", "v154", "v155", "v156", "v157", "v158", "v159");
    if (!live || w < 3) return;
  } else {
    u64 tb;
    asm volatile("s_setprio 3\n\ts_waitcnt lgkmcnt(0)\n\ts_barrier\n\ts_memrealtime %0\n\ts_waitcnt lgkmcnt(0)" : "=s"(tb)::"memory");
    if (w == 8 || w == 11) {
      const int cg = w == 11, gid = 2 * mt + cg;
      const char* sp = sst - hg * 1024;
      const char* sp2 = sp + (cg ? 0 : 7936);
      const u32 vq = (lane >> 3) * 1024 + (lane & 7) * 16 + (cg ? 640 : 768);
      u32x4 pq[3];
      u64 t2;
#define PLOADS "global_load_dwordx4 %0, %4, %5 sc1\n\tglobal_load_dwordx4 %1, %4, %5 offset:128 sc1\n\tglobal_load_dwordx4 %2, %4, %6 sc1\n\t"
      asm volatile("1:\n\ts_memrealtime %3\n\ts_waitcnt lgkmcnt(0)\n\ts_sub_u32 s40, %7, s40\n\ts_cmp_gt_i32 s40, 0\n\ts_cbranch_scc1 1b\n\t" PLOADS "s_setprio 1"
                   : "=&v"(pq[0]), "=&v"(pq[1]), "=&v"(pq[2]), "=&{s[40:41]}"(t2) : "v"(vq), "s"(sp), "s"(sp2), "s"((u32)tb + PDT) : "memory", "scc");
      int lane2 = lane;
      asm volatile("" : "+v"(lane2));
      const int n16 = lane2 & 15;
      u32x4 sl;
      u32x2 km;
#pragma unroll
      for (int e = 0; e < 4; ++e) sl[e] = n16 == 2 * e ? 0x3F80u : (n16 == 2 * e + 1 ? 0x3F800000u : 0u);
      u32 rid = (u32)((((lane2 >> 4) & 1) * 2 + (lane2 >> 5)) * 4 + (lane2 & 3)) << 10;
      u32 rid1 = rid + (16 << 10), rid2 = rid + (cg ? 0 : 32 << 10), msk = n16 < 4 ? 0xFFFF0000u : 0u;
      u32 ah = HIM + cg * 7680 + (lane2 < 10 ? lane2 : 9) * 16;
      km[1] = *(const u32*)(smem + CTL_OFF);
      const char* kp = ws + WS_PK + gid * 6144 + hg * 768;
      const char* cp = ws + WS_PC + gid * 1280 + hg * 160;
      asm volatile("" : "+s"(kp), "+s"(cp));
      km[0] = ((u32)tb >> 1) & 1023u;
      asm volatile("s_setprio " "3" "\n\ts_waitcnt vmcnt(0)" : "+v"(pq[0]), "+v"(pq[1]), "+v"(pq[2]), "+v"(sl), "+v"(rid), "+v"(rid1), "+v"(rid2), "+v"(msk), "+v"(ah), "+v"(km)::"memory");
#define PMISS __any(max(max(max(pq[0][0], pq[0][2]), max(pq[1][0], pq[1][2])), max(pq[2][0], pq[2][2])) == SENT)
      if (__builtin_expect(PMISS, 0)) {
#pragma nounroll
        for (int it = 0; it < SPIN_CAP; ++it) {
          asm volatile(PLOADS "s_waitcnt vmcnt(0)" : "=&v"(pq[0]), "=&v"(pq[1]), "=&v"(pq[2]), "=&s"(t2) : "v"(vq), "s"(sp), "s"(sp2) : "memory");
          if (!PMISS) break;
          __builtin_amdgcn_s_sleep(2);
        }
      }
      u32 pkey = 0;
#pragma unroll
      for (int t = 0; t < 3; ++t) {
        const f32x4 ys = MFMA(__builtin_bit_cast(bf16x8, pq[t]), __builtin_bit_cast(bf16x8, sl), ((f32x4){0.f, 0.f, 0.f, 0.f}));
        const float y = tr_sum(ys);
        pkey = max(pkey, (mono(y * __builtin_amdgcn_rcpf(dppf<0x104>(y))) & msk) | (t == 0 ? rid : (t == 1 ? rid1 : rid2)));
      }
      pkey = max(pkey, dppu<0xB1>(pkey));
      pkey = max(pkey, dppu<0x4E>(pkey));
      pkey = max(max((u32)__builtin_amdgcn_readlane(pkey, 0), (u32)__builtin_amdgcn_readlane(pkey, 16)),
                 max((u32)__builtin_amdgcn_readlane(pkey, 32), (u32)__builtin_amdgcn_readlane(pkey, 48)));
      km[0] |= pkey;
      asm volatile("s_mov_b32 exec_lo, 0x01010101\n\ts_mov_b32 exec_hi, 0x0101\n\tglobal_store_dwordx2 %0, %1, %2 sc1\n\ts_mov_b64 exec, -1\n\ts_setprio 2" ::"v"(l16), "v"(km), "s"(kp) : "memory");
      ah += ((pkey >> 10) & 63u) * 160;
      if (lane == 0) *(u32*)(smem + CTL_OFF + 24 + cg * 4) = ah;
      __builtin_amdgcn_s_setprio(0);
#pragma nounroll
      for (int it = 0; it < 32; ++it) {
        if ((int)((u32)__builtin_amdgcn_s_memrealtime() - (u32)tb) >= PSL) break;
        __builtin_amdgcn_s_sleep(6);
      }
      __builtin_amdgcn_s_setprio(2);
      asm volatile("s_mov_b32 s40, %[cap]\n"
                   "1:\n\tds_read_b32 v100, %[ac]\n\tds_read_b32 v101, %[ac] offset:24\n\tds_read_b128 v[96:99], %[ah]\n\ts_sub_u32 s40, s40, 1\n\ts_cbranch_scc1 2f\n\ts_waitcnt lgkmcnt(0)\n\t"
                   "v_cmp_gt_u32 vcc, %[nf], v100\n\ts_cbranch_vccz 2f\n\ts_sleep 2\n\ts_branch 1b\n"
                   "2:\n\ts_waitcnt lgkmcnt(0)\n\tv_cmp_eq_u32 vcc, 0, v101\n\ts_cbranch_vccz 3f\n\t"
                   "s_mov_b64 exec, 0x3ff\n\tglobal_store_dwordx4 %[l16], v[96:99], %[cp] sc1\n\ts_mov_b64 exec, -1\n3:"
                   ::[cap] "n"(SPIN_CAP * 8), [ac] "v"((u32)(CTL_OFF + 8 + cg * 4)), [ah] "v"(ah), [nf] "s"(3 - cg), [l16] "v"(l16), [cp] "s"(cp)
                   : "memory", "vcc", "scc", "s40", "v96", "v97", "v98", "v99", "v100", "v101");
      return;
    }
    __builtin_amdgcn_s_setprio(0);
    if (w == 10 && mt == 25) return;
    const int cg = w - 9, gid = 2 * mt + cg;
    const int nr = cg ? 31 : 47;
    const int lr = lane < nr ? lane : nr;
    const int lrow = cg * 48 + lr;
    const int li = (lane & 31) < 20 ? (lane & 31) : 19;
    asm volatile(COL_ASM
                 :
                 : [lq] "v"((u32)(lr * 16 + hg * 1024)), [ac] "v"((u32)(CTL_OFF + 8 + cg * 4)), [nf] "s"(3 - cg),
                   [sp] "s"(ws + WS_STAT + (size_t)gid * 8192), [ao] "v"((u32)(PST_OFF + lrow * 16)), [rdm] "s"(1.0f / DM),
                   [swr] "v"(*(const float*)(smem + CTL_OFF + 40)), [msk] "s"(0xFFFFF000u), [rid] "v"(0x800u | (u32)(2047 - (r0 + lrow))),
                   [at] "v"((u32)(TAB_OFF + lrow * 8)), [zr] "v"(l16), "{v99}"(*(const u32*)(smem + CTL_OFF)),
                   [kp] "s"(ws + WS_PK + gid * 6144 + hg * 768 + 8), [ak] "v"((u32)(CTL_OFF + 16 + cg * 4)), [rb] "s"(2047 - r0 - cg * 48),
                   [ah] "v"((u32)(HIM + cg * 7680 + li * 8)), [co] "v"((u32)((lane < 32 ? 1280 : 0) + li * 8)),
                   [cp] "s"(ws + WS_CAND + gid * 2560 + hg * 160), [pdel] "n"(PDEL2), [cap] "n"(SPIN_CAP)
                 : "memory", "vcc", "scc", "s40", "s41", "s42", "s43", "s44", "s45", "s46", "s47", "v64", "v65", "v66", "v67", "v68", "v69", "v70", "v71", "v72", "v73", "v74", "v75", "v76", "v77", "v78", "v79", "v80", "v81", "v82", "v83", "v84", "v85", "v86", "v87", "v88", "v89", "v90", "v91", "v92", "v93", "v94", "v95", "v96", "v97", "v98", "v100", "v101", "v102", "v103", "v104", "v105", "v106", "v107", "v108", "v109", "v110", "v111", "v112", "v113", "v114", "v115", "v116", "v117", "v118", "v119", "v120", "v121", "v122", "v123");
    return;
  }
  const u32 hw[10] = {hwa[0], hwa[1], hwa[2], hwa[3], hwb[0], hwb[1], hwb[2], hwb[3], hwc[0], hwc[1]};
  f32x2 hf[10];
#pragma unroll
  for (int e = 0; e < 10; ++e) {
    hf[e] = unpk(hw[e]);
    asm volatile("" : "+v"(hf[e]));
  }
  const char* obase = (const char*)out + (size_t)r0 * 1280 + hg * 160;
  u32 tilekey;
  asm volatile("" : "+s"(obase));
  u32 aow = EXO + rt * 5120 + r16 * 160 + g * 40, aor = EXO + rt * 5120 + l16, vo3[3], rr3[3];
#pragma unroll
  for (int i = 0; i < 3; ++i) {
    const u32 c = i * 64 + lane, r = c / 10u;
    vo3[i] = (rt * 16 + r) * 1120 + rt * 2560 + c * 16;
    rr3[i] = c < 160u ? 2047 - (r0 + rt * 16 + r) : 4096u;
    asm volatile("" : "+v"(vo3[i]), "+v"(rr3[i]));
  }
  asm volatile("" : "+v"(aow), "+v"(aor));
  f32x2 st2;
  asm volatile("s_mov_b32 s40, %[cap]\n"
               "1:\n\ts_sleep 1\n\tds_read_b32 %[k], %[ak]\n\tds_read_b64 %[s], %[at]\n\ts_sub_u32 s40, s40, 1\n\ts_waitcnt lgkmcnt(0)\n\ts_cbranch_scc1 2f\n\t"
               "v_cmp_eq_u32 vcc, 0, %[k]\n\ts_cbranch_vccnz 1b\n"
               "2:"
               : [k] "=&v"(tilekey), [s] "=&v"(st2) : [cap] "n"(SPIN_CAP * 8), [ak] "v"((u32)(CTL_OFF + 16 + grp * 4)), [at] "v"((u32)(TAB_OFF + (rt * 16 + r16) * 8))
               : "memory", "vcc", "scc", "s40");
  u32 ok[10];
#pragma unroll
  for (int e = 0; e < 10; ++e) ok[e] = cvt2(__builtin_elementwise_fma(hf[e] - (f32x2){st2[0], st2[0]}, (f32x2){st2[1], st2[1]}, hf[e]));
  {
#pragma unroll
    for (int e = 0; e < 5; ++e) asm volatile("ds_write_b64 %0, %1 offset:%2" ::"v"(aow), "v"((u32x2){ok[2 * e], ok[2 * e + 1]}), "n"(e * 8) : "memory");
    u32x4 o3[3];
    asm volatile("ds_read_b128 %0, %3\n\tds_read_b128 %1, %3 offset:1024\n\tds_read_b128 %2, %3 offset:2048\n\ts_waitcnt lgkmcnt(0)"
                 : "=&v"(o3[0]), "=&v"(o3[1]), "=&v"(o3[2]) : "v"(aor) : "memory");
    if (OSL) __builtin_amdgcn_s_sleep(OSL);
#pragma unroll
    for (int i = 0; i < 3; ++i)
      if (rr3[i] != (tilekey & 2047u) && rr3[i] != 4096u)
        asm volatile("global_store_dwordx4 %0, %1, %2" " sc1" "\n\ts_nop 1" ::"v"(vo3[i]), "v"(o3[i]), "s"(obase) : "memory");
  }
}

extern "C" void kernel_launch(void* const* d_in, const int* in_sizes, int n_in,
                              void* d_out, int out_size, void* d_ws, size_t ws_size,
                              hipStream_t stream) {
  const u16* x = (const u16*)d_in[0];
  const u16* wqkv = (const u16*)d_in[1];
  const u16* wo = (const u16*)d_in[2];
  const u16* w_r = (const u16*)d_in[4];
  const u16* w_gate = (const u16*)d_in[5];
  const u16* w_up = (const u16*)d_in[6];
  const u16* w_down = (const u16*)d_in[7];
  (void)hipFuncSetAttribute((const void*)fused_block, hipFuncAttributeMaxDynamicSharedMemorySize, LDS_TOTAL);
  fused_block<<<256, 768, LDS_TOTAL, stream>>>(x, wqkv, wo, w_r, w_gate, w_up, w_down, (u16*)d_out, (char*)d_ws);
}
```

```cpp
#include <hip/hip_runtime.h>
#include <hip/hip_bf16.h>
#include <stdint.h>


#define SEQ 2048
#define DM 640
#define FF 1536

typedef __attribute__((ext_vector_type(8))) short bf16x8;
typedef __attribute__((ext_vector_type(4))) short s16x4;
typedef __attribute__((ext_vector_type(4))) float f32x4;
typedef __attribute__((ext_vector_type(2))) float f32x2;
typedef __attribute__((ext_vector_type(2))) __bf16 bf16x2_t;
typedef __attribute__((address_space(3))) s16x4* lds_s16x4_p;
typedef unsigned short u16;
typedef unsigned int u32;
typedef unsigned long long u64;
typedef __attribute__((ext_vector_type(4))) unsigned int u32x4;
typedef __attribute__((ext_vector_type(2))) unsigned int u32x2;

#define HK0 15
#define CGAP 6
#define KGAP 7
#define CDEL 10
#define PDEL2 7
#define OBACK 2
#define PDELAY 220
#define FDELAY 450
#define KSTART 1050
#define PGAP 5
#define PDT 18
#define PSL 185
#define VDELAY 150
#define CCD 312
#define TQD 160
#define KCD 306
#define KOLD 316
#define TWS 12
#define PCD 230
#define PTHR 0.91f
#define GADD 61
#define OORD 0, 4, 5, 6, 7, 1, 2, 3
#define CODE_BYTES 35192

#define LDS_BARRIER() do { __builtin_amdgcn_sched_barrier(0); asm volatile("s_waitcnt lgkmcnt(0)\n\ts_barrier" ::: "memory"); __builtin_amdgcn_sched_barrier(0); } while (0)
__device__ __forceinline__ float bf2f(u16 v) { return __uint_as_float(((u32)v) << 16); }
__device__ __forceinline__ u16 f2bf(float f) { return __builtin_bit_cast(u16, (__bf16)f); }
__device__ __forceinline__ float rbf(float f) { return bf2f(f2bf(f)); }
__device__ __forceinline__ float lo_f(u32 w) { return __uint_as_float(w << 16); }
__device__ __forceinline__ float hi_f(u32 w) { return __uint_as_float(w & 0xFFFF0000u); }
__device__ __forceinline__ u32 cvt2(f32x2 v) { return __builtin_bit_cast(u32, __builtin_convertvector(v, bf16x2_t)); }
__device__ __forceinline__ f32x2 unpk(u32 w) { return (f32x2){lo_f(w), hi_f(w)}; }
__device__ __forceinline__ s16x4 tr_read(const char* p) {
  return __builtin_amdgcn_ds_read_tr16_b64_v4i16((lds_s16x4_p)(p));
}
template <int V> struct icst { static constexpr int value = V; };
template <int CTRL>
__device__ __forceinline__ float dppf(float v) {
  return __builtin_bit_cast(float, __builtin_amdgcn_update_dpp(0, __builtin_bit_cast(int, v), CTRL, 0xf, 0xf, true));
}
__device__ __forceinline__ float row8_sum(float v) {
  v += dppf<0xB1>(v);
  v += dppf<0x4E>(v);
  v += dppf<0x141>(v);
  return v;
}
__device__ __forceinline__ float row16_sum(float v) {
  v = row8_sum(v);
  v += dppf<0x140>(v);
  return v;
}
__device__ __forceinline__ float xg_sum(float v) {
  u32 a = __float_as_uint(v);
  auto r = __builtin_amdgcn_permlane32_swap(a, a, false, false);
  v = __uint_as_float(r[0]) + __uint_as_float(r[1]);
  a = __float_as_uint(v);
  auto r2 = __builtin_amdgcn_permlane16_swap(a, a, false, false);
  return __uint_as_float(r2[0]) + __uint_as_float(r2[1]);
}
__device__ __forceinline__ float tr_sum(f32x4 ys) {
  auto r01 = __builtin_amdgcn_permlane32_swap(__float_as_uint(ys[0]), __float_as_uint(ys[1]), false, false);
  auto r23 = __builtin_amdgcn_permlane32_swap(__float_as_uint(ys[2]), __float_as_uint(ys[3]), false, false);
  const u32 u = __float_as_uint(__uint_as_float(r01[0]) + __uint_as_float(r01[1]));
  const u32 vv = __float_as_uint(__uint_as_float(r23[0]) + __uint_as_float(r23[1]));
  auto r2 = __builtin_amdgcn_permlane16_swap(u, vv, false, false);
  return __uint_as_float(r2[0]) + __uint_as_float(r2[1]);
}
__device__ __forceinline__ float wave_sum(float v) { return xg_sum(row16_sum(v)); }
template <int CTRL>
__device__ __forceinline__ u32 dppu(u32 v) { return (u32)__builtin_amdgcn_update_dpp(0, (int)v, CTRL, 0xf, 0xf, true); }
__device__ __forceinline__ u32 wave_max(u32 v) {
  v = max(v, dppu<0xB1>(v)); v = max(v, dppu<0x4E>(v)); v = max(v, dppu<0x141>(v)); v = max(v, dppu<0x140>(v));
  return max(max((u32)__builtin_amdgcn_readlane(v, 0), (u32)__builtin_amdgcn_readlane(v, 16)),
             max((u32)__builtin_amdgcn_readlane(v, 32), (u32)__builtin_amdgcn_readlane(v, 48)));
}
__device__ __forceinline__ u32 mono(float f) {
  const u32 b = __float_as_uint(f);
  return b ^ ((u32)((int)b >> 31) | 0x80000000u);
}
__device__ __forceinline__ float dot2b(u32 a, u32 b, float c) {
  return __builtin_amdgcn_fdot2_f32_bf16(__builtin_bit_cast(bf16x2_t, a), __builtin_bit_cast(bf16x2_t, b), c, false);
}
template <int OFF>
__device__ __forceinline__ void dma_m0(u32 voff, const char* sbase, u32 dst) {
  asm volatile("s_mov_b32 m0, %2\n\ts_nop 0\n\tglobal_load_lds_dwordx4 %0, %1 offset:%3"
               :: "v"(voff), "s"(sbase), "s"(dst), "n"(OFF) : "memory");
}
template <int OFF>
__device__ __forceinline__ void dma_nx(u32 voff, const char* sbase) {
  asm volatile("global_load_lds_dwordx4 %0, %1 offset:%2" :: "v"(voff), "s"(sbase), "n"(OFF) : "memory");
}
__device__ __forceinline__ u32 lds_ld32(u32 addr) {
  u32 v;
  asm volatile("ds_read_b32 %0, %1\n\ts_waitcnt lgkmcnt(0)" : "=v"(v) : "v"(addr) : "memory");
  return v;
}
__device__ __forceinline__ u32x4 lds_ld128(u32 addr) {
  u32x4 v;
  asm volatile("ds_read_b128 %0, %1\n\ts_waitcnt lgkmcnt(0)" : "=v"(v) : "v"(addr) : "memory");
  return v;
}
__device__ __forceinline__ bf16x8 mk8(u32 a, u32 b, u32 c, u32 d) {
  return __builtin_bit_cast(bf16x8, (u32x4){a, b, c, d});
}
__device__ __forceinline__ bf16x8 tr_frag(const char* p, int off2) {
  s16x4 b0 = tr_read(p);
  s16x4 b1 = tr_read(p + off2);
  bf16x8 r;
  r[0] = b0[0]; r[1] = b0[1]; r[2] = b0[2]; r[3] = b0[3];
  r[4] = b1[0]; r[5] = b1[1]; r[6] = b1[2]; r[7] = b1[3];
  return r;
}
#define SPIN_CAP 400000
#define MFMA(A, B, C) __builtin_amdgcn_mfma_f32_16x16x32_bf16(A, B, C, 0, 0, 0)

#define HSLOT 16384
#define XRING 98304
#define XSLOT 12288
#define LDS_TOTAL 163840
#define EXO 102400
#define HIM 148480
#define PST_OFF (EXO + 40960)
#define TAB_OFF 161280
#define ONES_OFF 162048
#define CTL_OFF 162176
#define WR_OFF 162304
#define REC_OFF 162816
#define HN_OFF 124928
#define HSL 1344
#define ASL 1056
#define TCH_OFF 162560
#define ACT_OFF 135168
#define KEY_OFF (ACT_OFF + 5120)
#define POST_OFF (KEY_OFF + 256)
#define DOWN_OFF 81920
#define WS_EPOCH 0
#define WS_HFLAG 4096
#define WS_VP 40960
#define WS_LN1 16384
#define WS_FILL 131072
#define WS_O WS_FILL
#define WS_STAT (WS_O + 2621440)
#define WS_CAND (WS_STAT + 425984)
#define WS_PART (WS_CAND + 133120)
#define WS_BXF (WS_PART + 122880)
#define WS_PC (WS_BXF + 26624)
#define WS_PART2 (WS_PC + 66560)
#define WS_PK (4u << 20)
#define SENT 0xFFFFFFFFu

#define GST4(VO, SB, O, V) asm volatile("global_store_dwordx4 %0, %1, %2 offset:%3 sc1\n\ts_nop 1" ::"v"(VO), "v"(V), "s"(SB), "n"(O) : "memory")
#define GST2(VO, SB, O, V) asm volatile("global_store_dwordx2 %0, %1, %2 offset:%3 sc1\n\ts_nop 1" ::"v"(VO), "v"(V), "s"(SB), "n"(O) : "memory")
#define GLD2(D, VO, SB, O) asm volatile("global_load_dwordx2 %0, %1, %2 offset:%3 sc1" : "=v"(D) : "v"(VO), "s"(SB), "n"(O) : "memory")

__device__ __forceinline__ void touch_line(const void* p) {
  asm volatile("s_mov_b32 m0, %1\n\ts_nop 0\n\tglobal_load_lds_dword %0, off" ::"v"(p), "s"(TCH_OFF) : "memory");
}
__device__ __forceinline__ u32 w_off(int i, int lane, int hg) {
  int cc = (lane & 31) ^ (((2 * i + (lane >> 5)) & 7) << 1);
  cc = cc > 29 ? 29 : cc;
  return (lane >> 5) * 3840 + ((cc >= 10 ? 1120 : 0) + (cc >= 20 ? 1120 : 0)) + hg * 160 + cc * 16;
}
#define W_ROW(I) (((((2 * (I)) & 0x13) | (((2 * (I)) & 4) << 1) | (((2 * (I)) & 8) >> 1))) * 3840)
__device__ __forceinline__ u32 x_off(int i, int lane, int r0) {
  const int xr = i * 8 + (lane >> 3);
  int grow = r0 - 16 + xr;
  grow = grow < 0 ? 0 : (grow > SEQ - 1 ? SEQ - 1 : grow);
  return grow * 1280 + (((lane & 7) ^ ((xr >> 1) & 7)) * 16);
}

#define ATT_ASM \
  "ds_read_b32 v28, %[aq] offset:64\n\tds_read_b32 v30, %[aka]\n\tds_read_b32 v31, %[akb]\n\tv_mov_b64 v[38:39], 0\n\tv_mov_b64 v[42:43], 0\n\t" \
  "s_lshl_b32 s41, %[kh], 5\n\ts_lshl_b64 s[46:47], 0xffff, s41\n\ts_lshl_b64 s[48:49], s[46:47], 16\n\ts_mov_b32 s50, 0xaaaaaaaa\n\t" \
  "s_mov_b32 s51, 0xaaaaaaaa\n\ts_setprio 1\n\ts_waitcnt lgkmcnt(0)\n\ts_barrier\n\tds_read_b64 v[36:37], %[eh] offset:512\n\t" \
  "ds_read_b64 v[44:45], %[eh] offset:15360\n\tds_read_b64 v[46:47], %[e4] offset:27648\n\tds_read_b64 v[48:49], %[eh] offset:15872\n\t" \
  "ds_read_b64 v[50:51], %[e4] offset:28160\n\ts_mov_b64 exec, s[46:47]\n\tds_read_b64 v[38:39], %[e4] offset:12800\n\ts_mov_b64 exec, s[48:49]\n\t" \
  "ds_read_b64 v[42:43], %[e4] offset:12800\n\ts_mov_b64 exec, -1\n\tds_read_b64 v[40:41], %[eh] offset:3584\n\t" \
  "ds_read_b64 v[52:53], %[eh] offset:18432\n\tds_read_b64 v[54:55], %[e4] offset:27648\n\tds_read_b64 v[56:57], %[eh] offset:18944\n\t" \
  "ds_read_b64 v[58:59], %[e4] offset:28160\n\tv_mul_f32 v28, 0x3ea52b5b, v28\n\tv_pk_mul_f32 v[32:33], v[28:29], v[30:31] op_sel_hi:[0,1]\n\t" \
  "s_waitcnt lgkmcnt(6)\n\tv_mfma_f32_16x16x32_bf16 v[44:47], v[44:47], v[36:39], 0\n\tds_read2st64_b64 v[60:63], %[eh] offset0:60 offset1:61\n\t" \
  "v_mfma_f32_16x16x32_bf16 v[48:51], v[48:51], v[36:39], 0\n\tds_read2st64_b64 v[64:67], %[eh] offset0:66 offset1:67\n\ts_waitcnt lgkmcnt(2)\n\t" \
  "v_mfma_f32_16x16x32_bf16 v[52:55], v[52:55], v[40:43], 0\n\tds_read2st64_b64 v[68:71], %[e4] offset0:84 offset1:85\n\t" \
  "v_mfma_f32_16x16x32_bf16 v[56:59], v[56:59], v[40:43], 0\n\ts_mov_b32 exec_lo, 0xfff0ffff\n\ts_mov_b32 exec_hi, 0xf000ff00\n\ts_nop 0\n\t" \
  "v_mov_b64 v[44:45], v[48:49]\n\ts_mov_b32 exec_lo, 0xffc0fffc\n\ts_mov_b32 exec_hi, 0xc000fc00\n\tv_mov_b64 v[46:47], v[50:51]\n\ts_nop 0\n\t" \
  "v_mov_b64 v[54:55], v[58:59]\n\ts_mov_b32 exec_lo, 0xfff0ffff\n\ts_mov_b32 exec_hi, 0xf000ff00\n\tv_mov_b64 v[52:53], v[56:57]\n\t" \
  "s_mov_b64 exec, -1\n\tv_cndmask_b32 v44, v44, v45, s[50:51]\n\tv_cndmask_b32 v46, v46, v47, s[50:51]\n\tv_cndmask_b32 v52, v52, v53, s[50:51]\n\t" \
  "v_cndmask_b32 v54, v54, v55, s[50:51]\n\tv_fma_f32 v72, v44, v32, %[b0]\n\tv_fma_f32 v73, v46, v33, %[b1]\n\tv_fma_f32 v74, v52, v32, %[b0]\n\t" \
  "v_fma_f32 v75, v54, v33, %[b1]\n\tv_max_f32 v34, v72, v73\n\tv_max_f32 v35, v74, v75\n\tds_read_b128 v[76:79], %[arec]\n\t" \
  "ds_read_b128 v[80:83], %[arec] offset:16\n\tv_permlane32_swap_b32 v34, v35\n\tv_max_f32 v34, v34, v35\n\tv_mov_b32 v35, v34\n\t" \
  "ds_read_b128 v[84:87], %[arec] offset:32\n\ts_nop 0\n\tv_permlane16_swap_b32 v34, v35\n\tv_max_f32 v34, v34, v35\n\tv_mov_b32 v35, v34\n\ts_nop 1\n\t" \
  "v_permlane32_swap_b32 v34, v35\n\tv_sub_f32 v72, v72, v34\n\tv_sub_f32 v73, v73, v34\n\tv_sub_f32 v74, v74, v35\n\tv_sub_f32 v75, v75, v35\n\t" \
  "v_exp_f32 v72, v72\n\tv_exp_f32 v73, v73\n\tv_exp_f32 v74, v74\n\tv_exp_f32 v75, v75\n\tv_add_f32 v34, v72, v73\n\tv_add_f32 v35, v74, v75\n\t" \
  "v_pk_mul_f32 v[72:73], v[72:73], v[30:31]\n\tv_pk_mul_f32 v[74:75], v[74:75], v[30:31]\n\tv_permlane32_swap_b32 v34, v35\n\t" \
  "v_add_f32 v34, v34, v35\n\tv_mov_b32 v35, v34\n\ts_nop 1\n\tv_permlane16_swap_b32 v34, v35\n\tv_add_f32 v34, v34, v35\n\tv_rcp_f32 v34, v34\n\t" \
  "s_nop 0\n\tv_mov_b32 v30, v34\n\ts_waitcnt lgkmcnt(0)\n\ts_nop 0\n\tv_permlane32_swap_b32 v34, v30\n\t" \
  "v_pk_mul_f32 v[72:73], v[34:35], v[72:73] op_sel_hi:[0,1]\n\tv_pk_mul_f32 v[74:75], v[30:31], v[74:75] op_sel_hi:[0,1]\n\t" \
  "v_cvt_pk_bf16_f32 v72, v72, v73\n\tv_cvt_pk_bf16_f32 v74, v74, v75\n\tv_perm_b32 v36, 0, v72, %[s0]\n\tv_perm_b32 v37, 0, v72, %[s1]\n\t" \
  "v_perm_b32 v38, 0, v72, %[s2]\n\tv_perm_b32 v39, 0, v72, %[s3]\n\tv_perm_b32 v40, 0, v74, %[s0]\n\tv_perm_b32 v41, 0, v74, %[s1]\n\t" \
  "v_mfma_f32_16x16x32_bf16 v[60:63], v[60:63], v[36:39], 0\n\tv_perm_b32 v42, 0, v74, %[s2]\n\tv_perm_b32 v43, 0, v74, %[s3]\n\t" \
  "v_mfma_f32_16x16x32_bf16 v[52:55], v[68:71], v[36:39], 0\n\ts_nop 0\n\tv_mfma_f32_16x16x32_bf16 v[64:67], v[64:67], v[40:43], 0\n\t" \
  "v_mfma_f32_16x16x32_bf16 v[56:59], v[68:71], v[40:43], 0\n\ts_nop 1\n\tv_cvt_pk_bf16_f32 v24, v60, v61\n\tv_cvt_pk_bf16_f32 v25, v62, v63\n\t" \
  "s_nop 0\n\tv_cvt_pk_bf16_f32 v52, v52, v53\n\tv_cvt_pk_bf16_f32 v53, v54, v55\n\tv_cvt_pk_bf16_f32 v26, v64, v65\n\t" \
  "v_cvt_pk_bf16_f32 v27, v66, v67\n\tglobal_store_dwordx4 %[l16], v[24:27], %[os] sc1\n\ts_mov_b64 exec, s[48:49]\n\tv_cvt_pk_bf16_f32 v52, v56, v57\n\t" \
  "v_cvt_pk_bf16_f32 v53, v58, v59\n\ts_or_b64 exec, s[46:47], s[48:49]\n\tglobal_store_dwordx2 %[l16], v[52:53], %[os4] sc1\n\ts_mov_b64 exec, -1\n\t" \
  "s_setprio 0\n\tv_mfma_f32_16x16x32_bf16 v[60:63], v[24:27], v[76:79], 0\n\tv_and_b32 v54, 0x7fff7fff, v52\n\tv_and_b32 v55, 0x7fff7fff, v53\n\t" \
  "v_and_b32 v64, 0x7fff7fff, v24\n\tv_and_b32 v65, 0x7fff7fff, v25\n\tv_and_b32 v66, 0x7fff7fff, v26\n\tv_and_b32 v67, 0x7fff7fff, v27\n\t" \
  "v_mfma_f32_16x16x32_bf16 v[60:63], v[52:55], v[80:83], v[60:63]\n\tds_write2st64_b64 %[eh], v[24:25], v[26:27] offset0:1 offset1:7\n\t" \
  "v_mfma_f32_16x16x32_bf16 v[60:63], v[64:67], v[84:87], v[60:63]\n\ts_mov_b32 exec_lo, 0x01010101\n\ts_mov_b32 exec_hi, 0x01010101\n\ts_nop 5\n\t" \
  "v_cvt_pk_bf16_f32 v60, v60, v61\n\tv_cvt_pk_bf16_f32 v61, v62, v63\n\tv_and_b32 v60, -2, v60\n\tglobal_store_dwordx2 %[vpp], v[60:61], %[sst] sc1\n\t" \
  "s_mov_b64 exec, -1\n\t" \
  ""
#define OP_ASM \
  "s_mov_b32 s43, %[cap]\n\ts_cmp_lt_u32 %[w], 4\n\ts_cbranch_scc1 LH_%=\n\ts_setprio 3\n\tglobal_load_dwordx4 v[124:127], %[vd], %[oa1] sc1\n\t" \
  "global_load_dwordx4 v[128:131], %[vk], %[oa1] sc1\n\tglobal_load_dwordx4 v[132:135], %[vd], %[oa2] sc1\n\t" \
  "global_load_dwordx4 v[136:139], %[vk], %[oa2] sc1\n\tv_add_u32 %[tk], %[sk], %[e4]\n\tv_add_u32 %[ta], %[so0], %[pm]\n\t" \
  "ds_read2st64_b64 v[104:107], %[tk] offset0:1 offset1:7\n\tv_xor_b32 %[tk], 512, %[tk]\n\tds_read2st64_b64 v[108:111], %[tk] offset0:1 offset1:7\n\t" \
  "ds_read_b64_tr_b16 v[64:65], %[ta] offset:0\n\tds_read_b64_tr_b16 v[66:67], %[ta] offset:3200\n\tds_read_b64_tr_b16 v[68:69], %[ta] offset:8\n\t" \
  "ds_read_b64_tr_b16 v[70:71], %[ta] offset:3208\n\tds_read_b64_tr_b16 v[72:73], %[ta] offset:16\n\tds_read_b64_tr_b16 v[74:75], %[ta] offset:3216\n\t" \
  "ds_read_b64_tr_b16 v[76:77], %[ta] offset:24\n\tds_read_b64_tr_b16 v[78:79], %[ta] offset:3224\n\tds_read_b64_tr_b16 v[80:81], %[ta] offset:32\n\t" \
  "ds_read_b64_tr_b16 v[82:83], %[ta] offset:3232\n\tds_read_b64 v[112:113], %[ra] offset:0\n\tds_read_b64 v[114:115], %[ra] offset:8\n\t" \
  "ds_read_b64 v[116:117], %[ra] offset:16\n\tds_read_b64 v[118:119], %[ra] offset:24\n\tds_read_b64 v[120:121], %[ra] offset:32\n\t" \
  "global_load_dwordx4 v[140:143], %[vd], %[oa3] sc1\n\tv_add_u32 %[ta], %[so1], %[pm]\n\ts_waitcnt lgkmcnt(5)\n\t" \
  "v_mfma_f32_16x16x32_bf16 v[44:47], v[64:67], v[108:111], 0\n\tds_read_b64_tr_b16 v[84:85], %[ta] offset:0\n\t" \
  "ds_read_b64_tr_b16 v[86:87], %[ta] offset:3200\n\tv_mfma_f32_16x16x32_bf16 v[48:51], v[68:71], v[108:111], 0\n\t" \
  "ds_read_b64_tr_b16 v[88:89], %[ta] offset:8\n\tds_read_b64_tr_b16 v[90:91], %[ta] offset:3208\n\t" \
  "v_mfma_f32_16x16x32_bf16 v[52:55], v[72:75], v[108:111], 0\n\tds_read_b64_tr_b16 v[92:93], %[ta] offset:16\n\t" \
  "ds_read_b64_tr_b16 v[94:95], %[ta] offset:3216\n\tv_mfma_f32_16x16x32_bf16 v[56:59], v[76:79], v[108:111], 0\n\t" \
  "ds_read_b64_tr_b16 v[96:97], %[ta] offset:24\n\tds_read_b64_tr_b16 v[98:99], %[ta] offset:3224\n\t" \
  "v_mfma_f32_16x16x32_bf16 v[60:63], v[80:83], v[108:111], 0\n\tds_read_b64_tr_b16 v[100:101], %[ta] offset:32\n\t" \
  "ds_read_b64_tr_b16 v[102:103], %[ta] offset:3232\n\ts_waitcnt lgkmcnt(10)\n\tv_lshlrev_b32 v24, 16, v112\n\tv_and_b32 v25, 0xffff0000, v112\n\t" \
  "v_lshlrev_b32 v26, 16, v113\n\tv_and_b32 v27, 0xffff0000, v113\n\tv_lshlrev_b32 v28, 16, v114\n\tv_and_b32 v29, 0xffff0000, v114\n\t" \
  "v_lshlrev_b32 v30, 16, v115\n\tv_and_b32 v31, 0xffff0000, v115\n\tv_mfma_f32_16x16x32_bf16 v[24:27], v[64:67], v[104:107], v[24:27]\n\t" \
  "v_lshlrev_b32 v32, 16, v116\n\tv_and_b32 v33, 0xffff0000, v116\n\tv_lshlrev_b32 v34, 16, v117\n\tv_and_b32 v35, 0xffff0000, v117\n\t" \
  "v_mfma_f32_16x16x32_bf16 v[28:31], v[68:71], v[104:107], v[28:31]\n\tv_lshlrev_b32 v36, 16, v118\n\tv_and_b32 v37, 0xffff0000, v118\n\t" \
  "v_lshlrev_b32 v38, 16, v119\n\tv_and_b32 v39, 0xffff0000, v119\n\tv_mfma_f32_16x16x32_bf16 v[32:35], v[72:75], v[104:107], v[32:35]\n\t" \
  "v_lshlrev_b32 v40, 16, v120\n\tv_and_b32 v41, 0xffff0000, v120\n\tv_lshlrev_b32 v42, 16, v121\n\tv_and_b32 v43, 0xffff0000, v121\n\t" \
  "v_mfma_f32_16x16x32_bf16 v[36:39], v[76:79], v[104:107], v[36:39]\n\ts_nop 0\n\tv_mfma_f32_16x16x32_bf16 v[40:43], v[80:83], v[104:107], v[40:43]\n\t" \
  "v_add_u32 %[ta], %[so2], %[pm]\n\tds_read_b64_tr_b16 v[104:105], %[ta] offset:0\n\tds_read_b64_tr_b16 v[106:107], %[ta] offset:3200\n\t" \
  "ds_read_b64_tr_b16 v[108:109], %[ta] offset:8\n\tds_read_b64_tr_b16 v[110:111], %[ta] offset:3208\n\t" \
  "ds_read_b64_tr_b16 v[112:113], %[ta] offset:16\n\tds_read_b64_tr_b16 v[114:115], %[ta] offset:3216\n\t" \
  "ds_read_b64_tr_b16 v[116:117], %[ta] offset:24\n\tds_read_b64_tr_b16 v[118:119], %[ta] offset:3224\n\t" \
  "ds_read_b64_tr_b16 v[120:121], %[ta] offset:32\n\tds_read_b64_tr_b16 v[122:123], %[ta] offset:3232\n\t" \
  "global_load_dwordx4 v[144:147], %[vk], %[oa3] sc1\n\tglobal_load_dwordx4 v[148:151], %[vd], %[oa4] sc1\n\ts_waitcnt vmcnt(5)\n\t" \
  "v_max_u32 %[tk], v124, v128\n\tv_cmp_eq_u32 vcc, -1, %[tk]\n\ts_cbranch_vccnz LSs1_%=\n\tLQs1_%=:\n\ts_waitcnt lgkmcnt(10)\n\t" \
  "v_mfma_f32_16x16x32_bf16 v[44:47], v[84:87], v[124:127], v[44:47]\n\tv_add_u32 %[ta], %[so3], %[pm]\n\tds_read_b64_tr_b16 v[64:65], %[ta] offset:0\n\t" \
  "v_mfma_f32_16x16x32_bf16 v[24:27], v[84:87], v[128:131], v[24:27]\n\tds_read_b64_tr_b16 v[66:67], %[ta] offset:3200\n\t" \
  "global_load_dwordx4 v[152:155], %[vk], %[oa4] sc1\n\tv_mfma_f32_16x16x32_bf16 v[48:51], v[88:91], v[124:127], v[48:51]\n\t" \
  "ds_read_b64_tr_b16 v[68:69], %[ta] offset:8\n\tv_mfma_f32_16x16x32_bf16 v[28:31], v[88:91], v[128:131], v[28:31]\n\t" \
  "ds_read_b64_tr_b16 v[70:71], %[ta] offset:3208\n\tglobal_load_dwordx4 v[156:159], %[vd], %[oa5] sc1\n\t" \
  "v_mfma_f32_16x16x32_bf16 v[52:55], v[92:95], v[124:127], v[52:55]\n\tds_read_b64_tr_b16 v[72:73], %[ta] offset:16\n\t" \
  "v_mfma_f32_16x16x32_bf16 v[32:35], v[92:95], v[128:131], v[32:35]\n\tds_read_b64_tr_b16 v[74:75], %[ta] offset:3216\n\t" \
  "v_mfma_f32_16x16x32_bf16 v[56:59], v[96:99], v[124:127], v[56:59]\n\tds_read_b64_tr_b16 v[76:77], %[ta] offset:24\n\t" \
  "v_mfma_f32_16x16x32_bf16 v[36:39], v[96:99], v[128:131], v[36:39]\n\tds_read_b64_tr_b16 v[78:79], %[ta] offset:3224\n\ts_waitcnt vmcnt(5)\n\t" \
  "v_max_u32 %[tk], v132, v136\n\tv_cmp_eq_u32 vcc, -1, %[tk]\n\tv_mfma_f32_16x16x32_bf16 v[60:63], v[100:103], v[124:127], v[60:63]\n\t" \
  "ds_read_b64_tr_b16 v[80:81], %[ta] offset:32\n\tv_mfma_f32_16x16x32_bf16 v[40:43], v[100:103], v[128:131], v[40:43]\n\t" \
  "ds_read_b64_tr_b16 v[82:83], %[ta] offset:3232\n\ts_cbranch_vccnz LSs2_%=\n\tLQs2_%=:\n\ts_waitcnt lgkmcnt(10)\n\t" \
  "v_mfma_f32_16x16x32_bf16 v[44:47], v[104:107], v[132:135], v[44:47]\n\tv_add_u32 %[ta], %[so4], %[pf]\n\t" \
  "ds_read_b64_tr_b16 v[84:85], %[ta] offset:0\n\tv_mfma_f32_16x16x32_bf16 v[24:27], v[104:107], v[136:139], v[24:27]\n\t" \
  "ds_read_b64_tr_b16 v[86:87], %[ta] offset:12800\n\tglobal_load_dwordx4 v[124:127], %[vk], %[oa5] sc1\n\t" \
  "v_mfma_f32_16x16x32_bf16 v[48:51], v[108:111], v[132:135], v[48:51]\n\tds_read_b64_tr_b16 v[88:89], %[ta] offset:8\n\t" \
  "v_mfma_f32_16x16x32_bf16 v[28:31], v[108:111], v[136:139], v[28:31]\n\tds_read_b64_tr_b16 v[90:91], %[ta] offset:12808\n\t" \
  "global_load_dwordx4 v[128:131], %[vd], %[oa6] sc1\n\tv_mfma_f32_16x16x32_bf16 v[52:55], v[112:115], v[132:135], v[52:55]\n\t" \
  "ds_read_b64_tr_b16 v[92:93], %[ta] offset:16\n\tv_mfma_f32_16x16x32_bf16 v[32:35], v[112:115], v[136:139], v[32:35]\n\t" \
  "ds_read_b64_tr_b16 v[94:95], %[ta] offset:12816\n\tv_mfma_f32_16x16x32_bf16 v[56:59], v[116:119], v[132:135], v[56:59]\n\t" \
  "ds_read_b64_tr_b16 v[96:97], %[ta] offset:24\n\tv_mfma_f32_16x16x32_bf16 v[36:39], v[116:119], v[136:139], v[36:39]\n\t" \
  "ds_read_b64_tr_b16 v[98:99], %[ta] offset:12824\n\ts_waitcnt vmcnt(5)\n\tv_max_u32 %[tk], v140, v144\n\tv_cmp_eq_u32 vcc, -1, %[tk]\n\t" \
  "v_mfma_f32_16x16x32_bf16 v[60:63], v[120:123], v[132:135], v[60:63]\n\tds_read_b64_tr_b16 v[100:101], %[ta] offset:32\n\t" \
  "v_mfma_f32_16x16x32_bf16 v[40:43], v[120:123], v[136:139], v[40:43]\n\tds_read_b64_tr_b16 v[102:103], %[ta] offset:12832\n\t" \
  "s_cbranch_vccnz LSs3_%=\n\tLQs3_%=:\n\ts_waitcnt lgkmcnt(10)\n\tv_mfma_f32_16x16x32_bf16 v[44:47], v[64:67], v[140:143], v[44:47]\n\t" \
  "v_add_u32 %[ta], %[so5], %[pm]\n\tds_read_b64_tr_b16 v[104:105], %[ta] offset:0\n\t" \
  "v_mfma_f32_16x16x32_bf16 v[24:27], v[64:67], v[144:147], v[24:27]\n\tds_read_b64_tr_b16 v[106:107], %[ta] offset:3200\n\t" \
  "global_load_dwordx4 v[132:135], %[vk], %[oa6] sc1\n\tv_mfma_f32_16x16x32_bf16 v[48:51], v[68:71], v[140:143], v[48:51]\n\t" \
  "ds_read_b64_tr_b16 v[108:109], %[ta] offset:8\n\tv_mfma_f32_16x16x32_bf16 v[28:31], v[68:71], v[144:147], v[28:31]\n\t" \
  "ds_read_b64_tr_b16 v[110:111], %[ta] offset:3208\n\tglobal_load_dwordx4 v[136:139], %[vd], %[oa7] sc1\n\t" \
  "v_mfma_f32_16x16x32_bf16 v[52:55], v[72:75], v[140:143], v[52:55]\n\tds_read_b64_tr_b16 v[112:113], %[ta] offset:16\n\t" \
  "v_mfma_f32_16x16x32_bf16 v[32:35], v[72:75], v[144:147], v[32:35]\n\tds_read_b64_tr_b16 v[114:115], %[ta] offset:3216\n\t" \
  "v_mfma_f32_16x16x32_bf16 v[56:59], v[76:79], v[140:143], v[56:59]\n\tds_read_b64_tr_b16 v[116:117], %[ta] offset:24\n\t" \
  "v_mfma_f32_16x16x32_bf16 v[36:39], v[76:79], v[144:147], v[36:39]\n\tds_read_b64_tr_b16 v[118:119], %[ta] offset:3224\n\ts_waitcnt vmcnt(5)\n\t" \
  "v_max3_u32 %[tk], v148, v150, v152\n\tv_max_u32 %[tk], %[tk], v154\n\tv_cmp_eq_u32 vcc, -1, %[tk]\n\t" \
  "v_mfma_f32_16x16x32_bf16 v[60:63], v[80:83], v[140:143], v[60:63]\n\tds_read_b64_tr_b16 v[120:121], %[ta] offset:32\n\t" \
  "v_mfma_f32_16x16x32_bf16 v[40:43], v[80:83], v[144:147], v[40:43]\n\tds_read_b64_tr_b16 v[122:123], %[ta] offset:3232\n\ts_cbranch_vccnz LSs4_%=\n\t" \
  "LQs4_%=:\n\ts_waitcnt lgkmcnt(10)\n\tv_mfma_f32_16x16x32_bf16 v[44:47], v[84:87], v[148:151], v[44:47]\n\tv_add_u32 %[ta], %[so6], %[pm]\n\t" \
  "ds_read_b64_tr_b16 v[64:65], %[ta] offset:0\n\tv_mfma_f32_16x16x32_bf16 v[24:27], v[84:87], v[152:155], v[24:27]\n\t" \
  "ds_read_b64_tr_b16 v[66:67], %[ta] offset:3200\n\tglobal_load_dwordx4 v[140:143], %[vd], %[oa8] sc1\n\t" \
  "v_mfma_f32_16x16x32_bf16 v[48:51], v[88:91], v[148:151], v[48:51]\n\tds_read_b64_tr_b16 v[68:69], %[ta] offset:8\n\t" \
  "v_mfma_f32_16x16x32_bf16 v[28:31], v[88:91], v[152:155], v[28:31]\n\tds_read_b64_tr_b16 v[70:71], %[ta] offset:3208\n\t" \
  "global_load_dwordx4 v[144:147], %[vd], %[oa9] sc1\n\tv_mfma_f32_16x16x32_bf16 v[52:55], v[92:95], v[148:151], v[52:55]\n\t" \
  "ds_read_b64_tr_b16 v[72:73], %[ta] offset:16\n\tv_mfma_f32_16x16x32_bf16 v[32:35], v[92:95], v[152:155], v[32:35]\n\t" \
  "ds_read_b64_tr_b16 v[74:75], %[ta] offset:3216\n\tv_mfma_f32_16x16x32_bf16 v[56:59], v[96:99], v[148:151], v[56:59]\n\t" \
  "ds_read_b64_tr_b16 v[76:77], %[ta] offset:24\n\tv_mfma_f32_16x16x32_bf16 v[36:39], v[96:99], v[152:155], v[36:39]\n\t" \
  "ds_read_b64_tr_b16 v[78:79], %[ta] offset:3224\n\ts_waitcnt vmcnt(5)\n\tv_max_u32 %[tk], v156, v124\n\tv_cmp_eq_u32 vcc, -1, %[tk]\n\t" \
  "v_mfma_f32_16x16x32_bf16 v[60:63], v[100:103], v[148:151], v[60:63]\n\tds_read_b64_tr_b16 v[80:81], %[ta] offset:32\n\t" \
  "v_mfma_f32_16x16x32_bf16 v[40:43], v[100:103], v[152:155], v[40:43]\n\tds_read_b64_tr_b16 v[82:83], %[ta] offset:3232\n\ts_cbranch_vccnz LSs5_%=\n\t" \
  "LQs5_%=:\n\ts_waitcnt lgkmcnt(10)\n\tv_mfma_f32_16x16x32_bf16 v[44:47], v[104:107], v[156:159], v[44:47]\n\tv_add_u32 %[ta], %[so7], %[pm]\n\t" \
  "ds_read_b64_tr_b16 v[84:85], %[ta] offset:0\n\tv_mfma_f32_16x16x32_bf16 v[24:27], v[104:107], v[124:127], v[24:27]\n\t" \
  "ds_read_b64_tr_b16 v[86:87], %[ta] offset:3200\n\tglobal_load_dwordx4 v[148:151], %[vk], %[oa7] sc1\n\t" \
  "v_mfma_f32_16x16x32_bf16 v[48:51], v[108:111], v[156:159], v[48:51]\n\tds_read_b64_tr_b16 v[88:89], %[ta] offset:8\n\t" \
  "v_mfma_f32_16x16x32_bf16 v[28:31], v[108:111], v[124:127], v[28:31]\n\tds_read_b64_tr_b16 v[90:91], %[ta] offset:3208\n\t" \
  "global_load_dwordx4 v[152:155], %[vk], %[oa8] sc1\n\tv_mfma_f32_16x16x32_bf16 v[52:55], v[112:115], v[156:159], v[52:55]\n\t" \
  "ds_read_b64_tr_b16 v[92:93], %[ta] offset:16\n\tv_mfma_f32_16x16x32_bf16 v[32:35], v[112:115], v[124:127], v[32:35]\n\t" \
  "ds_read_b64_tr_b16 v[94:95], %[ta] offset:3216\n\tv_mfma_f32_16x16x32_bf16 v[56:59], v[116:119], v[156:159], v[56:59]\n\t" \
  "ds_read_b64_tr_b16 v[96:97], %[ta] offset:24\n\tv_mfma_f32_16x16x32_bf16 v[36:39], v[116:119], v[124:127], v[36:39]\n\t" \
  "ds_read_b64_tr_b16 v[98:99], %[ta] offset:3224\n\ts_waitcnt vmcnt(5)\n\tv_max_u32 %[tk], v128, v132\n\tv_cmp_eq_u32 vcc, -1, %[tk]\n\t" \
  "v_mfma_f32_16x16x32_bf16 v[60:63], v[120:123], v[156:159], v[60:63]\n\tds_read_b64_tr_b16 v[100:101], %[ta] offset:32\n\t" \
  "v_mfma_f32_16x16x32_bf16 v[40:43], v[120:123], v[124:127], v[40:43]\n\tds_read_b64_tr_b16 v[102:103], %[ta] offset:3232\n\ts_cbranch_vccnz LSs6_%=\n\t" \
  "LQs6_%=:\n\ts_waitcnt lgkmcnt(10)\n\tv_mfma_f32_16x16x32_bf16 v[44:47], v[64:67], v[128:131], v[44:47]\n\tv_add_u32 %[ta], %[so8], %[pm]\n\t" \
  "ds_read_b64_tr_b16 v[104:105], %[ta] offset:0\n\tv_mfma_f32_16x16x32_bf16 v[24:27], v[64:67], v[132:135], v[24:27]\n\t" \
  "ds_read_b64_tr_b16 v[106:107], %[ta] offset:3200\n\tglobal_load_dwordx4 v[156:159], %[vk], %[oa9] sc1\n\t" \
  "v_mfma_f32_16x16x32_bf16 v[48:51], v[68:71], v[128:131], v[48:51]\n\tds_read_b64_tr_b16 v[108:109], %[ta] offset:8\n\t" \
  "v_mfma_f32_16x16x32_bf16 v[28:31], v[68:71], v[132:135], v[28:31]\n\tds_read_b64_tr_b16 v[110:111], %[ta] offset:3208\n\t" \
  "v_mfma_f32_16x16x32_bf16 v[52:55], v[72:75], v[128:131], v[52:55]\n\tds_read_b64_tr_b16 v[112:113], %[ta] offset:16\n\t" \
  "v_mfma_f32_16x16x32_bf16 v[32:35], v[72:75], v[132:135], v[32:35]\n\tds_read_b64_tr_b16 v[114:115], %[ta] offset:3216\n\t" \
  "v_mfma_f32_16x16x32_bf16 v[56:59], v[76:79], v[128:131], v[56:59]\n\tds_read_b64_tr_b16 v[116:117], %[ta] offset:24\n\t" \
  "v_mfma_f32_16x16x32_bf16 v[36:39], v[76:79], v[132:135], v[36:39]\n\tds_read_b64_tr_b16 v[118:119], %[ta] offset:3224\n\ts_waitcnt vmcnt(4)\n\t" \
  "v_max_u32 %[tk], v136, v140\n\tv_cmp_eq_u32 vcc, -1, %[tk]\n\tv_mfma_f32_16x16x32_bf16 v[60:63], v[80:83], v[128:131], v[60:63]\n\t" \
  "ds_read_b64_tr_b16 v[120:121], %[ta] offset:32\n\tv_mfma_f32_16x16x32_bf16 v[40:43], v[80:83], v[132:135], v[40:43]\n\t" \
  "ds_read_b64_tr_b16 v[122:123], %[ta] offset:3232\n\ts_cbranch_vccnz LSd7_%=\n\tLQd7_%=:\n\ts_waitcnt lgkmcnt(10)\n\t" \
  "v_mfma_f32_16x16x32_bf16 v[44:47], v[84:87], v[136:139], v[44:47]\n\tv_add_u32 %[ta], %[so9], %[pf]\n\tds_read_b64_tr_b16 v[64:65], %[ta] offset:0\n\t" \
  "ds_read_b64_tr_b16 v[66:67], %[ta] offset:12800\n\tv_mfma_f32_16x16x32_bf16 v[48:51], v[88:91], v[136:139], v[48:51]\n\t" \
  "ds_read_b64_tr_b16 v[68:69], %[ta] offset:8\n\tds_read_b64_tr_b16 v[70:71], %[ta] offset:12808\n\t" \
  "v_mfma_f32_16x16x32_bf16 v[52:55], v[92:95], v[136:139], v[52:55]\n\tds_read_b64_tr_b16 v[72:73], %[ta] offset:16\n\t" \
  "ds_read_b64_tr_b16 v[74:75], %[ta] offset:12816\n\tv_mfma_f32_16x16x32_bf16 v[56:59], v[96:99], v[136:139], v[56:59]\n\t" \
  "ds_read_b64_tr_b16 v[76:77], %[ta] offset:24\n\tds_read_b64_tr_b16 v[78:79], %[ta] offset:12824\n\t" \
  "v_mfma_f32_16x16x32_bf16 v[60:63], v[100:103], v[136:139], v[60:63]\n\tds_read_b64_tr_b16 v[80:81], %[ta] offset:32\n\t" \
  "ds_read_b64_tr_b16 v[82:83], %[ta] offset:12832\n\ts_waitcnt lgkmcnt(10)\n\tv_mfma_f32_16x16x32_bf16 v[44:47], v[104:107], v[140:143], v[44:47]\n\t" \
  "v_mfma_f32_16x16x32_bf16 v[48:51], v[108:111], v[140:143], v[48:51]\n\tv_mfma_f32_16x16x32_bf16 v[52:55], v[112:115], v[140:143], v[52:55]\n\t" \
  "s_waitcnt vmcnt(2)\n\tv_max3_u32 %[tk], v144, v146, v148\n\tv_cmp_eq_u32 vcc, -1, %[tk]\n\t" \
  "v_mfma_f32_16x16x32_bf16 v[56:59], v[116:119], v[140:143], v[56:59]\n\tv_mfma_f32_16x16x32_bf16 v[60:63], v[120:123], v[140:143], v[60:63]\n\t" \
  "s_cbranch_vccnz LSd9_%=\n\tLQd9_%=:\n\ts_waitcnt lgkmcnt(0)\n\tv_mfma_f32_16x16x32_bf16 v[44:47], v[64:67], v[144:147], v[44:47]\n\t" \
  "ds_read2_b64 v[124:127], %[aw] offset0:0 offset1:1\n\tds_read2_b64 v[128:131], %[aw] offset0:2 offset1:3\n\t" \
  "ds_read_b64 v[132:133], %[aw] offset:32\n\tv_mov_b64 v[134:135], 0\n\tv_mfma_f32_16x16x32_bf16 v[48:51], v[68:71], v[144:147], v[48:51]\n\t" \
  "v_mfma_f32_16x16x32_bf16 v[52:55], v[72:75], v[144:147], v[52:55]\n\tv_add_u32 %[ta], %[sd], %[l16]\n\t" \
  "v_mfma_f32_16x16x32_bf16 v[56:59], v[76:79], v[144:147], v[56:59]\n\tv_mfma_f32_16x16x32_bf16 v[60:63], v[80:83], v[144:147], v[60:63]\n\t" \
  "v_mov_b32 v137, %[sc]\n\tv_mov_b32 v136, 1\n\tv_mfma_f32_16x16x32_bf16 v[24:27], v[84:87], v[148:151], v[24:27]\n\t" \
  "ds_write_b128 %[ta], v[44:47] offset:0\n\tv_mfma_f32_16x16x32_bf16 v[28:31], v[88:91], v[148:151], v[28:31]\n\t" \
  "ds_write_b128 %[ta], v[48:51] offset:1024\n\tv_mfma_f32_16x16x32_bf16 v[32:35], v[92:95], v[148:151], v[32:35]\n\t" \
  "ds_write_b128 %[ta], v[52:55] offset:2048\n\ts_waitcnt vmcnt(0)\n\tv_max3_u32 %[tk], v152, v156, v158\n\tv_cmp_eq_u32 vcc, -1, %[tk]\n\t" \
  "v_mfma_f32_16x16x32_bf16 v[36:39], v[96:99], v[148:151], v[36:39]\n\tds_write_b128 %[ta], v[56:59] offset:3072\n\t" \
  "v_mfma_f32_16x16x32_bf16 v[40:43], v[100:103], v[148:151], v[40:43]\n\tds_write_b128 %[ta], v[60:63] offset:4096\n\ts_mov_b64 exec, 1\n\t" \
  "ds_add_u32 v137, v136\n\ts_mov_b64 exec, -1\n\ts_cbranch_vccnz LSk8_%=\n\tLQk8_%=:\n\t" \
  "v_mfma_f32_16x16x32_bf16 v[24:27], v[104:107], v[152:155], v[24:27]\n\tv_mov_b64 v[94:95], 0\n\t" \
  "v_mfma_f32_16x16x32_bf16 v[28:31], v[108:111], v[152:155], v[28:31]\n\tv_xor_b32 v96, 4, v137\n\t" \
  "v_mfma_f32_16x16x32_bf16 v[32:35], v[112:115], v[152:155], v[32:35]\n\tv_add_u32 %[tk], %[sr], %[l16]\n\t" \
  "v_mfma_f32_16x16x32_bf16 v[36:39], v[116:119], v[152:155], v[36:39]\n\tv_mfma_f32_16x16x32_bf16 v[40:43], v[120:123], v[152:155], v[40:43]\n\t" \
  "v_mfma_f32_16x16x32_bf16 v[24:27], v[64:67], v[156:159], v[24:27]\n\tds_read_b32 v97, v96\n\tds_read_b128 v[44:47], %[tk] offset:0\n\t" \
  "ds_read_b128 v[48:51], %[tk] offset:1024\n\tds_read_b128 v[52:55], %[tk] offset:2048\n\tds_read_b128 v[56:59], %[tk] offset:3072\n\t" \
  "ds_read_b128 v[60:63], %[tk] offset:4096\n\tv_mfma_f32_16x16x32_bf16 v[28:31], v[68:71], v[156:159], v[28:31]\n\t" \
  "v_mfma_f32_16x16x32_bf16 v[32:35], v[72:75], v[156:159], v[32:35]\n\tv_mfma_f32_16x16x32_bf16 v[36:39], v[76:79], v[156:159], v[36:39]\n\t" \
  "v_mfma_f32_16x16x32_bf16 v[40:43], v[80:83], v[156:159], v[40:43]\n\tLD1_%=:\n\ts_waitcnt lgkmcnt(0)\n\tv_cmp_eq_u32 vcc, 0, v97\n\t" \
  "s_cbranch_vccnz LD2_%=\n\tLT_%=:\n\tv_mov_b32 v137, %[sa]\n\tds_read_b32 v138, v137 offset:16\n\tv_add_f32 v24, v24, v44\n\t" \
  "v_add_f32 v25, v25, v45\n\tv_add_f32 v26, v26, v46\n\tv_add_f32 v27, v27, v47\n\tv_cvt_pk_bf16_f32 v84, v24, v25\n\t" \
  "v_cvt_pk_bf16_f32 v85, v26, v27\n\tv_add_f32 v28, v28, v48\n\tv_add_f32 v29, v29, v49\n\tv_add_f32 v30, v30, v50\n\tv_add_f32 v31, v31, v51\n\t" \
  "v_cvt_pk_bf16_f32 v86, v28, v29\n\tv_cvt_pk_bf16_f32 v87, v30, v31\n\tv_add_f32 v32, v32, v52\n\tv_add_f32 v33, v33, v53\n\t" \
  "v_add_f32 v34, v34, v54\n\tv_add_f32 v35, v35, v55\n\tv_cvt_pk_bf16_f32 v88, v32, v33\n\tv_cvt_pk_bf16_f32 v89, v34, v35\n\t" \
  "v_mfma_f32_16x16x32_bf16 v[100:103], v[84:87], v[84:87], 0\n\tv_mfma_f32_16x16x32_bf16 v[96:99], v[124:127], v[84:87], 0\n\t" \
  "v_add_f32 v36, v36, v56\n\tv_add_f32 v37, v37, v57\n\tv_add_f32 v38, v38, v58\n\tv_add_f32 v39, v39, v59\n\tv_cvt_pk_bf16_f32 v90, v36, v37\n\t" \
  "v_cvt_pk_bf16_f32 v91, v38, v39\n\tv_add_f32 v40, v40, v60\n\tv_add_f32 v41, v41, v61\n\tv_add_f32 v42, v42, v62\n\tv_add_f32 v43, v43, v63\n\t" \
  "v_cvt_pk_bf16_f32 v92, v40, v41\n\tv_cvt_pk_bf16_f32 v93, v42, v43\n\tv_mfma_f32_16x16x32_bf16 v[100:103], v[88:91], v[88:91], v[100:103]\n\t" \
  "v_mfma_f32_16x16x32_bf16 v[96:99], v[128:131], v[88:91], v[96:99]\n\tv_mfma_f32_16x16x32_bf16 v[100:103], v[92:95], v[92:95], v[100:103]\n\t" \
  "v_mfma_f32_16x16x32_bf16 v[96:99], v[132:135], v[92:95], v[96:99]\n\ts_waitcnt lgkmcnt(0)\n\tv_sub_u32 v138, %[ra], v138\n\t" \
  "v_cmp_gt_u32 vcc, 0xa0, v138\n\tv_and_b32 %[ta], 0xff, %[l16]\n\tv_add_u32 %[tk], %[sp], %[ta]\n\tv_mov_b32 v136, 1\n\ts_cbranch_vccz LP1_%=\n\t" \
  "s_mov_b64 exec, vcc\n\tglobal_store_dwordx4 v138, v[84:87], %[cp] sc1\n\tglobal_store_dwordx4 v138, v[88:91], %[cp] offset:16 sc1\n\t" \
  "global_store_dwordx2 v138, v[92:93], %[cp] offset:32 sc1\n\tds_write_b32 v137, v136 offset:24\n\tLP1_%=:\n\ts_mov_b32 exec_lo, 0x00200002\n\t" \
  "s_mov_b32 exec_hi, 0x20000200\n\tv_mov_b32 v100, v101\n\ts_lshl_b64 exec, exec, 1\n\tv_mov_b32 v100, v102\n\ts_lshl_b64 exec, exec, 1\n\t" \
  "v_mov_b32 v100, v103\n\ts_mov_b32 exec_lo, 0x00f0000f\n\ts_mov_b32 exec_hi, 0xf0000f00\n\tv_mov_b32 v98, v100\n\t" \
  "global_store_dwordx4 %[ta], v[96:99], %[sst] sc1\n\tds_write_b128 %[tk], v[96:99]\n\ts_mov_b64 exec, -1\n\tds_write_b64 %[ra], v[84:85] offset:0\n\t" \
  "ds_write_b64 %[ra], v[86:87] offset:8\n\tds_write_b64 %[ra], v[88:89] offset:16\n\tds_write_b64 %[ra], v[90:91] offset:24\n\t" \
  "ds_write_b64 %[ra], v[92:93] offset:32\n\ts_mov_b64 exec, 1\n\tds_add_u32 v137, v136\n\ts_mov_b64 exec, -1\n\ts_setprio 0\n\ts_branch LZ_%=\n\t" \
  "LD2_%=:\n\ts_sub_u32 s43, s43, 1\n\ts_cbranch_scc1 LT_%=\n\tds_read_b32 v97, v96\n\tds_read_b128 v[44:47], %[tk] offset:0\n\t" \
  "ds_read_b128 v[48:51], %[tk] offset:1024\n\tds_read_b128 v[52:55], %[tk] offset:2048\n\tds_read_b128 v[56:59], %[tk] offset:3072\n\t" \
  "ds_read_b128 v[60:63], %[tk] offset:4096\n\ts_branch LD1_%=\n\tLH_%=:\n\ts_sleep 3\n\tglobal_load_dwordx4 v[140:143], %[vk], %[oa1] sc1\n\t" \
  "global_load_dwordx4 v[144:147], %[vk], %[oa2] sc1\n\tglobal_load_dwordx4 v[148:151], %[vk], %[oa3] sc1\n\t" \
  "global_load_dwordx4 v[152:155], %[vk], %[oa4] sc1\n\tglobal_load_dwordx4 v[156:159], %[vk], %[oa0] sc1\n\ts_cmp_eq_u32 %[w], 3\n\t" \
  "s_cbranch_scc0 LG_%=\n\tds_read_b64 v[112:113], %[ra] offset:0\n\tds_read_b64 v[114:115], %[ra] offset:8\n\t" \
  "ds_read_b64 v[116:117], %[ra] offset:16\n\tds_read_b64 v[118:119], %[ra] offset:24\n\tds_read_b64 v[120:121], %[ra] offset:32\n\t" \
  "ds_read2_b64 v[124:127], %[aw] offset0:0 offset1:1\n\tds_read2_b64 v[128:131], %[aw] offset0:2 offset1:3\n\t" \
  "ds_read_b64 v[132:133], %[aw] offset:32\n\tv_mov_b64 v[134:135], 0\n\ts_waitcnt lgkmcnt(3)\n\tv_lshlrev_b32 v24, 16, v112\n\t" \
  "v_and_b32 v25, 0xffff0000, v112\n\tv_lshlrev_b32 v26, 16, v113\n\tv_and_b32 v27, 0xffff0000, v113\n\tv_lshlrev_b32 v28, 16, v114\n\t" \
  "v_and_b32 v29, 0xffff0000, v114\n\tv_lshlrev_b32 v30, 16, v115\n\tv_and_b32 v31, 0xffff0000, v115\n\tv_lshlrev_b32 v32, 16, v116\n\t" \
  "v_and_b32 v33, 0xffff0000, v116\n\tv_lshlrev_b32 v34, 16, v117\n\tv_and_b32 v35, 0xffff0000, v117\n\tv_lshlrev_b32 v36, 16, v118\n\t" \
  "v_and_b32 v37, 0xffff0000, v118\n\tv_lshlrev_b32 v38, 16, v119\n\tv_and_b32 v39, 0xffff0000, v119\n\tv_lshlrev_b32 v40, 16, v120\n\t" \
  "v_and_b32 v41, 0xffff0000, v120\n\tv_lshlrev_b32 v42, 16, v121\n\tv_and_b32 v43, 0xffff0000, v121\n\ts_branch LG2_%=\n\tLG_%=:\n\t" \
  "v_mov_b64 v[24:25], 0\n\tv_mov_b64 v[26:27], 0\n\tv_mov_b64 v[28:29], 0\n\tv_mov_b64 v[30:31], 0\n\tv_mov_b64 v[32:33], 0\n\tv_mov_b64 v[34:35], 0\n\t" \
  "v_mov_b64 v[36:37], 0\n\tv_mov_b64 v[38:39], 0\n\tv_mov_b64 v[40:41], 0\n\tv_mov_b64 v[42:43], 0\n\tLG2_%=:\n\tv_add_u32 %[ta], %[so1], %[pm]\n\t" \
  "ds_read_b64_tr_b16 v[64:65], %[ta] offset:0\n\tds_read_b64_tr_b16 v[66:67], %[ta] offset:3200\n\tds_read_b64_tr_b16 v[68:69], %[ta] offset:8\n\t" \
  "ds_read_b64_tr_b16 v[70:71], %[ta] offset:3208\n\tds_read_b64_tr_b16 v[72:73], %[ta] offset:16\n\tds_read_b64_tr_b16 v[74:75], %[ta] offset:3216\n\t" \
  "ds_read_b64_tr_b16 v[76:77], %[ta] offset:24\n\tds_read_b64_tr_b16 v[78:79], %[ta] offset:3224\n\tds_read_b64_tr_b16 v[80:81], %[ta] offset:32\n\t" \
  "ds_read_b64_tr_b16 v[82:83], %[ta] offset:3232\n\tv_add_u32 %[ta], %[so2], %[pm]\n\tds_read_b64_tr_b16 v[84:85], %[ta] offset:0\n\t" \
  "ds_read_b64_tr_b16 v[86:87], %[ta] offset:3200\n\tds_read_b64_tr_b16 v[88:89], %[ta] offset:8\n\tds_read_b64_tr_b16 v[90:91], %[ta] offset:3208\n\t" \
  "ds_read_b64_tr_b16 v[92:93], %[ta] offset:16\n\tds_read_b64_tr_b16 v[94:95], %[ta] offset:3216\n\tds_read_b64_tr_b16 v[96:97], %[ta] offset:24\n\t" \
  "ds_read_b64_tr_b16 v[98:99], %[ta] offset:3224\n\tds_read_b64_tr_b16 v[100:101], %[ta] offset:32\n\t" \
  "ds_read_b64_tr_b16 v[102:103], %[ta] offset:3232\n\ts_waitcnt vmcnt(4)\n\tv_cmp_eq_u32 vcc, -1, v140\n\ts_cbranch_vccnz LSh1_%=\n\tLQh1_%=:\n\t" \
  "s_waitcnt lgkmcnt(10)\n\tv_mfma_f32_16x16x32_bf16 v[24:27], v[64:67], v[140:143], v[24:27]\n\tv_add_u32 %[ta], %[so3], %[pm]\n\t" \
  "ds_read_b64_tr_b16 v[104:105], %[ta] offset:0\n\tds_read_b64_tr_b16 v[106:107], %[ta] offset:3200\n\t" \
  "v_mfma_f32_16x16x32_bf16 v[28:31], v[68:71], v[140:143], v[28:31]\n\tds_read_b64_tr_b16 v[108:109], %[ta] offset:8\n\t" \
  "ds_read_b64_tr_b16 v[110:111], %[ta] offset:3208\n\tv_mfma_f32_16x16x32_bf16 v[32:35], v[72:75], v[140:143], v[32:35]\n\t" \
  "ds_read_b64_tr_b16 v[112:113], %[ta] offset:16\n\tds_read_b64_tr_b16 v[114:115], %[ta] offset:3216\n\t" \
  "v_mfma_f32_16x16x32_bf16 v[36:39], v[76:79], v[140:143], v[36:39]\n\tds_read_b64_tr_b16 v[116:117], %[ta] offset:24\n\t" \
  "ds_read_b64_tr_b16 v[118:119], %[ta] offset:3224\n\ts_waitcnt vmcnt(3)\n\tv_cmp_eq_u32 vcc, -1, v144\n\t" \
  "v_mfma_f32_16x16x32_bf16 v[40:43], v[80:83], v[140:143], v[40:43]\n\tds_read_b64_tr_b16 v[120:121], %[ta] offset:32\n\t" \
  "ds_read_b64_tr_b16 v[122:123], %[ta] offset:3232\n\ts_cbranch_vccnz LSh2_%=\n\tLQh2_%=:\n\ts_waitcnt lgkmcnt(10)\n\t" \
  "v_mfma_f32_16x16x32_bf16 v[24:27], v[84:87], v[144:147], v[24:27]\n\tv_add_u32 %[ta], %[so4], %[pf]\n\tds_read_b64_tr_b16 v[64:65], %[ta] offset:0\n\t" \
  "ds_read_b64_tr_b16 v[66:67], %[ta] offset:12800\n\tv_mfma_f32_16x16x32_bf16 v[28:31], v[88:91], v[144:147], v[28:31]\n\t" \
  "ds_read_b64_tr_b16 v[68:69], %[ta] offset:8\n\tds_read_b64_tr_b16 v[70:71], %[ta] offset:12808\n\t" \
  "v_mfma_f32_16x16x32_bf16 v[32:35], v[92:95], v[144:147], v[32:35]\n\tds_read_b64_tr_b16 v[72:73], %[ta] offset:16\n\t" \
  "ds_read_b64_tr_b16 v[74:75], %[ta] offset:12816\n\tv_mfma_f32_16x16x32_bf16 v[36:39], v[96:99], v[144:147], v[36:39]\n\t" \
  "ds_read_b64_tr_b16 v[76:77], %[ta] offset:24\n\tds_read_b64_tr_b16 v[78:79], %[ta] offset:12824\n\ts_waitcnt vmcnt(2)\n\t" \
  "v_cmp_eq_u32 vcc, -1, v148\n\tv_mfma_f32_16x16x32_bf16 v[40:43], v[100:103], v[144:147], v[40:43]\n\tds_read_b64_tr_b16 v[80:81], %[ta] offset:32\n\t" \
  "ds_read_b64_tr_b16 v[82:83], %[ta] offset:12832\n\ts_cbranch_vccnz LSh3_%=\n\tLQh3_%=:\n\ts_waitcnt lgkmcnt(10)\n\t" \
  "v_mfma_f32_16x16x32_bf16 v[24:27], v[104:107], v[148:151], v[24:27]\n\tv_add_u32 %[ta], %[so0], %[pm]\n\t" \
  "ds_read_b64_tr_b16 v[84:85], %[ta] offset:0\n\tds_read_b64_tr_b16 v[86:87], %[ta] offset:3200\n\t" \
  "v_mfma_f32_16x16x32_bf16 v[28:31], v[108:111], v[148:151], v[28:31]\n\tds_read_b64_tr_b16 v[88:89], %[ta] offset:8\n\t" \
  "ds_read_b64_tr_b16 v[90:91], %[ta] offset:3208\n\tv_mfma_f32_16x16x32_bf16 v[32:35], v[112:115], v[148:151], v[32:35]\n\t" \
  "ds_read_b64_tr_b16 v[92:93], %[ta] offset:16\n\tds_read_b64_tr_b16 v[94:95], %[ta] offset:3216\n\t" \
  "v_mfma_f32_16x16x32_bf16 v[36:39], v[116:119], v[148:151], v[36:39]\n\tds_read_b64_tr_b16 v[96:97], %[ta] offset:24\n\t" \
  "ds_read_b64_tr_b16 v[98:99], %[ta] offset:3224\n\ts_waitcnt vmcnt(1)\n\tv_max_u32 %[tk], v152, v154\n\tv_cmp_eq_u32 vcc, -1, %[tk]\n\t" \
  "v_mfma_f32_16x16x32_bf16 v[40:43], v[120:123], v[148:151], v[40:43]\n\tds_read_b64_tr_b16 v[100:101], %[ta] offset:32\n\t" \
  "ds_read_b64_tr_b16 v[102:103], %[ta] offset:3232\n\ts_cbranch_vccnz LSh4_%=\n\tLQh4_%=:\n\ts_waitcnt lgkmcnt(10)\n\t" \
  "v_mfma_f32_16x16x32_bf16 v[24:27], v[64:67], v[152:155], v[24:27]\n\tv_mfma_f32_16x16x32_bf16 v[28:31], v[68:71], v[152:155], v[28:31]\n\t" \
  "v_mfma_f32_16x16x32_bf16 v[32:35], v[72:75], v[152:155], v[32:35]\n\tv_mfma_f32_16x16x32_bf16 v[36:39], v[76:79], v[152:155], v[36:39]\n\t" \
  "s_waitcnt vmcnt(0)\n\tv_cmp_eq_u32 vcc, -1, v156\n\tv_mfma_f32_16x16x32_bf16 v[40:43], v[80:83], v[152:155], v[40:43]\n\ts_cbranch_vccnz LSh0_%=\n\t" \
  "LQh0_%=:\n\ts_waitcnt lgkmcnt(0)\n\tv_mfma_f32_16x16x32_bf16 v[24:27], v[84:87], v[156:159], v[24:27]\n\t" \
  "v_mfma_f32_16x16x32_bf16 v[28:31], v[88:91], v[156:159], v[28:31]\n\tv_mfma_f32_16x16x32_bf16 v[32:35], v[92:95], v[156:159], v[32:35]\n\t" \
  "v_mfma_f32_16x16x32_bf16 v[36:39], v[96:99], v[156:159], v[36:39]\n\tv_mfma_f32_16x16x32_bf16 v[40:43], v[100:103], v[156:159], v[40:43]\n\t" \
  "s_cmp_eq_u32 %[w], 3\n\ts_cbranch_scc1 LF_%=\n\ts_setprio 3\n\tv_add_u32 %[ta], %[sd], %[l16]\n\tv_mov_b32 v137, %[sc]\n\tv_mov_b32 v136, 1\n\t" \
  "ds_write_b128 %[ta], v[24:27] offset:0\n\tds_write_b128 %[ta], v[28:31] offset:1024\n\tds_write_b128 %[ta], v[32:35] offset:2048\n\t" \
  "ds_write_b128 %[ta], v[36:39] offset:3072\n\tds_write_b128 %[ta], v[40:43] offset:4096\n\ts_mov_b64 exec, 1\n\tds_add_u32 v137, v136\n\t" \
  "s_mov_b64 exec, -1\n\ts_branch LZ_%=\n\tLF_%=:\n\ts_setprio 3\n\tv_mov_b64 v[94:95], 0\n\tv_mov_b32 v96, %[sc]\n\tv_add_u32 %[ta], %[sr], %[l16]\n\t" \
  "LF1_%=:\n\tds_read_b32 v97, v96\n\ts_waitcnt lgkmcnt(0)\n\tv_cmp_gt_u32 vcc, 3, v97\n\ts_cbranch_vccz LF2_%=\n\ts_sleep 1\n\ts_sub_u32 s43, s43, 1\n\t" \
  "s_cbranch_scc0 LF1_%=\n\tLF2_%=:\n\tds_read_b128 v[64:67], %[ta] offset:0\n\tds_read_b128 v[68:71], %[ta] offset:1024\n\t" \
  "ds_read_b128 v[72:75], %[ta] offset:2048\n\tds_read_b128 v[76:79], %[ta] offset:3072\n\tds_read_b128 v[80:83], %[ta] offset:4096\n\t" \
  "ds_read_b128 v[104:107], %[ta] offset:5120\n\tds_read_b128 v[108:111], %[ta] offset:6144\n\tds_read_b128 v[112:115], %[ta] offset:7168\n\t" \
  "ds_read_b128 v[116:119], %[ta] offset:8192\n\tds_read_b128 v[120:123], %[ta] offset:9216\n\tds_read_b128 v[44:47], %[ta] offset:10240\n\t" \
  "ds_read_b128 v[48:51], %[ta] offset:11264\n\tds_read_b128 v[52:55], %[ta] offset:12288\n\tds_read_b128 v[56:59], %[ta] offset:13312\n\t" \
  "ds_read_b128 v[60:63], %[ta] offset:14336\n\ts_waitcnt lgkmcnt(10)\n\tv_pk_add_f32 v[24:25], v[24:25], v[64:65]\n\t" \
  "v_pk_add_f32 v[26:27], v[26:27], v[66:67]\n\tv_pk_add_f32 v[28:29], v[28:29], v[68:69]\n\tv_pk_add_f32 v[30:31], v[30:31], v[70:71]\n\t" \
  "v_pk_add_f32 v[32:33], v[32:33], v[72:73]\n\tv_pk_add_f32 v[34:35], v[34:35], v[74:75]\n\tv_pk_add_f32 v[36:37], v[36:37], v[76:77]\n\t" \
  "v_pk_add_f32 v[38:39], v[38:39], v[78:79]\n\tv_pk_add_f32 v[40:41], v[40:41], v[80:81]\n\tv_pk_add_f32 v[42:43], v[42:43], v[82:83]\n\t" \
  "s_waitcnt lgkmcnt(5)\n\tv_pk_add_f32 v[24:25], v[24:25], v[104:105]\n\tv_pk_add_f32 v[26:27], v[26:27], v[106:107]\n\t" \
  "v_pk_add_f32 v[28:29], v[28:29], v[108:109]\n\tv_pk_add_f32 v[30:31], v[30:31], v[110:111]\n\tv_pk_add_f32 v[32:33], v[32:33], v[112:113]\n\t" \
  "v_pk_add_f32 v[34:35], v[34:35], v[114:115]\n\tv_pk_add_f32 v[36:37], v[36:37], v[116:117]\n\tv_pk_add_f32 v[38:39], v[38:39], v[118:119]\n\t" \
  "v_pk_add_f32 v[40:41], v[40:41], v[120:121]\n\tv_pk_add_f32 v[42:43], v[42:43], v[122:123]\n\ts_waitcnt lgkmcnt(0)\n\ts_branch LT_%=\n\tLSs1_%=:\n\t" \
  "s_sub_u32 s43, s43, 1\n\ts_cbranch_scc1 LQs1_%=\n\ts_sleep %[oback]\n\tglobal_load_dwordx4 v[124:127], %[vd], %[oa1] sc1\n\t" \
  "global_load_dwordx4 v[128:131], %[vk], %[oa1] sc1\n\ts_waitcnt vmcnt(0)\n\tv_max_u32 %[tk], v124, v128\n\tv_cmp_eq_u32 vcc, -1, %[tk]\n\t" \
  "s_cbranch_vccnz LSs1_%=\n\ts_branch LQs1_%=\n\tLSs2_%=:\n\ts_sub_u32 s43, s43, 1\n\ts_cbranch_scc1 LQs2_%=\n\ts_sleep %[oback]\n\t" \
  "global_load_dwordx4 v[132:135], %[vd], %[oa2] sc1\n\tglobal_load_dwordx4 v[136:139], %[vk], %[oa2] sc1\n\ts_waitcnt vmcnt(0)\n\t" \
  "v_max_u32 %[tk], v132, v136\n\tv_cmp_eq_u32 vcc, -1, %[tk]\n\ts_cbranch_vccnz LSs2_%=\n\ts_branch LQs2_%=\n\tLSs3_%=:\n\ts_sub_u32 s43, s43, 1\n\t" \
  "s_cbranch_scc1 LQs3_%=\n\ts_sleep %[oback]\n\tglobal_load_dwordx4 v[140:143], %[vd], %[oa3] sc1\n\t" \
  "global_load_dwordx4 v[144:147], %[vk], %[oa3] sc1\n\ts_waitcnt vmcnt(0)\n\tv_max_u32 %[tk], v140, v144\n\tv_cmp_eq_u32 vcc, -1, %[tk]\n\t" \
  "s_cbranch_vccnz LSs3_%=\n\ts_branch LQs3_%=\n\tLSs4_%=:\n\ts_sub_u32 s43, s43, 1\n\ts_cbranch_scc1 LQs4_%=\n\ts_sleep %[oback]\n\t" \
  "global_load_dwordx4 v[148:151], %[vd], %[oa4] sc1\n\tglobal_load_dwordx4 v[152:155], %[vk], %[oa4] sc1\n\ts_waitcnt vmcnt(0)\n\t" \
  "v_max3_u32 %[tk], v148, v150, v152\n\tv_max_u32 %[tk], %[tk], v154\n\tv_cmp_eq_u32 vcc, -1, %[tk]\n\ts_cbranch_vccnz LSs4_%=\n\ts_branch LQs4_%=\n\t" \
  "LSs5_%=:\n\ts_sub_u32 s43, s43, 1\n\ts_cbranch_scc1 LQs5_%=\n\ts_sleep %[oback]\n\tglobal_load_dwordx4 v[156:159], %[vd], %[oa5] sc1\n\t" \
  "global_load_dwordx4 v[124:127], %[vk], %[oa5] sc1\n\ts_waitcnt vmcnt(0)\n\tv_max_u32 %[tk], v156, v124\n\tv_cmp_eq_u32 vcc, -1, %[tk]\n\t" \
  "s_cbranch_vccnz LSs5_%=\n\ts_branch LQs5_%=\n\tLSs6_%=:\n\ts_sub_u32 s43, s43, 1\n\ts_cbranch_scc1 LQs6_%=\n\ts_sleep %[oback]\n\t" \
  "global_load_dwordx4 v[128:131], %[vd], %[oa6] sc1\n\tglobal_load_dwordx4 v[132:135], %[vk], %[oa6] sc1\n\ts_waitcnt vmcnt(0)\n\t" \
  "v_max_u32 %[tk], v128, v132\n\tv_cmp_eq_u32 vcc, -1, %[tk]\n\ts_cbranch_vccnz LSs6_%=\n\ts_branch LQs6_%=\n\tLSd7_%=:\n\ts_sub_u32 s43, s43, 1\n\t" \
  "s_cbranch_scc1 LQd7_%=\n\ts_sleep %[oback]\n\tglobal_load_dwordx4 v[136:139], %[vd], %[oa7] sc1\n\t" \
  "global_load_dwordx4 v[140:143], %[vd], %[oa8] sc1\n\ts_waitcnt vmcnt(0)\n\tv_max_u32 %[tk], v136, v140\n\tv_cmp_eq_u32 vcc, -1, %[tk]\n\t" \
  "s_cbranch_vccnz LSd7_%=\n\ts_branch LQd7_%=\n\tLSd9_%=:\n\ts_sub_u32 s43, s43, 1\n\ts_cbranch_scc1 LQd9_%=\n\ts_sleep %[oback]\n\t" \
  "global_load_dwordx4 v[144:147], %[vd], %[oa9] sc1\n\tglobal_load_dwordx4 v[148:151], %[vk], %[oa7] sc1\n\ts_waitcnt vmcnt(0)\n\t" \
  "v_max3_u32 %[tk], v144, v146, v148\n\tv_cmp_eq_u32 vcc, -1, %[tk]\n\ts_cbranch_vccnz LSd9_%=\n\ts_branch LQd9_%=\n\tLSk8_%=:\n\t" \
  "s_sub_u32 s43, s43, 1\n\ts_cbranch_scc1 LQk8_%=\n\ts_sleep %[oback]\n\tglobal_load_dwordx4 v[152:155], %[vk], %[oa8] sc1\n\t" \
  "global_load_dwordx4 v[156:159], %[vk], %[oa9] sc1\n\ts_waitcnt vmcnt(0)\n\tv_max3_u32 %[tk], v152, v156, v158\n\tv_cmp_eq_u32 vcc, -1, %[tk]\n\t" \
  "s_cbranch_vccnz LSk8_%=\n\ts_branch LQk8_%=\n\tLSh1_%=:\n\ts_sub_u32 s43, s43, 1\n\ts_cbranch_scc1 LQh1_%=\n\ts_sleep %[oback]\n\t" \
  "global_load_dwordx4 v[140:143], %[vk], %[oa1] sc1\n\ts_waitcnt vmcnt(0)\n\tv_cmp_eq_u32 vcc, -1, v140\n\ts_cbranch_vccnz LSh1_%=\n\t" \
  "s_branch LQh1_%=\n\tLSh2_%=:\n\ts_sub_u32 s43, s43, 1\n\ts_cbranch_scc1 LQh2_%=\n\ts_sleep %[oback]\n\t" \
  "global_load_dwordx4 v[144:147], %[vk], %[oa2] sc1\n\ts_waitcnt vmcnt(0)\n\tv_cmp_eq_u32 vcc, -1, v144\n\ts_cbranch_vccnz LSh2_%=\n\t" \
  "s_branch LQh2_%=\n\tLSh3_%=:\n\ts_sub_u32 s43, s43, 1\n\ts_cbranch_scc1 LQh3_%=\n\ts_sleep %[oback]\n\t" \
  "global_load_dwordx4 v[148:151], %[vk], %[oa3] sc1\n\ts_waitcnt vmcnt(0)\n\tv_cmp_eq_u32 vcc, -1, v148\n\ts_cbranch_vccnz LSh3_%=\n\t" \
  "s_branch LQh3_%=\n\tLSh4_%=:\n\ts_sub_u32 s43, s43, 1\n\ts_cbranch_scc1 LQh4_%=\n\ts_sleep %[oback]\n\t" \
  "global_load_dwordx4 v[152:155], %[vk], %[oa4] sc1\n\ts_waitcnt vmcnt(0)\n\tv_max_u32 %[tk], v152, v154\n\tv_cmp_eq_u32 vcc, -1, %[tk]\n\t" \
  "s_cbranch_vccnz LSh4_%=\n\ts_branch LQh4_%=\n\tLSh0_%=:\n\ts_sub_u32 s43, s43, 1\n\ts_cbranch_scc1 LQh0_%=\n\ts_sleep %[oback]\n\t" \
  "global_load_dwordx4 v[156:159], %[vk], %[oa0] sc1\n\ts_waitcnt vmcnt(0)\n\tv_cmp_eq_u32 vcc, -1, v156\n\ts_cbranch_vccnz LSh0_%=\n\t" \
  "s_branch LQh0_%=\n\tLZ_%=:\n\t" \
  ""
#define COL_ASM \
  "s_mov_b32 s43, %[cap]\n\tv_xor_b32 v101, 0x400, %[lq]\n\tv_xor_b32 v102, 0x800, %[lq]\n\tv_xor_b32 v103, 0xc00, %[lq]\n\t" \
  "v_xor_b32 v104, 0x1000, %[lq]\n\tv_xor_b32 v105, 0x1400, %[lq]\n\tv_xor_b32 v106, 0x1800, %[lq]\n\tv_xor_b32 v107, 0x1c00, %[lq]\n\tLC0_%=:\n\t" \
  "ds_read_b32 v96, %[ac]\n\ts_waitcnt lgkmcnt(0)\n\tv_cmp_gt_u32 vcc, %[nf], v96\n\ts_cbranch_vccz LC1_%=\n\ts_sleep 1\n\ts_sub_u32 s43, s43, 1\n\t" \
  "s_cbranch_scc0 LC0_%=\n\tLC1_%=:\n\ts_setprio 2\n\ts_sleep %[pdel]\n\tglobal_load_dwordx4 v[68:71], v101, %[sp] sc1\n\t" \
  "global_load_dwordx4 v[72:75], v102, %[sp] sc1\n\tglobal_load_dwordx4 v[76:79], v103, %[sp] sc1\n\tglobal_load_dwordx4 v[80:83], v104, %[sp] sc1\n\t" \
  "global_load_dwordx4 v[84:87], v105, %[sp] sc1\n\tglobal_load_dwordx4 v[88:91], v106, %[sp] sc1\n\tglobal_load_dwordx4 v[92:95], v107, %[sp] sc1\n\t" \
  "ds_read_b128 v[64:67], %[ao]\n\ts_waitcnt lgkmcnt(0)\n\tLC2_%=:\n\ts_waitcnt vmcnt(4)\n\tv_pk_add_f32 v[108:109], v[64:65], v[68:69]\n\t" \
  "v_add_f32 v110, v66, v70\n\tv_pk_add_f32 v[112:113], v[72:73], v[76:77]\n\tv_add_f32 v114, v74, v78\n\t" \
  "v_pk_add_f32 v[108:109], v[108:109], v[112:113]\n\tv_add_f32 v110, v110, v114\n\ts_waitcnt vmcnt(0)\n\tv_pk_add_f32 v[116:117], v[80:81], v[84:85]\n\t" \
  "v_add_f32 v118, v82, v86\n\tv_pk_add_f32 v[120:121], v[88:89], v[92:93]\n\tv_add_f32 v122, v90, v94\n\t" \
  "v_pk_add_f32 v[116:117], v[116:117], v[120:121]\n\tv_add_f32 v118, v118, v122\n\tv_pk_add_f32 v[108:109], v[108:109], v[116:117]\n\t" \
  "v_add_f32 v110, v110, v118\n\tv_mul_f32 v112, 0x3acccccd, v108\n\tv_add_f32 v111, v108, v109\n\tv_mul_f32 v113, v112, v112\n\t" \
  "v_cmp_u_f32 vcc, v111, v110\n\tv_fma_f32 v113, v110, %[rdm], -v113\n\ts_cbranch_vccnz LC5_%=\n\tLC3_%=:\n\tv_max_f32 v113, 0, v113\n\t" \
  "v_cvt_pk_bf16_f32 v113, 0, v113\n\tv_add_f32 v113, 0x3727c5ac, v113\n\tv_cvt_pk_bf16_f32 v113, 0, v113\n\tv_sqrt_f32 v113, v113\n\t" \
  "v_cvt_pk_bf16_f32 v112, 0, v112\n\tv_cvt_pk_bf16_f32 v113, 0, v113\n\tv_rcp_f32 v113, v113\n\tv_fma_f32 v114, -%[swr], v112, v109\n\t" \
  "v_mul_f32 v114, v114, v113\n\tv_ashrrev_i32 v115, 31, v114\n\tv_or_b32 v115, 0x80000000, v115\n\tv_xor_b32 v114, v114, v115\n\t" \
  "v_and_or_b32 v114, v114, %[msk], %[rid]\n\ts_nop 1\n\tv_max_u32_dpp v114, v114, v114 quad_perm:[1,0,3,2] row_mask:0xf bank_mask:0xf bound_ctrl:1\n\t" \
  "ds_write_b64 %[at], v[112:113]\n\ts_nop 0\n\tv_max_u32_dpp v114, v114, v114 quad_perm:[2,3,0,1] row_mask:0xf bank_mask:0xf bound_ctrl:1\n\ts_nop 1\n\t" \
  "v_max_u32_dpp v114, v114, v114 row_half_mirror row_mask:0xf bank_mask:0xf bound_ctrl:1\n\ts_nop 1\n\t" \
  "v_max_u32_dpp v114, v114, v114 row_mirror row_mask:0xf bank_mask:0xf bound_ctrl:1\n\ts_nop 0\n\tv_readlane_b32 s40, v114, 0\n\t" \
  "v_readlane_b32 s41, v114, 16\n\tv_readlane_b32 s42, v114, 32\n\ts_max_u32 s40, s40, s41\n\ts_max_u32 s40, s40, s42\n\tv_mov_b32 v98, s40\n\t" \
  "s_mov_b32 exec_lo, 0x01010101\n\ts_mov_b32 exec_hi, 0x0101\n\tglobal_store_dwordx2 %[zr], v[98:99], %[kp] sc1\n\tds_write_b32 %[ak], v98\n\t" \
  "s_mov_b64 exec, -1\n\ts_and_b32 s41, s40, 0x7ff\n\ts_sub_u32 s41, %[rb], s41\n\ts_mul_i32 s42, s41, 160\n\tv_add_u32 v115, s42, %[ah]\n\t" \
  "ds_read_b64 v[116:117], v115\n\tv_readlane_b32 s44, v112, s41\n\tv_readlane_b32 s46, v113, s41\n\ts_waitcnt lgkmcnt(0)\n\ts_mov_b32 exec_hi, 0\n\t" \
  "v_lshlrev_b32 v118, 16, v116\n\tv_and_b32 v119, 0xffff0000, v116\n\tv_lshlrev_b32 v120, 16, v117\n\tv_and_b32 v121, 0xffff0000, v117\n\t" \
  "v_pk_add_f32 v[122:123], v[118:119], s[44:45] op_sel_hi:[1,0] neg_lo:[0,1] neg_hi:[0,1]\n\t" \
  "v_pk_add_f32 v[114:115], v[120:121], s[44:45] op_sel_hi:[1,0] neg_lo:[0,1] neg_hi:[0,1]\n\t" \
  "v_pk_fma_f32 v[118:119], v[122:123], s[46:47], v[118:119] op_sel_hi:[1,0,1]\n\t" \
  "v_pk_fma_f32 v[120:121], v[114:115], s[46:47], v[120:121] op_sel_hi:[1,0,1]\n\tv_cvt_pk_bf16_f32 v116, v118, v119\n\t" \
  "v_cvt_pk_bf16_f32 v117, v120, v121\n\ts_mov_b32 exec_hi, -1\n\tglobal_store_dwordx2 %[co], v[116:117], %[cp] sc1\n\ts_branch LC9_%=\n\tLC5_%=:\n\t" \
  "s_sub_u32 s43, s43, 1\n\ts_cbranch_scc1 LC3_%=\n\ts_mov_b32 s40, 0\n\tv_max3_u32 v96, v68, v69, v70\n\tv_cmp_eq_u32 vcc, -1, v96\n\t" \
  "s_cbranch_vccz LC61_%=\n\tglobal_load_dwordx4 v[68:71], v101, %[sp] sc1\n\ts_mov_b32 s40, 1\n\tLC61_%=:\n\tv_max3_u32 v96, v72, v73, v74\n\t" \
  "v_cmp_eq_u32 vcc, -1, v96\n\ts_cbranch_vccz LC62_%=\n\tglobal_load_dwordx4 v[72:75], v102, %[sp] sc1\n\ts_mov_b32 s40, 1\n\tLC62_%=:\n\t" \
  "v_max3_u32 v96, v76, v77, v78\n\tv_cmp_eq_u32 vcc, -1, v96\n\ts_cbranch_vccz LC63_%=\n\tglobal_load_dwordx4 v[76:79], v103, %[sp] sc1\n\t" \
  "s_mov_b32 s40, 1\n\tLC63_%=:\n\tv_max3_u32 v96, v80, v81, v82\n\tv_cmp_eq_u32 vcc, -1, v96\n\ts_cbranch_vccz LC64_%=\n\t" \
  "global_load_dwordx4 v[80:83], v104, %[sp] sc1\n\ts_mov_b32 s40, 1\n\tLC64_%=:\n\tv_max3_u32 v96, v84, v85, v86\n\tv_cmp_eq_u32 vcc, -1, v96\n\t" \
  "s_cbranch_vccz LC65_%=\n\tglobal_load_dwordx4 v[84:87], v105, %[sp] sc1\n\ts_mov_b32 s40, 1\n\tLC65_%=:\n\tv_max3_u32 v96, v88, v89, v90\n\t" \
  "v_cmp_eq_u32 vcc, -1, v96\n\ts_cbranch_vccz LC66_%=\n\tglobal_load_dwordx4 v[88:91], v106, %[sp] sc1\n\ts_mov_b32 s40, 1\n\tLC66_%=:\n\t" \
  "v_max3_u32 v96, v92, v93, v94\n\tv_cmp_eq_u32 vcc, -1, v96\n\ts_cbranch_vccz LC67_%=\n\tglobal_load_dwordx4 v[92:95], v107, %[sp] sc1\n\t" \
  "s_mov_b32 s40, 1\n\tLC67_%=:\n\ts_cmp_eq_u32 s40, 0\n\ts_cbranch_scc1 LC3_%=\n\ts_waitcnt vmcnt(0)\n\ts_branch LC2_%=\n\tLC9_%=:\n\t" \
  ""
extern "C" __global__ __launch_bounds__(768, 3) void fused_block(const u16* x, const u16* wqkv, const u16* wo, const u16* w_r,
                                                   const u16* w_gate, const u16* w_up, const u16* w_down, u16* out,
                                                   char* ws) {
  extern __shared__ __attribute__((aligned(16))) char smem[];
  const int tid = threadIdx.x;
  const int lane = tid & 63;
  const int w = __builtin_amdgcn_readfirstlane(tid >> 6);
  const int g = lane >> 4, r16 = lane & 15, q4 = (lane & 15) >> 2, p4 = lane & 3;
  const int bid = blockIdx.x;
  const int xcd = bid & 7;
  const int bidx = bid >> 3;
  const u32 l16 = lane * 16;
  if (w == 11) {
    u64 pc;
    asm volatile("s_getpc_b64 %0" : "=s"(pc));
    if (lane < 10) touch_line((const void*)(pc + min((bidx * 10 + lane) * 128, CODE_BYTES)));
  }

  if (w == 0 && lane == 0) {
    const int q = bidx & 3;
    touch_line(((q & 2) ? (const char*)x : (const char*)wqkv) + (q & 1) * 2359296 + bidx * 128);
  }
  if (bidx >= 26) {
    const int fb = xcd * 6 + (bidx - 26);
    const u32 tstart = (u32)__builtin_amdgcn_s_memrealtime();
    const bool hurry = ((((u32)(u64)w_gate + fb * 64) >> 7) & 7u) == 3u;
#define WAIT_UNTIL(T)                                                                      \
  _Pragma("nounroll") for (int it = 0; it < 1000; ++it) {                                  \
    if ((int)((u32)__builtin_amdgcn_s_memrealtime() - tstart) >= (T)) break;               \
    __builtin_amdgcn_s_sleep(4);                                                           \
  }
    if (w < 3) {
      int li = (bidx - 26) * 160 + w * 64 + lane;
      if (w < 2 || lane < 32) touch_line((const char*)x + (size_t)(((xcd >> 2) * 13 + 6) * 80 - 16) * 1280 + li * 128);
    }
    u32x2 ep;
    u32 tag0;
    asm volatile("global_load_dwordx2 %0, %1, off sc1" : "=v"(ep) : "v"(ws + WS_EPOCH) : "memory");
    {
      int wi = (((xcd >> 1) & 1) * 6 + (bidx - 26)) * 12 + w;
      wi = wi > 129 ? 129 : wi;
      int row = (xcd >> 2) * 1040 + (xcd & 1) * 520 + wi * 4 + g;
      row = row > SEQ - 1 ? SEQ - 1 : row;
      const u32 vo = row * 1280 + r16 * 16;
      u32x4 xr[5];
#pragma unroll
      for (int i = 0; i < 5; ++i)
        asm volatile("global_load_dwordx4 %0, %1, %2 offset:%3" : "=v"(xr[i]) : "v"(vo), "s"(x), "n"(i * 256) : "memory");
      asm volatile("s_waitcnt vmcnt(0)" ::: "memory");
      asm volatile("" : "+v"(xr[0]), "+v"(xr[1]), "+v"(xr[2]), "+v"(xr[3]), "+v"(xr[4]), "+v"(ep)::"memory");
      tag0 = ep[0] + (ep[0] == SENT - 1u ? 2u : 1u);
      float sm = 0.f;
#pragma unroll
      for (int i = 0; i < 5; ++i)
#pragma unroll
        for (int e = 0; e < 4; ++e) sm = dot2b(xr[i][e], 0x3F803F80u, sm);
      const float mean = row16_sum(sm) * (1.0f / DM);
      float ss = 0.f;
#pragma unroll
      for (int i = 0; i < 5; ++i)
#pragma unroll
        for (int e = 0; e < 4; ++e) {
          const float a = lo_f(xr[i][e]) - mean, b = hi_f(xr[i][e]) - mean;
          ss = __builtin_fmaf(a, a, ss);
          ss = __builtin_fmaf(b, b, ss);
        }
      const float var = row16_sum(ss) * (1.0f / DM);
      const float den = rbf(__builtin_amdgcn_sqrtf(rbf(rbf(var) + 1e-5f)));
      if (r16 == 0) GST2((u32)(row * 8), ws + WS_LN1, 0, ((u32x2){cvt2((f32x2){mean, den}), tag0}));
    }
    u32x4 va[2][5], vb[5];
    const int vrk = (fb >> 4) * 14 + (int)(((((u32)(u64)w_gate + fb * 64) >> 6) - 8u) & 15u);
    const int vq0 = vrk * 4 + w * 2;
    u32x4 ra;
    u32 rb5;
    asm volatile("global_load_dwordx4 %0, %2, %4\n\tglobal_load_dword %1, %3, %4 offset:1024"
                   : "=&v"(ra), "=&v"(rb5) : "v"(l16), "v"((u32)(lane * 4)), "s"(w_r) : "memory");
    if (w >= 2) {
      WAIT_UNTIL(hurry ? 0 : PDELAY + 12 * (w - 2))
      const u32x4 s4 = (u32x4){SENT, SENT, SENT, SENT};
      const char* fp = ws + WS_FILL + (size_t)fb * 73728 + (w - 2) * 7168 + 4096;
      GST4(l16, fp, -4096, s4); GST4(l16, fp, -3072, s4); GST4(l16, fp, -2048, s4); GST4(l16, fp, -1024, s4);
      GST4(l16, fp, 0, s4); GST4(l16, fp, 1024, s4); GST4(l16, fp, 2048, s4);
      if (w < 4) GST4(l16, ws + WS_FILL + (size_t)fb * 73728 + 69632 + w * 1024, 0, s4);
      if (w >= 5) GST4((u32)(((w - 5) * 8 + (lane >> 3)) * 6144 + (lane & 7) * 16), ws + WS_PK + fb * 128, 0, s4);
    }
    if (w == 0) {
      const u64 e1 = (((u64)ep[1] << 32) | ep[0]) + (ep[0] == SENT - 1u ? 2ull : 1ull);
      if (lane == 0) *(u32x4*)(smem + CTL_OFF) = (u32x4){(u32)e1, (u32)(e1 >> 32), 0u, 0u};
      if (lane >= 1 && lane < 8) *(u32x4*)(smem + CTL_OFF + lane * 16) = (u32x4){0u, 0u, 0u, 0u};
      if (lane >= 32) *(u32x4*)(smem + KEY_OFF - 512 + lane * 16) = (u32x4){0u, 0u, 0u, 0u};
    }
    if (w >= 6) {
      const u32 o1 = tid >= 636 ? 0x3F803F80u : 0u;
      if (tid < 720) *(u32x4*)(smem + HN_OFF + (tid - 384) * 16) = (u32x4){o1, o1, o1, o1};
    }
    if (w < 2 && !hurry && vrk < 40) {
      WAIT_UNTIL(VDELAY)
      const u32 vo = (vq0 * 4 + g) * 1280 + r16 * 16;
#pragma unroll
      for (int i = 0; i < 5; ++i)
        asm volatile("global_load_dwordx4 %0, %3, %4 offset:%8\n\tglobal_load_dwordx4 %1, %3, %5 offset:%8\n\tglobal_load_dwordx4 %2, %6, %7 offset:%8"
                     : "=&v"(va[0][i]), "=&v"(va[1][i]), "=&v"(vb[i]) : "v"(vo), "s"(wo), "s"((const char*)wo + 5120), "v"((u32)(r16 * 16)), "s"(w_r), "n"(i * 256) : "memory");
      asm volatile("s_waitcnt vmcnt(0)" : "+v"(va[0][0]), "+v"(va[0][1]), "+v"(va[0][2]), "+v"(va[0][3]), "+v"(va[0][4]), "+v"(va[1][0]), "+v"(va[1][1]),
                   "+v"(va[1][2]), "+v"(va[1][3]), "+v"(va[1][4]), "+v"(vb[0]), "+v"(vb[1]), "+v"(vb[2]), "+v"(vb[3]), "+v"(vb[4])::"memory");
      float sb = 0.f;
#pragma unroll
      for (int i = 0; i < 20; ++i) sb = dot2b(vb[i >> 2][i & 3], 0x3F803F80u, sb);
      sb = row16_sum(sb) * (1.0f / DM);
      u32x4 pk2[2];
#pragma unroll
      for (int k = 0; k < 2; ++k) {
        float d = 0.f, sa = 0.f;
#pragma unroll
        for (int i = 0; i < 20; ++i) {
          d = dot2b(va[k][i >> 2][i & 3], vb[i >> 2][i & 3], d);
          sa = dot2b(va[k][i >> 2][i & 3], 0x3F803F80u, sa);
        }
        const u32 vp = __float_as_uint(row16_sum(d - sa * sb));
        pk2[k] = (u32x4){cvt2((f32x2){__uint_as_float(__builtin_amdgcn_readlane(vp, 0)), __uint_as_float(__builtin_amdgcn_readlane(vp, 16))}), tag0,
                          cvt2((f32x2){__uint_as_float(__builtin_amdgcn_readlane(vp, 32)), __uint_as_float(__builtin_amdgcn_readlane(vp, 48))}), tag0};
      }
      if (lane < 8) GST4(l16, ws + WS_VP + vq0 * 64, 0, ((lane & 1) ? pk2[1] : pk2[0]));
    } else
      asm volatile("s_waitcnt vmcnt(0)" ::: "memory");
    asm volatile("" : "+v"(ra), "+v"(rb5)::"memory");
    LDS_BARRIER();
    const u32x2 tg = *(const u32x2*)(smem + CTL_OFF);
    const u32 tag = tg[0];
    if (w == 2 && lane == 0) GST2(0u, ws + WS_HFLAG + fb * 128, 0, tg);
    if (w >= 3) {
      int li = (bidx - 26) * 534 + (w - 3) * 64 + lane;
      li = li > 3199 ? 3199 : li;
      const int rr = li / 5;
      if (w < 11 || lane < 22) touch_line((const char*)wo + rr * 1280 + ((xcd >> 1) & 1) * 640 + (li - rr * 5) * 128);
    }
    WAIT_UNTIL(hurry ? 0 : FDELAY)
    asm volatile("s_barrier" ::: "memory");
    if (w >= 2) {
      const int ch = (lane & 7) ^ (((lane >> 4) & 1) * 2 + ((w + fb) & 1) * 4);
      const u32 vg = (lane >> 3) * (FF * 2) + fb * 64 + (ch & 3) * 16;
      const u64 gsel = (ch & 4) ? (u64)w_up : (u64)w_gate;
#pragma nounroll
      for (int n0 = w - 2; n0 < 80; n0 += 10) {
        const int n = (fb & 1) ? 79 - n0 : n0;
        const u64 p = gsel + vg + (u32)(n * 8 * FF * 2);
        asm volatile("s_mov_b32 m0, %1\n\ts_nop 0\n\tglobal_load_lds_dwordx4 %0, off" ::"v"(p), "s"(n * 1024) : "memory");
      }
#pragma nounroll
      for (int n = 11 - w; n < 40; n += 10) {
        const u32 row = (u32)(n * 64 + lane) / 80u;
        asm volatile("s_mov_b32 m0, %2\n\ts_nop 0\n\tglobal_load_lds_dwordx4 %0, %1" " nt"
                     ::"v"(l16 ^ ((row & 3u) * 32u + (row & 8u) * 16u)), "s"((const char*)w_down + (size_t)fb * 40960 + n * 1024), "s"(DOWN_OFF + n * 1024) : "memory");
      }
      asm volatile("s_waitcnt vmcnt(0)" ::: "memory");
      if (lane == 0) asm volatile("ds_add_u32 %0, %1" ::"v"(CTL_OFF + 48), "v"(1u) : "memory");
    } else {
      if (w == 1 && lane < 4)
        touch_line(lane == 3 ? ws + WS_PK - 65536 + bid * 128 : (const char*)out + lane * 1277952 + fb * 1280);
      WAIT_UNTIL(KSTART)
    }
    if (w >= 4) {
      const int v = w - 4, kq = v & 3, m = v >> 2;
#pragma nounroll
      for (int it = 0; it < SPIN_CAP; ++it) {
        if (__builtin_amdgcn_readfirstlane(lds_ld32(CTL_OFF + 48)) >= 10u) break;
        __builtin_amdgcn_s_sleep(1);
      }
      bf16x8 gfr[5], ufr[5], dfr[5];
      {
        const int gx = (160 * kq + 8 * g + q4) * 128 + ((m ^ (q4 >> 1) ^ ((g & 1) * 2)) * 32) + p4 * 8;
        const char* db = smem + DOWN_OFF + (8 * g + q4) * 1280 + p4 * 8;
#pragma unroll
        for (int j = 0; j < 5; ++j) {
          gfr[j] = tr_frag(smem + gx + j * 4096, 512);
          ufr[j] = tr_frag(smem + (gx ^ 64) + j * 4096, 512);
          dfr[j] = tr_frag(db + ((v * 160 + j * 32) ^ (q4 * 32 + (g & 1) * 128)), 5120);
        }
#pragma unroll
        for (int j = 0; j < 5; ++j) asm volatile("" : "+v"(gfr[j]), "+v"(ufr[j]), "+v"(dfr[j]));
      }
      u32 ahn = HN_OFF + p4 * HSL + kq * 320 + g * 16, asw = ACT_OFF + p4 * ASL + kq * 256 + (q4 & 1) * 128 + m * 64 + g * 16, asr = ACT_OFF + (lane & 31) * 4;
      u32 aav = ACT_OFF + 4352 + v * 64 + (lane & 31) * 2, aar = ACT_OFF + 4352 + v * 64 + g * 16, vpo = v * 320 + r16 * 64 + g * 16, up3 = lane >= 32 ? 3u : 0u;
      asm volatile("" : "+v"(up3), "+v"(aar));
      const char* pdst = ws + WS_PART + fb * 2560;
      asm volatile("" : "+v"(ahn), "+v"(asw), "+v"(asr), "+v"(aav), "+v"(vpo));
      asm volatile("" : "+s"(pdst));
      int pass = 0;
      asm volatile("" : "+s"(pass));
#pragma nounroll
      for (; pass < 2; ++pass) {
        asm volatile("s_barrier" ::: "memory");
        u32x4 bs;
#define GU(J, C0, C1) "v_mfma_f32_16x16x32_bf16 v[120:123], %[g" #J "], v[100+4*" #J ":103+4*" #J "], " C0 "\n\t" \
                      "v_mfma_f32_16x16x32_bf16 v[124:127], %[u" #J "], v[100+4*" #J ":103+4*" #J "], " C1 "\n\t"
#define GU1(J) GU(J, "v[120:123]", "v[124:127]")
        asm volatile("ds_read_b128 v[100:103], %[ah]\n\tds_read_b128 v[104:107], %[ah] offset:64\n\tds_read_b128 v[108:111], %[ah] offset:128\n\t"
                     "ds_read_b128 v[112:115], %[ah] offset:192\n\tds_read_b128 v[116:119], %[ah] offset:256\n\t"
                     "s_waitcnt lgkmcnt(4)\n\t" GU(0, "0", "0") "s_waitcnt lgkmcnt(3)\n\t" GU1(1) "s_waitcnt lgkmcnt(2)\n\t" GU1(2)
                     "s_waitcnt lgkmcnt(1)\n\t" GU1(3) "s_waitcnt lgkmcnt(0)\n\t" GU1(4)
                     "ds_read_b128 %[bs], %[ab]\n\ts_mov_b32 exec_lo, 0x00f000f0\n\ts_mov_b32 exec_hi, 0x00f000f0\n\ts_nop 4\n\t"
                     "v_mov_b64 v[120:121], v[124:125]\n\tv_mov_b64 v[122:123], v[126:127]\n\t"
                     "s_mov_b32 exec_lo, 0x00ff00ff\n\ts_mov_b32 exec_hi, 0x00ff00ff\n\tds_write_b128 %[aw], v[120:123]\n\ts_mov_b64 exec, -1\n\t"
                     "s_setprio 3\n\ts_waitcnt lgkmcnt(0)\n\tv_cmp_eq_u32 vcc, 3, v129\n\ts_barrier\n\t"
                     "s_cbranch_vccnz 1f\n\tds_read_b128 %[bs], %[ab]\n\ts_waitcnt lgkmcnt(0)\n1:"
                     : [bs] "=&{v[128:131]}"(bs)
                     : [ah] "v"(ahn), [ab] "v"((u32)(CTL_OFF + 112)), [aw] "v"(asw), [g0] "v"(gfr[0]), [g1] "v"(gfr[1]), [g2] "v"(gfr[2]),
                       [g3] "v"(gfr[3]), [g4] "v"(gfr[4]), [u0] "v"(ufr[0]), [u1] "v"(ufr[1]), [u2] "v"(ufr[2]), [u3] "v"(ufr[3]), [u4] "v"(ufr[4])
                     : "memory", "vcc", "scc", "v100", "v101", "v102", "v103", "v104", "v105", "v106", "v107", "v108", "v109", "v110", "v111", "v112", "v113",
                       "v114", "v115", "v116", "v117", "v118", "v119", "v120", "v121", "v122", "v123", "v124", "v125", "v126", "v127");
        const u32x2 best = {bs[2], bs[3]};
        f32x2 s0, s1, s2, s3;
        asm volatile("ds_read2_b32 %0, %4 offset0:0 offset1:32\n\tds_read2_b32 %1, %4 offset0:64 offset1:96\n\t"
                     "ds_read2_b32 %2, %4 offset0:128 offset1:160\n\tds_read2_b32 %3, %4 offset0:192 offset1:224"
                     : "=&v"(s0), "=&v"(s1), "=&v"(s2), "=&v"(s3) : "v"(asr + ((best[1] & 3u) | up3) * ASL) : "memory");
        float rd = __builtin_amdgcn_rcpf(hi_f(best[0])), mb = lo_f(best[0]);
        asm volatile("s_waitcnt lgkmcnt(0)" : "+v"(s0), "+v"(s1), "+v"(s2), "+v"(s3), "+v"(rd), "+v"(mb)::"memory");
        const f32x2 sm = (s0 + s1) + (s2 + s3);
        auto xg = __builtin_amdgcn_permlane32_swap(__float_as_uint(sm[0]), __float_as_uint(sm[0]), false, false);
        auto xu = __builtin_amdgcn_permlane32_swap(__float_as_uint(sm[1]), __float_as_uint(sm[1]), false, false);
        const f32x2 gu = unpk(cvt2((f32x2){(__uint_as_float(xg[0]) - mb * __uint_as_float(xg[1])) * rd,
                                          (__uint_as_float(xu[0]) - mb * __uint_as_float(xu[1])) * rd}));
        const float sg = rbf(gu[0] * __builtin_amdgcn_rcpf(1.0f + __expf(-gu[0])));
        const u32 av = f2bf(sg * gu[1]);
#define DN(J) "v_mfma_f32_16x16x32_bf16 v[104+4*" #J ":107+4*" #J "], %[d" #J "], v[100:103], 0\n\t"
#define MV(J) "s_lshl_b64 exec, exec, 1\n\tv_mov_b64 v[104:105], v[104+4*" #J ":105+4*" #J "]\n\tv_mov_b64 v[106:107], v[106+4*" #J ":107+4*" #J "]\n\t"
        asm volatile("ds_write_b16 %[aw], %[av]\n\tds_read_b128 v[100:103], %[ar]\n\ts_waitcnt lgkmcnt(0)\n\t" DN(0) DN(1) DN(2) DN(3) DN(4)
                     "s_mov_b32 exec_lo, 0x00010001\n\ts_mov_b32 exec_hi, 0x00010001\n\ts_nop 1\n\t" MV(1) MV(2) MV(3) MV(4)
                     "s_mov_b32 exec_lo, 0x001f001f\n\ts_mov_b32 exec_hi, 0x001f001f\n\tglobal_store_dwordx4 %[vp], v[104:107], %[pd] sc1\n\t"
                     "s_mov_b64 exec, -1\n\ts_setprio 0"
                     ::[aw] "v"(aav), [av] "v"(av), [ar] "v"(aar), [vp] "v"(vpo), [pd] "s"(pdst), [d0] "v"(dfr[0]), [d1] "v"(dfr[1]),
                       [d2] "v"(dfr[2]), [d3] "v"(dfr[3]), [d4] "v"(dfr[4])
                     : "memory", "scc", "v100", "v101", "v102", "v103", "v104", "v105", "v106", "v107", "v108", "v109", "v110", "v111", "v112", "v113",
                       "v114", "v115", "v116", "v117", "v118", "v119", "v120", "v121", "v122", "v123");
        if (pass) break;
        u32 fin = 0;
#pragma nounroll
        for (int it = 0; it < SPIN_CAP * 8; ++it) {
          fin = __builtin_amdgcn_readfirstlane(lds_ld32(CTL_OFF + 40));
          if (fin) break;
          __builtin_amdgcn_s_sleep(4);
        }
        if (fin != 2u) break;
        pdst += WS_PART2 - WS_PART;
      }
      return;
    }
    if (w == 0) {
      u32 pk6;
      asm volatile(
          "s_mov_b32 s41, %[cap]\n\ts_mov_b32 s40, 1\n\ts_mov_b64 s[46:47], 0\n\tv_mov_b32 v5, %[ln]\n\t"
          "s_mov_b32 s54, -1\n\ts_mov_b32 s55, 0x7ffff\n\tv_mov_b32 v12, 0\n\t"
#define PKLD(D) "s_mov_b64 exec, s[54:55]\n\tglobal_atomic_or_x2 " D ", %[vk], %[z], %[kb] sc0 sc1\n\ts_mov_b64 exec, -1\n\t"
          PKLD("v[8:9]") "s_sleep %[gap]\n\t" PKLD("v[10:11]") "s_sleep %[gap]\n\t" PKLD("v[14:15]")
#define MDPP(C) "s_nop 1\n\tv_max_u32_dpp v13, v13, v13 " C " row_mask:0xf bank_mask:0xf bound_ctrl:1\n\t"
#define PSTEP(L, D0, D1, DR)                                                                        \
          L "0:\n\t"                                                                               \
          "s_waitcnt vmcnt(2)\n\t"                                                                 \
          "v_cmp_eq_u32 s[42:43], " D1 ", %[tag]\n\t"                                              \
          "s_and_b64 exec, s[42:43], s[54:55]\n\ts_andn2_b64 s[54:55], s[54:55], s[42:43]\n\t"      \
          "v_and_or_b32 v12, " D0 ", %[msk], %[lid]\n\t"                                           \
          "v_mov_b32 v6, " D0 "\n\ts_mov_b64 exec, -1\n\t"                                         \
          "v_cmp_le_u32 vcc, s40, v12\n\t"                                                         \
          "s_cmp_eq_u64 vcc, s[46:47]\n\ts_cbranch_scc1 " L "1f\n\t"                              \
          "s_swappc_b64 s[50:51], s[48:49]\n"                                                      \
          L "1:\n\t"                                                                               \
          "s_cmp_eq_u64 s[54:55], 0\n\ts_cbranch_scc1 9f\n\t"                                     \
          PKLD(DR)
          "s_getpc_b64 s[48:49]\n\ts_add_u32 s48, s48, 12\n\ts_addc_u32 s49, s49, 0\n\ts_branch 10f\n\t"
          "v_mov_b32 v13, v12\n\t"
          MDPP("quad_perm:[1,0,3,2]") MDPP("quad_perm:[2,3,0,1]") MDPP("row_half_mirror") MDPP("row_mirror")
          "v_bfe_u32 v4, v6, 10, 6\n\tv_readlane_b32 s44, v13, 0\n\tv_readlane_b32 s45, v13, 16\n\tv_readlane_b32 s46, v13, 32\n\tv_readlane_b32 s47, v13, 48\n\t"
          "v_sub_u32 v4, %[rb], v4\n\ts_max_u32 s44, s44, s45\n\ts_max_u32 s46, s46, s47\n\ts_max_u32 s44, s44, s46\n\t"
          "s_and_b32 s45, s44, 0x7fffff\n\ts_bitset1_b32 s45, 23\n\ts_mul_hi_u32 s45, s45, %[shr]\n"
          "5:\n\ts_sub_u32 s40, s44, s45\n\ts_cselect_b32 s40, 1, s40\n\ts_max_u32 s40, s40, 1\n\tv_cmp_le_u32 s[46:47], s40, v12\n\ts_mul_i32 s45, s45, 3\n\ts_lshr_b32 s45, s45, 2\n\t"
          "s_bcnt1_i32_b64 s52, s[46:47]\n\ts_cmp_gt_u32 s52, 3\n\ts_cbranch_scc1 5b\n\t"
          "v_mbcnt_lo_u32_b32 v13, s46, 0\n\tv_mbcnt_hi_u32_b32 v13, s47, v13\n\tv_sub_u32 v7, %[ln], v13\n\tv_add_u32 v7, s52, v7\n\t"
          "v_cndmask_b32 v13, v7, v13, s[46:47]\n\tv_cndmask_b32 v7, 2, 1, s[46:47]\n\tv_cmp_gt_u32 s[44:45], 3, v13\n\t"
          "v_lshl_add_u32 v13, v13, 4, %[pa]\n\ts_mov_b64 exec, s[44:45]\n\tds_write_b128 v13, v[4:7]\n\ts_mov_b64 exec, -1\n\t"
          "s_setpc_b64 s[50:51]\n"
          PSTEP("1", "v8", "v9", "v[8:9]")
          PSTEP("2", "v10", "v11", "v[10:11]")
          PSTEP("3", "v14", "v15", "v[14:15]")
          "s_sub_u32 s41, s41, 1\n\ts_cbranch_scc0 10b\n\ts_swappc_b64 s[50:51], s[48:49]\n"
          "9:\n\t"  "v_or_b32 v7, 4, v7\n\ts_mov_b64 exec, s[44:45]\n\tds_write_b32 v13, v7 offset:12\n\ts_mov_b64 exec, -1\n\tds_write_b32 %[at], v6\n\ts_waitcnt lgkmcnt(0)\n\ts_barrier\n\ts_waitcnt vmcnt(0)"
          : "=&{v6}"(pk6) : [cap] "n"(SPIN_CAP), [gap] "n"(PGAP), [vk] "v"((u32)(lane * 6144)), [kb] "s"(ws + WS_PK + fb * 128), [tag] "v"(tag), [z] "v"(0ull), [ln] "v"(lane),
            [msk] "s"(~1023u), [lid] "v"(64u | lane), [rb] "v"(2047 - ((lane >> 1) * 80 + (lane & 1) * 48)),
            [shr] "s"((u32)((1.0 - PTHR) * 4294967296.0)), [pa] "s"(POST_OFF), [at] "v"((u32)(KEY_OFF - 256 + lane * 4))
          : "memory", "vcc", "scc", "s40", "s41", "s42", "s43", "s44", "s45", "s46", "s47", "s48", "s49", "s50", "s51", "s52", "s54", "s55",
            "v4", "v5", "v7", "v8", "v9", "v10", "v11", "v12", "v13", "v14", "v15");
      if (fb == 47 && lane < 8) GST4(l16, ws + WS_EPOCH, 0, ((u32x4){tg[0], tg[1], tg[0], tg[1]}));
      const u32 now = (u32)__builtin_amdgcn_s_memrealtime();
      const u32 nag = (pk6 - (now >> 1) - 1u) & 1023u;
      u32 kgo = __builtin_amdgcn_readfirstlane(now + KCD - 2 * (1023u - wave_max(lane < 51 ? nag : 0u)));
      u64 old = __ballot(lane < 51 && nag < 1023u - (KOLD - 8) / 2);
      asm volatile("" : "+s"(kgo), "+s"(old));
      asm volatile(
          "s_mov_b32 s41, %[cap]\n\ts_mov_b32 s54, -1\n\ts_mov_b32 s55, 0x7ffff\n\tv_mov_b32 v12, 0\n\t"
          "s_mov_b64 exec, %[old]\n\ts_cbranch_execz 3f\n\tglobal_load_dwordx2 v[14:15], %[vk], %[kb] sc1\n"
          "3:\n\ts_setprio 3\n\ts_barrier\n"
          "4:\n\ts_memrealtime s[42:43]\n\ts_waitcnt lgkmcnt(0)\n\ts_sub_u32 s42, s42, %[go]\n\ts_cmp_lt_i32 s42, 0\n\ts_cbranch_scc1 4b\n\t"
          "s_andn2_b64 exec, s[54:55], %[old]\n\ts_cbranch_execz 5f\n\tglobal_load_dwordx2 v[14:15], %[vk], %[kb] sc1\n"
          "5:\n\ts_mov_b64 exec, s[54:55]\n\tds_read_b32 v4, %[ctl] offset:92\n\ts_waitcnt lgkmcnt(0)\n\tv_and_b32 v4, 3, v4\n\t"
          "v_lshl_add_u32 v4, v4, 4, %[pa]\n\tds_read_b64 v[6:7], v4\n\ts_waitcnt lgkmcnt(0)\n\t"
          "v_readfirstlane_b32 s48, v6\n\tv_readfirstlane_b32 s49, v7\n\tv_mov_b32 v6, 1\n\ts_waitcnt vmcnt(0)\n\tv_cmp_ne_u32 s[54:55], v15, %[tag]\n\tv_mov_b32 v12, v14\n\ts_mov_b64 exec, -1\n\t"
          "s_cmp_eq_u64 s[54:55], 0\n\ts_cbranch_scc1 9f\n\t"
          PKLD("v[8:9]") "s_sleep %[gap]\n\t" PKLD("v[10:11]")
#define KSTEP(L, D0, D1, DR)                                                                        \
          L "0:\n\t"                                                                               \
          "s_waitcnt vmcnt(1)\n\t"                                                                 \
          "v_cmp_eq_u32 s[42:43], " D1 ", %[tag]\n\t"                                              \
          "s_and_b64 exec, s[42:43], s[54:55]\n\tv_mov_b32 v12, " D0 "\n\ts_mov_b64 exec, -1\n\t"   \
          "s_andn2_b64 s[54:55], s[54:55], s[42:43]\n\ts_cbranch_scc0 9f\n\t"                     \
          PKLD(DR)
          KSTEP("1", "v8", "v9", "v[8:9]")
          KSTEP("2", "v10", "v11", "v[10:11]")
          "s_sub_u32 s41, s41, 1\n\ts_cbranch_scc0 10b\n"
          "9:\n\t"
          "v_readlane_b32 s40, v12, s49\n\tds_write_b32 %[ak], v12\n\tv_mov_b32 v13, v12\n\t"
          "s_and_b32 s45, s40, 0x7ff\n\tv_cmp_lt_u32 vcc, s40, v12\n\ts_cmp_eq_u32 s45, s48\n\ts_cselect_b64 vcc, vcc, -1\n\ts_cbranch_vccz 8f\n\t"
          MDPP("quad_perm:[1,0,3,2]") MDPP("quad_perm:[2,3,0,1]") MDPP("row_half_mirror") MDPP("row_mirror")
          "s_nop 0\n\tv_readlane_b32 s44, v13, 0\n\tv_readlane_b32 s45, v13, 16\n\tv_readlane_b32 s46, v13, 32\n\tv_readlane_b32 s47, v13, 48\n\t"
          "s_max_u32 s44, s44, s45\n\ts_max_u32 s46, s46, s47\n\ts_max_u32 s40, s44, s46\n\t"
          "v_cmp_eq_u32 vcc, s40, v12\n\tv_mov_b32 v6, 2\n\ts_ff1_i32_b64 s44, vcc\n\tv_mov_b32 v7, s44\n"
          "8:\n\tv_mov_b32 v4, s40\n\ts_mov_b64 exec, 1\n\tds_write_b128 %[ctl], v[4:7]\n\ts_mov_b64 exec, -1\n\t"
          "s_waitcnt vmcnt(0) lgkmcnt(0)"
          :: [cap] "n"(SPIN_CAP), [gap] "n"(KGAP), [go] "s"(kgo), [old] "s"(old), [vk] "v"((u32)(lane * 6144)), [kb] "s"(ws + WS_PK + fb * 128 + 8), [tag] "v"(tag),
            [ctl] "v"((u32)(CTL_OFF + 32)), [ak] "v"((u32)(KEY_OFF + lane * 4)), [pa] "s"(POST_OFF), [z] "v"(0ull)
          : "memory", "vcc", "scc", "s40", "s41", "s42", "s43", "s44", "s45", "s46", "s47", "s48", "s49", "s54", "s55", "v4", "v5", "v6", "v7", "v8", "v9", "v10",
            "v11", "v12", "v13", "v14", "v15");
      return;
    }
    float swr = dot2b(rb5, 0x3F803F80u, 0.f);
#pragma unroll
    for (int e = 0; e < 4; ++e) swr = dot2b(ra[e], 0x3F803F80u, swr);
    swr = wave_sum(swr);
    u32 ah16 = HN_OFF + (w - 1) * HSL + l16, ah4 = HN_OFF + (w - 1) * HSL + 1024 + lane * 4, l4 = lane * 4;
    asm volatile("" : "+v"(swr), "+v"(ah16), "+v"(ah4), "+v"(l4));
#define DOT3(H, W) "v_dot2c_f32_bf16 v102, 0x3f803f80, " H "\n\tv_dot2c_f32_bf16 v103, " H ", " H "\n\tv_dot2c_f32_bf16 v104, " H ", " W "\n\t"
#define ADPP(C) "s_nop 1\n\tv_add_f32_dpp v102, v102, v102 " C " row_mask:0xf bank_mask:0xf bound_ctrl:1\n\t"
#define XDPP(C) "s_nop 1\n\tv_max_u32_dpp v102, v102, v102 " C " row_mask:0xf bank_mask:0xf bound_ctrl:1\n\t"
    const char* rp = (const char*)w_r;
    u32 second = 0;
#pragma nounroll
    for (;;) {
      asm volatile(
          "s_mov_b32 s47, %[cap]\n\ts_mov_b32 s43, -1\n\ts_sub_u32 s46, %[sec], 1\n"
          "7:\n\ts_mov_b64 s[48:49], %[rp]\n\ts_mov_b32 s45, 0\n\ts_branch 4f\n"
          "18:\n\ts_sleep 1\n"
          "1:\n\ts_sub_u32 s47, s47, 1\n\ts_cbranch_scc1 8f\n\ts_cmp_lg_u32 %[sec], 0\n\ts_cbranch_scc1 7b\n\t"
          "ds_read_b128 v[108:111], %[ap]\n\ts_memrealtime s[40:41]\n\ts_waitcnt lgkmcnt(0)\n\t"
          "v_readfirstlane_b32 s45, v111\n\tv_readfirstlane_b32 s46, v109\n\tv_readfirstlane_b32 s50, v110\n\ts_lshr_b32 s41, s40, 1\n\t"
          "s_cmp_eq_u32 s45, 6\n\ts_cbranch_scc1 8f\n\ts_bitcmp0_b32 s45, 0\n\ts_cbranch_scc1 18b\n\t"
          "s_cmp_eq_u32 s46, s42\n\ts_cbranch_scc1 6f\n\ts_cmp_eq_u32 s46, s43\n\ts_cbranch_scc1 2f\n\t"
          "s_sub_u32 s41, s41, s50\n\ts_and_b32 s41, s41, 0x3ff\n\ts_cmp_lt_u32 s41, %[tq]\n\ts_cbranch_scc1 18b\n\t"
          "s_mov_b32 s43, s46\n\ts_add_u32 s44, s50, 8\n\ts_cmp_ge_u32 s41, %[pc] - 20\n\ts_cbranch_scc1 1b\n\t"
          "s_mul_i32 s48, s46, 6144\n\tv_mov_b64 v[104:105], 0\n\tv_mov_b32 v101, s48\n\tv_mad_u32_u24 v101, %[l16], 48, v101\n\t"
          "s_mov_b64 exec, 0xff\n\tglobal_atomic_or_x2 v[102:103], v101, v[104:105], %[pk] sc0 sc1\n\ts_sub_u32 s50, s50, 512\n\ts_waitcnt vmcnt(0)\n\t"
          "v_cmp_eq_u32 vcc, v103, %[tag]\n\tv_subrev_u32 v102, s50, v102\n\tv_and_b32 v102, 0x3ff, v102\n\tv_cndmask_b32 v102, 0, v102, vcc\n\t"
          XDPP("quad_perm:[1,0,3,2]") XDPP("quad_perm:[2,3,0,1]") XDPP("row_half_mirror")
          "s_mov_b64 exec, -1\n\tv_readfirstlane_b32 s44, v102\n\ts_min_u32 s44, s44, 562\n\ts_add_u32 s44, s44, s50\n\ts_branch 1b\n"
          "2:\n\ts_sub_u32 s41, s41, s44\n\ts_and_b32 s41, s41, 0x3ff\n\ts_cmp_lt_u32 s41, %[pc] - 8\n\ts_cbranch_scc1 1b\n\t"
          "s_sub_u32 s41, %[pc], s41\n\ts_lshl1_add_u32 s51, s41, s40\n\ts_mul_i32 s48, s46, 1280\n\ts_add_u32 s48, s48, %[lo]\n\ts_addc_u32 s49, %[hi], 0\n\t"
          "3:\n\ts_memrealtime s[40:41]\n\ts_waitcnt lgkmcnt(0)\n\ts_sub_u32 s40, s40, s51\n\ts_cmp_lt_i32 s40, 0\n\ts_cbranch_scc1 3b\n"
          "4:\n\tglobal_load_dwordx4 v[96:99], %[l16], s[48:49] sc1\n\tglobal_load_dword v100, %[l4], s[48:49] offset:1024 sc1\n\t"
          "v_mov_b64 v[102:103], 0\n\tv_mov_b32 v104, 0\n\ts_mov_b32 s42, -2\n\ts_waitcnt vmcnt(0)\n\t"
          "ds_read_b128 v[108:111], %[ap]\n\tds_read_b32 v105, %[ab] offset:4\n\tds_write_b128 %[a16], v[96:99]\n\tds_write_b32 %[a4], v100\n\t"
          "v_max3_u32 v101, v96, v97, v98\n\tv_max3_u32 v101, v101, v99, v100\n\tv_cmp_eq_u32 vcc, -1, v101\n\t"
          DOT3("v96", "v92") DOT3("v97", "v93")
          "s_cbranch_vccnz 1b\n\ts_mov_b32 s42, s46\n\ts_waitcnt lgkmcnt(2)\n\tv_lshl_or_b32 v109, v111, 8, v109\n\tv_lshl_or_b32 v109, v105, 16, v109\n\t"
          "s_or_b32 s45, s46, 0x20500\n\tv_cmp_eq_u32 vcc, s45, v109\n\ts_cbranch_vccz 5f\n\ts_waitcnt lgkmcnt(0)\n\ts_barrier\n"
          "5:\n\t" DOT3("v98", "v94") DOT3("v99", "v95") DOT3("v100", "%[rb]")
          "s_nop 2\n\tv_mov_b32 v105, v104\n\tv_permlane32_swap_b32 v102, v103\n\tv_add_f32 v102, v102, v103\n\tv_permlane32_swap_b32 v104, v105\n\t"
          "v_add_f32 v104, v104, v105\n\ts_nop 1\n\tv_permlane16_swap_b32 v102, v104\n\tv_add_f32 v102, v102, v104\n\t"
          ADPP("quad_perm:[1,0,3,2]") ADPP("quad_perm:[2,3,0,1]") ADPP("row_half_mirror") ADPP("row_mirror")
          "v_mul_f32 v103, 0x3acccccd, v102\n\tv_readlane_b32 s41, v102, 16\n\tv_readlane_b32 s40, v103, 32\n\t"
          "s_nop 1\n\tv_fma_f32 v102, -v103, v103, s40\n\tv_max_f32 v102, 0, v102\n\tv_cvt_pk_bf16_f32 v102, 0, v102\n\t"
          "v_add_f32 v102, 0x3727c5ac, v102\n\tv_cvt_pk_bf16_f32 v102, 0, v102\n\tv_sqrt_f32 v102, v102\n\tv_cvt_pk_bf16_f32 v105, 0, v103\n\t"
          "v_fma_f32 v105, -%[swr], v105, s41\n\tv_cvt_pk_bf16_f32 v106, v103, v102\n\tv_and_b32 v102, 0xffff0000, v106\n\tv_rcp_f32 v102, v102\n\t"
          "v_mov_b32 v104, 1\n\tv_mul_f32 v102, v102, v105\n\tv_ashrrev_i32 v103, 31, v102\n\tv_or_b32 v103, 0x80000000, v103\n\t"
          "v_xor_b32 v102, v102, v103\n\tv_and_or_b32 v107, v102, -4, %[sl]\n\ts_cbranch_vccz 1b\n\t"
          "s_mov_b64 exec, 1\n\tds_max_u64 %[ab], v[106:107] offset:8\n\tds_add_u32 %[ab], v104 offset:4\n\ts_branch 9f\n"
          "6:\n\ts_bitcmp0_b32 s45, 2\n\ts_cbranch_scc1 1b\n\ts_mov_b64 exec, 1\n\tds_max_u64 %[ab], v[106:107] offset:8\n"
          "8:\n\tv_mov_b32 v104, 1\n\ts_mov_b64 exec, 1\n\tds_add_u32 %[ab], v104 offset:4\n\ts_barrier\n"
          "9:\n\ts_mov_b64 exec, -1\n\ts_waitcnt lgkmcnt(0)\n\ts_barrier"
          :
          : [cap] "n"(SPIN_CAP * 8), [rp] "s"(rp), [sec] "s"(second), [ap] "v"((u32)(POST_OFF + (w - 1) * 16)), [tq] "n"(TQD / 2), [pc] "n"(PCD / 2),
            [pk] "s"(ws + WS_PK + (bidx - 26) * 128), [tag] "v"(tag), [lo] "s"((u32)(u64)(ws + WS_PC)), [hi] "s"((u32)((u64)(ws + WS_PC) >> 32)),
            [l16] "v"(l16), [l4] "v"(l4), [a16] "v"(ah16), [a4] "v"(ah4), "{v[92:95]}"(ra), [rb] "v"(rb5), [swr] "v"(swr), [sl] "s"(w - 1),
            [ab] "v"((u32)(CTL_OFF + 112))
          : "memory", "vcc", "scc", "s40", "s41", "s42", "s43", "s44", "s45", "s46", "s47", "s48", "s49", "s50", "s51", "v96", "v97", "v98", "v99",
            "v100", "v101", "v102", "v103", "v104", "v105", "v106", "v107", "v108", "v109", "v110", "v111");
      if (w != 1 || second) break;
      u32x4 wst;
#pragma nounroll
      for (int it = 0; it < SPIN_CAP * 8; ++it) {
        wst = lds_ld128(CTL_OFF + 32);
        if (__builtin_amdgcn_readfirstlane(wst[2])) break;
        __builtin_amdgcn_s_sleep(1);
      }
      if (__builtin_amdgcn_readfirstlane(wst[2]) != 2u) break;
      if (lane < 2) *(u32x4*)(smem + (lane ? POST_OFF : CTL_OFF + 112)) = lane ? (u32x4){0u, 0u, 0u, 5u} : (u32x4){0u, 2u, 0x3F800000u, 0u};
      rp = ws + WS_CAND + __builtin_amdgcn_readfirstlane(wst[3]) * 2560;
      second = 1;
    }
    if (w == 1) {
    } else if (w == 3 || fb >= 40) {
      const int c = w == 3 ? fb : fb + 8;
      if (c < 51) {
        const u32 now = (u32)__builtin_amdgcn_s_memrealtime();
        const u32 age = ((now >> 1) - __builtin_amdgcn_readfirstlane(*(const u32*)(smem + KEY_OFF - 256 + c * 4))) & 1023u;
        asm volatile("s_mov_b32 s42, %[cap]\n"
                     "3:\n\ts_memrealtime s[40:41]\n\ts_waitcnt lgkmcnt(0)\n\ts_sub_u32 s40, s40, %[go]\n\ts_cmp_lt_i32 s40, 0\n\ts_cbranch_scc1 3b\n"
                     "4:\n\tglobal_load_dwordx4 v[96:99], %[l16], %[cp] sc1\n\tglobal_load_dword v103, %[l4], %[cp] offset:1024 sc1\n\t"
                     "s_sub_u32 s42, s42, 8\n\ts_waitcnt vmcnt(0)\n\ts_cbranch_scc1 2f\n\t"
                     "v_max3_u32 v101, v96, v98, v103\n\tv_cmp_eq_u32 vcc, -1, v101\n\ts_cbranch_vccnz 4b\n"
                     "1:\n\tds_read_b64 v[100:101], %[af]\n\tds_read_b32 v102, %[ak]\n\ts_sub_u32 s42, s42, 1\n\ts_cbranch_scc1 2f\n\ts_waitcnt lgkmcnt(0)\n\t"
                     "v_cmp_eq_u32 vcc, 0, v100\n\ts_cbranch_vccnz 1b\n\t"
                     "v_cmp_eq_u32 vcc, %[c], v101\n\tv_bfi_b32 v102, v102, 0, %[m]\n\ts_cbranch_vccnz 2f\n\t"
                     "v_mad_u32_u24 v100, v102, %[rs], %[l16]\n\tv_mad_u32_u24 v101, v102, %[rs], %[l4]\n\t"
                     "global_store_dwordx4 v100, v[96:99], %[out]" "\n\tglobal_store_dword v101, v103, %[out] offset:1024" "\n"
                     "2:\n\ts_waitcnt lgkmcnt(0)"
                     ::[cap] "n"(SPIN_CAP * 8), [go] "s"(now + CCD - 2 * age), [cp] "s"(ws + WS_CAND + c * 2560 + 1280), [af] "v"((u32)(CTL_OFF + 40)),
                       [ak] "v"((u32)(KEY_OFF + c * 4)), [c] "s"(c), [m] "s"(2047), [rs] "s"(1280), [l16] "v"(l16), [l4] "v"(l4), [out] "s"(out)
                     : "memory", "vcc", "scc", "s40", "s41", "s42", "v96", "v97", "v98", "v99", "v100", "v101", "v102", "v103");
      }
    } else {
      const char* pp = ws + WS_PART + fb * 64;
      int pass = 0;
      asm volatile("" : "+s"(pass));
#pragma nounroll
      for (; pass < 2; ++pass) {
        const u32x2 best = *(const u32x2*)(smem + CTL_OFF + 120);
        const u32 slot = best[1] & 3u;
        const u32 wkey = __builtin_amdgcn_readfirstlane(*(const u32*)(smem + (pass ? CTL_OFF + 32 : POST_OFF + slot * 16)));
        const float rr = rbf(__uint_as_float((best[1] & ~3u) ^ ((u32)((int)~best[1] >> 31) | 0x80000000u)));
        float rs = rbf(__builtin_amdgcn_rcpf(1.0f + __expf(-rr)));
        const int ccol = (g & 1) * 2 + (g >> 1);
        float hv = bf2f(*(const u16*)(smem + HN_OFF + slot * HSL + fb * 32 + p4 * 8 + ccol * 2));
        hv = __builtin_fmaf(hv - lo_f(best[0]), __builtin_amdgcn_rcpf(hi_f(best[0])), hv);
        const char* op_ = (const char*)out + (size_t)(2047 - (int)(wkey & 2047u)) * 1280 + fb * 32;
        u32 vo_ = p4 * 8 + ccol * 2;
        asm volatile("" : "+v"(vo_), "+v"(hv), "+v"(rs));
        asm volatile("" : "+s"(op_));
        const u32 vp = (lane >> 2) * 2560 + p4 * 16;
        typedef __attribute__((ext_vector_type(16))) unsigned int u32x16;
        u32x16 keepa, keepb;
        f32x4 ys;
        u32 fin;
        asm volatile(
#define PLD(B0, B1, B2)                                                                             \
            "global_load_dwordx4 " B0 ", %[vp], %[p0] sc1\n\t"                                       \
            "global_load_dwordx4 " B1 ", %[vp], %[p1] sc1\n\tglobal_load_dwordx4 " B2 ", %[vp], %[p2] sc1\n\t"
#define PCHK(A0, A1, B0, B1, C0, C1)                                                                \
            "s_waitcnt vmcnt(3)\n\t"                                                                 \
            "v_pk_add_f32 v[96:97], " A0 ", " B0 "\n\tv_pk_add_f32 v[98:99], " A1 ", " B1 "\n\t"    \
            "v_pk_add_f32 v[96:97], v[96:97], " C0 "\n\tv_pk_add_f32 v[98:99], v[98:99], " C1 "\n\t" \
            "v_pk_add_f32 v[102:103], v[96:97], v[98:99]\n\t"                                        \
            "v_cmp_u_f32 vcc, v102, v103\n\ts_cbranch_vccz 9f\n\t"
            "s_mov_b32 s42, %[cap]\n\ts_sleep %[cdel]\n\t"
            PLD("v[64:67]", "v[68:71]", "v[72:75]")
            "s_sleep %[gap]\n\t"
            PLD("v[80:83]", "v[84:87]", "v[88:91]")
            "1:\n\t"
            PCHK("v[64:65]", "v[66:67]", "v[68:69]", "v[70:71]", "v[72:73]", "v[74:75]")
            PLD("v[64:67]", "v[68:71]", "v[72:75]")
            PCHK("v[80:81]", "v[82:83]", "v[84:85]", "v[86:87]", "v[88:89]", "v[90:91]")
            PLD("v[80:83]", "v[84:87]", "v[88:91]")
            "s_sub_u32 s42, s42, 1\n\ts_cbranch_scc0 1b\n"
            "9:\n\tds_read_b32 %[fin], %[af]"
            : "=&{v[96:99]}"(ys), "=&{v[64:79]}"(keepa), "=&{v[80:95]}"(keepb), [fin] "=&v"(fin)
            : [cap] "n"(SPIN_CAP), [gap] "n"(CGAP), [cdel] "n"(CDEL), [vp] "v"(vp), [p0] "s"(pp),
              [p1] "s"(pp + 40960), [p2] "s"(pp + 81920), [af] "v"((u32)(CTL_OFF + 40))
            : "memory", "vcc", "scc", "s42", "v102", "v103");
        float y = tr_sum(ys);
        y += dppf<0x124>(y);
        y += dppf<0x128>(y);
        const u32 res = cvt2((f32x2){__builtin_fmaf(y, rs, hv), 0.f});
        asm volatile("s_mov_b32 s42, %[cap]\n"
                     "1:\n\ts_waitcnt lgkmcnt(0)\n\tv_cmp_eq_u32 vcc, %[ok], %[fin]\n\ts_cbranch_vccz 2f\n\t"
                     "s_mov_b32 exec_lo, 0x000f000f\n\ts_mov_b32 exec_hi, 0x000f000f\n\tglobal_store_short %[vo], %[res], %[op]" "\n\ts_mov_b64 exec, -1\n"
                     "2:\n\tv_cmp_eq_u32 vcc, 0, %[fin]\n\ts_cbranch_vccz 3f\n\tds_read_b32 %[fin], %[af]\n\ts_sub_u32 s42, s42, 1\n\ts_cbranch_scc0 1b\n"
                     "3:\n\ts_waitcnt lgkmcnt(0)\n\tv_cmp_lt_u32 vcc, %[ok], %[fin]\n\ts_cbranch_vccz 4f\n\ts_waitcnt vmcnt(0)\n4:"
                     : [fin] "+v"(fin) : [cap] "n"(SPIN_CAP * 8), [ok] "s"(pass + 1), [vo] "v"(vo_), [res] "v"(res), [op] "s"(op_),
                       [af] "v"((u32)(CTL_OFF + 40)), "{v[64:79]}"(keepa), "{v[80:95]}"(keepb) : "memory", "vcc", "scc", "s42");
        if (pass || __builtin_amdgcn_readfirstlane(fin) != 2u) break;
        asm volatile("s_barrier\n\ts_barrier" ::: "memory");
        pp += WS_PART2 - WS_PART;
      }
    }
    return;
  }

  const int idx2 = (xcd & 1) * 26 + bidx;
  const int hg = ((xcd >> 1) & 1) * 4 + (idx2 & 3);
  const int mt = (xcd >> 2) * 13 + (idx2 >> 2);
#define ROTF (((idx2 >> 2) * 3 / 2 + (idx2 & 3) * 3) % 10)
  const int kb0 = ROTF;
  const int r0 = mt * 80;
  if (w >= 4) {
    const int cw = w - 4;
    u32 vo = w_off(cw, lane, hg), vx = x_off(cw, lane, r0), vx2 = x_off(8 + (cw & 3), lane, r0);
    asm volatile("" : "+v"(vo), "+v"(vx), "+v"(vx2));
    __builtin_amdgcn_sched_barrier(0);
    const char* wb = (const char*)wqkv + (size_t)kb0 * 245760 + W_ROW(cw);
    dma_m0<0>(vo, wb, cw * 1024);
    dma_m0<0>(vx, (const char*)x + kb0 * 128, XRING + cw * 1024);
    dma_m0<0>(vo, wb + 61440, 8192 + cw * 1024);
    if (cw < 4) dma_m0<0>(vx2, (const char*)x + kb0 * 128, XRING + 8192 + cw * 1024);
    asm volatile("s_barrier" ::: "memory");
  }
  const int rt = w >= 4 ? (w & 3) : 4, kh = w >= 4 ? (w - 4) >> 2 : (w & 1);
  float bias[2];
  u32 sel[4], e4, eh, aq, aka, akb, pm, pf, hi, arec, vpp;
  const char *os, *os4, *sst, *oa[10];
  int so_[10];
  int lane_ = lane, mt_ = mt, hg_ = hg;
  auto prepv = [&](const int lane) __attribute__((always_inline)) {
    const int g = lane >> 4, r16 = lane & 15, q4 = (lane & 15) >> 2, p4 = lane & 3;
    const int par = lane & 1;
    const bool ca = r16 >= 4 * g, cb = r16 >= 4 * g + 2;
    bias[0] = (ca || r0 + rt > 0) ? 0.f : -__builtin_inff();
    bias[1] = (cb || r0 + rt > 0) ? 0.f : -__builtin_inff();
    const u32 sa = par ? 0x01000C0Cu : 0x0C0C0100u, sb = par ? 0x03020C0Cu : 0x0C0C0302u;
    sel[0] = ca ? 0x0C0C0C0Cu : sa;
    sel[1] = cb ? 0x0C0C0C0Cu : sb;
    sel[2] = ca ? sa : 0x0C0C0C0Cu;
    sel[3] = cb ? sb : 0x0C0C0C0Cu;
    e4 = EXO + rt * 512 + lane * 8;
    eh = e4 + kh * 6144;
    aq = TAB_OFF + 384 + (rt * 16 + r16) * 4;
    aka = TAB_OFF + 384 + (rt * 16 + 4 * g + par + (ca ? 16 : 0)) * 4;
    akb = TAB_OFF + 384 + (rt * 16 + 4 * g + par + 2 + (cb ? 16 : 0)) * 4;
    pm = (4 * g + q4) * 160 + p4 * 40;
    pf = (20 * g + 16 + q4) * 160 + p4 * 40;
    hi = HIM + (rt * 16 + r16) * 160 + g * 40;
    arec = REC_OFF + (r16 == 0 ? kh * 192 + g * 48 : (r16 == 8 ? ((g >> 1) == kh ? 480 : 384) : 432));
    vpp = lane + kh * 64 + (rt == 0 ? 768 : (rt == 2 ? 8192 : (rt == 4 ? 512 : 640)));
    asm volatile("" : "+v"(arec), "+v"(vpp));
    asm volatile("" : "+v"(bias[0]), "+v"(bias[1]), "+v"(sel[0]), "+v"(sel[1]), "+v"(sel[2]), "+v"(sel[3]), "+v"(e4), "+v"(eh),
                 "+v"(aq), "+v"(aka), "+v"(akb), "+v"(pm), "+v"(pf), "+v"(hi));
  };
  auto preps = [&](const int mt, const int hg) __attribute__((always_inline)) {
    const char* ob = ws + WS_O + (size_t)mt * 102400;
    os = ob + rt * 20480 + (2 * hg + kh) * 1024;
    os4 = ob + rt * 20480 + (16 + (hg >> 1)) * 1024 + (hg & 1) * 8;
    sst = ws + WS_STAT + (size_t)(2 * mt + (rt < 3 ? 0 : 1)) * 8192 + hg * 1024 + (rt < 3 ? rt : rt - 3) * 256;
    asm volatile("" : "+s"(os), "+s"(os4), "+s"(sst));
    const int k2 = w & 1;
#pragma unroll
    for (int j = 0; j < 10; ++j) {
      const bool oth = j >= 5 || w < 2;
      const int mo[8] = {OORD};
      const int ch = j % 5 < 4 ? (2 * hg + k2) ^ (oth ? 2 * mo[4 + j % 5] : 2 * mo[j % 5]) : (hg >> 1) ^ ((oth != ((hg & 1) != 0)) ? (k2 ? 0 : 3) : 1 + k2);
      oa[j] = ob + (j % 5 < 4 ? ch : 16 + ch) * 1024;
      so_[j] = ch * (j % 5 < 4 ? 6400 : 25600);
      asm volatile("" : "+s"(oa[j]), "+s"(so_[j]));
    }
  };
#define WO_OFFS                                                                                    \
  u32 woff[5];                                                                                     \
  _Pragma("unroll") for (int i = 0; i < 5; ++i) {                                                  \
    const int c = i * 64 + lane;                                                                   \
    const int row = c / 10;                                                                        \
    woff[i] = row * 1120 + hg * 160 + c * 16 + (4 - i) * 1024;                                     \
  }
#define WO_RUN(RHO)                                                                                \
  {                                                                                                \
    const char* wq_ = (const char*)wo + (RHO) * 40960; \
    dma_m0<-4096>(woff[0], wq_, (RHO) * 5120 + 4096);                                               \
    dma_nx<-3072>(woff[1], wq_); dma_nx<-2048>(woff[2], wq_); dma_nx<-1024>(woff[3], wq_); dma_nx<0>(woff[4], wq_); \
  }

#define BAR asm volatile("s_barrier" ::: "memory");
#define PREP_V __builtin_amdgcn_sched_barrier(0); asm volatile("" : "+v"(lane_)); prepv(lane_); __builtin_amdgcn_sched_barrier(0);
#define PREP_S __builtin_amdgcn_sched_barrier(0); asm volatile("" : "+s"(mt_), "+s"(hg_)); preps(mt_, hg_); __builtin_amdgcn_sched_barrier(0);
  if (w < 2) {
    const int l = w;
    __builtin_amdgcn_s_setprio(3);
    u32 vw[4];
#pragma unroll
    for (int i = 0; i < 4; ++i) vw[i] = w_off(i, lane, hg) + 4096;
    u32 go = (u32)kb0 * 245760u + 122880u + l * 61440u;
    u32 so = HSLOT + l * 8192 + 4096;
#define W1(I) dma_nx<((I) - 4) * 1024>(vw[(I) & 3], wb + (W_ROW(I) - (I) * 1024));
#define ISSUE_W                                                                                    \
  {                                                                                                \
    const char* wb = (const char*)wqkv + go;                                                       \
    dma_m0<-4096>(vw[0], wb, so);                                                                  \
    W1(1) W1(2) W1(3) W1(4) W1(5) W1(6) W1(7)                                                      \
    go += 122880u;                                                                                 \
    if (go >= 2457600u) go -= 2457600u;                                                            \
    so = so >= 5 * HSLOT ? so - 5 * HSLOT : so + HSLOT;                                            \
  }
    asm volatile("" : "+v"(vw[0]), "+v"(vw[1]), "+v"(vw[2]), "+v"(vw[3]), "+s"(go));
    BAR
    ISSUE_W
    __builtin_amdgcn_sched_barrier(0);
    WO_OFFS
    asm volatile("" : "+v"(woff[0]), "+v"(woff[1]), "+v"(woff[2]), "+v"(woff[3]), "+v"(woff[4]));
    BAR
    ISSUE_W
    asm volatile("s_waitcnt vmcnt(8)\n\ts_barrier" ::: "memory");
#pragma unroll 1
    for (int h = 0; h < 17; ++h) {
      ISSUE_W
      asm volatile("s_waitcnt vmcnt(8)\n\ts_barrier" ::: "memory");
    }
    asm volatile("s_waitcnt vmcnt(0)\n\ts_barrier" ::: "memory");
    WO_RUN(12 + l)
    const u32 tagw = lds_ld32(CTL_OFF);
    const char* bxf = ws + WS_BXF + mt * 1024;
    BAR
    const u32 town = (u32)__builtin_amdgcn_s_memrealtime();
    if (l == 0) {
      if (lane == 0) GST2((u32)(hg * 128), bxf, 0, ((u32x2){town, tagw}));
    }
    WO_RUN(18 + l)
    WO_RUN(8 + l)
    WO_RUN(l)
    const u32 vfl = (((lane & 7) == hg ? hg ^ 1 : (lane & 7))) * 128;
    u32x2 f;
    asm volatile("" : "=v"(f));
    BAR
    int gate = (int)((u32)__builtin_amdgcn_s_memrealtime() - town) + GADD;
    gate = gate > 200 ? 200 : gate;
    WO_RUN(2 + l)
    WO_RUN(4 + l)
    WO_RUN(6 + l)
    if (l == 1) asm volatile("global_load_dwordx2 %0, %1, %2 sc1" : "+v"(f) : "v"(vfl), "s"(bxf) : "memory");
    __builtin_amdgcn_s_setprio(0);
    pm = (4 * g + q4) * 160 + p4 * 40;
    pf = (20 * g + 16 + q4) * 160 + p4 * 40;
    asm volatile("" : "=v"(bias[0]), "=v"(bias[1]), "=v"(sel[0]), "=v"(sel[1]), "=v"(sel[2]), "=v"(sel[3]), "=v"(e4), "=v"(eh),
                 "=v"(aq), "=v"(aka), "=v"(akb), "+v"(pm), "+v"(pf), "=v"(hi), "=v"(arec), "=v"(vpp));
    PREP_S
    if (l == 1) {
      asm volatile("s_waitcnt vmcnt(0)" : "+v"(f)::"memory");
#pragma nounroll
      for (int it = 0; it < SPIN_CAP; ++it) {
        if (__all(f[1] == tagw)) break;
        asm volatile("global_load_dwordx2 %0, %1, %2 sc1\n\ts_waitcnt vmcnt(0)" : "=&v"(f) : "v"(vfl), "s"(bxf) : "memory");
      }
#pragma nounroll
      for (int it = 0; it < 64; ++it) {
        if (__all((int)((u32)__builtin_amdgcn_s_memrealtime() - f[0]) >= gate)) break;
      }
    }
    __builtin_amdgcn_s_setprio(0);
  } else if (w < 4) {
    __builtin_amdgcn_s_setprio(3);
    if (w == 2) {
      u32 vx[12];
#pragma unroll
      for (int i = 0; i < 12; ++i) vx[i] = x_off(i, lane, r0) + (4 - (i % 6)) * 1024;
      int kb = kb0 == 9 ? 0 : kb0 + 1;
      u32 xo = XRING + XSLOT + 4096;
#define ISSUE_X0                                                                                   \
  {                                                                                                \
    const char* xb = (const char*)x + kb * 128;                                                    \
    dma_m0<-4096>(vx[0], xb, xo);                                                                  \
    dma_nx<-3072>(vx[1], xb); dma_nx<-2048>(vx[2], xb); dma_nx<-1024>(vx[3], xb); dma_nx<0>(vx[4], xb); dma_nx<1024>(vx[5], xb); \
  }
#define ISSUE_X1                                                                                   \
  {                                                                                                \
    const char* xb = (const char*)x + kb * 128;                                                    \
    dma_m0<-4096>(vx[6], xb, xo + 6144);                                                           \
    dma_nx<-3072>(vx[7], xb); dma_nx<-2048>(vx[8], xb); dma_nx<-1024>(vx[9], xb); dma_nx<0>(vx[10], xb); dma_nx<1024>(vx[11], xb); \
    kb = kb == 9 ? 0 : kb + 1;                                                                     \
    xo = xo >= XRING + 3 * XSLOT ? xo - 3 * XSLOT : xo + XSLOT;                                    \
  }
      asm volatile("" : "+v"(vx[0]), "+v"(vx[1]), "+v"(vx[2]), "+v"(vx[3]), "+v"(vx[4]), "+v"(vx[5]));
      BAR
      __builtin_amdgcn_s_sleep(4);
      ISSUE_X0
      __builtin_amdgcn_sched_barrier(0);
      WO_OFFS
      asm volatile("" : "+v"(woff[0]), "+v"(woff[1]), "+v"(woff[2]), "+v"(woff[3]), "+v"(woff[4]));
      BAR
      ISSUE_X1
      ISSUE_X0
      BAR
      ISSUE_X1
      asm volatile("s_waitcnt vmcnt(12)\n\ts_barrier" ::: "memory");
#pragma unroll 1
      for (int j = 3; j < 10; ++j) {
        ISSUE_X0
        BAR
        ISSUE_X1
        asm volatile("s_waitcnt vmcnt(12)\n\ts_barrier" ::: "memory");
      }
      const char* xq = (const char*)x + (size_t)r0 * 1280;
      const int nv = mt == 25 ? 480 : 800;
#define RES1(I) if ((I) < 7 || (I) * 64 + lane < nv) dma_m0<(((I) % 5) - 4) * 1024>(woff[(I) % 5], xq + ((I) / 5) * 40960, HIM + ((I) / 5) * 5120 + 4096);
      PREP_V
      BAR
      PREP_S
      asm volatile("s_waitcnt vmcnt(0)\n\ts_barrier" ::: "memory");
      RES1(0) RES1(1) RES1(2) RES1(3) RES1(4) RES1(5) RES1(6)
      BAR
      RES1(7) RES1(8) RES1(9) RES1(10) RES1(11) RES1(12)
      if (lane < 10) dma_m0<0>(l16, (const char*)w_r + hg * 160, WR_OFF);
      BAR
    } else {
      u32x2 ep;
      u32x4 ra;
      u32 rb5;
      BAR
      if (lane < 8) {
        *(u32x4*)(smem + ONES_OFF + lane * 16) = (u32x4){0x3F803F80u, 0x3F803F80u, 0x3F803F80u, 0x3F803F80u};
        *(u32x4*)(smem + CTL_OFF + lane * 16) = (u32x4){0u, 0u, 0u, 0u};
      }
      {
        const u32 o1 = lane == 26 || lane == 32 ? 0x3F803F80u : 0u;
        const u32 o2 = lane == 31 ? 0x3F803F80u : o1;
        if (lane < 33) *(u32x4*)(smem + REC_OFF + lane * 16) = (u32x4){o1, o1, o2, o2};
      }
      WO_OFFS
      asm volatile("" : "+v"(woff[0]), "+v"(woff[1]), "+v"(woff[2]), "+v"(woff[3]), "+v"(woff[4]));
      __builtin_amdgcn_s_sleep(TWS);
      if (lane < 5)
        touch_line(lane == 4 ? (const char*)wo + hg * 160
                             : (lane >= 2 ? ws + WS_PK - 65536 + (lane - 2) * 524288 + bid * 128
                                          : (const char*)out + (size_t)(r0 + lane * 47) * 1280 + hg * 160));
      LDS_BARRIER();
      BAR BAR
      asm volatile("global_load_dwordx2 %0, %1, off sc1" : "=v"(ep) : "v"(ws + WS_EPOCH) : "memory");
      asm volatile("global_load_dwordx4 %0, %2, %4\n\tglobal_load_dword %1, %3, %4 offset:1024"
                   : "=&v"(ra), "=&v"(rb5) : "v"(l16), "v"((u32)(lane * 4)), "s"(w_r) : "memory");
      BAR BAR BAR
      PREP_V
      BAR
      PREP_S
      BAR
      asm volatile("s_waitcnt vmcnt(0)" : "+v"(ep), "+v"(ra), "+v"(rb5)::"memory");
      const u64 e1 = (((u64)ep[1] << 32) | ep[0]) + (ep[0] == SENT - 1u ? 2ull : 1ull);
      float swr = lo_f(rb5) + hi_f(rb5);
#pragma unroll
      for (int e = 0; e < 4; ++e) swr += lo_f(ra[e]) + hi_f(ra[e]);
      swr = wave_sum(swr);
      if (lane == 0) {
        *(u32x2*)(smem + CTL_OFF) = (u32x2){(u32)e1, (u32)(e1 >> 32)};
        *(float*)(smem + CTL_OFF + 40) = swr;
      }
      int ra0 = r0 - 16 + lane, ra1 = r0 + 48 + (lane & 31);
      ra0 = ra0 < 0 ? 0 : (ra0 > SEQ - 1 ? SEQ - 1 : ra0);
      ra1 = ra1 > SEQ - 1 ? SEQ - 1 : ra1;
      const int hd = lane / 5, pt = lane - hd * 5;
      u32 avp = REC_OFF + (hd >> 1) * 192 + (pt < 4 ? pt * 48 + (hd & 1) * 8 : hd * 48 + 16), vvp = lane < 20 ? (lane >> 1) * 128 + (lane & 1) * 16 : 0;
      u32 vf = (lane < 48 ? lane : 0) * 128, va0 = ra0 * 8, va1 = ra1 * 8, vt0 = TAB_OFF + lane * 4, vt1 = TAB_OFF + 256 + (lane & 31) * 4;
      asm volatile("" : "+v"(avp), "+v"(vvp), "+v"(vf), "+v"(va0), "+v"(va1), "+v"(vt0), "+v"(vt1));
#pragma nounroll
      for (int h = 7; h <= HK0; ++h) BAR
      u32x2 f, t0, t1;
      u32x4 vp4;
      GLD2(f, vf, ws + WS_HFLAG, 0);
      GLD2(t0, va0, ws + WS_LN1, 0);
      GLD2(t1, va1, ws + WS_LN1, 0);
      asm volatile("global_load_dwordx4 %0, %1, %2 sc1" : "=v"(vp4) : "v"(vvp), "s"(ws + WS_VP + hg * 1280) : "memory");
#pragma unroll
      for (int h = HK0 + 1; h <= 17; ++h) BAR
      WO_RUN(10)
      WO_RUN(11)
      BAR
      asm volatile("s_waitcnt vmcnt(10)" : "+v"(t0), "+v"(t1), "+v"(f), "+v"(vp4)::"memory");
#pragma nounroll
      for (int it = 0; it < SPIN_CAP; ++it) {
        if (__all(t0[1] == (u32)e1 && t1[1] == (u32)e1 && f[0] == (u32)e1 && f[1] == (u32)(e1 >> 32) && vp4[1] == (u32)e1 && vp4[3] == (u32)e1)) break;
        asm volatile("global_load_dwordx2 %2, %6, %9 sc1\n\tglobal_load_dwordx2 %0, %4, %8 sc1\n\tglobal_load_dwordx2 %1, %5, %8 sc1\n\t"
                     "global_load_dwordx4 %3, %7, %10 sc1\n\ts_waitcnt vmcnt(0)"
                     : "=&v"(t0), "=&v"(t1), "=&v"(f), "=&v"(vp4)
                     : "v"(va0), "v"(va1), "v"(vf), "v"(vvp), "s"(ws + WS_LN1), "s"(ws + WS_HFLAG), "s"(ws + WS_VP + hg * 1280) : "memory");
      }
      *(u32*)(smem + vt0) = t0[0] << 16;
      *(u32*)(smem + vt1) = t1[0] << 16;
      *(float*)(smem + vt0 + 384) = __builtin_amdgcn_rcpf(hi_f(t0[0]));
      *(float*)(smem + vt1 + 384) = __builtin_amdgcn_rcpf(hi_f(t1[0]));
      if (lane < 20) *(u32x2*)(smem + avp) = (u32x2){vp4[0], vp4[2]};
      asm volatile("" ::: "memory");
      WO_RUN(14)
      WO_RUN(15)
      LDS_BARRIER();
      WO_RUN(16)
      WO_RUN(17)
    }
    __builtin_amdgcn_s_setprio(0);
  } else {
    const int cw = w - 4;
    prepv(lane);
    preps(mt, hg);
    if (cw >= 4) __builtin_amdgcn_s_setprio(1);
    const int xa0 = XRING + r16 * 128 + ((g ^ ((r16 >> 1) & 7)) * 16);
    const u32 m1 = r16 == 0 ? 0u : 1u;
    const u32 xz0 = r16 == 0 ? (u32)ONES_OFF : (u32)xa0;
    int wa[2];
#pragma unroll
    for (int c = 0; c < 2; ++c) {
      const int ct = cw < 7 ? 2 * cw + c : 14;
      const int seg = ct / 5, tt = ct - seg * 5;
      const int G = tt < 4 ? 5 * tt + p4 : 5 * p4 + 4;
      wa[c] = (16 * (g >> 1) + 4 * (g & 1) + q4) * 512 + ((seg * 160 + G * 8) ^ ((4 * (g & 1) + q4) << 5));
    }
    auto run = [&](auto tyc) __attribute__((always_inline)) {
      constexpr int TY = decltype(tyc)::value;
      constexpr int NW = TY == 2 ? 1 : 2;
      f32x4 acc[2][6];
#pragma unroll
      for (int c = 0; c < 2; ++c)
#pragma unroll
        for (int r = 0; r < 6; ++r) acc[c][r] = (f32x4){0.f, 0.f, 0.f, 0.f};
      u32x4 xA[6], xB[6];
      u32x2 wA[4], wB[4];
#define RX(D, A, O) asm volatile("ds_read_b128 %0, %1 offset:%2" : "=v"(D) : "v"(A), "n"(O) : "memory");
#define RW(D, A, O) asm volatile("ds_read_b64_tr_b16 %0, %1 offset:%2" : "=v"(D) : "v"(A), "n"(O) : "memory");
#define MM(C, R, XF, WF)                                                                           \
  if ((C) < NW) {                                                                                    \
    const bf16x8 wf_ = mk8(WF[2 * (C)][0], WF[2 * (C)][1], WF[2 * (C) + 1][0], WF[2 * (C) + 1][1]); \
    const bf16x8 xf_ = __builtin_bit_cast(bf16x8, XF[R]);                                          \
    acc[C][R] = TY == 0 ? MFMA(wf_, xf_, acc[C][R]) : MFMA(xf_, wf_, acc[C][R]);                   \
    __builtin_amdgcn_sched_barrier(0);                                                             \
  }
#define READS(QX, QW)                                                                              \
  RW(QW[0], aw0, 0) RW(QW[1], aw0, 4096) RX(QX[1], ax, 2048)                                       \
  if (NW == 2) { RW(QW[2], aw1, 0) RW(QW[3], aw1, 4096) }                                          \
  RX(QX[2], ax, 4096) RX(QX[3], ax, 6144) RX(QX[4], ax, 8192) RX(QX[5], ax, 10240) RX(QX[0], az, 0)
#define ARRIVED(PX, PW)                                                                            \
  if (NW == 2)                                                                                     \
    asm volatile("s_waitcnt lgkmcnt(0)" : "+v"(PX[0]), "+v"(PX[1]), "+v"(PX[2]), "+v"(PX[3]), "+v"(PX[4]), "+v"(PX[5]), \
                 "+v"(PW[0]), "+v"(PW[1]), "+v"(PW[2]), "+v"(PW[3])::"memory");                    \
  else                                                                                             \
    asm volatile("s_waitcnt lgkmcnt(0)" : "+v"(PX[0]), "+v"(PX[1]), "+v"(PX[2]), "+v"(PX[3]), "+v"(PX[4]), "+v"(PX[5]), \
                 "+v"(PW[0]), "+v"(PW[1])::"memory");                                              \
  __builtin_amdgcn_sched_barrier(0);
#define SB __builtin_amdgcn_sched_barrier(0);
#define WT(N, ...) asm volatile("s_waitcnt lgkmcnt(" #N ")" : __VA_ARGS__::"memory"); SB
#define HSTAGE2(PX, PW, QX, QW, NX)                                                                \
  BAR                                                                                              \
  WT(7, "+v"(PW[0]), "+v"(PW[1]), "+v"(PX[1]))                                                     \
  MM(0, 1, PX, PW) RW(QW[0], aw0, 0)                                                               \
  WT(6, "+v"(PW[2]), "+v"(PW[3]))                                                                  \
  MM(1, 1, PX, PW) RW(QW[1], aw0, 4096)                                                            \
  WT(6, "+v"(PX[2]))                                                                               \
  MM(0, 2, PX, PW) RX(QX[1], ax, 2048) SB                                                          \
  MM(1, 2, PX, PW) RW(QW[2], aw1, 0)                                                               \
  WT(7, "+v"(PX[3]))                                                                               \
  MM(0, 3, PX, PW) RW(QW[3], aw1, 4096) SB                                                         \
  MM(1, 3, PX, PW) RX(QX[2], ax, 4096)                                                             \
  WT(6, "+v"(PX[4]), "+v"(PX[5]), "+v"(PX[0]))                                                     \
  MM(0, 4, PX, PW) RX(QX[3], ax, 6144) SB                                                          \
  MM(1, 4, PX, PW) RX(QX[4], ax, 8192) SB                                                          \
  MM(0, 5, PX, PW) RX(QX[5], ax, 10240) SB                                                         \
  MM(1, 5, PX, PW) RX(QX[0], az, 0) SB                                                             \
  MM(0, 0, PX, PW)                                                                                 \
  NX asm volatile("" : "+v"(aw0), "+v"(aw1), "+v"(ax), "+v"(az), "+s"(so), "+s"(xo)); SB           \
  MM(1, 0, PX, PW)
#define HSTAGE1(PX, PW, QX, QW, NX)                                                                \
  BAR                                                                                              \
  ARRIVED(PX, PW)                                                                                  \
  MM(0, 1, PX, PW) RW(QW[0], aw0, 0) RW(QW[1], aw0, 4096) SB                                       \
  MM(0, 2, PX, PW) RX(QX[1], ax, 2048) RX(QX[2], ax, 4096) SB                                      \
  MM(0, 3, PX, PW) RX(QX[3], ax, 6144) RX(QX[4], ax, 8192) SB                                      \
  MM(0, 4, PX, PW) RX(QX[5], ax, 10240) RX(QX[0], az, 0) SB                                        \
  MM(0, 5, PX, PW)                                                                                 \
  NX asm volatile("" : "+v"(aw0), "+v"(ax), "+v"(az), "+s"(so), "+s"(xo)); SB                      \
  MM(0, 0, PX, PW)
#define HSTAGE(...) if (NW == 2) { HSTAGE2(__VA_ARGS__) } else { HSTAGE1(__VA_ARGS__) }
      u32 aw0 = wa[0], aw1 = wa[1], ax = xa0, az = xz0;
      asm volatile("s_waitcnt vmcnt(0)\n\ts_barrier" ::: "memory");
      READS(xA, wA)
      aw0 += HSLOT; aw1 += HSLOT; ax ^= 64; az ^= 64;
      int so = 2 * HSLOT, xo = XSLOT;
#pragma unroll 1
      for (int s = 0; s < 9; ++s) {
        HSTAGE(xA, wA, xB, wB, aw0 = wa[0] + so; aw1 = wa[1] + so; ax = xa0 + xo; az = xz0 + xo * m1;
               so = so >= 4 * HSLOT ? 0 : so + 2 * HSLOT; xo = xo >= 3 * XSLOT ? 0 : xo + XSLOT;)
        HSTAGE(xB, wB, xA, wA, aw0 += HSLOT; aw1 += HSLOT; ax ^= 64; az ^= 64;)
      }
      {
        HSTAGE(xA, wA, xB, wB, )
        ARRIVED(xB, wB)
        BAR
        SB
      }
      char* ex = smem + EXO + cw * 6144 + lane * 8;
      if (TY == 0) {
        float st[6];
        const u32 ta = TAB_OFF + r16 * 4;
#define RT(R) asm volatile("ds_read_b32 %0, %1 offset:%2" : "=v"(st[R]) : "v"(ta), "n"(64 * (R)) : "memory");
        RT(1) RT(2) RT(3) RT(4) RT(5) RT(0)
        float cs[2][4];
#define FMS(A, M, CS) ((A) - (M) * (CS))
#define EP0(C, R)                                                                                  \
  {                                                                                                \
    const u32 a = cvt2((f32x2){FMS(acc[C][R][0], st[R], cs[C][0]), FMS(acc[C][R][1], st[R], cs[C][1])}); \
    const u32 b = cvt2((f32x2){FMS(acc[C][R][2], st[R], cs[C][2]), FMS(acc[C][R][3], st[R], cs[C][3])}); \
    *(u32x2*)(ex + (C) * 3072 + (R) * 512) = (u32x2){a, b};                                        \
    SB                                                                                             \
  }
        MM(0, 0, xB, wB) MM(1, 0, xB, wB) MM(0, 1, xB, wB) MM(1, 1, xB, wB)
#pragma unroll
        for (int i = 0; i < 4; ++i) cs[0][i] = dppf<0x150>(acc[0][0][i]);
        SB
        MM(0, 2, xB, wB)
#pragma unroll
        for (int i = 0; i < 4; ++i) cs[1][i] = dppf<0x150>(acc[1][0][i]);
        WT(0, "+v"(st[0]), "+v"(st[1]), "+v"(st[2]), "+v"(st[3]), "+v"(st[4]), "+v"(st[5]))
        MM(1, 2, xB, wB) EP0(0, 1)
        MM(0, 3, xB, wB) EP0(1, 1)
        MM(1, 3, xB, wB) EP0(0, 2)
        MM(0, 4, xB, wB) EP0(1, 2)
        MM(1, 4, xB, wB) EP0(0, 3)
        MM(0, 5, xB, wB) EP0(1, 3)
        MM(1, 5, xB, wB)
        if (cw >= 2) {
          EP0(1, 0)
          if (cw >= 3) EP0(0, 0)
        }
        EP0(0, 4) EP0(1, 4) EP0(0, 5) EP0(1, 5)
      } else {
        f32x4 mbv[6];
        const u32 ta = TAB_OFF + g * 16;
#define RV(R) RX(mbv[R], ta, 64 * (R))
        RV(1) RV(2) RV(3) RV(4) RV(5) RV(0)
        float cs[2];
#define CS1(C)                                                                                     \
  if ((C) < NW) {                                                                                  \
    const u32 v0 = __float_as_uint(acc[C][0][0]);                                                  \
    const u32 v1 = __builtin_amdgcn_permlane16_swap(v0, v0, false, false)[0];                      \
    cs[C] = __uint_as_float(__builtin_amdgcn_permlane32_swap(v1, v1, false, false)[0]);            \
    SB                                                                                             \
  }
#define EP1(C, R)                                                                                  \
  if ((C) < NW) {                                                                                  \
    const f32x4 v = (f32x4){FMS(acc[C][R][0], mbv[R][0], cs[C]), FMS(acc[C][R][1], mbv[R][1], cs[C]), FMS(acc[C][R][2], mbv[R][2], cs[C]), FMS(acc[C][R][3], mbv[R][3], cs[C])}; \
    *(u32x2*)(ex + (C) * 3072 + (R) * 512) = (u32x2){cvt2((f32x2){v[0], v[1]}), cvt2((f32x2){v[2], v[3]})}; \
    SB                                                                                             \
  }
        MM(0, 0, xB, wB) MM(1, 0, xB, wB) MM(0, 1, xB, wB) MM(1, 1, xB, wB)
        CS1(0)
        MM(0, 2, xB, wB)
        CS1(1)
        WT(0, "+v"(mbv[0]), "+v"(mbv[1]), "+v"(mbv[2]), "+v"(mbv[3]), "+v"(mbv[4]), "+v"(mbv[5]))
        MM(1, 2, xB, wB) EP1(0, 1)
        MM(0, 3, xB, wB) EP1(1, 1)
        MM(1, 3, xB, wB) EP1(0, 2)
        MM(0, 4, xB, wB) EP1(1, 2)
        MM(1, 4, xB, wB) EP1(0, 3)
        MM(0, 5, xB, wB) EP1(1, 3)
        MM(1, 5, xB, wB)
        EP1(0, 0) EP1(1, 0) EP1(0, 4) EP1(1, 4) EP1(0, 5) EP1(1, 5)
      }
    };
    if (cw < 5) run(icst<0>{});
    else if (cw < 7) run(icst<1>{});
    else run(icst<2>{});
  }
  __builtin_amdgcn_s_setprio(0);
  const bool live = r0 + rt * 16 < SEQ;
  if (!live) {
    if (w >= 2) LDS_BARRIER();
    if (w != 7) {
      asm volatile("s_waitcnt vmcnt(0)" ::: "memory");
      return;
    }
  } else if (w >= 2)
    asm volatile(ATT_ASM
                 :: [kh] "s"(kh), [l16] "v"(l16), [os] "s"(os), [os4] "s"(os4), [b0] "v"(bias[0]), [b1] "v"(bias[1]),
                    [s0] "v"(sel[0]), [s1] "v"(sel[1]), [s2] "v"(sel[2]), [s3] "v"(sel[3]), [e4] "v"(e4), [eh] "v"(eh), [aq] "v"(aq),
                    [aka] "v"(aka), [akb] "v"(akb), [arec] "v"(arec), [vpp] "v"(vpp), [sst] "s"(sst), [w] "s"(w)
                 : "memory", "s41", "s46", "s47", "s48", "s49", "s50", "s51", "scc", "v24", "v25", "v26", "v27", "v28", "v29", "v30", "v31", "v32", "v33", "v34", "v35", "v36", "v37", "v38", "v39", "v40", "v41", "v42", "v43", "v44", "v45", "v46", "v47", "v48", "v49", "v50", "v51", "v52", "v53", "v54", "v55", "v56", "v57", "v58", "v59", "v60", "v61", "v62", "v63", "v64", "v65", "v66", "v67", "v68", "v69", "v70", "v71", "v72", "v73", "v74", "v75", "v76", "v77", "v78", "v79", "v80", "v81", "v82", "v83", "v84", "v85", "v86", "v87");
  u32x4 hwa, hwb;
  u32x2 hwc;
  const int grp = rt < 3 ? 0 : 1;
  if (w < 8) {
    const int rmax = mt == 25 ? 2 : 4;
    const int rk = rt < rmax ? rt : rmax, rd = (rt ^ 1) < rmax ? (rt ^ 1) : rmax;
    u32 ta, tk;
    if (w == 0) asm volatile("s_waitcnt vmcnt(0)" ::: "memory");
    if (w == 2 || w == 3) asm volatile("s_waitcnt vmcnt(3)" ::: "memory");
    asm volatile("s_waitcnt lgkmcnt(0)\n\ts_barrier\n\t" OP_ASM
                 : "=&{v[84:87]}"(hwa), "=&{v[88:91]}"(hwb), "=&{v[92:93]}"(hwc), [ta] "=&v"(ta), [tk] "=&v"(tk)
                 : [w] "s"(w), [l16] "v"(l16), [oa0] "s"(oa[0]), [so0] "s"(so_[0]), [oa1] "s"(oa[1]), [so1] "s"(so_[1]), [oa2] "s"(oa[2]), [so2] "s"(so_[2]), [oa3] "s"(oa[3]), [so3] "s"(so_[3]), [oa4] "s"(oa[4]), [so4] "s"(so_[4]), [oa5] "s"(oa[5]), [so5] "s"(so_[5]), [oa6] "s"(oa[6]), [so6] "s"(so_[6]), [oa7] "s"(oa[7]), [so7] "s"(so_[7]), [oa8] "s"(oa[8]), [so8] "s"(so_[8]), [oa9] "s"(oa[9]), [so9] "s"(so_[9]),
                   [pm] "v"(pm), [pf] "v"(pf), [ra] "v"(w < 3 ? (u32)(CTL_OFF + 72) : hi), [e4] "v"(e4),
                   [aw] "v"((lane & 1) ? (u32)(WR_OFF + g * 40) : (u32)ONES_OFF),
                   [vk] "v"(l16 + rk * 20480), [vd] "v"(l16 + rd * 20480), [sk] "s"((w & 1) * 6144),
                   [sd] "s"(EXO + (w < 4 ? 4 + w : rt ^ 1) * 5120), [sr] "s"(EXO + rt * 5120),
                   [sc] "s"(CTL_OFF + 48 + (w < 4 ? 4 : rt ^ 1) * 4), [sp] "s"(PST_OFF + rt * 256), [sa] "s"(CTL_OFF + 8 + grp * 4),
                   [sst] "s"(sst), [cp] "s"(ws + WS_PC + (2 * mt + grp) * 1280 + hg * 160), [oback] "n"(OBACK), [cap] "n"(SPIN_CAP)
                 : "memory", "vcc", "scc", "s43", "v24", "v25", "v26", "v27", "v28", "v29", "v30", "v31", "v32", "v33", "v34", "v35", "v36", "v37", "v38", "v39", "v40", "v41", "v42", "v43", "v44", "v45", "v46", "v47", "v48", "v49", "v50", "v51", "v52", "v53", "v54", "v55", "v56", "v57", "v58", "v59", "v60", "v61", "v62", "v63", "v64", "v65", "v66", "v67", "v68", "v69", "v70", "v71", "v72", "v73", "v74", "v75", "v76", "v77", "v78", "v79", "v80", "v81", "v82", "v83", "v94", "v95", "v96", "v97", "v98", "v99", "v100", "v101", "v102", "v103", "v104", "v105", "v106", "v107", "v108", "v109", "v110", "v111", "v112", "v113", "v114", "v115", "v116", "v117", "v118", "v119", "v120", "v121", "v122", "v123", "v124", "v125", "v126", "v127", "v128", "v129", "v130", "v131", "v132", "v133", "v134", "v135", "v136", "v137", "v138", "v139", "v140", "v141", "v142", "v143", "v144", "v145", "v146", "v147", "v148", "v149", "v150", "v151", "v152", "v153", "v154", "v155", "v156", "v157", "v158", "v159");
    if (!live || w < 3) return;
  } else {
    u64 tb;
    asm volatile("s_setprio 3\n\ts_waitcnt lgkmcnt(0)\n\ts_barrier\n\ts_memrealtime %0\n\ts_waitcnt lgkmcnt(0)" : "=s"(tb)::"memory");
    if (w == 8 || w == 11) {
      const int cg = w == 11, gid = 2 * mt + cg;
      const char* sp = sst - hg * 1024;
      const char* sp2 = sp + (cg ? 0 : 7936);
      const u32 vq = (lane >> 3) * 1024 + (lane & 7) * 16 + (cg ? 640 : 768);
      u32x4 pq[3];
      u64 t2;
#define PLOADS "global_load_dwordx4 %0, %4, %5 sc1\n\tglobal_load_dwordx4 %1, %4, %5 offset:128 sc1\n\tglobal_load_dwordx4 %2, %4, %6 sc1\n\t"
      asm volatile("1:\n\ts_memrealtime %3\n\ts_waitcnt lgkmcnt(0)\n\ts_sub_u32 s40, %7, s40\n\ts_cmp_gt_i32 s40, 0\n\ts_cbranch_scc1 1b\n\t" PLOADS "s_setprio 1"
                   : "=&v"(pq[0]), "=&v"(pq[1]), "=&v"(pq[2]), "=&{s[40:41]}"(t2) : "v"(vq), "s"(sp), "s"(sp2), "s"((u32)tb + PDT) : "memory", "scc");
      int lane2 = lane;
      asm volatile("" : "+v"(lane2));
      const int n16 = lane2 & 15;
      u32x4 sl;
      u32x2 km;
#pragma unroll
      for (int e = 0; e < 4; ++e) sl[e] = n16 == 2 * e ? 0x3F80u : (n16 == 2 * e + 1 ? 0x3F800000u : 0u);
      u32 rid = (u32)((((lane2 >> 4) & 1) * 2 + (lane2 >> 5)) * 4 + (lane2 & 3)) << 10;
      u32 rid1 = rid + (16 << 10), rid2 = rid + (cg ? 0 : 32 << 10), msk = n16 < 4 ? 0xFFFF0000u : 0u;
      u32 ah = HIM + cg * 7680 + (lane2 < 10 ? lane2 : 9) * 16;
      km[1] = *(const u32*)(smem + CTL_OFF);
      const char* kp = ws + WS_PK + gid * 6144 + hg * 768;
      const char* cp = ws + WS_PC + gid * 1280 + hg * 160;
      asm volatile("" : "+s"(kp), "+s"(cp));
      km[0] = ((u32)tb >> 1) & 1023u;
      asm volatile("s_setprio " "3" "\n\ts_waitcnt vmcnt(0)" : "+v"(pq[0]), "+v"(pq[1]), "+v"(pq[2]), "+v"(sl), "+v"(rid), "+v"(rid1), "+v"(rid2), "+v"(msk), "+v"(ah), "+v"(km)::"memory");
#define PMISS __any(max(max(max(pq[0][0], pq[0][2]), max(pq[1][0], pq[1][2])), max(pq[2][0], pq[2][2])) == SENT)
      if (__builtin_expect(PMISS, 0)) {
#pragma nounroll
        for (int it = 0; it < SPIN_CAP; ++it) {
          asm volatile(PLOADS "s_waitcnt vmcnt(0)" : "=&v"(pq[0]), "=&v"(pq[1]), "=&v"(pq[2]), "=&s"(t2) : "v"(vq), "s"(sp), "s"(sp2) : "memory");
          if (!PMISS) break;
          __builtin_amdgcn_s_sleep(2);
        }
      }
      u32 pkey = 0;
#pragma unroll
      for (int t = 0; t < 3; ++t) {
        const f32x4 ys = MFMA(__builtin_bit_cast(bf16x8, pq[t]), __builtin_bit_cast(bf16x8, sl), ((f32x4){0.f, 0.f, 0.f, 0.f}));
        const float y = tr_sum(ys);
        pkey = max(pkey, (mono(y * __builtin_amdgcn_rcpf(dppf<0x104>(y))) & msk) | (t == 0 ? rid : (t == 1 ? rid1 : rid2)));
      }
      pkey = max(pkey, dppu<0xB1>(pkey));
      pkey = max(pkey, dppu<0x4E>(pkey));
      pkey = max(max((u32)__builtin_amdgcn_readlane(pkey, 0), (u32)__builtin_amdgcn_readlane(pkey, 16)),
                 max((u32)__builtin_amdgcn_readlane(pkey, 32), (u32)__builtin_amdgcn_readlane(pkey, 48)));
      km[0] |= pkey;
      asm volatile("s_mov_b32 exec_lo, 0x01010101\n\ts_mov_b32 exec_hi, 0x0101\n\tglobal_store_dwordx2 %0, %1, %2 sc1\n\ts_mov_b64 exec, -1\n\ts_setprio 2" ::"v"(l16), "v"(km), "s"(kp) : "memory");
      ah += ((pkey >> 10) & 63u) * 160;
      if (lane == 0) *(u32*)(smem + CTL_OFF + 24 + cg * 4) = ah;
      __builtin_amdgcn_s_setprio(0);
#pragma nounroll
      for (int it = 0; it < 32; ++it) {
        if ((int)((u32)__builtin_amdgcn_s_memrealtime() - (u32)tb) >= PSL) break;
        __builtin_amdgcn_s_sleep(6);
      }
      __builtin_amdgcn_s_setprio(2);
      asm volatile("s_mov_b32 s40, %[cap]\n"
                   "1:\n\tds_read_b32 v100, %[ac]\n\tds_read_b32 v101, %[ac] offset:24\n\tds_read_b128 v[96:99], %[ah]\n\ts_sub_u32 s40, s40, 1\n\ts_cbranch_scc1 2f\n\ts_waitcnt lgkmcnt(0)\n\t"
                   "v_cmp_gt_u32 vcc, %[nf], v100\n\ts_cbranch_vccz 2f\n\ts_sleep 2\n\ts_branch 1b\n"
                   "2:\n\ts_waitcnt lgkmcnt(0)\n\tv_cmp_eq_u32 vcc, 0, v101\n\ts_cbranch_vccz 3f\n\t"
                   "s_mov_b64 exec, 0x3ff\n\tglobal_store_dwordx4 %[l16], v[96:99], %[cp] sc1\n\ts_mov_b64 exec, -1\n3:"
                   ::[cap] "n"(SPIN_CAP * 8), [ac] "v"((u32)(CTL_OFF + 8 + cg * 4)), [ah] "v"(ah), [nf] "s"(3 - cg), [l16] "v"(l16), [cp] "s"(cp)
                   : "memory", "vcc", "scc", "s40", "v96", "v97", "v98", "v99", "v100", "v101");
      return;
    }
    __builtin_amdgcn_s_setprio(0);
    if (w == 10 && mt == 25) return;
    const int cg = w - 9, gid = 2 * mt + cg;
    const int nr = cg ? 31 : 47;
    const int lr = lane < nr ? lane : nr;
    const int lrow = cg * 48 + lr;
    const int li = (lane & 31) < 20 ? (lane & 31) : 19;
    asm volatile(COL_ASM
                 :
                 : [lq] "v"((u32)(lr * 16 + hg * 1024)), [ac] "v"((u32)(CTL_OFF + 8 + cg * 4)), [nf] "s"(3 - cg),
                   [sp] "s"(ws + WS_STAT + (size_t)gid * 8192), [ao] "v"((u32)(PST_OFF + lrow * 16)), [rdm] "s"(1.0f / DM),
                   [swr] "v"(*(const float*)(smem + CTL_OFF + 40)), [msk] "s"(0xFFFFF000u), [rid] "v"(0x800u | (u32)(2047 - (r0 + lrow))),
                   [at] "v"((u32)(TAB_OFF + lrow * 8)), [zr] "v"(l16), "{v99}"(*(const u32*)(smem + CTL_OFF)),
                   [kp] "s"(ws + WS_PK + gid * 6144 + hg * 768 + 8), [ak] "v"((u32)(CTL_OFF + 16 + cg * 4)), [rb] "s"(2047 - r0 - cg * 48),
                   [ah] "v"((u32)(HIM + cg * 7680 + li * 8)), [co] "v"((u32)((lane < 32 ? 1280 : 0) + li * 8)),
                   [cp] "s"(ws + WS_CAND + gid * 2560 + hg * 160), [pdel] "n"(PDEL2), [cap] "n"(SPIN_CAP)
                 : "memory", "vcc", "scc", "s40", "s41", "s42", "s43", "s44", "s45", "s46", "s47", "v64", "v65", "v66", "v67", "v68", "v69", "v70", "v71", "v72", "v73", "v74", "v75", "v76", "v77", "v78", "v79", "v80", "v81", "v82", "v83", "v84", "v85", "v86", "v87", "v88", "v89", "v90", "v91", "v92", "v93", "v94", "v95", "v96", "v97", "v98", "v100", "v101", "v102", "v103", "v104", "v105", "v106", "v107", "v108", "v109", "v110", "v111", "v112", "v113", "v114", "v115", "v116", "v117", "v118", "v119", "v120", "v121", "v122", "v123");
    return;
  }
  const u32 hw[10] = {hwa[0], hwa[1], hwa[2], hwa[3], hwb[0], hwb[1], hwb[2], hwb[3], hwc[0], hwc[1]};
  f32x2 hf[10];
#pragma unroll
  for (int e = 0; e < 10; ++e) {
    hf[e] = unpk(hw[e]);
    asm volatile("" : "+v"(hf[e]));
  }
  const char* obase = (const char*)out + (size_t)r0 * 1280 + hg * 160;
  u32 tilekey;
  asm volatile("" : "+s"(obase));
  u32 aow = EXO + rt * 5120 + r16 * 160 + g * 40, aor = EXO + rt * 5120 + l16, vo3[3], rr3[3];
#pragma unroll
  for (int i = 0; i < 3; ++i) {
    const u32 c = i * 64 + lane, r = c / 10u;
    vo3[i] = (rt * 16 + r) * 1120 + rt * 2560 + c * 16;
    rr3[i] = c < 160u ? 2047 - (r0 + rt * 16 + r) : 4096u;
    asm volatile("" : "+v"(vo3[i]), "+v"(rr3[i]));
  }
  asm volatile("" : "+v"(aow), "+v"(aor));
  f32x2 st2;
  asm volatile("s_mov_b32 s40, %[cap]\n"
               "1:\n\ts_sleep 1\n\tds_read_b32 %[k], %[ak]\n\tds_read_b64 %[s], %[at]\n\ts_sub_u32 s40, s40, 1\n\ts_waitcnt lgkmcnt(0)\n\ts_cbranch_scc1 2f\n\t"
               "v_cmp_eq_u32 vcc, 0, %[k]\n\ts_cbranch_vccnz 1b\n"
               "2:"
               : [k] "=&v"(tilekey), [s] "=&v"(st2) : [cap] "n"(SPIN_CAP * 8), [ak] "v"((u32)(CTL_OFF + 16 + grp * 4)), [at] "v"((u32)(TAB_OFF + (rt * 16 + r16) * 8))
               : "memory", "vcc", "scc", "s40");
  u32 ok[10];
#pragma unroll
  for (int e = 0; e < 10; ++e) ok[e] = cvt2(__builtin_elementwise_fma(hf[e] - (f32x2){st2[0], st2[0]}, (f32x2){st2[1], st2[1]}, hf[e]));
  {
#pragma unroll
    for (int e = 0; e < 5; ++e) asm volatile("ds_write_b64 %0, %1 offset:%2" ::"v"(aow), "v"((u32x2){ok[2 * e], ok[2 * e + 1]}), "n"(e * 8) : "memory");
    u32x4 o3[3];
    asm volatile("ds_read_b128 %0, %3\n\tds_read_b128 %1, %3 offset:1024\n\tds_read_b128 %2, %3 offset:2048\n\ts_waitcnt lgkmcnt(0)"
                 : "=&v"(o3[0]), "=&v"(o3[1]), "=&v"(o3[2]) : "v"(aor) : "memory");
#pragma unroll
    for (int i = 0; i < 3; ++i)
      if (rr3[i] != (tilekey & 2047u) && rr3[i] != 4096u)
        asm volatile("global_store_dwordx4 %0, %1, %2" " sc1" "\n\ts_nop 1" ::"v"(vo3[i]), "v"(o3[i]), "s"(obase) : "memory");
  }
}

extern "C" void kernel_launch(void* const* d_in, const int* in_sizes, int n_in,
                              void* d_out, int out_size, void* d_ws, size_t ws_size,
                              hipStream_t stream) {
  const u16* x = (const u16*)d_in[0];
  const u16* wqkv = (const u16*)d_in[1];
  const u16* wo = (const u16*)d_in[2];
  const u16* w_r = (const u16*)d_in[4];
  const u16* w_gate = (const u16*)d_in[5];
  const u16* w_up = (const u16*)d_in[6];
  const u16* w_down = (const u16*)d_in[7];
  (void)hipFuncSetAttribute((const void*)fused_block, hipFuncAttributeMaxDynamicSharedMemorySize, LDS_TOTAL);
  fused_block<<<256, 768, LDS_TOTAL, stream>>>(x, wqkv, wo, w_r, w_gate, w_up, w_down, (u16*)d_out, (char*)d_ws);
}
```

```cpp
#include <hip/hip_runtime.h>
#include <hip/hip_bf16.h>
#include <stdint.h>


#define SEQ 2048
#define DM 640
#define FF 1536

typedef __attribute__((ext_vector_type(8))) short bf16x8;
typedef __attribute__((ext_vector_type(4))) short s16x4;
typedef __attribute__((ext_vector_type(4))) float f32x4;
typedef __attribute__((ext_vector_type(2))) float f32x2;
typedef __attribute__((ext_vector_type(2))) __bf16 bf16x2_t;
typedef __attribute__((address_space(3))) s16x4* lds_s16x4_p;
typedef unsigned short u16;
typedef unsigned int u32;
typedef unsigned long long u64;
typedef __attribute__((ext_vector_type(4))) unsigned int u32x4;
typedef __attribute__((ext_vector_type(2))) unsigned int u32x2;

#define HK0 15
#define CGAP 6
#define KGAP 7
#define CDEL 10
#define PDEL2 7
#define OBACK 2
#define PDELAY 220
#define FDELAY 450
#define KSTART 1050
#define PGAP 5
#define PDT 20
#define PSL 185
#define VDELAY 150
#define CCD 312
#define TQD 160
#define KCD 306
#define KOLD 316
#define TWS 12
#define PCD 230
#define PTHR 0.91f
#define GADD 80
#define OORD 0, 4, 5, 6, 7, 1, 2, 3
#define CODE_BYTES 35232

#define LDS_BARRIER() do { __builtin_amdgcn_sched_barrier(0); asm volatile("s_waitcnt lgkmcnt(0)\n\ts_barrier" ::: "memory"); __builtin_amdgcn_sched_barrier(0); } while (0)
__device__ __forceinline__ float bf2f(u16 v) { return __uint_as_float(((u32)v) << 16); }
__device__ __forceinline__ u16 f2bf(float f) { return __builtin_bit_cast(u16, (__bf16)f); }
__device__ __forceinline__ float rbf(float f) { return bf2f(f2bf(f)); }
__device__ __forceinline__ float lo_f(u32 w) { return __uint_as_float(w << 16); }
__device__ __forceinline__ float hi_f(u32 w) { return __uint_as_float(w & 0xFFFF0000u); }
__device__ __forceinline__ u32 cvt2(f32x2 v) { return __builtin_bit_cast(u32, __builtin_convertvector(v, bf16x2_t)); }
__device__ __forceinline__ f32x2 unpk(u32 w) { return (f32x2){lo_f(w), hi_f(w)}; }
__device__ __forceinline__ s16x4 tr_read(const char* p) {
  return __builtin_amdgcn_ds_read_tr16_b64_v4i16((lds_s16x4_p)(p));
}
template <int V> struct icst { static constexpr int value = V; };
template <int CTRL>
__device__ __forceinline__ float dppf(float v) {
  return __builtin_bit_cast(float, __builtin_amdgcn_update_dpp(0, __builtin_bit_cast(int, v), CTRL, 0xf, 0xf, true));
}
__device__ __forceinline__ float row8_sum(float v) {
  v += dppf<0xB1>(v);
  v += dppf<0x4E>(v);
  v += dppf<0x141>(v);
  return v;
}
__device__ __forceinline__ float row16_sum(float v) {
  v = row8_sum(v);
  v += dppf<0x140>(v);
  return v;
}
__device__ __forceinline__ float xg_sum(float v) {
  u32 a = __float_as_uint(v);
  auto r = __builtin_amdgcn_permlane32_swap(a, a, false, false);
  v = __uint_as_float(r[0]) + __uint_as_float(r[1]);
  a = __float_as_uint(v);
  auto r2 = __builtin_amdgcn_permlane16_swap(a, a, false, false);
  return __uint_as_float(r2[0]) + __uint_as_float(r2[1]);
}
__device__ __forceinline__ float tr_sum(f32x4 ys) {
  auto r01 = __builtin_amdgcn_permlane32_swap(__float_as_uint(ys[0]), __float_as_uint(ys[1]), false, false);
  auto r23 = __builtin_amdgcn_permlane32_swap(__float_as_uint(ys[2]), __float_as_uint(ys[3]), false, false);
  const u32 u = __float_as_uint(__uint_as_float(r01[0]) + __uint_as_float(r01[1]));
  const u32 vv = __float_as_uint(__uint_as_float(r23[0]) + __uint_as_float(r23[1]));
  auto r2 = __builtin_amdgcn_permlane16_swap(u, vv, false, false);
  return __uint_as_float(r2[0]) + __uint_as_float(r2[1]);
}
__device__ __forceinline__ float wave_sum(float v) { return xg_sum(row16_sum(v)); }
template <int CTRL>
__device__ __forceinline__ u32 dppu(u32 v) { return (u32)__builtin_amdgcn_update_dpp(0, (int)v, CTRL, 0xf, 0xf, true); }
__device__ __forceinline__ u32 wave_max(u32 v) {
  v = max(v, dppu<0xB1>(v)); v = max(v, dppu<0x4E>(v)); v = max(v, dppu<0x141>(v)); v = max(v, dppu<0x140>(v));
  return max(max((u32)__builtin_amdgcn_readlane(v, 0), (u32)__builtin_amdgcn_readlane(v, 16)),
             max((u32)__builtin_amdgcn_readlane(v, 32), (u32)__builtin_amdgcn_readlane(v, 48)));
}
__device__ __forceinline__ u32 mono(float f) {
  const u32 b = __float_as_uint(f);
  return b ^ ((u32)((int)b >> 31) | 0x80000000u);
}
__device__ __forceinline__ float dot2b(u32 a, u32 b, float c) {
  return __builtin_amdgcn_fdot2_f32_bf16(__builtin_bit_cast(bf16x2_t, a), __builtin_bit_cast(bf16x2_t, b), c, false);
}
template <int OFF>
__device__ __forceinline__ void dma_m0(u32 voff, const char* sbase, u32 dst) {
  asm volatile("s_mov_b32 m0, %2\n\ts_nop 0\n\tglobal_load_lds_dwordx4 %0, %1 offset:%3"
               :: "v"(voff), "s"(sbase), "s"(dst), "n"(OFF) : "memory");
}
template <int OFF>
__device__ __forceinline__ void dma_nx(u32 voff, const char* sbase) {
  asm volatile("global_load_lds_dwordx4 %0, %1 offset:%2" :: "v"(voff), "s"(sbase), "n"(OFF) : "memory");
}
__device__ __forceinline__ u32 lds_ld32(u32 addr) {
  u32 v;
  asm volatile("ds_read_b32 %0, %1\n\ts_waitcnt lgkmcnt(0)" : "=v"(v) : "v"(addr) : "memory");
  return v;
}
__device__ __forceinline__ u32x4 lds_ld128(u32 addr) {
  u32x4 v;
  asm volatile("ds_read_b128 %0, %1\n\ts_waitcnt lgkmcnt(0)" : "=v"(v) : "v"(addr) : "memory");
  return v;
}
__device__ __forceinline__ bf16x8 mk8(u32 a, u32 b, u32 c, u32 d) {
  return __builtin_bit_cast(bf16x8, (u32x4){a, b, c, d});
}
__device__ __forceinline__ bf16x8 tr_frag(const char* p, int off2) {
  s16x4 b0 = tr_read(p);
  s16x4 b1 = tr_read(p + off2);
  bf16x8 r;
  r[0] = b0[0]; r[1] = b0[1]; r[2] = b0[2]; r[3] = b0[3];
  r[4] = b1[0]; r[5] = b1[1]; r[6] = b1[2]; r[7] = b1[3];
  return r;
}
#define SPIN_CAP 400000
#define MFMA(A, B, C) __builtin_amdgcn_mfma_f32_16x16x32_bf16(A, B, C, 0, 0, 0)

#define HSLOT 16384
#define XRING 98304
#define XSLOT 12288
#define LDS_TOTAL 163840
#define EXO 102400
#define HIM 148480
#define PST_OFF (EXO + 40960)
#define TAB_OFF 161280
#define ONES_OFF 162048
#define CTL_OFF 162176
#define WR_OFF 162304
#define REC_OFF 162816
#define HN_OFF 124928
#define HSL 1344
#define ASL 1056
#define TCH_OFF 162560
#define ACT_OFF 135168
#define KEY_OFF (ACT_OFF + 5120)
#define POST_OFF (KEY_OFF + 256)
#define DOWN_OFF 81920
#define WS_EPOCH 0
#define WS_HFLAG 4096
#define WS_VP 40960
#define WS_LN1 16384
#define WS_FILL 131072
#define WS_O WS_FILL
#define WS_STAT (WS_O + 2621440)
#define WS_CAND (WS_STAT + 425984)
#define WS_PART (WS_CAND + 133120)
#define WS_BXF (WS_PART + 122880)
#define WS_PC (WS_BXF + 26624)
#define WS_PART2 (WS_PC + 66560)
#define WS_PK (4u << 20)
#define SENT 0xFFFFFFFFu

#define GST4(VO, SB, O, V) asm volatile("global_store_dwordx4 %0, %1, %2 offset:%3 sc1\n\ts_nop 1" ::"v"(VO), "v"(V), "s"(SB), "n"(O) : "memory")
#define GST2(VO, SB, O, V) asm volatile("global_store_dwordx2 %0, %1, %2 offset:%3 sc1\n\ts_nop 1" ::"v"(VO), "v"(V), "s"(SB), "n"(O) : "memory")
#define GLD2(D, VO, SB, O) asm volatile("global_load_dwordx2 %0, %1, %2 offset:%3 sc1" : "=v"(D) : "v"(VO), "s"(SB), "n"(O) : "memory")

__device__ __forceinline__ void touch_line(const void* p) {
  asm volatile("s_mov_b32 m0, %1\n\ts_nop 0\n\tglobal_load_lds_dword %0, off" ::"v"(p), "s"(TCH_OFF) : "memory");
}
__device__ __forceinline__ u32 w_off(int i, int lane, int hg) {
  int cc = (lane & 31) ^ (((2 * i + (lane >> 5)) & 7) << 1);
  cc = cc > 29 ? 29 : cc;
  return (lane >> 5) * 3840 + ((cc >= 10 ? 1120 : 0) + (cc >= 20 ? 1120 : 0)) + hg * 160 + cc * 16;
}
#define W_ROW(I) (((((2 * (I)) & 0x13) | (((2 * (I)) & 4) << 1) | (((2 * (I)) & 8) >> 1))) * 3840)
__device__ __forceinline__ u32 x_off(int i, int lane, int r0) {
  const int xr = i * 8 + (lane >> 3);
  int grow = r0 - 16 + xr;
  grow = grow < 0 ? 0 : (grow > SEQ - 1 ? SEQ - 1 : grow);
  return grow * 1280 + (((lane & 7) ^ ((xr >> 1) & 7)) * 16);
}

#define ATT_ASM \
  "ds_read_b32 v28, %[aq] offset:64\n\tds_read_b32 v30, %[aka]\n\tds_read_b32 v31, %[akb]\n\tv_mov_b64 v[38:39], 0\n\tv_mov_b64 v[42:43], 0\n\t" \
  "s_lshl_b32 s41, %[kh], 5\n\ts_lshl_b64 s[46:47], 0xffff, s41\n\ts_lshl_b64 s[48:49], s[46:47], 16\n\ts_mov_b32 s50, 0xaaaaaaaa\n\t" \
  "s_mov_b32 s51, 0xaaaaaaaa\n\ts_setprio 1\n\ts_waitcnt lgkmcnt(0)\n\ts_barrier\n\tds_read_b64 v[36:37], %[eh] offset:512\n\t" \
  "ds_read_b64 v[44:45], %[eh] offset:15360\n\tds_read_b64 v[46:47], %[e4] offset:27648\n\tds_read_b64 v[48:49], %[eh] offset:15872\n\t" \
  "ds_read_b64 v[50:51], %[e4] offset:28160\n\ts_mov_b64 exec, s[46:47]\n\tds_read_b64 v[38:39], %[e4] offset:12800\n\ts_mov_b64 exec, s[48:49]\n\t" \
  "ds_read_b64 v[42:43], %[e4] offset:12800\n\ts_mov_b64 exec, -1\n\tds_read_b64 v[40:41], %[eh] offset:3584\n\t" \
  "ds_read_b64 v[52:53], %[eh] offset:18432\n\tds_read_b64 v[54:55], %[e4] offset:27648\n\tds_read_b64 v[56:57], %[eh] offset:18944\n\t" \
  "ds_read_b64 v[58:59], %[e4] offset:28160\n\tv_mul_f32 v28, 0x3ea52b5b, v28\n\tv_pk_mul_f32 v[32:33], v[28:29], v[30:31] op_sel_hi:[0,1]\n\t" \
  "s_waitcnt lgkmcnt(6)\n\tv_mfma_f32_16x16x32_bf16 v[44:47], v[44:47], v[36:39], 0\n\tds_read2st64_b64 v[60:63], %[eh] offset0:60 offset1:61\n\t" \
  "v_mfma_f32_16x16x32_bf16 v[48:51], v[48:51], v[36:39], 0\n\tds_read2st64_b64 v[64:67], %[eh] offset0:66 offset1:67\n\ts_waitcnt lgkmcnt(2)\n\t" \
  "v_mfma_f32_16x16x32_bf16 v[52:55], v[52:55], v[40:43], 0\n\tds_read2st64_b64 v[68:71], %[e4] offset0:84 offset1:85\n\t" \
  "v_mfma_f32_16x16x32_bf16 v[56:59], v[56:59], v[40:43], 0\n\ts_mov_b32 exec_lo, 0xfff0ffff\n\ts_mov_b32 exec_hi, 0xf000ff00\n\ts_nop 0\n\t" \
  "v_mov_b64 v[44:45], v[48:49]\n\ts_mov_b32 exec_lo, 0xffc0fffc\n\ts_mov_b32 exec_hi, 0xc000fc00\n\tv_mov_b64 v[46:47], v[50:51]\n\ts_nop 0\n\t" \
  "v_mov_b64 v[54:55], v[58:59]\n\ts_mov_b32 exec_lo, 0xfff0ffff\n\ts_mov_b32 exec_hi, 0xf000ff00\n\tv_mov_b64 v[52:53], v[56:57]\n\t" \
  "s_mov_b64 exec, -1\n\tv_cndmask_b32 v44, v44, v45, s[50:51]\n\tv_cndmask_b32 v46, v46, v47, s[50:51]\n\tv_cndmask_b32 v52, v52, v53, s[50:51]\n\t" \
  "v_cndmask_b32 v54, v54, v55, s[50:51]\n\tv_fma_f32 v72, v44, v32, %[b0]\n\tv_fma_f32 v73, v46, v33, %[b1]\n\tv_fma_f32 v74, v52, v32, %[b0]\n\t" \
  "v_fma_f32 v75, v54, v33, %[b1]\n\tv_max_f32 v34, v72, v73\n\tv_max_f32 v35, v74, v75\n\tds_read_b128 v[76:79], %[arec]\n\t" \
  "ds_read_b128 v[80:83], %[arec] offset:16\n\tv_permlane32_swap_b32 v34, v35\n\tv_max_f32 v34, v34, v35\n\tv_mov_b32 v35, v34\n\t" \
  "ds_read_b128 v[84:87], %[arec] offset:32\n\ts_nop 0\n\tv_permlane16_swap_b32 v34, v35\n\tv_max_f32 v34, v34, v35\n\tv_mov_b32 v35, v34\n\ts_nop 1\n\t" \
  "v_permlane32_swap_b32 v34, v35\n\tv_sub_f32 v72, v72, v34\n\tv_sub_f32 v73, v73, v34\n\tv_sub_f32 v74, v74, v35\n\tv_sub_f32 v75, v75, v35\n\t" \
  "v_exp_f32 v72, v72\n\tv_exp_f32 v73, v73\n\tv_exp_f32 v74, v74\n\tv_exp_f32 v75, v75\n\tv_add_f32 v34, v72, v73\n\tv_add_f32 v35, v74, v75\n\t" \
  "v_pk_mul_f32 v[72:73], v[72:73], v[30:31]\n\tv_pk_mul_f32 v[74:75], v[74:75], v[30:31]\n\tv_permlane32_swap_b32 v34, v35\n\t" \
  "v_add_f32 v34, v34, v35\n\tv_mov_b32 v35, v34\n\ts_nop 1\n\tv_permlane16_swap_b32 v34, v35\n\tv_add_f32 v34, v34, v35\n\tv_rcp_f32 v34, v34\n\t" \
  "s_nop 0\n\tv_mov_b32 v30, v34\n\ts_waitcnt lgkmcnt(0)\n\ts_nop 0\n\tv_permlane32_swap_b32 v34, v30\n\t" \
  "v_pk_mul_f32 v[72:73], v[34:35], v[72:73] op_sel_hi:[0,1]\n\tv_pk_mul_f32 v[74:75], v[30:31], v[74:75] op_sel_hi:[0,1]\n\t" \
  "v_cvt_pk_bf16_f32 v72, v72, v73\n\tv_cvt_pk_bf16_f32 v74, v74, v75\n\tv_perm_b32 v36, 0, v72, %[s0]\n\tv_perm_b32 v37, 0, v72, %[s1]\n\t" \
  "v_perm_b32 v38, 0, v72, %[s2]\n\tv_perm_b32 v39, 0, v72, %[s3]\n\tv_perm_b32 v40, 0, v74, %[s0]\n\tv_perm_b32 v41, 0, v74, %[s1]\n\t" \
  "v_mfma_f32_16x16x32_bf16 v[60:63], v[60:63], v[36:39], 0\n\tv_perm_b32 v42, 0, v74, %[s2]\n\tv_perm_b32 v43, 0, v74, %[s3]\n\t" \
  "v_mfma_f32_16x16x32_bf16 v[52:55], v[68:71], v[36:39], 0\n\ts_nop 0\n\tv_mfma_f32_16x16x32_bf16 v[64:67], v[64:67], v[40:43], 0\n\t" \
  "v_mfma_f32_16x16x32_bf16 v[56:59], v[68:71], v[40:43], 0\n\ts_nop 1\n\tv_cvt_pk_bf16_f32 v24, v60, v61\n\tv_cvt_pk_bf16_f32 v25, v62, v63\n\t" \
  "s_nop 0\n\tv_cvt_pk_bf16_f32 v52, v52, v53\n\tv_cvt_pk_bf16_f32 v53, v54, v55\n\tv_cvt_pk_bf16_f32 v26, v64, v65\n\t" \
  "v_cvt_pk_bf16_f32 v27, v66, v67\n\tglobal_store_dwordx4 %[l16], v[24:27], %[os] sc1\n\ts_mov_b64 exec, s[48:49]\n\tv_cvt_pk_bf16_f32 v52, v56, v57\n\t" \
  "v_cvt_pk_bf16_f32 v53, v58, v59\n\ts_or_b64 exec, s[46:47], s[48:49]\n\tglobal_store_dwordx2 %[l16], v[52:53], %[os4] sc1\n\ts_mov_b64 exec, -1\n\t" \
  "s_setprio 0\n\tv_mfma_f32_16x16x32_bf16 v[60:63], v[24:27], v[76:79], 0\n\tv_and_b32 v54, 0x7fff7fff, v52\n\tv_and_b32 v55, 0x7fff7fff, v53\n\t" \
  "v_and_b32 v64, 0x7fff7fff, v24\n\tv_and_b32 v65, 0x7fff7fff, v25\n\tv_and_b32 v66, 0x7fff7fff, v26\n\tv_and_b32 v67, 0x7fff7fff, v27\n\t" \
  "v_mfma_f32_16x16x32_bf16 v[60:63], v[52:55], v[80:83], v[60:63]\n\tds_write2st64_b64 %[eh], v[24:25], v[26:27] offset0:1 offset1:7\n\t" \
  "v_mfma_f32_16x16x32_bf16 v[60:63], v[64:67], v[84:87], v[60:63]\n\ts_mov_b32 exec_lo, 0x01010101\n\ts_mov_b32 exec_hi, 0x01010101\n\ts_nop 5\n\t" \
  "v_cvt_pk_bf16_f32 v60, v60, v61\n\tv_cvt_pk_bf16_f32 v61, v62, v63\n\tv_and_b32 v60, -2, v60\n\tglobal_store_dwordx2 %[vpp], v[60:61], %[sst] sc1\n\t" \
  "s_mov_b64 exec, -1\n\t" \
  ""
#define OP_ASM \
  "s_mov_b32 s43, %[cap]\n\ts_cmp_lt_u32 %[w], 4\n\ts_cbranch_scc1 LH_%=\n\ts_setprio 3\n\tglobal_load_dwordx4 v[124:127], %[vd], %[oa1] sc1\n\t" \
  "global_load_dwordx4 v[128:131], %[vk], %[oa1] sc1\n\tglobal_load_dwordx4 v[132:135], %[vd], %[oa2] sc1\n\t" \
  "global_load_dwordx4 v[136:139], %[vk], %[oa2] sc1\n\tv_add_u32 %[tk], %[sk], %[e4]\n\tv_add_u32 %[ta], %[so0], %[pm]\n\t" \
  "ds_read2st64_b64 v[104:107], %[tk] offset0:1 offset1:7\n\tv_xor_b32 %[tk], 512, %[tk]\n\tds_read2st64_b64 v[108:111], %[tk] offset0:1 offset1:7\n\t" \
  "ds_read_b64_tr_b16 v[64:65], %[ta] offset:0\n\tds_read_b64_tr_b16 v[66:67], %[ta] offset:3200\n\tds_read_b64_tr_b16 v[68:69], %[ta] offset:8\n\t" \
  "ds_read_b64_tr_b16 v[70:71], %[ta] offset:3208\n\tds_read_b64_tr_b16 v[72:73], %[ta] offset:16\n\tds_read_b64_tr_b16 v[74:75], %[ta] offset:3216\n\t" \
  "ds_read_b64_tr_b16 v[76:77], %[ta] offset:24\n\tds_read_b64_tr_b16 v[78:79], %[ta] offset:3224\n\tds_read_b64_tr_b16 v[80:81], %[ta] offset:32\n\t" \
  "ds_read_b64_tr_b16 v[82:83], %[ta] offset:3232\n\tds_read_b64 v[112:113], %[ra] offset:0\n\tds_read_b64 v[114:115], %[ra] offset:8\n\t" \
  "ds_read_b64 v[116:117], %[ra] offset:16\n\tds_read_b64 v[118:119], %[ra] offset:24\n\tds_read_b64 v[120:121], %[ra] offset:32\n\t" \
  "global_load_dwordx4 v[140:143], %[vd], %[oa3] sc1\n\tv_add_u32 %[ta], %[so1], %[pm]\n\ts_waitcnt lgkmcnt(5)\n\t" \
  "v_mfma_f32_16x16x32_bf16 v[44:47], v[64:67], v[108:111], 0\n\tds_read_b64_tr_b16 v[84:85], %[ta] offset:0\n\t" \
  "ds_read_b64_tr_b16 v[86:87], %[ta] offset:3200\n\tv_mfma_f32_16x16x32_bf16 v[48:51], v[68:71], v[108:111], 0\n\t" \
  "ds_read_b64_tr_b16 v[88:89], %[ta] offset:8\n\tds_read_b64_tr_b16 v[90:91], %[ta] offset:3208\n\t" \
  "v_mfma_f32_16x16x32_bf16 v[52:55], v[72:75], v[108:111], 0\n\tds_read_b64_tr_b16 v[92:93], %[ta] offset:16\n\t" \
  "ds_read_b64_tr_b16 v[94:95], %[ta] offset:3216\n\tv_mfma_f32_16x16x32_bf16 v[56:59], v[76:79], v[108:111], 0\n\t" \
  "ds_read_b64_tr_b16 v[96:97], %[ta] offset:24\n\tds_read_b64_tr_b16 v[98:99], %[ta] offset:3224\n\t" \
  "v_mfma_f32_16x16x32_bf16 v[60:63], v[80:83], v[108:111], 0\n\tds_read_b64_tr_b16 v[100:101], %[ta] offset:32\n\t" \
  "ds_read_b64_tr_b16 v[102:103], %[ta] offset:3232\n\ts_waitcnt lgkmcnt(10)\n\tv_lshlrev_b32 v24, 16, v112\n\tv_and_b32 v25, 0xffff0000, v112\n\t" \
  "v_lshlrev_b32 v26, 16, v113\n\tv_and_b32 v27, 0xffff0000, v113\n\tv_lshlrev_b32 v28, 16, v114\n\tv_and_b32 v29, 0xffff0000, v114\n\t" \
  "v_lshlrev_b32 v30, 16, v115\n\tv_and_b32 v31, 0xffff0000, v115\n\tv_mfma_f32_16x16x32_bf16 v[24:27], v[64:67], v[104:107], v[24:27]\n\t" \
  "v_lshlrev_b32 v32, 16, v116\n\tv_and_b32 v33, 0xffff0000, v116\n\tv_lshlrev_b32 v34, 16, v117\n\tv_and_b32 v35, 0xffff0000, v117\n\t" \
  "v_mfma_f32_16x16x32_bf16 v[28:31], v[68:71], v[104:107], v[28:31]\n\tv_lshlrev_b32 v36, 16, v118\n\tv_and_b32 v37, 0xffff0000, v118\n\t" \
  "v_lshlrev_b32 v38, 16, v119\n\tv_and_b32 v39, 0xffff0000, v119\n\tv_mfma_f32_16x16x32_bf16 v[32:35], v[72:75], v[104:107], v[32:35]\n\t" \
  "v_lshlrev_b32 v40, 16, v120\n\tv_and_b32 v41, 0xffff0000, v120\n\tv_lshlrev_b32 v42, 16, v121\n\tv_and_b32 v43, 0xffff0000, v121\n\t" \
  "v_mfma_f32_16x16x32_bf16 v[36:39], v[76:79], v[104:107], v[36:39]\n\ts_nop 0\n\tv_mfma_f32_16x16x32_bf16 v[40:43], v[80:83], v[104:107], v[40:43]\n\t" \
  "v_add_u32 %[ta], %[so2], %[pm]\n\tds_read_b64_tr_b16 v[104:105], %[ta] offset:0\n\tds_read_b64_tr_b16 v[106:107], %[ta] offset:3200\n\t" \
  "ds_read_b64_tr_b16 v[108:109], %[ta] offset:8\n\tds_read_b64_tr_b16 v[110:111], %[ta] offset:3208\n\t" \
  "ds_read_b64_tr_b16 v[112:113], %[ta] offset:16\n\tds_read_b64_tr_b16 v[114:115], %[ta] offset:3216\n\t" \
  "ds_read_b64_tr_b16 v[116:117], %[ta] offset:24\n\tds_read_b64_tr_b16 v[118:119], %[ta] offset:3224\n\t" \
  "ds_read_b64_tr_b16 v[120:121], %[ta] offset:32\n\tds_read_b64_tr_b16 v[122:123], %[ta] offset:3232\n\t" \
  "global_load_dwordx4 v[144:147], %[vk], %[oa3] sc1\n\tglobal_load_dwordx4 v[148:151], %[vd], %[oa4] sc1\n\ts_waitcnt vmcnt(5)\n\t" \
  "v_max_u32 %[tk], v124, v128\n\tv_cmp_eq_u32 vcc, -1, %[tk]\n\ts_cbranch_vccnz LSs1_%=\n\tLQs1_%=:\n\ts_waitcnt lgkmcnt(10)\n\t" \
  "v_mfma_f32_16x16x32_bf16 v[44:47], v[84:87], v[124:127], v[44:47]\n\tv_add_u32 %[ta], %[so3], %[pm]\n\tds_read_b64_tr_b16 v[64:65], %[ta] offset:0\n\t" \
  "v_mfma_f32_16x16x32_bf16 v[24:27], v[84:87], v[128:131], v[24:27]\n\tds_read_b64_tr_b16 v[66:67], %[ta] offset:3200\n\t" \
  "global_load_dwordx4 v[152:155], %[vk], %[oa4] sc1\n\tv_mfma_f32_16x16x32_bf16 v[48:51], v[88:91], v[124:127], v[48:51]\n\t" \
  "ds_read_b64_tr_b16 v[68:69], %[ta] offset:8\n\tv_mfma_f32_16x16x32_bf16 v[28:31], v[88:91], v[128:131], v[28:31]\n\t" \
  "ds_read_b64_tr_b16 v[70:71], %[ta] offset:3208\n\tglobal_load_dwordx4 v[156:159], %[vd], %[oa5] sc1\n\t" \
  "v_mfma_f32_16x16x32_bf16 v[52:55], v[92:95], v[124:127], v[52:55]\n\tds_read_b64_tr_b16 v[72:73], %[ta] offset:16\n\t" \
  "v_mfma_f32_16x16x32_bf16 v[32:35], v[92:95], v[128:131], v[32:35]\n\tds_read_b64_tr_b16 v[74:75], %[ta] offset:3216\n\t" \
  "v_mfma_f32_16x16x32_bf16 v[56:59], v[96:99], v[124:127], v[56:59]\n\tds_read_b64_tr_b16 v[76:77], %[ta] offset:24\n\t" \
  "v_mfma_f32_16x16x32_bf16 v[36:39], v[96:99], v[128:131], v[36:39]\n\tds_read_b64_tr_b16 v[78:79], %[ta] offset:3224\n\ts_waitcnt vmcnt(5)\n\t" \
  "v_max_u32 %[tk], v132, v136\n\tv_cmp_eq_u32 vcc, -1, %[tk]\n\tv_mfma_f32_16x16x32_bf16 v[60:63], v[100:103], v[124:127], v[60:63]\n\t" \
  "ds_read_b64_tr_b16 v[80:81], %[ta] offset:32\n\tv_mfma_f32_16x16x32_bf16 v[40:43], v[100:103], v[128:131], v[40:43]\n\t" \
  "ds_read_b64_tr_b16 v[82:83], %[ta] offset:3232\n\ts_cbranch_vccnz LSs2_%=\n\tLQs2_%=:\n\ts_waitcnt lgkmcnt(10)\n\t" \
  "v_mfma_f32_16x16x32_bf16 v[44:47], v[104:107], v[132:135], v[44:47]\n\tv_add_u32 %[ta], %[so4], %[pf]\n\t" \
  "ds_read_b64_tr_b16 v[84:85], %[ta] offset:0\n\tv_mfma_f32_16x16x32_bf16 v[24:27], v[104:107], v[136:139], v[24:27]\n\t" \
  "ds_read_b64_tr_b16 v[86:87], %[ta] offset:12800\n\tglobal_load_dwordx4 v[124:127], %[vk], %[oa5] sc1\n\t" \
  "v_mfma_f32_16x16x32_bf16 v[48:51], v[108:111], v[132:135], v[48:51]\n\tds_read_b64_tr_b16 v[88:89], %[ta] offset:8\n\t" \
  "v_mfma_f32_16x16x32_bf16 v[28:31], v[108:111], v[136:139], v[28:31]\n\tds_read_b64_tr_b16 v[90:91], %[ta] offset:12808\n\t" \
  "global_load_dwordx4 v[128:131], %[vd], %[oa6] sc1\n\tv_mfma_f32_16x16x32_bf16 v[52:55], v[112:115], v[132:135], v[52:55]\n\t" \
  "ds_read_b64_tr_b16 v[92:93], %[ta] offset:16\n\tv_mfma_f32_16x16x32_bf16 v[32:35], v[112:115], v[136:139], v[32:35]\n\t" \
  "ds_read_b64_tr_b16 v[94:95], %[ta] offset:12816\n\tv_mfma_f32_16x16x32_bf16 v[56:59], v[116:119], v[132:135], v[56:59]\n\t" \
  "ds_read_b64_tr_b16 v[96:97], %[ta] offset:24\n\tv_mfma_f32_16x16x32_bf16 v[36:39], v[116:119], v[136:139], v[36:39]\n\t" \
  "ds_read_b64_tr_b16 v[98:99], %[ta] offset:12824\n\ts_waitcnt vmcnt(5)\n\tv_max_u32 %[tk], v140, v144\n\tv_cmp_eq_u32 vcc, -1, %[tk]\n\t" \
  "v_mfma_f32_16x16x32_bf16 v[60:63], v[120:123], v[132:135], v[60:63]\n\tds_read_b64_tr_b16 v[100:101], %[ta] offset:32\n\t" \
  "v_mfma_f32_16x16x32_bf16 v[40:43], v[120:123], v[136:139], v[40:43]\n\tds_read_b64_tr_b16 v[102:103], %[ta] offset:12832\n\t" \
  "s_cbranch_vccnz LSs3_%=\n\tLQs3_%=:\n\ts_waitcnt lgkmcnt(10)\n\tv_mfma_f32_16x16x32_bf16 v[44:47], v[64:67], v[140:143], v[44:47]\n\t" \
  "v_add_u32 %[ta], %[so5], %[pm]\n\tds_read_b64_tr_b16 v[104:105], %[ta] offset:0\n\t" \
  "v_mfma_f32_16x16x32_bf16 v[24:27], v[64:67], v[144:147], v[24:27]\n\tds_read_b64_tr_b16 v[106:107], %[ta] offset:3200\n\t" \
  "global_load_dwordx4 v[132:135], %[vk], %[oa6] sc1\n\tv_mfma_f32_16x16x32_bf16 v[48:51], v[68:71], v[140:143], v[48:51]\n\t" \
  "ds_read_b64_tr_b16 v[108:109], %[ta] offset:8\n\tv_mfma_f32_16x16x32_bf16 v[28:31], v[68:71], v[144:147], v[28:31]\n\t" \
  "ds_read_b64_tr_b16 v[110:111], %[ta] offset:3208\n\tglobal_load_dwordx4 v[136:139], %[vd], %[oa7] sc1\n\t" \
  "v_mfma_f32_16x16x32_bf16 v[52:55], v[72:75], v[140:143], v[52:55]\n\tds_read_b64_tr_b16 v[112:113], %[ta] offset:16\n\t" \
  "v_mfma_f32_16x16x32_bf16 v[32:35], v[72:75], v[144:147], v[32:35]\n\tds_read_b64_tr_b16 v[114:115], %[ta] offset:3216\n\t" \
  "v_mfma_f32_16x16x32_bf16 v[56:59], v[76:79], v[140:143], v[56:59]\n\tds_read_b64_tr_b16 v[116:117], %[ta] offset:24\n\t" \
  "v_mfma_f32_16x16x32_bf16 v[36:39], v[76:79], v[144:147], v[36:39]\n\tds_read_b64_tr_b16 v[118:119], %[ta] offset:3224\n\ts_waitcnt vmcnt(5)\n\t" \
  "v_max3_u32 %[tk], v148, v150, v152\n\tv_max_u32 %[tk], %[tk], v154\n\tv_cmp_eq_u32 vcc, -1, %[tk]\n\t" \
  "v_mfma_f32_16x16x32_bf16 v[60:63], v[80:83], v[140:143], v[60:63]\n\tds_read_b64_tr_b16 v[120:121], %[ta] offset:32\n\t" \
  "v_mfma_f32_16x16x32_bf16 v[40:43], v[80:83], v[144:147], v[40:43]\n\tds_read_b64_tr_b16 v[122:123], %[ta] offset:3232\n\ts_cbranch_vccnz LSs4_%=\n\t" \
  "LQs4_%=:\n\ts_waitcnt lgkmcnt(10)\n\tv_mfma_f32_16x16x32_bf16 v[44:47], v[84:87], v[148:151], v[44:47]\n\tv_add_u32 %[ta], %[so6], %[pm]\n\t" \
  "ds_read_b64_tr_b16 v[64:65], %[ta] offset:0\n\tv_mfma_f32_16x16x32_bf16 v[24:27], v[84:87], v[152:155], v[24:27]\n\t" \
  "ds_read_b64_tr_b16 v[66:67], %[ta] offset:3200\n\tglobal_load_dwordx4 v[140:143], %[vd], %[oa8] sc1\n\t" \
  "v_mfma_f32_16x16x32_bf16 v[48:51], v[88:91], v[148:151], v[48:51]\n\tds_read_b64_tr_b16 v[68:69], %[ta] offset:8\n\t" \
  "v_mfma_f32_16x16x32_bf16 v[28:31], v[88:91], v[152:155], v[28:31]\n\tds_read_b64_tr_b16 v[70:71], %[ta] offset:3208\n\t" \
  "global_load_dwordx4 v[144:147], %[vd], %[oa9] sc1\n\tv_mfma_f32_16x16x32_bf16 v[52:55], v[92:95], v[148:151], v[52:55]\n\t" \
  "ds_read_b64_tr_b16 v[72:73], %[ta] offset:16\n\tv_mfma_f32_16x16x32_bf16 v[32:35], v[92:95], v[152:155], v[32:35]\n\t" \
  "ds_read_b64_tr_b16 v[74:75], %[ta] offset:3216\n\tv_mfma_f32_16x16x32_bf16 v[56:59], v[96:99], v[148:151], v[56:59]\n\t" \
  "ds_read_b64_tr_b16 v[76:77], %[ta] offset:24\n\tv_mfma_f32_16x16x32_bf16 v[36:39], v[96:99], v[152:155], v[36:39]\n\t" \
  "ds_read_b64_tr_b16 v[78:79], %[ta] offset:3224\n\ts_waitcnt vmcnt(5)\n\tv_max_u32 %[tk], v156, v124\n\tv_cmp_eq_u32 vcc, -1, %[tk]\n\t" \
  "v_mfma_f32_16x16x32_bf16 v[60:63], v[100:103], v[148:151], v[60:63]\n\tds_read_b64_tr_b16 v[80:81], %[ta] offset:32\n\t" \
  "v_mfma_f32_16x16x32_bf16 v[40:43], v[100:103], v[152:155], v[40:43]\n\tds_read_b64_tr_b16 v[82:83], %[ta] offset:3232\n\ts_cbranch_vccnz LSs5_%=\n\t" \
  "LQs5_%=:\n\ts_waitcnt lgkmcnt(10)\n\tv_mfma_f32_16x16x32_bf16 v[44:47], v[104:107], v[156:159], v[44:47]\n\tv_add_u32 %[ta], %[so7], %[pm]\n\t" \
  "ds_read_b64_tr_b16 v[84:85], %[ta] offset:0\n\tv_mfma_f32_16x16x32_bf16 v[24:27], v[104:107], v[124:127], v[24:27]\n\t" \
  "ds_read_b64_tr_b16 v[86:87], %[ta] offset:3200\n\tglobal_load_dwordx4 v[148:151], %[vk], %[oa7] sc1\n\t" \
  "v_mfma_f32_16x16x32_bf16 v[48:51], v[108:111], v[156:159], v[48:51]\n\tds_read_b64_tr_b16 v[88:89], %[ta] offset:8\n\t" \
  "v_mfma_f32_16x16x32_bf16 v[28:31], v[108:111], v[124:127], v[28:31]\n\tds_read_b64_tr_b16 v[90:91], %[ta] offset:3208\n\t" \
  "global_load_dwordx4 v[152:155], %[vk], %[oa8] sc1\n\tv_mfma_f32_16x16x32_bf16 v[52:55], v[112:115], v[156:159], v[52:55]\n\t" \
  "ds_read_b64_tr_b16 v[92:93], %[ta] offset:16\n\tv_mfma_f32_16x16x32_bf16 v[32:35], v[112:115], v[124:127], v[32:35]\n\t" \
  "ds_read_b64_tr_b16 v[94:95], %[ta] offset:3216\n\tv_mfma_f32_16x16x32_bf16 v[56:59], v[116:119], v[156:159], v[56:59]\n\t" \
  "ds_read_b64_tr_b16 v[96:97], %[ta] offset:24\n\tv_mfma_f32_16x16x32_bf16 v[36:39], v[116:119], v[124:127], v[36:39]\n\t" \
  "ds_read_b64_tr_b16 v[98:99], %[ta] offset:3224\n\ts_waitcnt vmcnt(5)\n\tv_max_u32 %[tk], v128, v132\n\tv_cmp_eq_u32 vcc, -1, %[tk]\n\t" \
  "v_mfma_f32_16x16x32_bf16 v[60:63], v[120:123], v[156:159], v[60:63]\n\tds_read_b64_tr_b16 v[100:101], %[ta] offset:32\n\t" \
  "v_mfma_f32_16x16x32_bf16 v[40:43], v[120:123], v[124:127], v[40:43]\n\tds_read_b64_tr_b16 v[102:103], %[ta] offset:3232\n\ts_cbranch_vccnz LSs6_%=\n\t" \
  "LQs6_%=:\n\ts_waitcnt lgkmcnt(10)\n\tv_mfma_f32_16x16x32_bf16 v[44:47], v[64:67], v[128:131], v[44:47]\n\tv_add_u32 %[ta], %[so8], %[pm]\n\t" \
  "ds_read_b64_tr_b16 v[104:105], %[ta] offset:0\n\tv_mfma_f32_16x16x32_bf16 v[24:27], v[64:67], v[132:135], v[24:27]\n\t" \
  "ds_read_b64_tr_b16 v[106:107], %[ta] offset:3200\n\tglobal_load_dwordx4 v[156:159], %[vk], %[oa9] sc1\n\t" \
  "v_mfma_f32_16x16x32_bf16 v[48:51], v[68:71], v[128:131], v[48:51]\n\tds_read_b64_tr_b16 v[108:109], %[ta] offset:8\n\t" \
  "v_mfma_f32_16x16x32_bf16 v[28:31], v[68:71], v[132:135], v[28:31]\n\tds_read_b64_tr_b16 v[110:111], %[ta] offset:3208\n\t" \
  "v_mfma_f32_16x16x32_bf16 v[52:55], v[72:75], v[128:131], v[52:55]\n\tds_read_b64_tr_b16 v[112:113], %[ta] offset:16\n\t" \
  "v_mfma_f32_16x16x32_bf16 v[32:35], v[72:75], v[132:135], v[32:35]\n\tds_read_b64_tr_b16 v[114:115], %[ta] offset:3216\n\t" \
  "v_mfma_f32_16x16x32_bf16 v[56:59], v[76:79], v[128:131], v[56:59]\n\tds_read_b64_tr_b16 v[116:117], %[ta] offset:24\n\t" \
  "v_mfma_f32_16x16x32_bf16 v[36:39], v[76:79], v[132:135], v[36:39]\n\tds_read_b64_tr_b16 v[118:119], %[ta] offset:3224\n\ts_waitcnt vmcnt(4)\n\t" \
  "v_max_u32 %[tk], v136, v140\n\tv_cmp_eq_u32 vcc, -1, %[tk]\n\tv_mfma_f32_16x16x32_bf16 v[60:63], v[80:83], v[128:131], v[60:63]\n\t" \
  "ds_read_b64_tr_b16 v[120:121], %[ta] offset:32\n\tv_mfma_f32_16x16x32_bf16 v[40:43], v[80:83], v[132:135], v[40:43]\n\t" \
  "ds_read_b64_tr_b16 v[122:123], %[ta] offset:3232\n\ts_cbranch_vccnz LSd7_%=\n\tLQd7_%=:\n\ts_waitcnt lgkmcnt(10)\n\t" \
  "v_mfma_f32_16x16x32_bf16 v[44:47], v[84:87], v[136:139], v[44:47]\n\tv_add_u32 %[ta], %[so9], %[pf]\n\tds_read_b64_tr_b16 v[64:65], %[ta] offset:0\n\t" \
  "ds_read_b64_tr_b16 v[66:67], %[ta] offset:12800\n\tv_mfma_f32_16x16x32_bf16 v[48:51], v[88:91], v[136:139], v[48:51]\n\t" \
  "ds_read_b64_tr_b16 v[68:69], %[ta] offset:8\n\tds_read_b64_tr_b16 v[70:71], %[ta] offset:12808\n\t" \
  "v_mfma_f32_16x16x32_bf16 v[52:55], v[92:95], v[136:139], v[52:55]\n\tds_read_b64_tr_b16 v[72:73], %[ta] offset:16\n\t" \
  "ds_read_b64_tr_b16 v[74:75], %[ta] offset:12816\n\tv_mfma_f32_16x16x32_bf16 v[56:59], v[96:99], v[136:139], v[56:59]\n\t" \
  "ds_read_b64_tr_b16 v[76:77], %[ta] offset:24\n\tds_read_b64_tr_b16 v[78:79], %[ta] offset:12824\n\t" \
  "v_mfma_f32_16x16x32_bf16 v[60:63], v[100:103], v[136:139], v[60:63]\n\tds_read_b64_tr_b16 v[80:81], %[ta] offset:32\n\t" \
  "ds_read_b64_tr_b16 v[82:83], %[ta] offset:12832\n\ts_waitcnt lgkmcnt(10)\n\tv_mfma_f32_16x16x32_bf16 v[44:47], v[104:107], v[140:143], v[44:47]\n\t" \
  "v_mfma_f32_16x16x32_bf16 v[48:51], v[108:111], v[140:143], v[48:51]\n\tv_mfma_f32_16x16x32_bf16 v[52:55], v[112:115], v[140:143], v[52:55]\n\t" \
  "s_waitcnt vmcnt(2)\n\tv_max3_u32 %[tk], v144, v146, v148\n\tv_cmp_eq_u32 vcc, -1, %[tk]\n\t" \
  "v_mfma_f32_16x16x32_bf16 v[56:59], v[116:119], v[140:143], v[56:59]\n\tv_mfma_f32_16x16x32_bf16 v[60:63], v[120:123], v[140:143], v[60:63]\n\t" \
  "s_cbranch_vccnz LSd9_%=\n\tLQd9_%=:\n\ts_waitcnt lgkmcnt(0)\n\tv_mfma_f32_16x16x32_bf16 v[44:47], v[64:67], v[144:147], v[44:47]\n\t" \
  "ds_read2_b64 v[124:127], %[aw] offset0:0 offset1:1\n\tds_read2_b64 v[128:131], %[aw] offset0:2 offset1:3\n\t" \
  "ds_read_b64 v[132:133], %[aw] offset:32\n\tv_mov_b64 v[134:135], 0\n\tv_mfma_f32_16x16x32_bf16 v[48:51], v[68:71], v[144:147], v[48:51]\n\t" \
  "v_mfma_f32_16x16x32_bf16 v[52:55], v[72:75], v[144:147], v[52:55]\n\tv_add_u32 %[ta], %[sd], %[l16]\n\t" \
  "v_mfma_f32_16x16x32_bf16 v[56:59], v[76:79], v[144:147], v[56:59]\n\tv_mfma_f32_16x16x32_bf16 v[60:63], v[80:83], v[144:147], v[60:63]\n\t" \
  "v_mov_b32 v137, %[sc]\n\tv_mov_b32 v136, 1\n\tv_mfma_f32_16x16x32_bf16 v[24:27], v[84:87], v[148:151], v[24:27]\n\t" \
  "ds_write_b128 %[ta], v[44:47] offset:0\n\tv_mfma_f32_16x16x32_bf16 v[28:31], v[88:91], v[148:151], v[28:31]\n\t" \
  "ds_write_b128 %[ta], v[48:51] offset:1024\n\tv_mfma_f32_16x16x32_bf16 v[32:35], v[92:95], v[148:151], v[32:35]\n\t" \
  "ds_write_b128 %[ta], v[52:55] offset:2048\n\ts_waitcnt vmcnt(0)\n\tv_max3_u32 %[tk], v152, v156, v158\n\tv_cmp_eq_u32 vcc, -1, %[tk]\n\t" \
  "v_mfma_f32_16x16x32_bf16 v[36:39], v[96:99], v[148:151], v[36:39]\n\tds_write_b128 %[ta], v[56:59] offset:3072\n\t" \
  "v_mfma_f32_16x16x32_bf16 v[40:43], v[100:103], v[148:151], v[40:43]\n\tds_write_b128 %[ta], v[60:63] offset:4096\n\ts_mov_b64 exec, 1\n\t" \
  "ds_add_u32 v137, v136\n\ts_mov_b64 exec, -1\n\ts_cbranch_vccnz LSk8_%=\n\tLQk8_%=:\n\t" \
  "v_mfma_f32_16x16x32_bf16 v[24:27], v[104:107], v[152:155], v[24:27]\n\tv_mov_b64 v[94:95], 0\n\t" \
  "v_mfma_f32_16x16x32_bf16 v[28:31], v[108:111], v[152:155], v[28:31]\n\tv_xor_b32 v96, 4, v137\n\t" \
  "v_mfma_f32_16x16x32_bf16 v[32:35], v[112:115], v[152:155], v[32:35]\n\tv_add_u32 %[tk], %[sr], %[l16]\n\t" \
  "v_mfma_f32_16x16x32_bf16 v[36:39], v[116:119], v[152:155], v[36:39]\n\tv_mfma_f32_16x16x32_bf16 v[40:43], v[120:123], v[152:155], v[40:43]\n\t" \
  "v_mfma_f32_16x16x32_bf16 v[24:27], v[64:67], v[156:159], v[24:27]\n\tds_read_b32 v97, v96\n\tds_read_b128 v[44:47], %[tk] offset:0\n\t" \
  "ds_read_b128 v[48:51], %[tk] offset:1024\n\tds_read_b128 v[52:55], %[tk] offset:2048\n\tds_read_b128 v[56:59], %[tk] offset:3072\n\t" \
  "ds_read_b128 v[60:63], %[tk] offset:4096\n\tv_mfma_f32_16x16x32_bf16 v[28:31], v[68:71], v[156:159], v[28:31]\n\t" \
  "v_mfma_f32_16x16x32_bf16 v[32:35], v[72:75], v[156:159], v[32:35]\n\tv_mfma_f32_16x16x32_bf16 v[36:39], v[76:79], v[156:159], v[36:39]\n\t" \
  "v_mfma_f32_16x16x32_bf16 v[40:43], v[80:83], v[156:159], v[40:43]\n\tLD1_%=:\n\ts_waitcnt lgkmcnt(0)\n\tv_cmp_eq_u32 vcc, 0, v97\n\t" \
  "s_cbranch_vccnz LD2_%=\n\tLT_%=:\n\tv_mov_b32 v137, %[sa]\n\tds_read_b32 v138, v137 offset:16\n\tv_add_f32 v24, v24, v44\n\t" \
  "v_add_f32 v25, v25, v45\n\tv_add_f32 v26, v26, v46\n\tv_add_f32 v27, v27, v47\n\tv_cvt_pk_bf16_f32 v84, v24, v25\n\t" \
  "v_cvt_pk_bf16_f32 v85, v26, v27\n\tv_add_f32 v28, v28, v48\n\tv_add_f32 v29, v29, v49\n\tv_add_f32 v30, v30, v50\n\tv_add_f32 v31, v31, v51\n\t" \
  "v_cvt_pk_bf16_f32 v86, v28, v29\n\tv_cvt_pk_bf16_f32 v87, v30, v31\n\tv_add_f32 v32, v32, v52\n\tv_add_f32 v33, v33, v53\n\t" \
  "v_add_f32 v34, v34, v54\n\tv_add_f32 v35, v35, v55\n\tv_cvt_pk_bf16_f32 v88, v32, v33\n\tv_cvt_pk_bf16_f32 v89, v34, v35\n\t" \
  "v_mfma_f32_16x16x32_bf16 v[100:103], v[84:87], v[84:87], 0\n\tv_mfma_f32_16x16x32_bf16 v[96:99], v[124:127], v[84:87], 0\n\t" \
  "v_add_f32 v36, v36, v56\n\tv_add_f32 v37, v37, v57\n\tv_add_f32 v38, v38, v58\n\tv_add_f32 v39, v39, v59\n\tv_cvt_pk_bf16_f32 v90, v36, v37\n\t" \
  "v_cvt_pk_bf16_f32 v91, v38, v39\n\tv_add_f32 v40, v40, v60\n\tv_add_f32 v41, v41, v61\n\tv_add_f32 v42, v42, v62\n\tv_add_f32 v43, v43, v63\n\t" \
  "v_cvt_pk_bf16_f32 v92, v40, v41\n\tv_cvt_pk_bf16_f32 v93, v42, v43\n\tv_mfma_f32_16x16x32_bf16 v[100:103], v[88:91], v[88:91], v[100:103]\n\t" \
  "v_mfma_f32_16x16x32_bf16 v[96:99], v[128:131], v[88:91], v[96:99]\n\tv_mfma_f32_16x16x32_bf16 v[100:103], v[92:95], v[92:95], v[100:103]\n\t" \
  "v_mfma_f32_16x16x32_bf16 v[96:99], v[132:135], v[92:95], v[96:99]\n\ts_waitcnt lgkmcnt(0)\n\tv_sub_u32 v138, %[ra], v138\n\t" \
  "v_cmp_gt_u32 vcc, 0xa0, v138\n\tv_and_b32 %[ta], 0xff, %[l16]\n\tv_add_u32 %[tk], %[sp], %[ta]\n\tv_mov_b32 v136, 1\n\ts_cbranch_vccz LP1_%=\n\t" \
  "s_mov_b64 exec, vcc\n\tglobal_store_dwordx4 v138, v[84:87], %[cp] sc1\n\tglobal_store_dwordx4 v138, v[88:91], %[cp] offset:16 sc1\n\t" \
  "global_store_dwordx2 v138, v[92:93], %[cp] offset:32 sc1\n\tds_write_b32 v137, v136 offset:24\n\tLP1_%=:\n\ts_mov_b32 exec_lo, 0x00200002\n\t" \
  "s_mov_b32 exec_hi, 0x20000200\n\tv_mov_b32 v100, v101\n\ts_lshl_b64 exec, exec, 1\n\tv_mov_b32 v100, v102\n\ts_lshl_b64 exec, exec, 1\n\t" \
  "v_mov_b32 v100, v103\n\ts_mov_b32 exec_lo, 0x00f0000f\n\ts_mov_b32 exec_hi, 0xf0000f00\n\tv_mov_b32 v98, v100\n\t" \
  "global_store_dwordx4 %[ta], v[96:99], %[sst] sc1\n\tds_write_b128 %[tk], v[96:99]\n\ts_mov_b64 exec, -1\n\tds_write_b64 %[ra], v[84:85] offset:0\n\t" \
  "ds_write_b64 %[ra], v[86:87] offset:8\n\tds_write_b64 %[ra], v[88:89] offset:16\n\tds_write_b64 %[ra], v[90:91] offset:24\n\t" \
  "ds_write_b64 %[ra], v[92:93] offset:32\n\ts_mov_b64 exec, 1\n\tds_add_u32 v137, v136\n\ts_mov_b64 exec, -1\n\ts_setprio 0\n\ts_branch LZ_%=\n\t" \
  "LD2_%=:\n\ts_sub_u32 s43, s43, 1\n\ts_cbranch_scc1 LT_%=\n\tds_read_b32 v97, v96\n\tds_read_b128 v[44:47], %[tk] offset:0\n\t" \
  "ds_read_b128 v[48:51], %[tk] offset:1024\n\tds_read_b128 v[52:55], %[tk] offset:2048\n\tds_read_b128 v[56:59], %[tk] offset:3072\n\t" \
  "ds_read_b128 v[60:63], %[tk] offset:4096\n\ts_branch LD1_%=\n\tLH_%=:\n\ts_sleep 3\n\tglobal_load_dwordx4 v[140:143], %[vk], %[oa1] sc1\n\t" \
  "global_load_dwordx4 v[144:147], %[vk], %[oa2] sc1\n\tglobal_load_dwordx4 v[148:151], %[vk], %[oa3] sc1\n\t" \
  "global_load_dwordx4 v[152:155], %[vk], %[oa4] sc1\n\tglobal_load_dwordx4 v[156:159], %[vk], %[oa0] sc1\n\ts_cmp_eq_u32 %[w], 3\n\t" \
  "s_cbranch_scc0 LG_%=\n\tds_read_b64 v[112:113], %[ra] offset:0\n\tds_read_b64 v[114:115], %[ra] offset:8\n\t" \
  "ds_read_b64 v[116:117], %[ra] offset:16\n\tds_read_b64 v[118:119], %[ra] offset:24\n\tds_read_b64 v[120:121], %[ra] offset:32\n\t" \
  "ds_read2_b64 v[124:127], %[aw] offset0:0 offset1:1\n\tds_read2_b64 v[128:131], %[aw] offset0:2 offset1:3\n\t" \
  "ds_read_b64 v[132:133], %[aw] offset:32\n\tv_mov_b64 v[134:135], 0\n\ts_waitcnt lgkmcnt(3)\n\tv_lshlrev_b32 v24, 16, v112\n\t" \
  "v_and_b32 v25, 0xffff0000, v112\n\tv_lshlrev_b32 v26, 16, v113\n\tv_and_b32 v27, 0xffff0000, v113\n\tv_lshlrev_b32 v28, 16, v114\n\t" \
  "v_and_b32 v29, 0xffff0000, v114\n\tv_lshlrev_b32 v30, 16, v115\n\tv_and_b32 v31, 0xffff0000, v115\n\tv_lshlrev_b32 v32, 16, v116\n\t" \
  "v_and_b32 v33, 0xffff0000, v116\n\tv_lshlrev_b32 v34, 16, v117\n\tv_and_b32 v35, 0xffff0000, v117\n\tv_lshlrev_b32 v36, 16, v118\n\t" \
  "v_and_b32 v37, 0xffff0000, v118\n\tv_lshlrev_b32 v38, 16, v119\n\tv_and_b32 v39, 0xffff0000, v119\n\tv_lshlrev_b32 v40, 16, v120\n\t" \
  "v_and_b32 v41, 0xffff0000, v120\n\tv_lshlrev_b32 v42, 16, v121\n\tv_and_b32 v43, 0xffff0000, v121\n\ts_branch LG2_%=\n\tLG_%=:\n\t" \
  "v_mov_b64 v[24:25], 0\n\tv_mov_b64 v[26:27], 0\n\tv_mov_b64 v[28:29], 0\n\tv_mov_b64 v[30:31], 0\n\tv_mov_b64 v[32:33], 0\n\tv_mov_b64 v[34:35], 0\n\t" \
  "v_mov_b64 v[36:37], 0\n\tv_mov_b64 v[38:39], 0\n\tv_mov_b64 v[40:41], 0\n\tv_mov_b64 v[42:43], 0\n\tLG2_%=:\n\tv_add_u32 %[ta], %[so1], %[pm]\n\t" \
  "ds_read_b64_tr_b16 v[64:65], %[ta] offset:0\n\tds_read_b64_tr_b16 v[66:67], %[ta] offset:3200\n\tds_read_b64_tr_b16 v[68:69], %[ta] offset:8\n\t" \
  "ds_read_b64_tr_b16 v[70:71], %[ta] offset:3208\n\tds_read_b64_tr_b16 v[72:73], %[ta] offset:16\n\tds_read_b64_tr_b16 v[74:75], %[ta] offset:3216\n\t" \
  "ds_read_b64_tr_b16 v[76:77], %[ta] offset:24\n\tds_read_b64_tr_b16 v[78:79], %[ta] offset:3224\n\tds_read_b64_tr_b16 v[80:81], %[ta] offset:32\n\t" \
  "ds_read_b64_tr_b16 v[82:83], %[ta] offset:3232\n\tv_add_u32 %[ta], %[so2], %[pm]\n\tds_read_b64_tr_b16 v[84:85], %[ta] offset:0\n\t" \
  "ds_read_b64_tr_b16 v[86:87], %[ta] offset:3200\n\tds_read_b64_tr_b16 v[88:89], %[ta] offset:8\n\tds_read_b64_tr_b16 v[90:91], %[ta] offset:3208\n\t" \
  "ds_read_b64_tr_b16 v[92:93], %[ta] offset:16\n\tds_read_b64_tr_b16 v[94:95], %[ta] offset:3216\n\tds_read_b64_tr_b16 v[96:97], %[ta] offset:24\n\t" \
  "ds_read_b64_tr_b16 v[98:99], %[ta] offset:3224\n\tds_read_b64_tr_b16 v[100:101], %[ta] offset:32\n\t" \
  "ds_read_b64_tr_b16 v[102:103], %[ta] offset:3232\n\ts_waitcnt vmcnt(4)\n\tv_cmp_eq_u32 vcc, -1, v140\n\ts_cbranch_vccnz LSh1_%=\n\tLQh1_%=:\n\t" \
  "s_waitcnt lgkmcnt(10)\n\tv_mfma_f32_16x16x32_bf16 v[24:27], v[64:67], v[140:143], v[24:27]\n\tv_add_u32 %[ta], %[so3], %[pm]\n\t" \
  "ds_read_b64_tr_b16 v[104:105], %[ta] offset:0\n\tds_read_b64_tr_b16 v[106:107], %[ta] offset:3200\n\t" \
  "v_mfma_f32_16x16x32_bf16 v[28:31], v[68:71], v[140:143], v[28:31]\n\tds_read_b64_tr_b16 v[108:109], %[ta] offset:8\n\t" \
  "ds_read_b64_tr_b16 v[110:111], %[ta] offset:3208\n\tv_mfma_f32_16x16x32_bf16 v[32:35], v[72:75], v[140:143], v[32:35]\n\t" \
  "ds_read_b64_tr_b16 v[112:113], %[ta] offset:16\n\tds_read_b64_tr_b16 v[114:115], %[ta] offset:3216\n\t" \
  "v_mfma_f32_16x16x32_bf16 v[36:39], v[76:79], v[140:143], v[36:39]\n\tds_read_b64_tr_b16 v[116:117], %[ta] offset:24\n\t" \
  "ds_read_b64_tr_b16 v[118:119], %[ta] offset:3224\n\ts_waitcnt vmcnt(3)\n\tv_cmp_eq_u32 vcc, -1, v144\n\t" \
  "v_mfma_f32_16x16x32_bf16 v[40:43], v[80:83], v[140:143], v[40:43]\n\tds_read_b64_tr_b16 v[120:121], %[ta] offset:32\n\t" \
  "ds_read_b64_tr_b16 v[122:123], %[ta] offset:3232\n\ts_cbranch_vccnz LSh2_%=\n\tLQh2_%=:\n\ts_waitcnt lgkmcnt(10)\n\t" \
  "v_mfma_f32_16x16x32_bf16 v[24:27], v[84:87], v[144:147], v[24:27]\n\tv_add_u32 %[ta], %[so4], %[pf]\n\tds_read_b64_tr_b16 v[64:65], %[ta] offset:0\n\t" \
  "ds_read_b64_tr_b16 v[66:67], %[ta] offset:12800\n\tv_mfma_f32_16x16x32_bf16 v[28:31], v[88:91], v[144:147], v[28:31]\n\t" \
  "ds_read_b64_tr_b16 v[68:69], %[ta] offset:8\n\tds_read_b64_tr_b16 v[70:71], %[ta] offset:12808\n\t" \
  "v_mfma_f32_16x16x32_bf16 v[32:35], v[92:95], v[144:147], v[32:35]\n\tds_read_b64_tr_b16 v[72:73], %[ta] offset:16\n\t" \
  "ds_read_b64_tr_b16 v[74:75], %[ta] offset:12816\n\tv_mfma_f32_16x16x32_bf16 v[36:39], v[96:99], v[144:147], v[36:39]\n\t" \
  "ds_read_b64_tr_b16 v[76:77], %[ta] offset:24\n\tds_read_b64_tr_b16 v[78:79], %[ta] offset:12824\n\ts_waitcnt vmcnt(2)\n\t" \
  "v_cmp_eq_u32 vcc, -1, v148\n\tv_mfma_f32_16x16x32_bf16 v[40:43], v[100:103], v[144:147], v[40:43]\n\tds_read_b64_tr_b16 v[80:81], %[ta] offset:32\n\t" \
  "ds_read_b64_tr_b16 v[82:83], %[ta] offset:12832\n\ts_cbranch_vccnz LSh3_%=\n\tLQh3_%=:\n\ts_waitcnt lgkmcnt(10)\n\t" \
  "v_mfma_f32_16x16x32_bf16 v[24:27], v[104:107], v[148:151], v[24:27]\n\tv_add_u32 %[ta], %[so0], %[pm]\n\t" \
  "ds_read_b64_tr_b16 v[84:85], %[ta] offset:0\n\tds_read_b64_tr_b16 v[86:87], %[ta] offset:3200\n\t" \
  "v_mfma_f32_16x16x32_bf16 v[28:31], v[108:111], v[148:151], v[28:31]\n\tds_read_b64_tr_b16 v[88:89], %[ta] offset:8\n\t" \
  "ds_read_b64_tr_b16 v[90:91], %[ta] offset:3208\n\tv_mfma_f32_16x16x32_bf16 v[32:35], v[112:115], v[148:151], v[32:35]\n\t" \
  "ds_read_b64_tr_b16 v[92:93], %[ta] offset:16\n\tds_read_b64_tr_b16 v[94:95], %[ta] offset:3216\n\t" \
  "v_mfma_f32_16x16x32_bf16 v[36:39], v[116:119], v[148:151], v[36:39]\n\tds_read_b64_tr_b16 v[96:97], %[ta] offset:24\n\t" \
  "ds_read_b64_tr_b16 v[98:99], %[ta] offset:3224\n\ts_waitcnt vmcnt(1)\n\tv_max_u32 %[tk], v152, v154\n\tv_cmp_eq_u32 vcc, -1, %[tk]\n\t" \
  "v_mfma_f32_16x16x32_bf16 v[40:43], v[120:123], v[148:151], v[40:43]\n\tds_read_b64_tr_b16 v[100:101], %[ta] offset:32\n\t" \
  "ds_read_b64_tr_b16 v[102:103], %[ta] offset:3232\n\ts_cbranch_vccnz LSh4_%=\n\tLQh4_%=:\n\ts_waitcnt lgkmcnt(10)\n\t" \
  "v_mfma_f32_16x16x32_bf16 v[24:27], v[64:67], v[152:155], v[24:27]\n\tv_mfma_f32_16x16x32_bf16 v[28:31], v[68:71], v[152:155], v[28:31]\n\t" \
  "v_mfma_f32_16x16x32_bf16 v[32:35], v[72:75], v[152:155], v[32:35]\n\tv_mfma_f32_16x16x32_bf16 v[36:39], v[76:79], v[152:155], v[36:39]\n\t" \
  "s_waitcnt vmcnt(0)\n\tv_cmp_eq_u32 vcc, -1, v156\n\tv_mfma_f32_16x16x32_bf16 v[40:43], v[80:83], v[152:155], v[40:43]\n\ts_cbranch_vccnz LSh0_%=\n\t" \
  "LQh0_%=:\n\ts_waitcnt lgkmcnt(0)\n\tv_mfma_f32_16x16x32_bf16 v[24:27], v[84:87], v[156:159], v[24:27]\n\t" \
  "v_mfma_f32_16x16x32_bf16 v[28:31], v[88:91], v[156:159], v[28:31]\n\tv_mfma_f32_16x16x32_bf16 v[32:35], v[92:95], v[156:159], v[32:35]\n\t" \
  "v_mfma_f32_16x16x32_bf16 v[36:39], v[96:99], v[156:159], v[36:39]\n\tv_mfma_f32_16x16x32_bf16 v[40:43], v[100:103], v[156:159], v[40:43]\n\t" \
  "s_cmp_eq_u32 %[w], 3\n\ts_cbranch_scc1 LF_%=\n\ts_setprio 3\n\tv_add_u32 %[ta], %[sd], %[l16]\n\tv_mov_b32 v137, %[sc]\n\tv_mov_b32 v136, 1\n\t" \
  "ds_write_b128 %[ta], v[24:27] offset:0\n\tds_write_b128 %[ta], v[28:31] offset:1024\n\tds_write_b128 %[ta], v[32:35] offset:2048\n\t" \
  "ds_write_b128 %[ta], v[36:39] offset:3072\n\tds_write_b128 %[ta], v[40:43] offset:4096\n\ts_mov_b64 exec, 1\n\tds_add_u32 v137, v136\n\t" \
  "s_mov_b64 exec, -1\n\ts_branch LZ_%=\n\tLF_%=:\n\ts_setprio 3\n\tv_mov_b64 v[94:95], 0\n\tv_mov_b32 v96, %[sc]\n\tv_add_u32 %[ta], %[sr], %[l16]\n\t" \
  "LF1_%=:\n\tds_read_b32 v97, v96\n\ts_waitcnt lgkmcnt(0)\n\tv_cmp_gt_u32 vcc, 3, v97\n\ts_cbranch_vccz LF2_%=\n\ts_sleep 1\n\ts_sub_u32 s43, s43, 1\n\t" \
  "s_cbranch_scc0 LF1_%=\n\tLF2_%=:\n\tds_read_b128 v[64:67], %[ta] offset:0\n\tds_read_b128 v[68:71], %[ta] offset:1024\n\t" \
  "ds_read_b128 v[72:75], %[ta] offset:2048\n\tds_read_b128 v[76:79], %[ta] offset:3072\n\tds_read_b128 v[80:83], %[ta] offset:4096\n\t" \
  "ds_read_b128 v[104:107], %[ta] offset:5120\n\tds_read_b128 v[108:111], %[ta] offset:6144\n\tds_read_b128 v[112:115], %[ta] offset:7168\n\t" \
  "ds_read_b128 v[116:119], %[ta] offset:8192\n\tds_read_b128 v[120:123], %[ta] offset:9216\n\tds_read_b128 v[44:47], %[ta] offset:10240\n\t" \
  "ds_read_b128 v[48:51], %[ta] offset:11264\n\tds_read_b128 v[52:55], %[ta] offset:12288\n\tds_read_b128 v[56:59], %[ta] offset:13312\n\t" \
  "ds_read_b128 v[60:63], %[ta] offset:14336\n\ts_waitcnt lgkmcnt(10)\n\tv_pk_add_f32 v[24:25], v[24:25], v[64:65]\n\t" \
  "v_pk_add_f32 v[26:27], v[26:27], v[66:67]\n\tv_pk_add_f32 v[28:29], v[28:29], v[68:69]\n\tv_pk_add_f32 v[30:31], v[30:31], v[70:71]\n\t" \
  "v_pk_add_f32 v[32:33], v[32:33], v[72:73]\n\tv_pk_add_f32 v[34:35], v[34:35], v[74:75]\n\tv_pk_add_f32 v[36:37], v[36:37], v[76:77]\n\t" \
  "v_pk_add_f32 v[38:39], v[38:39], v[78:79]\n\tv_pk_add_f32 v[40:41], v[40:41], v[80:81]\n\tv_pk_add_f32 v[42:43], v[42:43], v[82:83]\n\t" \
  "s_waitcnt lgkmcnt(5)\n\tv_pk_add_f32 v[24:25], v[24:25], v[104:105]\n\tv_pk_add_f32 v[26:27], v[26:27], v[106:107]\n\t" \
  "v_pk_add_f32 v[28:29], v[28:29], v[108:109]\n\tv_pk_add_f32 v[30:31], v[30:31], v[110:111]\n\tv_pk_add_f32 v[32:33], v[32:33], v[112:113]\n\t" \
  "v_pk_add_f32 v[34:35], v[34:35], v[114:115]\n\tv_pk_add_f32 v[36:37], v[36:37], v[116:117]\n\tv_pk_add_f32 v[38:39], v[38:39], v[118:119]\n\t" \
  "v_pk_add_f32 v[40:41], v[40:41], v[120:121]\n\tv_pk_add_f32 v[42:43], v[42:43], v[122:123]\n\ts_waitcnt lgkmcnt(0)\n\ts_branch LT_%=\n\tLSs1_%=:\n\t" \
  "s_sub_u32 s43, s43, 1\n\ts_cbranch_scc1 LQs1_%=\n\ts_sleep %[oback]\n\tglobal_load_dwordx4 v[124:127], %[vd], %[oa1] sc1\n\t" \
  "global_load_dwordx4 v[128:131], %[vk], %[oa1] sc1\n\ts_waitcnt vmcnt(0)\n\tv_max_u32 %[tk], v124, v128\n\tv_cmp_eq_u32 vcc, -1, %[tk]\n\t" \
  "s_cbranch_vccnz LSs1_%=\n\ts_branch LQs1_%=\n\tLSs2_%=:\n\ts_sub_u32 s43, s43, 1\n\ts_cbranch_scc1 LQs2_%=\n\ts_sleep %[oback]\n\t" \
  "global_load_dwordx4 v[132:135], %[vd], %[oa2] sc1\n\tglobal_load_dwordx4 v[136:139], %[vk], %[oa2] sc1\n\ts_waitcnt vmcnt(0)\n\t" \
  "v_max_u32 %[tk], v132, v136\n\tv_cmp_eq_u32 vcc, -1, %[tk]\n\ts_cbranch_vccnz LSs2_%=\n\ts_branch LQs2_%=\n\tLSs3_%=:\n\ts_sub_u32 s43, s43, 1\n\t" \
  "s_cbranch_scc1 LQs3_%=\n\ts_sleep %[oback]\n\tglobal_load_dwordx4 v[140:143], %[vd], %[oa3] sc1\n\t" \
  "global_load_dwordx4 v[144:147], %[vk], %[oa3] sc1\n\ts_waitcnt vmcnt(0)\n\tv_max_u32 %[tk], v140, v144\n\tv_cmp_eq_u32 vcc, -1, %[tk]\n\t" \
  "s_cbranch_vccnz LSs3_%=\n\ts_branch LQs3_%=\n\tLSs4_%=:\n\ts_sub_u32 s43, s43, 1\n\ts_cbranch_scc1 LQs4_%=\n\ts_sleep %[oback]\n\t" \
  "global_load_dwordx4 v[148:151], %[vd], %[oa4] sc1\n\tglobal_load_dwordx4 v[152:155], %[vk], %[oa4] sc1\n\ts_waitcnt vmcnt(0)\n\t" \
  "v_max3_u32 %[tk], v148, v150, v152\n\tv_max_u32 %[tk], %[tk], v154\n\tv_cmp_eq_u32 vcc, -1, %[tk]\n\ts_cbranch_vccnz LSs4_%=\n\ts_branch LQs4_%=\n\t" \
  "LSs5_%=:\n\ts_sub_u32 s43, s43, 1\n\ts_cbranch_scc1 LQs5_%=\n\ts_sleep %[oback]\n\tglobal_load_dwordx4 v[156:159], %[vd], %[oa5] sc1\n\t" \
  "global_load_dwordx4 v[124:127], %[vk], %[oa5] sc1\n\ts_waitcnt vmcnt(0)\n\tv_max_u32 %[tk], v156, v124\n\tv_cmp_eq_u32 vcc, -1, %[tk]\n\t" \
  "s_cbranch_vccnz LSs5_%=\n\ts_branch LQs5_%=\n\tLSs6_%=:\n\ts_sub_u32 s43, s43, 1\n\ts_cbranch_scc1 LQs6_%=\n\ts_sleep %[oback]\n\t" \
  "global_load_dwordx4 v[128:131], %[vd], %[oa6] sc1\n\tglobal_load_dwordx4 v[132:135], %[vk], %[oa6] sc1\n\ts_waitcnt vmcnt(0)\n\t" \
  "v_max_u32 %[tk], v128, v132\n\tv_cmp_eq_u32 vcc, -1, %[tk]\n\ts_cbranch_vccnz LSs6_%=\n\ts_branch LQs6_%=\n\tLSd7_%=:\n\ts_sub_u32 s43, s43, 1\n\t" \
  "s_cbranch_scc1 LQd7_%=\n\ts_sleep %[oback]\n\tglobal_load_dwordx4 v[136:139], %[vd], %[oa7] sc1\n\t" \
  "global_load_dwordx4 v[140:143], %[vd], %[oa8] sc1\n\ts_waitcnt vmcnt(0)\n\tv_max_u32 %[tk], v136, v140\n\tv_cmp_eq_u32 vcc, -1, %[tk]\n\t" \
  "s_cbranch_vccnz LSd7_%=\n\ts_branch LQd7_%=\n\tLSd9_%=:\n\ts_sub_u32 s43, s43, 1\n\ts_cbranch_scc1 LQd9_%=\n\ts_sleep %[oback]\n\t" \
  "global_load_dwordx4 v[144:147], %[vd], %[oa9] sc1\n\tglobal_load_dwordx4 v[148:151], %[vk], %[oa7] sc1\n\ts_waitcnt vmcnt(0)\n\t" \
  "v_max3_u32 %[tk], v144, v146, v148\n\tv_cmp_eq_u32 vcc, -1, %[tk]\n\ts_cbranch_vccnz LSd9_%=\n\ts_branch LQd9_%=\n\tLSk8_%=:\n\t" \
  "s_sub_u32 s43, s43, 1\n\ts_cbranch_scc1 LQk8_%=\n\ts_sleep %[oback]\n\tglobal_load_dwordx4 v[152:155], %[vk], %[oa8] sc1\n\t" \
  "global_load_dwordx4 v[156:159], %[vk], %[oa9] sc1\n\ts_waitcnt vmcnt(0)\n\tv_max3_u32 %[tk], v152, v156, v158\n\tv_cmp_eq_u32 vcc, -1, %[tk]\n\t" \
  "s_cbranch_vccnz LSk8_%=\n\ts_branch LQk8_%=\n\tLSh1_%=:\n\ts_sub_u32 s43, s43, 1\n\ts_cbranch_scc1 LQh1_%=\n\ts_sleep %[oback]\n\t" \
  "global_load_dwordx4 v[140:143], %[vk], %[oa1] sc1\n\ts_waitcnt vmcnt(0)\n\tv_cmp_eq_u32 vcc, -1, v140\n\ts_cbranch_vccnz LSh1_%=\n\t" \
  "s_branch LQh1_%=\n\tLSh2_%=:\n\ts_sub_u32 s43, s43, 1\n\ts_cbranch_scc1 LQh2_%=\n\ts_sleep %[oback]\n\t" \
  "global_load_dwordx4 v[144:147], %[vk], %[oa2] sc1\n\ts_waitcnt vmcnt(0)\n\tv_cmp_eq_u32 vcc, -1, v144\n\ts_cbranch_vccnz LSh2_%=\n\t" \
  "s_branch LQh2_%=\n\tLSh3_%=:\n\ts_sub_u32 s43, s43, 1\n\ts_cbranch_scc1 LQh3_%=\n\ts_sleep %[oback]\n\t" \
  "global_load_dwordx4 v[148:151], %[vk], %[oa3] sc1\n\ts_waitcnt vmcnt(0)\n\tv_cmp_eq_u32 vcc, -1, v148\n\ts_cbranch_vccnz LSh3_%=\n\t" \
  "s_branch LQh3_%=\n\tLSh4_%=:\n\ts_sub_u32 s43, s43, 1\n\ts_cbranch_scc1 LQh4_%=\n\ts_sleep %[oback]\n\t" \
  "global_load_dwordx4 v[152:155], %[vk], %[oa4] sc1\n\ts_waitcnt vmcnt(0)\n\tv_max_u32 %[tk], v152, v154\n\tv_cmp_eq_u32 vcc, -1, %[tk]\n\t" \
  "s_cbranch_vccnz LSh4_%=\n\ts_branch LQh4_%=\n\tLSh0_%=:\n\ts_sub_u32 s43, s43, 1\n\ts_cbranch_scc1 LQh0_%=\n\ts_sleep %[oback]\n\t" \
  "global_load_dwordx4 v[156:159], %[vk], %[oa0] sc1\n\ts_waitcnt vmcnt(0)\n\tv_cmp_eq_u32 vcc, -1, v156\n\ts_cbranch_vccnz LSh0_%=\n\t" \
  "s_branch LQh0_%=\n\tLZ_%=:\n\t" \
  ""
#define COL_ASM \
  "s_mov_b32 s43, %[cap]\n\tv_xor_b32 v101, 0x400, %[lq]\n\tv_xor_b32 v102, 0x800, %[lq]\n\tv_xor_b32 v103, 0xc00, %[lq]\n\t" \
  "v_xor_b32 v104, 0x1000, %[lq]\n\tv_xor_b32 v105, 0x1400, %[lq]\n\tv_xor_b32 v106, 0x1800, %[lq]\n\tv_xor_b32 v107, 0x1c00, %[lq]\n\tLC0_%=:\n\t" \
  "ds_read_b32 v96, %[ac]\n\ts_waitcnt lgkmcnt(0)\n\tv_cmp_gt_u32 vcc, %[nf], v96\n\ts_cbranch_vccz LC1_%=\n\ts_sleep 1\n\ts_sub_u32 s43, s43, 1\n\t" \
  "s_cbranch_scc0 LC0_%=\n\tLC1_%=:\n\ts_setprio 2\n\ts_sleep %[pdel]\n\tglobal_load_dwordx4 v[68:71], v101, %[sp] sc1\n\t" \
  "global_load_dwordx4 v[72:75], v102, %[sp] sc1\n\tglobal_load_dwordx4 v[76:79], v103, %[sp] sc1\n\tglobal_load_dwordx4 v[80:83], v104, %[sp] sc1\n\t" \
  "global_load_dwordx4 v[84:87], v105, %[sp] sc1\n\tglobal_load_dwordx4 v[88:91], v106, %[sp] sc1\n\tglobal_load_dwordx4 v[92:95], v107, %[sp] sc1\n\t" \
  "ds_read_b128 v[64:67], %[ao]\n\ts_waitcnt lgkmcnt(0)\n\tLC2_%=:\n\ts_waitcnt vmcnt(4)\n\tv_pk_add_f32 v[108:109], v[64:65], v[68:69]\n\t" \
  "v_add_f32 v110, v66, v70\n\tv_pk_add_f32 v[112:113], v[72:73], v[76:77]\n\tv_add_f32 v114, v74, v78\n\t" \
  "v_pk_add_f32 v[108:109], v[108:109], v[112:113]\n\tv_add_f32 v110, v110, v114\n\ts_waitcnt vmcnt(0)\n\tv_pk_add_f32 v[116:117], v[80:81], v[84:85]\n\t" \
  "v_add_f32 v118, v82, v86\n\tv_pk_add_f32 v[120:121], v[88:89], v[92:93]\n\tv_add_f32 v122, v90, v94\n\t" \
  "v_pk_add_f32 v[116:117], v[116:117], v[120:121]\n\tv_add_f32 v118, v118, v122\n\tv_pk_add_f32 v[108:109], v[108:109], v[116:117]\n\t" \
  "v_add_f32 v110, v110, v118\n\tv_mul_f32 v112, 0x3acccccd, v108\n\tv_add_f32 v111, v108, v109\n\tv_mul_f32 v113, v112, v112\n\t" \
  "v_cmp_u_f32 vcc, v111, v110\n\tv_fma_f32 v113, v110, %[rdm], -v113\n\ts_cbranch_vccnz LC5_%=\n\tLC3_%=:\n\tv_max_f32 v113, 0, v113\n\t" \
  "v_cvt_pk_bf16_f32 v113, 0, v113\n\tv_add_f32 v113, 0x3727c5ac, v113\n\tv_cvt_pk_bf16_f32 v113, 0, v113\n\tv_sqrt_f32 v113, v113\n\t" \
  "v_cvt_pk_bf16_f32 v112, 0, v112\n\tv_cvt_pk_bf16_f32 v113, 0, v113\n\tv_rcp_f32 v113, v113\n\tv_fma_f32 v114, -%[swr], v112, v109\n\t" \
  "v_mul_f32 v114, v114, v113\n\tv_ashrrev_i32 v115, 31, v114\n\tv_or_b32 v115, 0x80000000, v115\n\tv_xor_b32 v114, v114, v115\n\t" \
  "v_and_or_b32 v114, v114, %[msk], %[rid]\n\ts_nop 1\n\tv_max_u32_dpp v114, v114, v114 quad_perm:[1,0,3,2] row_mask:0xf bank_mask:0xf bound_ctrl:1\n\t" \
  "ds_write_b64 %[at], v[112:113]\n\ts_nop 0\n\tv_max_u32_dpp v114, v114, v114 quad_perm:[2,3,0,1] row_mask:0xf bank_mask:0xf bound_ctrl:1\n\ts_nop 1\n\t" \
  "v_max_u32_dpp v114, v114, v114 row_half_mirror row_mask:0xf bank_mask:0xf bound_ctrl:1\n\ts_nop 1\n\t" \
  "v_max_u32_dpp v114, v114, v114 row_mirror row_mask:0xf bank_mask:0xf bound_ctrl:1\n\ts_nop 0\n\tv_readlane_b32 s40, v114, 0\n\t" \
  "v_readlane_b32 s41, v114, 16\n\tv_readlane_b32 s42, v114, 32\n\ts_max_u32 s40, s40, s41\n\ts_max_u32 s40, s40, s42\n\tv_mov_b32 v98, s40\n\t" \
  "s_mov_b32 exec_lo, 0x01010101\n\ts_mov_b32 exec_hi, 0x0101\n\tglobal_store_dwordx2 %[zr], v[98:99], %[kp] sc1\n\tds_write_b32 %[ak], v98\n\t" \
  "s_mov_b64 exec, -1\n\ts_and_b32 s41, s40, 0x7ff\n\ts_sub_u32 s41, %[rb], s41\n\ts_mul_i32 s42, s41, 160\n\tv_add_u32 v115, s42, %[ah]\n\t" \
  "ds_read_b64 v[116:117], v115\n\tv_readlane_b32 s44, v112, s41\n\tv_readlane_b32 s46, v113, s41\n\ts_waitcnt lgkmcnt(0)\n\ts_mov_b32 exec_hi, 0\n\t" \
  "v_lshlrev_b32 v118, 16, v116\n\tv_and_b32 v119, 0xffff0000, v116\n\tv_lshlrev_b32 v120, 16, v117\n\tv_and_b32 v121, 0xffff0000, v117\n\t" \
  "v_pk_add_f32 v[122:123], v[118:119], s[44:45] op_sel_hi:[1,0] neg_lo:[0,1] neg_hi:[0,1]\n\t" \
  "v_pk_add_f32 v[114:115], v[120:121], s[44:45] op_sel_hi:[1,0] neg_lo:[0,1] neg_hi:[0,1]\n\t" \
  "v_pk_fma_f32 v[118:119], v[122:123], s[46:47], v[118:119] op_sel_hi:[1,0,1]\n\t" \
  "v_pk_fma_f32 v[120:121], v[114:115], s[46:47], v[120:121] op_sel_hi:[1,0,1]\n\tv_cvt_pk_bf16_f32 v116, v118, v119\n\t" \
  "v_cvt_pk_bf16_f32 v117, v120, v121\n\ts_mov_b32 exec_hi, -1\n\tglobal_store_dwordx2 %[co], v[116:117], %[cp] sc1\n\ts_branch LC9_%=\n\tLC5_%=:\n\t" \
  "s_sub_u32 s43, s43, 1\n\ts_cbranch_scc1 LC3_%=\n\ts_mov_b32 s40, 0\n\tv_max3_u32 v96, v68, v69, v70\n\tv_cmp_eq_u32 vcc, -1, v96\n\t" \
  "s_cbranch_vccz LC61_%=\n\tglobal_load_dwordx4 v[68:71], v101, %[sp] sc1\n\ts_mov_b32 s40, 1\n\tLC61_%=:\n\tv_max3_u32 v96, v72, v73, v74\n\t" \
  "v_cmp_eq_u32 vcc, -1, v96\n\ts_cbranch_vccz LC62_%=\n\tglobal_load_dwordx4 v[72:75], v102, %[sp] sc1\n\ts_mov_b32 s40, 1\n\tLC62_%=:\n\t" \
  "v_max3_u32 v96, v76, v77, v78\n\tv_cmp_eq_u32 vcc, -1, v96\n\ts_cbranch_vccz LC63_%=\n\tglobal_load_dwordx4 v[76:79], v103, %[sp] sc1\n\t" \
  "s_mov_b32 s40, 1\n\tLC63_%=:\n\tv_max3_u32 v96, v80, v81, v82\n\tv_cmp_eq_u32 vcc, -1, v96\n\ts_cbranch_vccz LC64_%=\n\t" \
  "global_load_dwordx4 v[80:83], v104, %[sp] sc1\n\ts_mov_b32 s40, 1\n\tLC64_%=:\n\tv_max3_u32 v96, v84, v85, v86\n\tv_cmp_eq_u32 vcc, -1, v96\n\t" \
  "s_cbranch_vccz LC65_%=\n\tglobal_load_dwordx4 v[84:87], v105, %[sp] sc1\n\ts_mov_b32 s40, 1\n\tLC65_%=:\n\tv_max3_u32 v96, v88, v89, v90\n\t" \
  "v_cmp_eq_u32 vcc, -1, v96\n\ts_cbranch_vccz LC66_%=\n\tglobal_load_dwordx4 v[88:91], v106, %[sp] sc1\n\ts_mov_b32 s40, 1\n\tLC66_%=:\n\t" \
  "v_max3_u32 v96, v92, v93, v94\n\tv_cmp_eq_u32 vcc, -1, v96\n\ts_cbranch_vccz LC67_%=\n\tglobal_load_dwordx4 v[92:95], v107, %[sp] sc1\n\t" \
  "s_mov_b32 s40, 1\n\tLC67_%=:\n\ts_cmp_eq_u32 s40, 0\n\ts_cbranch_scc1 LC3_%=\n\ts_waitcnt vmcnt(0)\n\ts_branch LC2_%=\n\tLC9_%=:\n\t" \
  ""
extern "C" __global__ __launch_bounds__(768, 3) void fused_block(const u16* x, const u16* wqkv, const u16* wo, const u16* w_r,
                                                   const u16* w_gate, const u16* w_up, const u16* w_down, u16* out,
                                                   char* ws) {
  extern __shared__ __attribute__((aligned(16))) char smem[];
  const int tid = threadIdx.x;
  const int lane = tid & 63;
  const int w = __builtin_amdgcn_readfirstlane(tid >> 6);
  const int g = lane >> 4, r16 = lane & 15, q4 = (lane & 15) >> 2, p4 = lane & 3;
  const int bid = blockIdx.x;
  const int xcd = bid & 7;
  const int bidx = bid >> 3;
  const u32 l16 = lane * 16;
  if (w == 11) {
    u64 pc;
    asm volatile("s_getpc_b64 %0" : "=s"(pc));
    if (lane < 10) touch_line((const void*)(pc + min((bidx * 10 + lane) * 128, CODE_BYTES)));
  }

  if (w == 0 && lane == 0) {
    const int q = bidx & 3;
    touch_line(((q & 2) ? (const char*)x : (const char*)wqkv) + (q & 1) * 2359296 + bidx * 128);
  }
  if (bidx >= 26) {
    const int fb = xcd * 6 + (bidx - 26);
    const u32 tstart = (u32)__builtin_amdgcn_s_memrealtime();
    const bool hurry = ((((u32)(u64)w_gate + fb * 64) >> 7) & 7u) == 3u;
#define WAIT_UNTIL(T)                                                                      \
  _Pragma("nounroll") for (int it = 0; it < 1000; ++it) {                                  \
    if ((int)((u32)__builtin_amdgcn_s_memrealtime() - tstart) >= (T)) break;               \
    __builtin_amdgcn_s_sleep(4);                                                           \
  }
    if (w < 3) {
      int li = (bidx - 26) * 160 + w * 64 + lane;
      if (w < 2 || lane < 32) touch_line((const char*)x + (size_t)(((xcd >> 2) * 13 + 6) * 80 - 16) * 1280 + li * 128);
    }
    u32x2 ep;
    u32 tag0;
    asm volatile("global_load_dwordx2 %0, %1, off sc1" : "=v"(ep) : "v"(ws + WS_EPOCH) : "memory");
    {
      int wi = (((xcd >> 1) & 1) * 6 + (bidx - 26)) * 12 + w;
      wi = wi > 129 ? 129 : wi;
      int row = (xcd >> 2) * 1040 + (xcd & 1) * 520 + wi * 4 + g;
      row = row > SEQ - 1 ? SEQ - 1 : row;
      const u32 vo = row * 1280 + r16 * 16;
      u32x4 xr[5];
#pragma unroll
      for (int i = 0; i < 5; ++i)
        asm volatile("global_load_dwordx4 %0, %1, %2 offset:%3" : "=v"(xr[i]) : "v"(vo), "s"(x), "n"(i * 256) : "memory");
      asm volatile("s_waitcnt vmcnt(0)" ::: "memory");
      asm volatile("" : "+v"(xr[0]), "+v"(xr[1]), "+v"(xr[2]), "+v"(xr[3]), "+v"(xr[4]), "+v"(ep)::"memory");
      tag0 = ep[0] + (ep[0] == SENT - 1u ? 2u : 1u);
      float sm = 0.f;
#pragma unroll
      for (int i = 0; i < 5; ++i)
#pragma unroll
        for (int e = 0; e < 4; ++e) sm = dot2b(xr[i][e], 0x3F803F80u, sm);
      const float mean = row16_sum(sm) * (1.0f / DM);
      float ss = 0.f;
#pragma unroll
      for (int i = 0; i < 5; ++i)
#pragma unroll
        for (int e = 0; e < 4; ++e) {
          const float a = lo_f(xr[i][e]) - mean, b = hi_f(xr[i][e]) - mean;
          ss = __builtin_fmaf(a, a, ss);
          ss = __builtin_fmaf(b, b, ss);
        }
      const float var = row16_sum(ss) * (1.0f / DM);
      const float den = rbf(__builtin_amdgcn_sqrtf(rbf(rbf(var) + 1e-5f)));
      if (r16 == 0) GST2((u32)(row * 8), ws + WS_LN1, 0, ((u32x2){cvt2((f32x2){mean, den}), tag0}));
    }
    u32x4 va[2][5], vb[5];
    const int vrk = (fb >> 4) * 14 + (int)(((((u32)(u64)w_gate + fb * 64) >> 6) - 8u) & 15u);
    const int vq0 = vrk * 4 + w * 2;
    u32x4 ra;
    u32 rb5;
    asm volatile("global_load_dwordx4 %0, %2, %4\n\tglobal_load_dword %1, %3, %4 offset:1024"
                   : "=&v"(ra), "=&v"(rb5) : "v"(l16), "v"((u32)(lane * 4)), "s"(w_r) : "memory");
    if (w >= 2) {
      WAIT_UNTIL(hurry ? 0 : PDELAY + 12 * (w - 2))
      const u32x4 s4 = (u32x4){SENT, SENT, SENT, SENT};
      const char* fp = ws + WS_FILL + (size_t)fb * 73728 + (w - 2) * 7168 + 4096;
      GST4(l16, fp, -4096, s4); GST4(l16, fp, -3072, s4); GST4(l16, fp, -2048, s4); GST4(l16, fp, -1024, s4);
      GST4(l16, fp, 0, s4); GST4(l16, fp, 1024, s4); GST4(l16, fp, 2048, s4);
      if (w < 4) GST4(l16, ws + WS_FILL + (size_t)fb * 73728 + 69632 + w * 1024, 0, s4);
      if (w >= 5) GST4((u32)(((w - 5) * 8 + (lane >> 3)) * 6144 + (lane & 7) * 16), ws + WS_PK + fb * 128, 0, s4);
    }
    if (w == 0) {
      const u64 e1 = (((u64)ep[1] << 32) | ep[0]) + (ep[0] == SENT - 1u ? 2ull : 1ull);
      if (lane == 0) *(u32x4*)(smem + CTL_OFF) = (u32x4){(u32)e1, (u32)(e1 >> 32), 0u, 0u};
      if (lane >= 1 && lane < 8) *(u32x4*)(smem + CTL_OFF + lane * 16) = (u32x4){0u, 0u, 0u, 0u};
      if (lane >= 32) *(u32x4*)(smem + KEY_OFF - 512 + lane * 16) = (u32x4){0u, 0u, 0u, 0u};
    }
    if (w >= 6) {
      const u32 o1 = tid >= 636 ? 0x3F803F80u : 0u;
      if (tid < 720) *(u32x4*)(smem + HN_OFF + (tid - 384) * 16) = (u32x4){o1, o1, o1, o1};
    }
    if (w < 2 && !hurry && vrk < 40) {
      WAIT_UNTIL(VDELAY)
      const u32 vo = (vq0 * 4 + g) * 1280 + r16 * 16;
#pragma unroll
      for (int i = 0; i < 5; ++i)
        asm volatile("global_load_dwordx4 %0, %3, %4 offset:%8\n\tglobal_load_dwordx4 %1, %3, %5 offset:%8\n\tglobal_load_dwordx4 %2, %6, %7 offset:%8"
                     : "=&v"(va[0][i]), "=&v"(va[1][i]), "=&v"(vb[i]) : "v"(vo), "s"(wo), "s"((const char*)wo + 5120), "v"((u32)(r16 * 16)), "s"(w_r), "n"(i * 256) : "memory");
      asm volatile("s_waitcnt vmcnt(0)" : "+v"(va[0][0]), "+v"(va[0][1]), "+v"(va[0][2]), "+v"(va[0][3]), "+v"(va[0][4]), "+v"(va[1][0]), "+v"(va[1][1]),
                   "+v"(va[1][2]), "+v"(va[1][3]), "+v"(va[1][4]), "+v"(vb[0]), "+v"(vb[1]), "+v"(vb[2]), "+v"(vb[3]), "+v"(vb[4])::"memory");
      float sb = 0.f;
#pragma unroll
      for (int i = 0; i < 20; ++i) sb = dot2b(vb[i >> 2][i & 3], 0x3F803F80u, sb);
      sb = row16_sum(sb) * (1.0f / DM);
      u32x4 pk2[2];
#pragma unroll
      for (int k = 0; k < 2; ++k) {
        float d = 0.f, sa = 0.f;
#pragma unroll
        for (int i = 0; i < 20; ++i) {
          d = dot2b(va[k][i >> 2][i & 3], vb[i >> 2][i & 3], d);
          sa = dot2b(va[k][i >> 2][i & 3], 0x3F803F80u, sa);
        }
        const u32 vp = __float_as_uint(row16_sum(d - sa * sb));
        pk2[k] = (u32x4){cvt2((f32x2){__uint_as_float(__builtin_amdgcn_readlane(vp, 0)), __uint_as_float(__builtin_amdgcn_readlane(vp, 16))}), tag0,
                          cvt2((f32x2){__uint_as_float(__builtin_amdgcn_readlane(vp, 32)), __uint_as_float(__builtin_amdgcn_readlane(vp, 48))}), tag0};
      }
      if (lane < 8) GST4(l16, ws + WS_VP + vq0 * 64, 0, ((lane & 1) ? pk2[1] : pk2[0]));
    } else
      asm volatile("s_waitcnt vmcnt(0)" ::: "memory");
    asm volatile("" : "+v"(ra), "+v"(rb5)::"memory");
    LDS_BARRIER();
    const u32x2 tg = *(const u32x2*)(smem + CTL_OFF);
    const u32 tag = tg[0];
    if (w == 2 && lane == 0) GST2(0u, ws + WS_HFLAG + fb * 128, 0, tg);
    if (w >= 3) {
      int li = (bidx - 26) * 534 + (w - 3) * 64 + lane;
      li = li > 3199 ? 3199 : li;
      const int rr = li / 5;
      if (w < 11 || lane < 22) touch_line((const char*)wo + rr * 1280 + ((xcd >> 1) & 1) * 640 + (li - rr * 5) * 128);
    }
    WAIT_UNTIL(hurry ? 0 : FDELAY)
    asm volatile("s_barrier" ::: "memory");
    if (w >= 2) {
      const int ch = (lane & 7) ^ (((lane >> 4) & 1) * 2 + ((w + fb) & 1) * 4);
      const u32 vg = (lane >> 3) * (FF * 2) + fb * 64 + (ch & 3) * 16;
      const u64 gsel = (ch & 4) ? (u64)w_up : (u64)w_gate;
#pragma nounroll
      for (int n0 = w - 2; n0 < 80; n0 += 10) {
        const int n = (fb & 1) ? 79 - n0 : n0;
        const u64 p = gsel + vg + (u32)(n * 8 * FF * 2);
        asm volatile("s_mov_b32 m0, %1\n\ts_nop 0\n\tglobal_load_lds_dwordx4 %0, off" ::"v"(p), "s"(n * 1024) : "memory");
      }
#pragma nounroll
      for (int n = 11 - w; n < 40; n += 10) {
        const u32 row = (u32)(n * 64 + lane) / 80u;
        asm volatile("s_mov_b32 m0, %2\n\ts_nop 0\n\tglobal_load_lds_dwordx4 %0, %1" " nt"
                     ::"v"(l16 ^ ((row & 3u) * 32u + (row & 8u) * 16u)), "s"((const char*)w_down + (size_t)fb * 40960 + n * 1024), "s"(DOWN_OFF + n * 1024) : "memory");
      }
      asm volatile("s_waitcnt vmcnt(0)" ::: "memory");
      if (lane == 0) asm volatile("ds_add_u32 %0, %1" ::"v"(CTL_OFF + 48), "v"(1u) : "memory");
    } else {
      if (w == 1 && lane < 4)
        touch_line(lane == 3 ? ws + WS_PK - 65536 + bid * 128 : (const char*)out + lane * 1277952 + fb * 1280);
      WAIT_UNTIL(KSTART)
    }
    if (w >= 4) {
      const int v = w - 4, kq = v & 3, m = v >> 2;
#pragma nounroll
      for (int it = 0; it < SPIN_CAP; ++it) {
        if (__builtin_amdgcn_readfirstlane(lds_ld32(CTL_OFF + 48)) >= 10u) break;
        __builtin_amdgcn_s_sleep(1);
      }
      bf16x8 gfr[5], ufr[5], dfr[5];
      {
        const int gx = (160 * kq + 8 * g + q4) * 128 + ((m ^ (q4 >> 1) ^ ((g & 1) * 2)) * 32) + p4 * 8;
        const char* db = smem + DOWN_OFF + (8 * g + q4) * 1280 + p4 * 8;
#pragma unroll
        for (int j = 0; j < 5; ++j) {
          gfr[j] = tr_frag(smem + gx + j * 4096, 512);
          ufr[j] = tr_frag(smem + (gx ^ 64) + j * 4096, 512);
          dfr[j] = tr_frag(db + ((v * 160 + j * 32) ^ (q4 * 32 + (g & 1) * 128)), 5120);
        }
#pragma unroll
        for (int j = 0; j < 5; ++j) asm volatile("" : "+v"(gfr[j]), "+v"(ufr[j]), "+v"(dfr[j]));
      }
      u32 ahn = HN_OFF + p4 * HSL + kq * 320 + g * 16, asw = ACT_OFF + p4 * ASL + kq * 256 + (q4 & 1) * 128 + m * 64 + g * 16, asr = ACT_OFF + (g * 8 + (lane & 7)) * 4;
      u32 vpo = v * 320 + r16 * 64 + g * 16, up3 = (lane & 8) ? 3u : 0u;
      asm volatile("" : "+v"(up3));
      const char* pdst = ws + WS_PART + fb * 2560;
      asm volatile("" : "+v"(ahn), "+v"(asw), "+v"(asr), "+v"(vpo));
      asm volatile("" : "+s"(pdst));
      int pass = 0;
      asm volatile("" : "+s"(pass));
#pragma nounroll
      for (; pass < 2; ++pass) {
        asm volatile("s_barrier" ::: "memory");
        u32x4 bs;
#define GU(J, C0, C1) "v_mfma_f32_16x16x32_bf16 v[120:123], %[g" #J "], v[100+4*" #J ":103+4*" #J "], " C0 "\n\t" \
                      "v_mfma_f32_16x16x32_bf16 v[124:127], %[u" #J "], v[100+4*" #J ":103+4*" #J "], " C1 "\n\t"
#define GU1(J) GU(J, "v[120:123]", "v[124:127]")
        asm volatile("ds_read_b128 v[100:103], %[ah]\n\tds_read_b128 v[104:107], %[ah] offset:64\n\tds_read_b128 v[108:111], %[ah] offset:128\n\t"
                     "ds_read_b128 v[112:115], %[ah] offset:192\n\tds_read_b128 v[116:119], %[ah] offset:256\n\t"
                     "s_waitcnt lgkmcnt(4)\n\t" GU(0, "0", "0") "s_waitcnt lgkmcnt(3)\n\t" GU1(1) "s_waitcnt lgkmcnt(2)\n\t" GU1(2)
                     "s_waitcnt lgkmcnt(1)\n\t" GU1(3) "s_waitcnt lgkmcnt(0)\n\t" GU1(4)
                     "ds_read_b128 %[bs], %[ab]\n\ts_mov_b32 exec_lo, 0x00f000f0\n\ts_mov_b32 exec_hi, 0x00f000f0\n\ts_nop 4\n\t"
                     "v_mov_b64 v[120:121], v[124:125]\n\tv_mov_b64 v[122:123], v[126:127]\n\t"
                     "s_mov_b32 exec_lo, 0x00ff00ff\n\ts_mov_b32 exec_hi, 0x00ff00ff\n\tds_write_b128 %[aw], v[120:123]\n\ts_mov_b64 exec, -1\n\t"
                     "s_setprio 3\n\ts_waitcnt lgkmcnt(0)\n\tv_cmp_eq_u32 vcc, 3, v129\n\ts_barrier\n\t"
                     "s_cbranch_vccnz 1f\n\tds_read_b128 %[bs], %[ab]\n\ts_waitcnt lgkmcnt(0)\n1:"
                     : [bs] "=&{v[128:131]}"(bs)
                     : [ah] "v"(ahn), [ab] "v"((u32)(CTL_OFF + 112)), [aw] "v"(asw), [g0] "v"(gfr[0]), [g1] "v"(gfr[1]), [g2] "v"(gfr[2]),
                       [g3] "v"(gfr[3]), [g4] "v"(gfr[4]), [u0] "v"(ufr[0]), [u1] "v"(ufr[1]), [u2] "v"(ufr[2]), [u3] "v"(ufr[3]), [u4] "v"(ufr[4])
                     : "memory", "vcc", "scc", "v100", "v101", "v102", "v103", "v104", "v105", "v106", "v107", "v108", "v109", "v110", "v111", "v112", "v113",
                       "v114", "v115", "v116", "v117", "v118", "v119", "v120", "v121", "v122", "v123", "v124", "v125", "v126", "v127");
        const u32x2 best = {bs[2], bs[3]};
        f32x2 s0, s1, s2, s3;
        asm volatile("ds_read2_b32 %0, %4 offset0:0 offset1:32\n\tds_read2_b32 %1, %4 offset0:64 offset1:96\n\t"
                     "ds_read2_b32 %2, %4 offset0:128 offset1:160\n\tds_read2_b32 %3, %4 offset0:192 offset1:224"
                     : "=&v"(s0), "=&v"(s1), "=&v"(s2), "=&v"(s3) : "v"(asr + ((best[1] & 3u) | up3) * ASL) : "memory");
        float rd = __builtin_amdgcn_rcpf(hi_f(best[0])), mb = lo_f(best[0]);
        asm volatile("s_waitcnt lgkmcnt(0)" : "+v"(s0), "+v"(s1), "+v"(s2), "+v"(s3), "+v"(rd), "+v"(mb)::"memory");
        const f32x2 sm = (s0 + s1) + (s2 + s3);
        const float gt = (sm[0] - mb * dppf<0x108>(sm[0])) * rd, up = (sm[1] - mb * dppf<0x108>(sm[1])) * rd;
        const float av = gt * up * __builtin_amdgcn_rcpf(1.0f + __expf(-gt));
        const u32 ap = cvt2((f32x2){av, dppf<0x101>(av)});
        const u32x4 bm = {dppu<0x150>(ap), dppu<0x152>(ap), dppu<0x154>(ap), dppu<0x156>(ap)};
#define DN(J) "v_mfma_f32_16x16x32_bf16 v[104+4*" #J ":107+4*" #J "], %[d" #J "], %[bm], 0\n\t"
#define MV(J) "s_lshl_b64 exec, exec, 1\n\tv_mov_b64 v[104:105], v[104+4*" #J ":105+4*" #J "]\n\tv_mov_b64 v[106:107], v[106+4*" #J ":107+4*" #J "]\n\t"
        asm volatile("s_nop 1\n\t" DN(0) DN(1) DN(2) DN(3) DN(4)
                     "s_mov_b32 exec_lo, 0x00010001\n\ts_mov_b32 exec_hi, 0x00010001\n\ts_nop 1\n\t" MV(1) MV(2) MV(3) MV(4)
                     "s_mov_b32 exec_lo, 0x001f001f\n\ts_mov_b32 exec_hi, 0x001f001f\n\tglobal_store_dwordx4 %[vp], v[104:107], %[pd] sc1\n\t"
                     "s_mov_b64 exec, -1\n\ts_setprio 0"
                     ::[bm] "v"(bm), [vp] "v"(vpo), [pd] "s"(pdst), [d0] "v"(dfr[0]), [d1] "v"(dfr[1]),
                       [d2] "v"(dfr[2]), [d3] "v"(dfr[3]), [d4] "v"(dfr[4])
                     : "memory", "scc", "v100", "v101", "v102", "v103", "v104", "v105", "v106", "v107", "v108", "v109", "v110", "v111", "v112", "v113",
                       "v114", "v115", "v116", "v117", "v118", "v119", "v120", "v121", "v122", "v123");
        if (pass) break;
        u32 fin = 0;
#pragma nounroll
        for (int it = 0; it < SPIN_CAP * 8; ++it) {
          fin = __builtin_amdgcn_readfirstlane(lds_ld32(CTL_OFF + 40));
          if (fin) break;
          __builtin_amdgcn_s_sleep(4);
        }
        if (fin != 2u) break;
        pdst += WS_PART2 - WS_PART;
      }
      return;
    }
    if (w == 0) {
      u32 pk6;
      asm volatile(
          "s_mov_b32 s41, %[cap]\n\ts_mov_b32 s40, 1\n\ts_mov_b64 s[46:47], 0\n\tv_mov_b32 v5, %[ln]\n\t"
          "s_mov_b32 s54, -1\n\ts_mov_b32 s55, 0x7ffff\n\tv_mov_b32 v12, 0\n\t"
#define PKLD(D) "s_mov_b64 exec, s[54:55]\n\tglobal_atomic_or_x2 " D ", %[vk], %[z], %[kb] sc0 sc1\n\ts_mov_b64 exec, -1\n\t"
          PKLD("v[8:9]") "s_sleep %[gap]\n\t" PKLD("v[10:11]") "s_sleep %[gap]\n\t" PKLD("v[14:15]")
#define MDPP(C) "s_nop 1\n\tv_max_u32_dpp v13, v13, v13 " C " row_mask:0xf bank_mask:0xf bound_ctrl:1\n\t"
#define PSTEP(L, D0, D1, DR)                                                                        \
          L "0:\n\t"                                                                               \
          "s_waitcnt vmcnt(2)\n\t"                                                                 \
          "v_cmp_eq_u32 s[42:43], " D1 ", %[tag]\n\t"                                              \
          "s_and_b64 exec, s[42:43], s[54:55]\n\ts_andn2_b64 s[54:55], s[54:55], s[42:43]\n\t"      \
          "v_and_or_b32 v12, " D0 ", %[msk], %[lid]\n\t"                                           \
          "v_mov_b32 v6, " D0 "\n\ts_mov_b64 exec, -1\n\t"                                         \
          "v_cmp_le_u32 vcc, s40, v12\n\t"                                                         \
          "s_cmp_eq_u64 vcc, s[46:47]\n\ts_cbranch_scc1 " L "1f\n\t"                              \
          "s_swappc_b64 s[50:51], s[48:49]\n"                                                      \
          L "1:\n\t"                                                                               \
          "s_cmp_eq_u64 s[54:55], 0\n\ts_cbranch_scc1 9f\n\t"                                     \
          PKLD(DR)
          "s_getpc_b64 s[48:49]\n\ts_add_u32 s48, s48, 12\n\ts_addc_u32 s49, s49, 0\n\ts_branch 10f\n\t"
          "v_mov_b32 v13, v12\n\t"
          MDPP("quad_perm:[1,0,3,2]") MDPP("quad_perm:[2,3,0,1]") MDPP("row_half_mirror") MDPP("row_mirror")
          "v_bfe_u32 v4, v6, 10, 6\n\tv_readlane_b32 s44, v13, 0\n\tv_readlane_b32 s45, v13, 16\n\tv_readlane_b32 s46, v13, 32\n\tv_readlane_b32 s47, v13, 48\n\t"
          "v_sub_u32 v4, %[rb], v4\n\ts_max_u32 s44, s44, s45\n\ts_max_u32 s46, s46, s47\n\ts_max_u32 s44, s44, s46\n\t"
          "s_and_b32 s45, s44, 0x7fffff\n\ts_bitset1_b32 s45, 23\n\ts_mul_hi_u32 s45, s45, %[shr]\n"
          "5:\n\ts_sub_u32 s40, s44, s45\n\ts_cselect_b32 s40, 1, s40\n\ts_max_u32 s40, s40, 1\n\tv_cmp_le_u32 s[46:47], s40, v12\n\ts_mul_i32 s45, s45, 3\n\ts_lshr_b32 s45, s45, 2\n\t"
          "s_bcnt1_i32_b64 s52, s[46:47]\n\ts_cmp_gt_u32 s52, 3\n\ts_cbranch_scc1 5b\n\t"
          "v_mbcnt_lo_u32_b32 v13, s46, 0\n\tv_mbcnt_hi_u32_b32 v13, s47, v13\n\tv_sub_u32 v7, %[ln], v13\n\tv_add_u32 v7, s52, v7\n\t"
          "v_cndmask_b32 v13, v7, v13, s[46:47]\n\tv_cndmask_b32 v7, 2, 1, s[46:47]\n\tv_cmp_gt_u32 s[44:45], 3, v13\n\t"
          "v_lshl_add_u32 v13, v13, 4, %[pa]\n\ts_mov_b64 exec, s[44:45]\n\tds_write_b128 v13, v[4:7]\n\ts_mov_b64 exec, -1\n\t"
          "s_setpc_b64 s[50:51]\n"
          PSTEP("1", "v8", "v9", "v[8:9]")
          PSTEP("2", "v10", "v11", "v[10:11]")
          PSTEP("3", "v14", "v15", "v[14:15]")
          "s_sub_u32 s41, s41, 1\n\ts_cbranch_scc0 10b\n\ts_swappc_b64 s[50:51], s[48:49]\n"
          "9:\n\t"  "v_or_b32 v7, 4, v7\n\ts_mov_b64 exec, s[44:45]\n\tds_write_b32 v13, v7 offset:12\n\ts_mov_b64 exec, -1\n\tds_write_b32 %[at], v6\n\ts_waitcnt lgkmcnt(0)\n\ts_barrier\n\ts_waitcnt vmcnt(0)"
          : "=&{v6}"(pk6) : [cap] "n"(SPIN_CAP), [gap] "n"(PGAP), [vk] "v"((u32)(lane * 6144)), [kb] "s"(ws + WS_PK + fb * 128), [tag] "v"(tag), [z] "v"(0ull), [ln] "v"(lane),
            [msk] "s"(~1023u), [lid] "v"(64u | lane), [rb] "v"(2047 - ((lane >> 1) * 80 + (lane & 1) * 48)),
            [shr] "s"((u32)((1.0 - PTHR) * 4294967296.0)), [pa] "s"(POST_OFF), [at] "v"((u32)(KEY_OFF - 256 + lane * 4))
          : "memory", "vcc", "scc", "s40", "s41", "s42", "s43", "s44", "s45", "s46", "s47", "s48", "s49", "s50", "s51", "s52", "s54", "s55",
            "v4", "v5", "v7", "v8", "v9", "v10", "v11", "v12", "v13", "v14", "v15");
      if (fb == 47 && lane < 8) GST4(l16, ws + WS_EPOCH, 0, ((u32x4){tg[0], tg[1], tg[0], tg[1]}));
      const u32 now = (u32)__builtin_amdgcn_s_memrealtime();
      const u32 nag = (pk6 - (now >> 1) - 1u) & 1023u;
      u32 kgo = __builtin_amdgcn_readfirstlane(now + KCD - 2 * (1023u - wave_max(lane < 51 ? nag : 0u)));
      u64 old = __ballot(lane < 51 && nag < 1023u - (KOLD - 8) / 2);
      asm volatile("" : "+s"(kgo), "+s"(old));
      asm volatile(
          "s_mov_b32 s41, %[cap]\n\ts_mov_b32 s54, -1\n\ts_mov_b32 s55, 0x7ffff\n\tv_mov_b32 v12, 0\n\t"
          "s_mov_b64 exec, %[old]\n\ts_cbranch_execz 3f\n\tglobal_load_dwordx2 v[14:15], %[vk], %[kb] sc1\n"
          "3:\n\ts_setprio 3\n\ts_barrier\n"
          "4:\n\ts_memrealtime s[42:43]\n\ts_waitcnt lgkmcnt(0)\n\ts_sub_u32 s42, s42, %[go]\n\ts_cmp_lt_i32 s42, 0\n\ts_cbranch_scc1 4b\n\t"
          "s_andn2_b64 exec, s[54:55], %[old]\n\ts_cbranch_execz 5f\n\tglobal_load_dwordx2 v[14:15], %[vk], %[kb] sc1\n"
          "5:\n\ts_mov_b64 exec, s[54:55]\n\tds_read_b32 v4, %[ctl] offset:92\n\ts_waitcnt lgkmcnt(0)\n\tv_and_b32 v4, 3, v4\n\t"
          "v_lshl_add_u32 v4, v4, 4, %[pa]\n\tds_read_b64 v[6:7], v4\n\ts_waitcnt lgkmcnt(0)\n\t"
          "v_readfirstlane_b32 s48, v6\n\tv_readfirstlane_b32 s49, v7\n\tv_mov_b32 v6, 1\n\ts_waitcnt vmcnt(0)\n\tv_cmp_ne_u32 s[54:55], v15, %[tag]\n\tv_mov_b32 v12, v14\n\ts_mov_b64 exec, -1\n\t"
          "s_cmp_eq_u64 s[54:55], 0\n\ts_cbranch_scc1 9f\n\t"
          PKLD("v[8:9]") "s_sleep %[gap]\n\t" PKLD("v[10:11]")
#define KSTEP(L, D0, D1, DR)                                                                        \
          L "0:\n\t"                                                                               \
          "s_waitcnt vmcnt(1)\n\t"                                                                 \
          "v_cmp_eq_u32 s[42:43], " D1 ", %[tag]\n\t"                                              \
          "s_and_b64 exec, s[42:43], s[54:55]\n\tv_mov_b32 v12, " D0 "\n\ts_mov_b64 exec, -1\n\t"   \
          "s_andn2_b64 s[54:55], s[54:55], s[42:43]\n\ts_cbranch_scc0 9f\n\t"                     \
          PKLD(DR)
          KSTEP("1", "v8", "v9", "v[8:9]")
          KSTEP("2", "v10", "v11", "v[10:11]")
          "s_sub_u32 s41, s41, 1\n\ts_cbranch_scc0 10b\n"
          "9:\n\t"
          "v_readlane_b32 s40, v12, s49\n\tds_write_b32 %[ak], v12\n\tv_mov_b32 v13, v12\n\t"
          "s_and_b32 s45, s40, 0x7ff\n\tv_cmp_lt_u32 vcc, s40, v12\n\ts_cmp_eq_u32 s45, s48\n\ts_cselect_b64 vcc, vcc, -1\n\ts_cbranch_vccz 8f\n\t"
          MDPP("quad_perm:[1,0,3,2]") MDPP("quad_perm:[2,3,0,1]") MDPP("row_half_mirror") MDPP("row_mirror")
          "s_nop 0\n\tv_readlane_b32 s44, v13, 0\n\tv_readlane_b32 s45, v13, 16\n\tv_readlane_b32 s46, v13, 32\n\tv_readlane_b32 s47, v13, 48\n\t"
          "s_max_u32 s44, s44, s45\n\ts_max_u32 s46, s46, s47\n\ts_max_u32 s40, s44, s46\n\t"
          "v_cmp_eq_u32 vcc, s40, v12\n\tv_mov_b32 v6, 2\n\ts_ff1_i32_b64 s44, vcc\n\tv_mov_b32 v7, s44\n"
          "8:\n\tv_mov_b32 v4, s40\n\ts_mov_b64 exec, 1\n\tds_write_b128 %[ctl], v[4:7]\n\ts_mov_b64 exec, -1\n\t"
          "s_waitcnt vmcnt(0) lgkmcnt(0)"
          :: [cap] "n"(SPIN_CAP), [gap] "n"(KGAP), [go] "s"(kgo), [old] "s"(old), [vk] "v"((u32)(lane * 6144)), [kb] "s"(ws + WS_PK + fb * 128 + 8), [tag] "v"(tag),
            [ctl] "v"((u32)(CTL_OFF + 32)), [ak] "v"((u32)(KEY_OFF + lane * 4)), [pa] "s"(POST_OFF), [z] "v"(0ull)
          : "memory", "vcc", "scc", "s40", "s41", "s42", "s43", "s44", "s45", "s46", "s47", "s48", "s49", "s54", "s55", "v4", "v5", "v6", "v7", "v8", "v9", "v10",
            "v11", "v12", "v13", "v14", "v15");
      return;
    }
    float swr = dot2b(rb5, 0x3F803F80u, 0.f);
#pragma unroll
    for (int e = 0; e < 4; ++e) swr = dot2b(ra[e], 0x3F803F80u, swr);
    swr = wave_sum(swr);
    u32 ah16 = HN_OFF + (w - 1) * HSL + l16, ah4 = HN_OFF + (w - 1) * HSL + 1024 + lane * 4, l4 = lane * 4;
    asm volatile("" : "+v"(swr), "+v"(ah16), "+v"(ah4), "+v"(l4));
#define DOT3(H, W) "v_dot2c_f32_bf16 v102, 0x3f803f80, " H "\n\tv_dot2c_f32_bf16 v103, " H ", " H "\n\tv_dot2c_f32_bf16 v104, " H ", " W "\n\t"
#define ADPP(C) "s_nop 1\n\tv_add_f32_dpp v102, v102, v102 " C " row_mask:0xf bank_mask:0xf bound_ctrl:1\n\t"
#define XDPP(C) "s_nop 1\n\tv_max_u32_dpp v102, v102, v102 " C " row_mask:0xf bank_mask:0xf bound_ctrl:1\n\t"
    const char* rp = (const char*)w_r;
    u32 second = 0;
#pragma nounroll
    for (;;) {
      asm volatile(
          "s_mov_b32 s47, %[cap]\n\ts_mov_b32 s43, -1\n\ts_sub_u32 s46, %[sec], 1\n"
          "7:\n\ts_mov_b64 s[48:49], %[rp]\n\ts_mov_b32 s45, 0\n\ts_branch 4f\n"
          "18:\n\ts_sleep 1\n"
          "1:\n\ts_sub_u32 s47, s47, 1\n\ts_cbranch_scc1 8f\n\ts_cmp_lg_u32 %[sec], 0\n\ts_cbranch_scc1 7b\n\t"
          "ds_read_b128 v[108:111], %[ap]\n\ts_memrealtime s[40:41]\n\ts_waitcnt lgkmcnt(0)\n\t"
          "v_readfirstlane_b32 s45, v111\n\tv_readfirstlane_b32 s46, v109\n\tv_readfirstlane_b32 s50, v110\n\ts_lshr_b32 s41, s40, 1\n\t"
          "s_cmp_eq_u32 s45, 6\n\ts_cbranch_scc1 8f\n\ts_bitcmp0_b32 s45, 0\n\ts_cbranch_scc1 18b\n\t"
          "s_cmp_eq_u32 s46, s42\n\ts_cbranch_scc1 6f\n\ts_cmp_eq_u32 s46, s43\n\ts_cbranch_scc1 2f\n\t"
          "s_sub_u32 s41, s41, s50\n\ts_and_b32 s41, s41, 0x3ff\n\ts_cmp_lt_u32 s41, %[tq]\n\ts_cbranch_scc1 18b\n\t"
          "s_mov_b32 s43, s46\n\ts_add_u32 s44, s50, 8\n\ts_cmp_ge_u32 s41, %[pc] - 20\n\ts_cbranch_scc1 1b\n\t"
          "s_mul_i32 s48, s46, 6144\n\tv_mov_b64 v[104:105], 0\n\tv_mov_b32 v101, s48\n\tv_mad_u32_u24 v101, %[l16], 48, v101\n\t"
          "s_mov_b64 exec, 0xff\n\tglobal_atomic_or_x2 v[102:103], v101, v[104:105], %[pk] sc0 sc1\n\ts_sub_u32 s50, s50, 512\n\ts_waitcnt vmcnt(0)\n\t"
          "v_cmp_eq_u32 vcc, v103, %[tag]\n\tv_subrev_u32 v102, s50, v102\n\tv_and_b32 v102, 0x3ff, v102\n\tv_cndmask_b32 v102, 0, v102, vcc\n\t"
          XDPP("quad_perm:[1,0,3,2]") XDPP("quad_perm:[2,3,0,1]") XDPP("row_half_mirror")
          "s_mov_b64 exec, -1\n\tv_readfirstlane_b32 s44, v102\n\ts_min_u32 s44, s44, 562\n\ts_add_u32 s44, s44, s50\n\ts_branch 1b\n"
          "2:\n\ts_sub_u32 s41, s41, s44\n\ts_and_b32 s41, s41, 0x3ff\n\ts_cmp_lt_u32 s41, %[pc] - 8\n\ts_cbranch_scc1 1b\n\t"
          "s_sub_u32 s41, %[pc], s41\n\ts_lshl1_add_u32 s51, s41, s40\n\ts_mul_i32 s48, s46, 1280\n\ts_add_u32 s48, s48, %[lo]\n\ts_addc_u32 s49, %[hi], 0\n\t"
          "3:\n\ts_memrealtime s[40:41]\n\ts_waitcnt lgkmcnt(0)\n\ts_sub_u32 s40, s40, s51\n\ts_cmp_lt_i32 s40, 0\n\ts_cbranch_scc1 3b\n"
          "4:\n\tglobal_load_dwordx4 v[96:99], %[l16], s[48:49] sc1\n\tglobal_load_dword v100, %[l4], s[48:49] offset:1024 sc1\n\t"
          "v_mov_b64 v[102:103], 0\n\tv_mov_b32 v104, 0\n\ts_mov_b32 s42, -2\n\ts_waitcnt vmcnt(0)\n\t"
          "ds_read_b128 v[108:111], %[ap]\n\tds_read_b32 v105, %[ab] offset:4\n\tds_write_b128 %[a16], v[96:99]\n\tds_write_b32 %[a4], v100\n\t"
          "v_max3_u32 v101, v96, v97, v98\n\tv_max3_u32 v101, v101, v99, v100\n\tv_cmp_eq_u32 vcc, -1, v101\n\t"
          DOT3("v96", "v92") DOT3("v97", "v93")
          "s_cbranch_vccnz 1b\n\ts_mov_b32 s42, s46\n\ts_waitcnt lgkmcnt(2)\n\tv_lshl_or_b32 v109, v111, 8, v109\n\tv_lshl_or_b32 v109, v105, 16, v109\n\t"
          "s_or_b32 s45, s46, 0x20500\n\tv_cmp_eq_u32 vcc, s45, v109\n\ts_cbranch_vccz 5f\n\ts_waitcnt lgkmcnt(0)\n\ts_barrier\n"
          "5:\n\t" DOT3("v98", "v94") DOT3("v99", "v95") DOT3("v100", "%[rb]")
          "s_nop 2\n\tv_mov_b32 v105, v104\n\tv_permlane32_swap_b32 v102, v103\n\tv_add_f32 v102, v102, v103\n\tv_permlane32_swap_b32 v104, v105\n\t"
          "v_add_f32 v104, v104, v105\n\ts_nop 1\n\tv_permlane16_swap_b32 v102, v104\n\tv_add_f32 v102, v102, v104\n\t"
          ADPP("quad_perm:[1,0,3,2]") ADPP("quad_perm:[2,3,0,1]") ADPP("row_half_mirror") ADPP("row_mirror")
          "v_mul_f32 v103, 0x3acccccd, v102\n\tv_readlane_b32 s41, v102, 16\n\tv_readlane_b32 s40, v103, 32\n\t"
          "s_nop 1\n\tv_fma_f32 v102, -v103, v103, s40\n\tv_max_f32 v102, 0, v102\n\tv_cvt_pk_bf16_f32 v102, 0, v102\n\t"
          "v_add_f32 v102, 0x3727c5ac, v102\n\tv_cvt_pk_bf16_f32 v102, 0, v102\n\tv_sqrt_f32 v102, v102\n\tv_cvt_pk_bf16_f32 v105, 0, v103\n\t"
          "v_fma_f32 v105, -%[swr], v105, s41\n\tv_cvt_pk_bf16_f32 v106, v103, v102\n\tv_and_b32 v102, 0xffff0000, v106\n\tv_rcp_f32 v102, v102\n\t"
          "v_mov_b32 v104, 1\n\tv_mul_f32 v102, v102, v105\n\tv_ashrrev_i32 v103, 31, v102\n\tv_or_b32 v103, 0x80000000, v103\n\t"
          "v_xor_b32 v102, v102, v103\n\tv_and_or_b32 v107, v102, -4, %[sl]\n\ts_cbranch_vccz 1b\n\t"
          "s_mov_b64 exec, 1\n\tds_max_u64 %[ab], v[106:107] offset:8\n\tds_add_u32 %[ab], v104 offset:4\n\ts_branch 9f\n"
          "6:\n\ts_bitcmp0_b32 s45, 2\n\ts_cbranch_scc1 1b\n\ts_mov_b64 exec, 1\n\tds_max_u64 %[ab], v[106:107] offset:8\n"
          "8:\n\tv_mov_b32 v104, 1\n\ts_mov_b64 exec, 1\n\tds_add_u32 %[ab], v104 offset:4\n\ts_barrier\n"
          "9:\n\ts_mov_b64 exec, -1\n\ts_waitcnt lgkmcnt(0)\n\ts_barrier"
          :
          : [cap] "n"(SPIN_CAP * 8), [rp] "s"(rp), [sec] "s"(second), [ap] "v"((u32)(POST_OFF + (w - 1) * 16)), [tq] "n"(TQD / 2), [pc] "n"(PCD / 2),
            [pk] "s"(ws + WS_PK + (bidx - 26) * 128), [tag] "v"(tag), [lo] "s"((u32)(u64)(ws + WS_PC)), [hi] "s"((u32)((u64)(ws + WS_PC) >> 32)),
            [l16] "v"(l16), [l4] "v"(l4), [a16] "v"(ah16), [a4] "v"(ah4), "{v[92:95]}"(ra), [rb] "v"(rb5), [swr] "v"(swr), [sl] "s"(w - 1),
            [ab] "v"((u32)(CTL_OFF + 112))
          : "memory", "vcc", "scc", "s40", "s41", "s42", "s43", "s44", "s45", "s46", "s47", "s48", "s49", "s50", "s51", "v96", "v97", "v98", "v99",
            "v100", "v101", "v102", "v103", "v104", "v105", "v106", "v107", "v108", "v109", "v110", "v111");
      if (w != 1 || second) break;
      u32x4 wst;
#pragma nounroll
      for (int it = 0; it < SPIN_CAP * 8; ++it) {
        wst = lds_ld128(CTL_OFF + 32);
        if (__builtin_amdgcn_readfirstlane(wst[2])) break;
        __builtin_amdgcn_s_sleep(1);
      }
      if (__builtin_amdgcn_readfirstlane(wst[2]) != 2u) break;
      if (lane < 2) *(u32x4*)(smem + (lane ? POST_OFF : CTL_OFF + 112)) = lane ? (u32x4){0u, 0u, 0u, 5u} : (u32x4){0u, 2u, 0x3F800000u, 0u};
      rp = ws + WS_CAND + __builtin_amdgcn_readfirstlane(wst[3]) * 2560;
      second = 1;
    }
    if (w == 1) {
    } else if (w == 3 || fb >= 40) {
      const int c = w == 3 ? fb : fb + 8;
      if (c < 51) {
        const u32 now = (u32)__builtin_amdgcn_s_memrealtime();
        const u32 age = ((now >> 1) - __builtin_amdgcn_readfirstlane(*(const u32*)(smem + KEY_OFF - 256 + c * 4))) & 1023u;
        asm volatile("s_mov_b32 s42, %[cap]\n"
                     "3:\n\ts_memrealtime s[40:41]\n\ts_waitcnt lgkmcnt(0)\n\ts_sub_u32 s40, s40, %[go]\n\ts_cmp_lt_i32 s40, 0\n\ts_cbranch_scc1 3b\n"
                     "4:\n\tglobal_load_dwordx4 v[96:99], %[l16], %[cp] sc1\n\tglobal_load_dword v103, %[l4], %[cp] offset:1024 sc1\n\t"
                     "s_sub_u32 s42, s42, 8\n\ts_waitcnt vmcnt(0)\n\ts_cbranch_scc1 2f\n\t"
                     "v_max3_u32 v101, v96, v98, v103\n\tv_cmp_eq_u32 vcc, -1, v101\n\ts_cbranch_vccnz 4b\n"
                     "1:\n\tds_read_b64 v[100:101], %[af]\n\tds_read_b32 v102, %[ak]\n\ts_sub_u32 s42, s42, 1\n\ts_cbranch_scc1 2f\n\ts_waitcnt lgkmcnt(0)\n\t"
                     "v_cmp_eq_u32 vcc, 0, v100\n\ts_cbranch_vccnz 1b\n\t"
                     "v_cmp_eq_u32 vcc, %[c], v101\n\tv_bfi_b32 v102, v102, 0, %[m]\n\ts_cbranch_vccnz 2f\n\t"
                     "v_mad_u32_u24 v100, v102, %[rs], %[l16]\n\tv_mad_u32_u24 v101, v102, %[rs], %[l4]\n\t"
                     "global_store_dwordx4 v100, v[96:99], %[out]" "\n\tglobal_store_dword v101, v103, %[out] offset:1024" "\n"
                     "2:\n\ts_waitcnt lgkmcnt(0)"
                     ::[cap] "n"(SPIN_CAP * 8), [go] "s"(now + CCD - 2 * age), [cp] "s"(ws + WS_CAND + c * 2560 + 1280), [af] "v"((u32)(CTL_OFF + 40)),
                       [ak] "v"((u32)(KEY_OFF + c * 4)), [c] "s"(c), [m] "s"(2047), [rs] "s"(1280), [l16] "v"(l16), [l4] "v"(l4), [out] "s"(out)
                     : "memory", "vcc", "scc", "s40", "s41", "s42", "v96", "v97", "v98", "v99", "v100", "v101", "v102", "v103");
      }
    } else {
      const char* pp = ws + WS_PART + fb * 64;
      int pass = 0;
      asm volatile("" : "+s"(pass));
#pragma nounroll
      for (; pass < 2; ++pass) {
        const u32x2 best = *(const u32x2*)(smem + CTL_OFF + 120);
        const u32 slot = best[1] & 3u;
        const u32 wkey = __builtin_amdgcn_readfirstlane(*(const u32*)(smem + (pass ? CTL_OFF + 32 : POST_OFF + slot * 16)));
        const float rr = rbf(__uint_as_float((best[1] & ~3u) ^ ((u32)((int)~best[1] >> 31) | 0x80000000u)));
        float rs = rbf(__builtin_amdgcn_rcpf(1.0f + __expf(-rr)));
        const int ccol = (g & 1) * 2 + (g >> 1);
        float hv = bf2f(*(const u16*)(smem + HN_OFF + slot * HSL + fb * 32 + p4 * 8 + ccol * 2));
        hv = __builtin_fmaf(hv - lo_f(best[0]), __builtin_amdgcn_rcpf(hi_f(best[0])), hv);
        const char* op_ = (const char*)out + (size_t)(2047 - (int)(wkey & 2047u)) * 1280 + fb * 32;
        u32 vo_ = p4 * 8 + ccol * 2;
        asm volatile("" : "+v"(vo_), "+v"(hv), "+v"(rs));
        asm volatile("" : "+s"(op_));
        const u32 vp = (lane >> 2) * 2560 + p4 * 16;
        typedef __attribute__((ext_vector_type(16))) unsigned int u32x16;
        u32x16 keepa, keepb;
        f32x4 ys;
        u32 fin;
        asm volatile(
#define PLD(B0, B1, B2)                                                                             \
            "global_load_dwordx4 " B0 ", %[vp], %[p0] sc1\n\t"                                       \
            "global_load_dwordx4 " B1 ", %[vp], %[p1] sc1\n\tglobal_load_dwordx4 " B2 ", %[vp], %[p2] sc1\n\t"
#define PCHK(A0, A1, B0, B1, C0, C1)                                                                \
            "s_waitcnt vmcnt(3)\n\t"                                                                 \
            "v_pk_add_f32 v[96:97], " A0 ", " B0 "\n\tv_pk_add_f32 v[98:99], " A1 ", " B1 "\n\t"    \
            "v_pk_add_f32 v[96:97], v[96:97], " C0 "\n\tv_pk_add_f32 v[98:99], v[98:99], " C1 "\n\t" \
            "v_pk_add_f32 v[102:103], v[96:97], v[98:99]\n\t"                                        \
            "v_cmp_u_f32 vcc, v102, v103\n\ts_cbranch_vccz 9f\n\t"
            "s_mov_b32 s42, %[cap]\n\ts_sleep %[cdel]\n\t"
            PLD("v[64:67]", "v[68:71]", "v[72:75]")
            "s_sleep %[gap]\n\t"
            PLD("v[80:83]", "v[84:87]", "v[88:91]")
            "1:\n\t"
            PCHK("v[64:65]", "v[66:67]", "v[68:69]", "v[70:71]", "v[72:73]", "v[74:75]")
            PLD("v[64:67]", "v[68:71]", "v[72:75]")
            PCHK("v[80:81]", "v[82:83]", "v[84:85]", "v[86:87]", "v[88:89]", "v[90:91]")
            PLD("v[80:83]", "v[84:87]", "v[88:91]")
            "s_sub_u32 s42, s42, 1\n\ts_cbranch_scc0 1b\n"
            "9:\n\tds_read_b32 %[fin], %[af]"
            : "=&{v[96:99]}"(ys), "=&{v[64:79]}"(keepa), "=&{v[80:95]}"(keepb), [fin] "=&v"(fin)
            : [cap] "n"(SPIN_CAP), [gap] "n"(CGAP), [cdel] "n"(CDEL), [vp] "v"(vp), [p0] "s"(pp),
              [p1] "s"(pp + 40960), [p2] "s"(pp + 81920), [af] "v"((u32)(CTL_OFF + 40))
            : "memory", "vcc", "scc", "s42", "v102", "v103");
        float y = tr_sum(ys);
        y += dppf<0x124>(y);
        y += dppf<0x128>(y);
        const u32 res = cvt2((f32x2){__builtin_fmaf(y, rs, hv), 0.f});
        asm volatile("s_mov_b32 s42, %[cap]\n"
                     "1:\n\ts_waitcnt lgkmcnt(0)\n\tv_cmp_eq_u32 vcc, %[ok], %[fin]\n\ts_cbranch_vccz 2f\n\t"
                     "s_mov_b32 exec_lo, 0x000f000f\n\ts_mov_b32 exec_hi, 0x000f000f\n\tglobal_store_short %[vo], %[res], %[op]" "\n\ts_mov_b64 exec, -1\n"
                     "2:\n\tv_cmp_eq_u32 vcc, 0, %[fin]\n\ts_cbranch_vccz 3f\n\tds_read_b32 %[fin], %[af]\n\ts_sub_u32 s42, s42, 1\n\ts_cbranch_scc0 1b\n"
                     "3:\n\ts_waitcnt lgkmcnt(0)\n\tv_cmp_lt_u32 vcc, %[ok], %[fin]\n\ts_cbranch_vccz 4f\n\ts_waitcnt vmcnt(0)\n4:"
                     : [fin] "+v"(fin) : [cap] "n"(SPIN_CAP * 8), [ok] "s"(pass + 1), [vo] "v"(vo_), [res] "v"(res), [op] "s"(op_),
                       [af] "v"((u32)(CTL_OFF + 40)), "{v[64:79]}"(keepa), "{v[80:95]}"(keepb) : "memory", "vcc", "scc", "s42");
        if (pass || __builtin_amdgcn_readfirstlane(fin) != 2u) break;
        asm volatile("s_barrier\n\ts_barrier" ::: "memory");
        pp += WS_PART2 - WS_PART;
      }
    }
    return;
  }

  const int idx2 = (xcd & 1) * 26 + bidx;
  const int hg = ((xcd >> 1) & 1) * 4 + (idx2 & 3);
  const int mt = (xcd >> 2) * 13 + (idx2 >> 2);
#define ROTF (((idx2 >> 2) * 3 / 2 + (idx2 & 3) * 3) % 10)
  const int kb0 = ROTF;
  const int r0 = mt * 80;
  if (w >= 4) {
    const int cw = w - 4;
    u32 vo = w_off(cw, lane, hg), vx = x_off(cw, lane, r0), vx2 = x_off(8 + (cw & 3), lane, r0);
    asm volatile("" : "+v"(vo), "+v"(vx), "+v"(vx2));
    __builtin_amdgcn_sched_barrier(0);
    const char* wb = (const char*)wqkv + (size_t)kb0 * 245760 + W_ROW(cw);
    dma_m0<0>(vo, wb, cw * 1024);
    dma_m0<0>(vx, (const char*)x + kb0 * 128, XRING + cw * 1024);
    dma_m0<0>(vo, wb + 61440, 8192 + cw * 1024);
    if (cw < 4) dma_m0<0>(vx2, (const char*)x + kb0 * 128, XRING + 8192 + cw * 1024);
    asm volatile("s_barrier" ::: "memory");
  }
  const int rt = w >= 4 ? (w & 3) : 4, kh = w >= 4 ? (w - 4) >> 2 : (w & 1);
  float bias[2];
  u32 sel[4], e4, eh, aq, aka, akb, pm, pf, hi, arec, vpp;
  const char *os, *os4, *sst, *oa[10];
  int so_[10];
  int lane_ = lane, mt_ = mt, hg_ = hg;
  u32 tgo = 0;
  auto prepv = [&](const int lane) __attribute__((always_inline)) {
    const int g = lane >> 4, r16 = lane & 15, q4 = (lane & 15) >> 2, p4 = lane & 3;
    const int par = lane & 1;
    const bool ca = r16 >= 4 * g, cb = r16 >= 4 * g + 2;
    bias[0] = (ca || r0 + rt > 0) ? 0.f : -__builtin_inff();
    bias[1] = (cb || r0 + rt > 0) ? 0.f : -__builtin_inff();
    const u32 sa = par ? 0x01000C0Cu : 0x0C0C0100u, sb = par ? 0x03020C0Cu : 0x0C0C0302u;
    sel[0] = ca ? 0x0C0C0C0Cu : sa;
    sel[1] = cb ? 0x0C0C0C0Cu : sb;
    sel[2] = ca ? sa : 0x0C0C0C0Cu;
    sel[3] = cb ? sb : 0x0C0C0C0Cu;
    e4 = EXO + rt * 512 + lane * 8;
    eh = e4 + kh * 6144;
    aq = TAB_OFF + 384 + (rt * 16 + r16) * 4;
    aka = TAB_OFF + 384 + (rt * 16 + 4 * g + par + (ca ? 16 : 0)) * 4;
    akb = TAB_OFF + 384 + (rt * 16 + 4 * g + par + 2 + (cb ? 16 : 0)) * 4;
    pm = (4 * g + q4) * 160 + p4 * 40;
    pf = (20 * g + 16 + q4) * 160 + p4 * 40;
    hi = HIM + (rt * 16 + r16) * 160 + g * 40;
    arec = REC_OFF + (r16 == 0 ? kh * 192 + g * 48 : (r16 == 8 ? ((g >> 1) == kh ? 480 : 384) : 432));
    vpp = lane + kh * 64 + (rt == 0 ? 768 : (rt == 2 ? 8192 : (rt == 4 ? 512 : 640)));
    asm volatile("" : "+v"(arec), "+v"(vpp));
    asm volatile("" : "+v"(bias[0]), "+v"(bias[1]), "+v"(sel[0]), "+v"(sel[1]), "+v"(sel[2]), "+v"(sel[3]), "+v"(e4), "+v"(eh),
                 "+v"(aq), "+v"(aka), "+v"(akb), "+v"(pm), "+v"(pf), "+v"(hi));
  };
  auto preps = [&](const int mt, const int hg) __attribute__((always_inline)) {
    const char* ob = ws + WS_O + (size_t)mt * 102400;
    os = ob + rt * 20480 + (2 * hg + kh) * 1024;
    os4 = ob + rt * 20480 + (16 + (hg >> 1)) * 1024 + (hg & 1) * 8;
    sst = ws + WS_STAT + (size_t)(2 * mt + (rt < 3 ? 0 : 1)) * 8192 + hg * 1024 + (rt < 3 ? rt : rt - 3) * 256;
    asm volatile("" : "+s"(os), "+s"(os4), "+s"(sst));
    const int k2 = w & 1;
#pragma unroll
    for (int j = 0; j < 10; ++j) {
      const bool oth = j >= 5 || w < 2;
      const int mo[8] = {OORD};
      const int ch = j % 5 < 4 ? (2 * hg + k2) ^ (oth ? 2 * mo[4 + j % 5] : 2 * mo[j % 5]) : (hg >> 1) ^ ((oth != ((hg & 1) != 0)) ? (k2 ? 0 : 3) : 1 + k2);
      oa[j] = ob + (j % 5 < 4 ? ch : 16 + ch) * 1024;
      so_[j] = ch * (j % 5 < 4 ? 6400 : 25600);
      asm volatile("" : "+s"(oa[j]), "+s"(so_[j]));
    }
  };
#define WO_OFFS                                                                                    \
  u32 woff[5];                                                                                     \
  _Pragma("unroll") for (int i = 0; i < 5; ++i) {                                                  \
    const int c = i * 64 + lane;                                                                   \
    const int row = c / 10;                                                                        \
    woff[i] = row * 1120 + hg * 160 + c * 16 + (4 - i) * 1024;                                     \
  }
#define WO_RUN(RHO)                                                                                \
  {                                                                                                \
    const char* wq_ = (const char*)wo + (RHO) * 40960; \
    dma_m0<-4096>(woff[0], wq_, (RHO) * 5120 + 4096);                                               \
    dma_nx<-3072>(woff[1], wq_); dma_nx<-2048>(woff[2], wq_); dma_nx<-1024>(woff[3], wq_); dma_nx<0>(woff[4], wq_); \
  }

#define BAR asm volatile("s_barrier" ::: "memory");
#define PREP_V __builtin_amdgcn_sched_barrier(0); asm volatile("" : "+v"(lane_)); prepv(lane_); __builtin_amdgcn_sched_barrier(0);
#define PREP_S __builtin_amdgcn_sched_barrier(0); asm volatile("" : "+s"(mt_), "+s"(hg_)); preps(mt_, hg_); __builtin_amdgcn_sched_barrier(0);
  if (w < 2) {
    const int l = w;
    __builtin_amdgcn_s_setprio(3);
    u32 vw[4];
#pragma unroll
    for (int i = 0; i < 4; ++i) vw[i] = w_off(i, lane, hg) + 4096;
    u32 go = (u32)kb0 * 245760u + 122880u + l * 61440u;
    u32 so = HSLOT + l * 8192 + 4096;
#define W1(I) dma_nx<((I) - 4) * 1024>(vw[(I) & 3], wb + (W_ROW(I) - (I) * 1024));
#define ISSUE_W                                                                                    \
  {                                                                                                \
    const char* wb = (const char*)wqkv + go;                                                       \
    dma_m0<-4096>(vw[0], wb, so);                                                                  \
    W1(1) W1(2) W1(3) W1(4) W1(5) W1(6) W1(7)                                                      \
    go += 122880u;                                                                                 \
    if (go >= 2457600u) go -= 2457600u;                                                            \
    so = so >= 5 * HSLOT ? so - 5 * HSLOT : so + HSLOT;                                            \
  }
    asm volatile("" : "+v"(vw[0]), "+v"(vw[1]), "+v"(vw[2]), "+v"(vw[3]), "+s"(go));
    BAR
    ISSUE_W
    __builtin_amdgcn_sched_barrier(0);
    WO_OFFS
    asm volatile("" : "+v"(woff[0]), "+v"(woff[1]), "+v"(woff[2]), "+v"(woff[3]), "+v"(woff[4]));
    BAR
    ISSUE_W
    asm volatile("s_waitcnt vmcnt(8)\n\ts_barrier" ::: "memory");
#pragma unroll 1
    for (int h = 0; h < 17; ++h) {
      ISSUE_W
      asm volatile("s_waitcnt vmcnt(8)\n\ts_barrier" ::: "memory");
    }
    asm volatile("s_waitcnt vmcnt(0)\n\ts_barrier" ::: "memory");
    WO_RUN(12 + l)
    const u32 tagw = lds_ld32(CTL_OFF);
    const char* bxf = ws + WS_BXF + mt * 1024;
    BAR
    const u32 town = (u32)__builtin_amdgcn_s_memrealtime();
    tgo = town;
    if (l == 0) {
      if (lane == 0) GST2((u32)(hg * 128), bxf, 0, ((u32x2){town, tagw}));
    }
    WO_RUN(18 + l)
    WO_RUN(8 + l)
    WO_RUN(l)
    const u32 vfl = (((lane & 7) == hg ? hg ^ 1 : (lane & 7))) * 128;
    u32x2 f;
    asm volatile("" : "=v"(f));
    BAR
    int gate = (int)((u32)__builtin_amdgcn_s_memrealtime() - town) + GADD;
    gate = gate > 240 ? 240 : gate;
    WO_RUN(2 + l)
    WO_RUN(4 + l)
    WO_RUN(6 + l)
    if (l == 1) asm volatile("global_load_dwordx2 %0, %1, %2 sc1" : "+v"(f) : "v"(vfl), "s"(bxf) : "memory");
    __builtin_amdgcn_s_setprio(0);
    pm = (4 * g + q4) * 160 + p4 * 40;
    pf = (20 * g + 16 + q4) * 160 + p4 * 40;
    asm volatile("" : "=v"(bias[0]), "=v"(bias[1]), "=v"(sel[0]), "=v"(sel[1]), "=v"(sel[2]), "=v"(sel[3]), "=v"(e4), "=v"(eh),
                 "=v"(aq), "=v"(aka), "=v"(akb), "+v"(pm), "+v"(pf), "=v"(hi), "=v"(arec), "=v"(vpp));
    PREP_S
    if (l == 1) {
      asm volatile("s_waitcnt vmcnt(0)" : "+v"(f)::"memory");
#pragma nounroll
      for (int it = 0; it < SPIN_CAP; ++it) {
        if (__all(f[1] == tagw)) break;
        asm volatile("global_load_dwordx2 %0, %1, %2 sc1\n\ts_waitcnt vmcnt(0)" : "=&v"(f) : "v"(vfl), "s"(bxf) : "memory");
      }
      u32 d = min(max((int)(f[0] - town), 0), 200);
      d = max(d, dppu<0xB1>(d)); d = max(d, dppu<0x4E>(d)); d = max(d, dppu<0x141>(d));
      tgo = town + gate + __builtin_amdgcn_readfirstlane(d);
    }
    __builtin_amdgcn_s_setprio(0);
  } else if (w < 4) {
    __builtin_amdgcn_s_setprio(3);
    if (w == 2) {
      u32 vx[12];
#pragma unroll
      for (int i = 0; i < 12; ++i) vx[i] = x_off(i, lane, r0) + (4 - (i % 6)) * 1024;
      int kb = kb0 == 9 ? 0 : kb0 + 1;
      u32 xo = XRING + XSLOT + 4096;
#define ISSUE_X0                                                                                   \
  {                                                                                                \
    const char* xb = (const char*)x + kb * 128;                                                    \
    dma_m0<-4096>(vx[0], xb, xo);                                                                  \
    dma_nx<-3072>(vx[1], xb); dma_nx<-2048>(vx[2], xb); dma_nx<-1024>(vx[3], xb); dma_nx<0>(vx[4], xb); dma_nx<1024>(vx[5], xb); \
  }
#define ISSUE_X1                                                                                   \
  {                                                                                                \
    const char* xb = (const char*)x + kb * 128;                                                    \
    dma_m0<-4096>(vx[6], xb, xo + 6144);                                                           \
    dma_nx<-3072>(vx[7], xb); dma_nx<-2048>(vx[8], xb); dma_nx<-1024>(vx[9], xb); dma_nx<0>(vx[10], xb); dma_nx<1024>(vx[11], xb); \
    kb = kb == 9 ? 0 : kb + 1;                                                                     \
    xo = xo >= XRING + 3 * XSLOT ? xo - 3 * XSLOT : xo + XSLOT;                                    \
  }
      asm volatile("" : "+v"(vx[0]), "+v"(vx[1]), "+v"(vx[2]), "+v"(vx[3]), "+v"(vx[4]), "+v"(vx[5]));
      BAR
      __builtin_amdgcn_s_sleep(4);
      ISSUE_X0
      __builtin_amdgcn_sched_barrier(0);
      WO_OFFS
      asm volatile("" : "+v"(woff[0]), "+v"(woff[1]), "+v"(woff[2]), "+v"(woff[3]), "+v"(woff[4]));
      BAR
      ISSUE_X1
      ISSUE_X0
      BAR
      ISSUE_X1
      asm volatile("s_waitcnt vmcnt(12)\n\ts_barrier" ::: "memory");
#pragma unroll 1
      for (int j = 3; j < 10; ++j) {
        ISSUE_X0
        BAR
        ISSUE_X1
        asm volatile("s_waitcnt vmcnt(12)\n\ts_barrier" ::: "memory");
      }
      const char* xq = (const char*)x + (size_t)r0 * 1280;
      const int nv = mt == 25 ? 480 : 800;
#define RES1(I) if ((I) < 7 || (I) * 64 + lane < nv) dma_m0<(((I) % 5) - 4) * 1024>(woff[(I) % 5], xq + ((I) / 5) * 40960, HIM + ((I) / 5) * 5120 + 4096);
      PREP_V
      BAR
      PREP_S
      asm volatile("s_waitcnt vmcnt(0)\n\ts_barrier" ::: "memory");
      RES1(0) RES1(1) RES1(2) RES1(3) RES1(4) RES1(5) RES1(6)
      BAR
      RES1(7) RES1(8) RES1(9) RES1(10) RES1(11) RES1(12)
      if (lane < 10) dma_m0<0>(l16, (const char*)w_r + hg * 160, WR_OFF);
      BAR
    } else {
      u32x2 ep;
      u32x4 ra;
      u32 rb5;
      BAR
      if (lane < 8) {
        *(u32x4*)(smem + ONES_OFF + lane * 16) = (u32x4){0x3F803F80u, 0x3F803F80u, 0x3F803F80u, 0x3F803F80u};
        *(u32x4*)(smem + CTL_OFF + lane * 16) = (u32x4){0u, 0u, 0u, 0u};
      }
      {
        const u32 o1 = lane == 26 || lane == 32 ? 0x3F803F80u : 0u;
        const u32 o2 = lane == 31 ? 0x3F803F80u : o1;
        if (lane < 33) *(u32x4*)(smem + REC_OFF + lane * 16) = (u32x4){o1, o1, o2, o2};
      }
      WO_OFFS
      asm volatile("" : "+v"(woff[0]), "+v"(woff[1]), "+v"(woff[2]), "+v"(woff[3]), "+v"(woff[4]));
      __builtin_amdgcn_s_sleep(TWS);
      if (lane < 5)
        touch_line(lane == 4 ? (const char*)wo + hg * 160
                             : (lane >= 2 ? ws + WS_PK - 65536 + (lane - 2) * 524288 + bid * 128
                                          : (const char*)out + (size_t)(r0 + lane * 47) * 1280 + hg * 160));
      LDS_BARRIER();
      BAR BAR
      asm volatile("global_load_dwordx2 %0, %1, off sc1" : "=v"(ep) : "v"(ws + WS_EPOCH) : "memory");
      asm volatile("global_load_dwordx4 %0, %2, %4\n\tglobal_load_dword %1, %3, %4 offset:1024"
                   : "=&v"(ra), "=&v"(rb5) : "v"(l16), "v"((u32)(lane * 4)), "s"(w_r) : "memory");
      BAR BAR BAR
      PREP_V
      BAR
      PREP_S
      BAR
      asm volatile("s_waitcnt vmcnt(0)" : "+v"(ep), "+v"(ra), "+v"(rb5)::"memory");
      const u64 e1 = (((u64)ep[1] << 32) | ep[0]) + (ep[0] == SENT - 1u ? 2ull : 1ull);
      float swr = lo_f(rb5) + hi_f(rb5);
#pragma unroll
      for (int e = 0; e < 4; ++e) swr += lo_f(ra[e]) + hi_f(ra[e]);
      swr = wave_sum(swr);
      if (lane == 0) {
        *(u32x2*)(smem + CTL_OFF) = (u32x2){(u32)e1, (u32)(e1 >> 32)};
        *(float*)(smem + CTL_OFF + 40) = swr;
      }
      int ra0 = r0 - 16 + lane, ra1 = r0 + 48 + (lane & 31);
      ra0 = ra0 < 0 ? 0 : (ra0 > SEQ - 1 ? SEQ - 1 : ra0);
      ra1 = ra1 > SEQ - 1 ? SEQ - 1 : ra1;
      const int hd = lane / 5, pt = lane - hd * 5;
      u32 avp = REC_OFF + (hd >> 1) * 192 + (pt < 4 ? pt * 48 + (hd & 1) * 8 : hd * 48 + 16), vvp = lane < 20 ? (lane >> 1) * 128 + (lane & 1) * 16 : 0;
      u32 vf = (lane < 48 ? lane : 0) * 128, va0 = ra0 * 8, va1 = ra1 * 8, vt0 = TAB_OFF + lane * 4, vt1 = TAB_OFF + 256 + (lane & 31) * 4;
      asm volatile("" : "+v"(avp), "+v"(vvp), "+v"(vf), "+v"(va0), "+v"(va1), "+v"(vt0), "+v"(vt1));
#pragma nounroll
      for (int h = 7; h <= HK0; ++h) BAR
      u32x2 f, t0, t1;
      u32x4 vp4;
      GLD2(f, vf, ws + WS_HFLAG, 0);
      GLD2(t0, va0, ws + WS_LN1, 0);
      GLD2(t1, va1, ws + WS_LN1, 0);
      asm volatile("global_load_dwordx4 %0, %1, %2 sc1" : "=v"(vp4) : "v"(vvp), "s"(ws + WS_VP + hg * 1280) : "memory");
#pragma unroll
      for (int h = HK0 + 1; h <= 17; ++h) BAR
      WO_RUN(10)
      WO_RUN(11)
      BAR
      asm volatile("s_waitcnt vmcnt(10)" : "+v"(t0), "+v"(t1), "+v"(f), "+v"(vp4)::"memory");
#pragma nounroll
      for (int it = 0; it < SPIN_CAP; ++it) {
        if (__all(t0[1] == (u32)e1 && t1[1] == (u32)e1 && f[0] == (u32)e1 && f[1] == (u32)(e1 >> 32) && vp4[1] == (u32)e1 && vp4[3] == (u32)e1)) break;
        asm volatile("global_load_dwordx2 %2, %6, %9 sc1\n\tglobal_load_dwordx2 %0, %4, %8 sc1\n\tglobal_load_dwordx2 %1, %5, %8 sc1\n\t"
                     "global_load_dwordx4 %3, %7, %10 sc1\n\ts_waitcnt vmcnt(0)"
                     : "=&v"(t0), "=&v"(t1), "=&v"(f), "=&v"(vp4)
                     : "v"(va0), "v"(va1), "v"(vf), "v"(vvp), "s"(ws + WS_LN1), "s"(ws + WS_HFLAG), "s"(ws + WS_VP + hg * 1280) : "memory");
      }
      *(u32*)(smem + vt0) = t0[0] << 16;
      *(u32*)(smem + vt1) = t1[0] << 16;
      *(float*)(smem + vt0 + 384) = __builtin_amdgcn_rcpf(hi_f(t0[0]));
      *(float*)(smem + vt1 + 384) = __builtin_amdgcn_rcpf(hi_f(t1[0]));
      if (lane < 20) *(u32x2*)(smem + avp) = (u32x2){vp4[0], vp4[2]};
      asm volatile("" ::: "memory");
      WO_RUN(14)
      WO_RUN(15)
      LDS_BARRIER();
      WO_RUN(16)
      WO_RUN(17)
    }
    __builtin_amdgcn_s_setprio(0);
  } else {
    const int cw = w - 4;
    prepv(lane);
    preps(mt, hg);
    if (cw >= 4) __builtin_amdgcn_s_setprio(1);
    const int xa0 = XRING + r16 * 128 + ((g ^ ((r16 >> 1) & 7)) * 16);
    const u32 m1 = r16 == 0 ? 0u : 1u;
    const u32 xz0 = r16 == 0 ? (u32)ONES_OFF : (u32)xa0;
    int wa[2];
#pragma unroll
    for (int c = 0; c < 2; ++c) {
      const int ct = cw < 7 ? 2 * cw + c : 14;
      const int seg = ct / 5, tt = ct - seg * 5;
      const int G = tt < 4 ? 5 * tt + p4 : 5 * p4 + 4;
      wa[c] = (16 * (g >> 1) + 4 * (g & 1) + q4) * 512 + ((seg * 160 + G * 8) ^ ((4 * (g & 1) + q4) << 5));
    }
    auto run = [&](auto tyc) __attribute__((always_inline)) {
      constexpr int TY = decltype(tyc)::value;
      constexpr int NW = TY == 2 ? 1 : 2;
      f32x4 acc[2][6];
#pragma unroll
      for (int c = 0; c < 2; ++c)
#pragma unroll
        for (int r = 0; r < 6; ++r) acc[c][r] = (f32x4){0.f, 0.f, 0.f, 0.f};
      u32x4 xA[6], xB[6];
      u32x2 wA[4], wB[4];
#define RX(D, A, O) asm volatile("ds_read_b128 %0, %1 offset:%2" : "=v"(D) : "v"(A), "n"(O) : "memory");
#define RW(D, A, O) asm volatile("ds_read_b64_tr_b16 %0, %1 offset:%2" : "=v"(D) : "v"(A), "n"(O) : "memory");
#define MM(C, R, XF, WF)                                                                           \
  if ((C) < NW) {                                                                                    \
    const bf16x8 wf_ = mk8(WF[2 * (C)][0], WF[2 * (C)][1], WF[2 * (C) + 1][0], WF[2 * (C) + 1][1]); \
    const bf16x8 xf_ = __builtin_bit_cast(bf16x8, XF[R]);                                          \
    acc[C][R] = TY == 0 ? MFMA(wf_, xf_, acc[C][R]) : MFMA(xf_, wf_, acc[C][R]);                   \
    __builtin_amdgcn_sched_barrier(0);                                                             \
  }
#define READS(QX, QW)                                                                              \
  RW(QW[0], aw0, 0) RW(QW[1], aw0, 4096) RX(QX[1], ax, 2048)                                       \
  if (NW == 2) { RW(QW[2], aw1, 0) RW(QW[3], aw1, 4096) }                                          \
  RX(QX[2], ax, 4096) RX(QX[3], ax, 6144) RX(QX[4], ax, 8192) RX(QX[5], ax, 10240) RX(QX[0], az, 0)
#define ARRIVED(PX, PW)                                                                            \
  if (NW == 2)                                                                                     \
    asm volatile("s_waitcnt lgkmcnt(0)" : "+v"(PX[0]), "+v"(PX[1]), "+v"(PX[2]), "+v"(PX[3]), "+v"(PX[4]), "+v"(PX[5]), \
                 "+v"(PW[0]), "+v"(PW[1]), "+v"(PW[2]), "+v"(PW[3])::"memory");                    \
  else                                                                                             \
    asm volatile("s_waitcnt lgkmcnt(0)" : "+v"(PX[0]), "+v"(PX[1]), "+v"(PX[2]), "+v"(PX[3]), "+v"(PX[4]), "+v"(PX[5]), \
                 "+v"(PW[0]), "+v"(PW[1])::"memory");                                              \
  __builtin_amdgcn_sched_barrier(0);
#define SB __builtin_amdgcn_sched_barrier(0);
#define WT(N, ...) asm volatile("s_waitcnt lgkmcnt(" #N ")" : __VA_ARGS__::"memory"); SB
#define HSTAGE2(PX, PW, QX, QW, NX)                                                                \
  BAR                                                                                              \
  WT(7, "+v"(PW[0]), "+v"(PW[1]), "+v"(PX[1]))                                                     \
  MM(0, 1, PX, PW) RW(QW[0], aw0, 0)                                                               \
  WT(6, "+v"(PW[2]), "+v"(PW[3]))                                                                  \
  MM(1, 1, PX, PW) RW(QW[1], aw0, 4096)                                                            \
  WT(6, "+v"(PX[2]))                                                                               \
  MM(0, 2, PX, PW) RX(QX[1], ax, 2048) SB                                                          \
  MM(1, 2, PX, PW) RW(QW[2], aw1, 0)                                                               \
  WT(7, "+v"(PX[3]))                                                                               \
  MM(0, 3, PX, PW) RW(QW[3], aw1, 4096) SB                                                         \
  MM(1, 3, PX, PW) RX(QX[2], ax, 4096)                                                             \
  WT(6, "+v"(PX[4]), "+v"(PX[5]), "+v"(PX[0]))                                                     \
  MM(0, 4, PX, PW) RX(QX[3], ax, 6144) SB                                                          \
  MM(1, 4, PX, PW) RX(QX[4], ax, 8192) SB                                                          \
  MM(0, 5, PX, PW) RX(QX[5], ax, 10240) SB                                                         \
  MM(1, 5, PX, PW) RX(QX[0], az, 0) SB                                                             \
  MM(0, 0, PX, PW)                                                                                 \
  NX asm volatile("" : "+v"(aw0), "+v"(aw1), "+v"(ax), "+v"(az), "+s"(so), "+s"(xo)); SB           \
  MM(1, 0, PX, PW)
#define HSTAGE1(PX, PW, QX, QW, NX)                                                                \
  BAR                                                                                              \
  ARRIVED(PX, PW)                                                                                  \
  MM(0, 1, PX, PW) RW(QW[0], aw0, 0) RW(QW[1], aw0, 4096) SB                                       \
  MM(0, 2, PX, PW) RX(QX[1], ax, 2048) RX(QX[2], ax, 4096) SB                                      \
  MM(0, 3, PX, PW) RX(QX[3], ax, 6144) RX(QX[4], ax, 8192) SB                                      \
  MM(0, 4, PX, PW) RX(QX[5], ax, 10240) RX(QX[0], az, 0) SB                                        \
  MM(0, 5, PX, PW)                                                                                 \
  NX asm volatile("" : "+v"(aw0), "+v"(ax), "+v"(az), "+s"(so), "+s"(xo)); SB                      \
  MM(0, 0, PX, PW)
#define HSTAGE(...) if (NW == 2) { HSTAGE2(__VA_ARGS__) } else { HSTAGE1(__VA_ARGS__) }
      u32 aw0 = wa[0], aw1 = wa[1], ax = xa0, az = xz0;
      asm volatile("s_waitcnt vmcnt(0)\n\ts_barrier" ::: "memory");
      READS(xA, wA)
      aw0 += HSLOT; aw1 += HSLOT; ax ^= 64; az ^= 64;
      int so = 2 * HSLOT, xo = XSLOT;
#pragma unroll 1
      for (int s = 0; s < 9; ++s) {
        HSTAGE(xA, wA, xB, wB, aw0 = wa[0] + so; aw1 = wa[1] + so; ax = xa0 + xo; az = xz0 + xo * m1;
               so = so >= 4 * HSLOT ? 0 : so + 2 * HSLOT; xo = xo >= 3 * XSLOT ? 0 : xo + XSLOT;)
        HSTAGE(xB, wB, xA, wA, aw0 += HSLOT; aw1 += HSLOT; ax ^= 64; az ^= 64;)
      }
      {
        HSTAGE(xA, wA, xB, wB, )
        ARRIVED(xB, wB)
        BAR
        SB
      }
      char* ex = smem + EXO + cw * 6144 + lane * 8;
      if (TY == 0) {
        float st[6];
        const u32 ta = TAB_OFF + r16 * 4;
#define RT(R) asm volatile("ds_read_b32 %0, %1 offset:%2" : "=v"(st[R]) : "v"(ta), "n"(64 * (R)) : "memory");
        RT(1) RT(2) RT(3) RT(4) RT(5) RT(0)
        float cs[2][4];
#define FMS(A, M, CS) ((A) - (M) * (CS))
#define EP0(C, R)                                                                                  \
  {                                                                                                \
    const u32 a = cvt2((f32x2){FMS(acc[C][R][0], st[R], cs[C][0]), FMS(acc[C][R][1], st[R], cs[C][1])}); \
    const u32 b = cvt2((f32x2){FMS(acc[C][R][2], st[R], cs[C][2]), FMS(acc[C][R][3], st[R], cs[C][3])}); \
    *(u32x2*)(ex + (C) * 3072 + (R) * 512) = (u32x2){a, b};                                        \
    SB                                                                                             \
  }
        MM(0, 0, xB, wB) MM(1, 0, xB, wB) MM(0, 1, xB, wB) MM(1, 1, xB, wB)
#pragma unroll
        for (int i = 0; i < 4; ++i) cs[0][i] = dppf<0x150>(acc[0][0][i]);
        SB
        MM(0, 2, xB, wB)
#pragma unroll
        for (int i = 0; i < 4; ++i) cs[1][i] = dppf<0x150>(acc[1][0][i]);
        WT(0, "+v"(st[0]), "+v"(st[1]), "+v"(st[2]), "+v"(st[3]), "+v"(st[4]), "+v"(st[5]))
        MM(1, 2, xB, wB) EP0(0, 1)
        MM(0, 3, xB, wB) EP0(1, 1)
        MM(1, 3, xB, wB) EP0(0, 2)
        MM(0, 4, xB, wB) EP0(1, 2)
        MM(1, 4, xB, wB) EP0(0, 3)
        MM(0, 5, xB, wB) EP0(1, 3)
        MM(1, 5, xB, wB)
        if (cw >= 2) {
          EP0(1, 0)
          if (cw >= 3) EP0(0, 0)
        }
        EP0(0, 4) EP0(1, 4) EP0(0, 5) EP0(1, 5)
      } else {
        f32x4 mbv[6];
        const u32 ta = TAB_OFF + g * 16;
#define RV(R) RX(mbv[R], ta, 64 * (R))
        RV(1) RV(2) RV(3) RV(4) RV(5) RV(0)
        float cs[2];
#define CS1(C)                                                                                     \
  if ((C) < NW) {                                                                                  \
    const u32 v0 = __float_as_uint(acc[C][0][0]);                                                  \
    const u32 v1 = __builtin_amdgcn_permlane16_swap(v0, v0, false, false)[0];                      \
    cs[C] = __uint_as_float(__builtin_amdgcn_permlane32_swap(v1, v1, false, false)[0]);            \
    SB                                                                                             \
  }
#define EP1(C, R)                                                                                  \
  if ((C) < NW) {                                                                                  \
    const f32x4 v = (f32x4){FMS(acc[C][R][0], mbv[R][0], cs[C]), FMS(acc[C][R][1], mbv[R][1], cs[C]), FMS(acc[C][R][2], mbv[R][2], cs[C]), FMS(acc[C][R][3], mbv[R][3], cs[C])}; \
    *(u32x2*)(ex + (C) * 3072 + (R) * 512) = (u32x2){cvt2((f32x2){v[0], v[1]}), cvt2((f32x2){v[2], v[3]})}; \
    SB                                                                                             \
  }
        MM(0, 0, xB, wB) MM(1, 0, xB, wB) MM(0, 1, xB, wB) MM(1, 1, xB, wB)
        CS1(0)
        MM(0, 2, xB, wB)
        CS1(1)
        WT(0, "+v"(mbv[0]), "+v"(mbv[1]), "+v"(mbv[2]), "+v"(mbv[3]), "+v"(mbv[4]), "+v"(mbv[5]))
        MM(1, 2, xB, wB) EP1(0, 1)
        MM(0, 3, xB, wB) EP1(1, 1)
        MM(1, 3, xB, wB) EP1(0, 2)
        MM(0, 4, xB, wB) EP1(1, 2)
        MM(1, 4, xB, wB) EP1(0, 3)
        MM(0, 5, xB, wB) EP1(1, 3)
        MM(1, 5, xB, wB)
        EP1(0, 0) EP1(1, 0) EP1(0, 4) EP1(1, 4) EP1(0, 5) EP1(1, 5)
      }
    };
    if (cw < 5) run(icst<0>{});
    else if (cw < 7) run(icst<1>{});
    else run(icst<2>{});
  }
  __builtin_amdgcn_s_setprio(0);
  const bool live = r0 + rt * 16 < SEQ;
  if (!live) {
    if (w >= 2) LDS_BARRIER();
    if (w != 7) {
      asm volatile("s_waitcnt vmcnt(0)" ::: "memory");
      return;
    }
  } else if (w >= 2)
    asm volatile(ATT_ASM
                 :: [kh] "s"(kh), [l16] "v"(l16), [os] "s"(os), [os4] "s"(os4), [b0] "v"(bias[0]), [b1] "v"(bias[1]),
                    [s0] "v"(sel[0]), [s1] "v"(sel[1]), [s2] "v"(sel[2]), [s3] "v"(sel[3]), [e4] "v"(e4), [eh] "v"(eh), [aq] "v"(aq),
                    [aka] "v"(aka), [akb] "v"(akb), [arec] "v"(arec), [vpp] "v"(vpp), [sst] "s"(sst), [w] "s"(w)
                 : "memory", "s41", "s46", "s47", "s48", "s49", "s50", "s51", "scc", "v24", "v25", "v26", "v27", "v28", "v29", "v30", "v31", "v32", "v33", "v34", "v35", "v36", "v37", "v38", "v39", "v40", "v41", "v42", "v43", "v44", "v45", "v46", "v47", "v48", "v49", "v50", "v51", "v52", "v53", "v54", "v55", "v56", "v57", "v58", "v59", "v60", "v61", "v62", "v63", "v64", "v65", "v66", "v67", "v68", "v69", "v70", "v71", "v72", "v73", "v74", "v75", "v76", "v77", "v78", "v79", "v80", "v81", "v82", "v83", "v84", "v85", "v86", "v87");
  u32x4 hwa, hwb;
  u32x2 hwc;
  const int grp = rt < 3 ? 0 : 1;
  if (w < 8) {
    const int rmax = mt == 25 ? 2 : 4;
    const int rk = rt < rmax ? rt : rmax, rd = (rt ^ 1) < rmax ? (rt ^ 1) : rmax;
    u32 ta, tk;
    if (w == 0) asm volatile("s_waitcnt vmcnt(0)" ::: "memory");
    if (w == 2 || w == 3) asm volatile("s_waitcnt vmcnt(3)" ::: "memory");
    asm volatile("s_cmp_lg_u32 %[w], 1\n\ts_cbranch_scc1 2f\n\ts_setprio 3\n"
                 "1:\n\ts_memrealtime vcc\n\ts_waitcnt lgkmcnt(0)\n\ts_sub_u32 vcc_lo, vcc_lo, %[go]\n\ts_cmp_lt_i32 vcc_lo, 0\n\ts_cbranch_scc1 1b\n\ts_setprio 0\n"
                 "2:\n\ts_waitcnt lgkmcnt(0)\n\ts_barrier\n\t" OP_ASM
                 : "=&{v[84:87]}"(hwa), "=&{v[88:91]}"(hwb), "=&{v[92:93]}"(hwc), [ta] "=&v"(ta), [tk] "=&v"(tk)
                 : [w] "s"(w), [go] "s"(tgo), [l16] "v"(l16), [oa0] "s"(oa[0]), [so0] "s"(so_[0]), [oa1] "s"(oa[1]), [so1] "s"(so_[1]), [oa2] "s"(oa[2]), [so2] "s"(so_[2]), [oa3] "s"(oa[3]), [so3] "s"(so_[3]), [oa4] "s"(oa[4]), [so4] "s"(so_[4]), [oa5] "s"(oa[5]), [so5] "s"(so_[5]), [oa6] "s"(oa[6]), [so6] "s"(so_[6]), [oa7] "s"(oa[7]), [so7] "s"(so_[7]), [oa8] "s"(oa[8]), [so8] "s"(so_[8]), [oa9] "s"(oa[9]), [so9] "s"(so_[9]),
                   [pm] "v"(pm), [pf] "v"(pf), [ra] "v"(w < 3 ? (u32)(CTL_OFF + 72) : hi), [e4] "v"(e4),
                   [aw] "v"((lane & 1) ? (u32)(WR_OFF + g * 40) : (u32)ONES_OFF),
                   [vk] "v"(l16 + rk * 20480), [vd] "v"(l16 + rd * 20480), [sk] "s"((w & 1) * 6144),
                   [sd] "s"(EXO + (w < 4 ? 4 + w : rt ^ 1) * 5120), [sr] "s"(EXO + rt * 5120),
                   [sc] "s"(CTL_OFF + 48 + (w < 4 ? 4 : rt ^ 1) * 4), [sp] "s"(PST_OFF + rt * 256), [sa] "s"(CTL_OFF + 8 + grp * 4),
                   [sst] "s"(sst), [cp] "s"(ws + WS_PC + (2 * mt + grp) * 1280 + hg * 160), [oback] "n"(OBACK), [cap] "n"(SPIN_CAP)
                 : "memory", "vcc", "scc", "s43", "v24", "v25", "v26", "v27", "v28", "v29", "v30", "v31", "v32", "v33", "v34", "v35", "v36", "v37", "v38", "v39", "v40", "v41", "v42", "v43", "v44", "v45", "v46", "v47", "v48", "v49", "v50", "v51", "v52", "v53", "v54", "v55", "v56", "v57", "v58", "v59", "v60", "v61", "v62", "v63", "v64", "v65", "v66", "v67", "v68", "v69", "v70", "v71", "v72", "v73", "v74", "v75", "v76", "v77", "v78", "v79", "v80", "v81", "v82", "v83", "v94", "v95", "v96", "v97", "v98", "v99", "v100", "v101", "v102", "v103", "v104", "v105", "v106", "v107", "v108", "v109", "v110", "v111", "v112", "v113", "v114", "v115", "v116", "v117", "v118", "v119", "v120", "v121", "v122", "v123", "v124", "v125", "v126", "v127", "v128", "v129", "v130", "v131", "v132", "v133", "v134", "v135", "v136", "v137", "v138", "v139", "v140", "v141", "v142", "v143", "v144", "v145", "v146", "v147", "v148", "v149", "v150", "v151", "v152", "v153", "v154", "v155", "v156", "v157", "v158", "v159");
    if (!live || w < 3) return;
  } else {
    u64 tb;
    asm volatile("s_setprio 3\n\ts_waitcnt lgkmcnt(0)\n\ts_barrier\n\ts_memrealtime %0\n\ts_waitcnt lgkmcnt(0)" : "=s"(tb)::"memory");
    if (w == 8 || w == 11) {
      const int cg = w == 11, gid = 2 * mt + cg;
      const char* sp = sst - hg * 1024;
      const char* sp2 = sp + (cg ? 0 : 7936);
      const u32 vq = (lane >> 3) * 1024 + (lane & 7) * 16 + (cg ? 640 : 768);
      u32x4 pq[3];
      u64 t2;
#define PLOADS "global_load_dwordx4 %0, %4, %5 sc1\n\tglobal_load_dwordx4 %1, %4, %5 offset:128 sc1\n\tglobal_load_dwordx4 %2, %4, %6 sc1\n\t"
      asm volatile("1:\n\ts_memrealtime %3\n\ts_waitcnt lgkmcnt(0)\n\ts_sub_u32 s40, %7, s40\n\ts_cmp_gt_i32 s40, 0\n\ts_cbranch_scc1 1b\n\t" PLOADS "s_setprio 1"
                   : "=&v"(pq[0]), "=&v"(pq[1]), "=&v"(pq[2]), "=&{s[40:41]}"(t2) : "v"(vq), "s"(sp), "s"(sp2), "s"((u32)tb + PDT) : "memory", "scc");
      int lane2 = lane;
      asm volatile("" : "+v"(lane2));
      const int n16 = lane2 & 15;
      u32x4 sl;
      u32x2 km;
#pragma unroll
      for (int e = 0; e < 4; ++e) sl[e] = n16 == 2 * e ? 0x3F80u : (n16 == 2 * e + 1 ? 0x3F800000u : 0u);
      u32 rid = (u32)((((lane2 >> 4) & 1) * 2 + (lane2 >> 5)) * 4 + (lane2 & 3)) << 10;
      u32 rid1 = rid + (16 << 10), rid2 = rid + (cg ? 0 : 32 << 10), msk = n16 < 4 ? 0xFFFF0000u : 0u;
      u32 ah = HIM + cg * 7680 + (lane2 < 10 ? lane2 : 9) * 16;
      km[1] = *(const u32*)(smem + CTL_OFF);
      const char* kp = ws + WS_PK + gid * 6144 + hg * 768;
      const char* cp = ws + WS_PC + gid * 1280 + hg * 160;
      asm volatile("" : "+s"(kp), "+s"(cp));
      km[0] = ((u32)tb >> 1) & 1023u;
      asm volatile("s_setprio " "3" "\n\ts_waitcnt vmcnt(0)" : "+v"(pq[0]), "+v"(pq[1]), "+v"(pq[2]), "+v"(sl), "+v"(rid), "+v"(rid1), "+v"(rid2), "+v"(msk), "+v"(ah), "+v"(km)::"memory");
#define PMISS __any(max(max(max(pq[0][0], pq[0][2]), max(pq[1][0], pq[1][2])), max(pq[2][0], pq[2][2])) == SENT)
      if (__builtin_expect(PMISS, 0)) {
#pragma nounroll
        for (int it = 0; it < SPIN_CAP; ++it) {
          asm volatile(PLOADS "s_waitcnt vmcnt(0)" : "=&v"(pq[0]), "=&v"(pq[1]), "=&v"(pq[2]), "=&s"(t2) : "v"(vq), "s"(sp), "s"(sp2) : "memory");
          if (!PMISS) break;
          __builtin_amdgcn_s_sleep(2);
        }
      }
      u32 pkey = 0;
#pragma unroll
      for (int t = 0; t < 3; ++t) {
        const f32x4 ys = MFMA(__builtin_bit_cast(bf16x8, pq[t]), __builtin_bit_cast(bf16x8, sl), ((f32x4){0.f, 0.f, 0.f, 0.f}));
        const float y = tr_sum(ys);
        pkey = max(pkey, (mono(y * __builtin_amdgcn_rcpf(dppf<0x104>(y))) & msk) | (t == 0 ? rid : (t == 1 ? rid1 : rid2)));
      }
      pkey = max(pkey, dppu<0xB1>(pkey));
      pkey = max(pkey, dppu<0x4E>(pkey));
      pkey = max(max((u32)__builtin_amdgcn_readlane(pkey, 0), (u32)__builtin_amdgcn_readlane(pkey, 16)),
                 max((u32)__builtin_amdgcn_readlane(pkey, 32), (u32)__builtin_amdgcn_readlane(pkey, 48)));
      km[0] |= pkey;
      asm volatile("s_mov_b32 exec_lo, 0x01010101\n\ts_mov_b32 exec_hi, 0x0101\n\tglobal_store_dwordx2 %0, %1, %2 sc1\n\ts_mov_b64 exec, -1\n\ts_setprio 2" ::"v"(l16), "v"(km), "s"(kp) : "memory");
      ah += ((pkey >> 10) & 63u) * 160;
      if (lane == 0) *(u32*)(smem + CTL_OFF + 24 + cg * 4) = ah;
      __builtin_amdgcn_s_setprio(0);
#pragma nounroll
      for (int it = 0; it < 32; ++it) {
        if ((int)((u32)__builtin_amdgcn_s_memrealtime() - (u32)tb) >= PSL) break;
        __builtin_amdgcn_s_sleep(6);
      }
      __builtin_amdgcn_s_setprio(2);
      asm volatile("s_mov_b32 s40, %[cap]\n"
                   "1:\n\tds_read_b32 v100, %[ac]\n\tds_read_b32 v101, %[ac] offset:24\n\tds_read_b128 v[96:99], %[ah]\n\ts_sub_u32 s40, s40, 1\n\ts_cbranch_scc1 2f\n\ts_waitcnt lgkmcnt(0)\n\t"
                   "v_cmp_gt_u32 vcc, %[nf], v100\n\ts_cbranch_vccz 2f\n\ts_sleep 2\n\ts_branch 1b\n"
                   "2:\n\ts_waitcnt lgkmcnt(0)\n\tv_cmp_eq_u32 vcc, 0, v101\n\ts_cbranch_vccz 3f\n\t"
                   "s_mov_b64 exec, 0x3ff\n\tglobal_store_dwordx4 %[l16], v[96:99], %[cp] sc1\n\ts_mov_b64 exec, -1\n3:"
                   ::[cap] "n"(SPIN_CAP * 8), [ac] "v"((u32)(CTL_OFF + 8 + cg * 4)), [ah] "v"(ah), [nf] "s"(3 - cg), [l16] "v"(l16), [cp] "s"(cp)
                   : "memory", "vcc", "scc", "s40", "v96", "v97", "v98", "v99", "v100", "v101");
      return;
    }
    __builtin_amdgcn_s_setprio(0);
    if (w == 10 && mt == 25) return;
    const int cg = w - 9, gid = 2 * mt + cg;
    const int nr = cg ? 31 : 47;
    const int lr = lane < nr ? lane : nr;
    const int lrow = cg * 48 + lr;
    const int li = (lane & 31) < 20 ? (lane & 31) : 19;
    asm volatile(COL_ASM
                 :
                 : [lq] "v"((u32)(lr * 16 + hg * 1024)), [ac] "v"((u32)(CTL_OFF + 8 + cg * 4)), [nf] "s"(3 - cg),
                   [sp] "s"(ws + WS_STAT + (size_t)gid * 8192), [ao] "v"((u32)(PST_OFF + lrow * 16)), [rdm] "s"(1.0f / DM),
                   [swr] "v"(*(const float*)(smem + CTL_OFF + 40)), [msk] "s"(0xFFFFF000u), [rid] "v"(0x800u | (u32)(2047 - (r0 + lrow))),
                   [at] "v"((u32)(TAB_OFF + lrow * 8)), [zr] "v"(l16), "{v99}"(*(const u32*)(smem + CTL_OFF)),
                   [kp] "s"(ws + WS_PK + gid * 6144 + hg * 768 + 8), [ak] "v"((u32)(CTL_OFF + 16 + cg * 4)), [rb] "s"(2047 - r0 - cg * 48),
                   [ah] "v"((u32)(HIM + cg * 7680 + li * 8)), [co] "v"((u32)((lane < 32 ? 1280 : 0) + li * 8)),
                   [cp] "s"(ws + WS_CAND + gid * 2560 + hg * 160), [pdel] "n"(PDEL2), [cap] "n"(SPIN_CAP)
                 : "memory", "vcc", "scc", "s40", "s41", "s42", "s43", "s44", "s45", "s46", "s47", "v64", "v65", "v66", "v67", "v68", "v69", "v70", "v71", "v72", "v73", "v74", "v75", "v76", "v77", "v78", "v79", "v80", "v81", "v82", "v83", "v84", "v85", "v86", "v87", "v88", "v89", "v90", "v91", "v92", "v93", "v94", "v95", "v96", "v97", "v98", "v100", "v101", "v102", "v103", "v104", "v105", "v106", "v107", "v108", "v109", "v110", "v111", "v112", "v113", "v114", "v115", "v116", "v117", "v118", "v119", "v120", "v121", "v122", "v123");
    return;
  }
  const u32 hw[10] = {hwa[0], hwa[1], hwa[2], hwa[3], hwb[0], hwb[1], hwb[2], hwb[3], hwc[0], hwc[1]};
  f32x2 hf[10];
#pragma unroll
  for (int e = 0; e < 10; ++e) {
    hf[e] = unpk(hw[e]);
    asm volatile("" : "+v"(hf[e]));
  }
  const char* obase = (const char*)out + (size_t)r0 * 1280 + hg * 160;
  u32 tilekey;
  asm volatile("" : "+s"(obase));
  u32 aow = EXO + rt * 5120 + r16 * 160 + g * 40, aor = EXO + rt * 5120 + l16, vo3[3], rr3[3];
#pragma unroll
  for (int i = 0; i < 3; ++i) {
    const u32 c = i * 64 + lane, r = c / 10u;
    vo3[i] = (rt * 16 + r) * 1120 + rt * 2560 + c * 16;
    rr3[i] = c < 160u ? 2047 - (r0 + rt * 16 + r) : 4096u;
    asm volatile("" : "+v"(vo3[i]), "+v"(rr3[i]));
  }
  asm volatile("" : "+v"(aow), "+v"(aor));
  f32x2 st2;
  asm volatile("s_mov_b32 s40, %[cap]\n"
               "1:\n\ts_sleep 1\n\tds_read_b32 %[k], %[ak]\n\tds_read_b64 %[s], %[at]\n\ts_sub_u32 s40, s40, 1\n\ts_waitcnt lgkmcnt(0)\n\ts_cbranch_scc1 2f\n\t"
               "v_cmp_eq_u32 vcc, 0, %[k]\n\ts_cbranch_vccnz 1b\n"
               "2:"
               : [k] "=&v"(tilekey), [s] "=&v"(st2) : [cap] "n"(SPIN_CAP * 8), [ak] "v"((u32)(CTL_OFF + 16 + grp * 4)), [at] "v"((u32)(TAB_OFF + (rt * 16 + r16) * 8))
               : "memory", "vcc", "scc", "s40");
  u32 ok[10];
#pragma unroll
  for (int e = 0; e < 10; ++e) ok[e] = cvt2(__builtin_elementwise_fma(hf[e] - (f32x2){st2[0], st2[0]}, (f32x2){st2[1], st2[1]}, hf[e]));
  {
#pragma unroll
    for (int e = 0; e < 5; ++e) asm volatile("ds_write_b64 %0, %1 offset:%2" ::"v"(aow), "v"((u32x2){ok[2 * e], ok[2 * e + 1]}), "n"(e * 8) : "memory");
    u32x4 o3[3];
    asm volatile("ds_read_b128 %0, %3\n\tds_read_b128 %1, %3 offset:1024\n\tds_read_b128 %2, %3 offset:2048\n\ts_waitcnt lgkmcnt(0)"
                 : "=&v"(o3[0]), "=&v"(o3[1]), "=&v"(o3[2]) : "v"(aor) : "memory");
#pragma unroll
    for (int i = 0; i < 3; ++i)
      if (rr3[i] != (tilekey & 2047u) && rr3[i] != 4096u)
        asm volatile("global_store_dwordx4 %0, %1, %2" " sc1" "\n\ts_nop 1" ::"v"(vo3[i]), "v"(o3[i]), "s"(obase) : "memory");
  }
}

extern "C" void kernel_launch(void* const* d_in, const int* in_sizes, int n_in,
                              void* d_out, int out_size, void* d_ws, size_t ws_size,
                              hipStream_t stream) {
  const u16* x = (const u16*)d_in[0];
  const u16* wqkv = (const u16*)d_in[1];
  const u16* wo = (const u16*)d_in[2];
  const u16* w_r = (const u16*)d_in[4];
  const u16* w_gate = (const u16*)d_in[5];
  const u16* w_up = (const u16*)d_in[6];
  const u16* w_down = (const u16*)d_in[7];
  (void)hipFuncSetAttribute((const void*)fused_block, hipFuncAttributeMaxDynamicSharedMemorySize, LDS_TOTAL);
  fused_block<<<256, 768, LDS_TOTAL, stream>>>(x, wqkv, wo, w_r, w_gate, w_up, w_down, (u16*)d_out, (char*)d_ws);
}
```

```cpp
#include <hip/hip_runtime.h>
#include <hip/hip_bf16.h>
#include <stdint.h>


#define SEQ 2048
#define DM 640
#define FF 1536

typedef __attribute__((ext_vector_type(8))) short bf16x8;
typedef __attribute__((ext_vector_type(4))) short s16x4;
typedef __attribute__((ext_vector_type(4))) float f32x4;
typedef __attribute__((ext_vector_type(2))) float f32x2;
typedef __attribute__((ext_vector_type(2))) __bf16 bf16x2_t;
typedef __attribute__((address_space(3))) s16x4* lds_s16x4_p;
typedef unsigned short u16;
typedef unsigned int u32;
typedef unsigned long long u64;
typedef __attribute__((ext_vector_type(4))) unsigned int u32x4;
typedef __attribute__((ext_vector_type(2))) unsigned int u32x2;

#define HK0 15
#define CGAP 6
#define KGAP 7
#define CDEL 10
#define PDEL2 7
#define OBACK 2
#define PDELAY 220
#define FDELAY 450
#define KSTART 1050
#define PGAP 5
#define PDT 20
#define PSL 185
#define VDELAY 150
#define CCD 312
#define TQD 160
#define KCD 306
#define KOLD 316
#define TWS 12
#define PCD 230
#define PTHR 0.91f
#define GADD 80
#define OORD 0, 4, 5, 6, 7, 1, 2, 3
#define CODE_BYTES 35264

#define LDS_BARRIER() do { __builtin_amdgcn_sched_barrier(0); asm volatile("s_waitcnt lgkmcnt(0)\n\ts_barrier" ::: "memory"); __builtin_amdgcn_sched_barrier(0); } while (0)
__device__ __forceinline__ float bf2f(u16 v) { return __uint_as_float(((u32)v) << 16); }
__device__ __forceinline__ u16 f2bf(float f) { return __builtin_bit_cast(u16, (__bf16)f); }
__device__ __forceinline__ float rbf(float f) { return bf2f(f2bf(f)); }
__device__ __forceinline__ float lo_f(u32 w) { return __uint_as_float(w << 16); }
__device__ __forceinline__ float hi_f(u32 w) { return __uint_as_float(w & 0xFFFF0000u); }
__device__ __forceinline__ u32 cvt2(f32x2 v) { return __builtin_bit_cast(u32, __builtin_convertvector(v, bf16x2_t)); }
__device__ __forceinline__ f32x2 unpk(u32 w) { return (f32x2){lo_f(w), hi_f(w)}; }
__device__ __forceinline__ s16x4 tr_read(const char* p) {
  return __builtin_amdgcn_ds_read_tr16_b64_v4i16((lds_s16x4_p)(p));
}
template <int V> struct icst { static constexpr int value = V; };
template <int CTRL>
__device__ __forceinline__ float dppf(float v) {
  return __builtin_bit_cast(float, __builtin_amdgcn_update_dpp(0, __builtin_bit_cast(int, v), CTRL, 0xf, 0xf, true));
}
__device__ __forceinline__ float row8_sum(float v) {
  v += dppf<0xB1>(v);
  v += dppf<0x4E>(v);
  v += dppf<0x141>(v);
  return v;
}
__device__ __forceinline__ float row16_sum(float v) {
  v = row8_sum(v);
  v += dppf<0x140>(v);
  return v;
}
__device__ __forceinline__ float xg_sum(float v) {
  u32 a = __float_as_uint(v);
  auto r = __builtin_amdgcn_permlane32_swap(a, a, false, false);
  v = __uint_as_float(r[0]) + __uint_as_float(r[1]);
  a = __float_as_uint(v);
  auto r2 = __builtin_amdgcn_permlane16_swap(a, a, false, false);
  return __uint_as_float(r2[0]) + __uint_as_float(r2[1]);
}
__device__ __forceinline__ float tr_sum(f32x4 ys) {
  auto r01 = __builtin_amdgcn_permlane32_swap(__float_as_uint(ys[0]), __float_as_uint(ys[1]), false, false);
  auto r23 = __builtin_amdgcn_permlane32_swap(__float_as_uint(ys[2]), __float_as_uint(ys[3]), false, false);
  const u32 u = __float_as_uint(__uint_as_float(r01[0]) + __uint_as_float(r01[1]));
  const u32 vv = __float_as_uint(__uint_as_float(r23[0]) + __uint_as_float(r23[1]));
  auto r2 = __builtin_amdgcn_permlane16_swap(u, vv, false, false);
  return __uint_as_float(r2[0]) + __uint_as_float(r2[1]);
}
__device__ __forceinline__ float wave_sum(float v) { return xg_sum(row16_sum(v)); }
template <int CTRL>
__device__ __forceinline__ u32 dppu(u32 v) { return (u32)__builtin_amdgcn_update_dpp(0, (int)v, CTRL, 0xf, 0xf, true); }
__device__ __forceinline__ u32 wave_max(u32 v) {
  v = max(v, dppu<0xB1>(v)); v = max(v, dppu<0x4E>(v)); v = max(v, dppu<0x141>(v)); v = max(v, dppu<0x140>(v));
  return max(max((u32)__builtin_amdgcn_readlane(v, 0), (u32)__builtin_amdgcn_readlane(v, 16)),
             max((u32)__builtin_amdgcn_readlane(v, 32), (u32)__builtin_amdgcn_readlane(v, 48)));
}
__device__ __forceinline__ u32 mono(float f) {
  const u32 b = __float_as_uint(f);
  return b ^ ((u32)((int)b >> 31) | 0x80000000u);
}
__device__ __forceinline__ float dot2b(u32 a, u32 b, float c) {
  return __builtin_amdgcn_fdot2_f32_bf16(__builtin_bit_cast(bf16x2_t, a), __builtin_bit_cast(bf16x2_t, b), c, false);
}
template <int OFF>
__device__ __forceinline__ void dma_m0(u32 voff, const char* sbase, u32 dst) {
  asm volatile("s_mov_b32 m0, %2\n\ts_nop 0\n\tglobal_load_lds_dwordx4 %0, %1 offset:%3"
               :: "v"(voff), "s"(sbase), "s"(dst), "n"(OFF) : "memory");
}
template <int OFF>
__device__ __forceinline__ void dma_nx(u32 voff, const char* sbase) {
  asm volatile("global_load_lds_dwordx4 %0, %1 offset:%2" :: "v"(voff), "s"(sbase), "n"(OFF) : "memory");
}
__device__ __forceinline__ u32 lds_ld32(u32 addr) {
  u32 v;
  asm volatile("ds_read_b32 %0, %1\n\ts_waitcnt lgkmcnt(0)" : "=v"(v) : "v"(addr) : "memory");
  return v;
}
__device__ __forceinline__ u32x4 lds_ld128(u32 addr) {
  u32x4 v;
  asm volatile("ds_read_b128 %0, %1\n\ts_waitcnt lgkmcnt(0)" : "=v"(v) : "v"(addr) : "memory");
  return v;
}
__device__ __forceinline__ bf16x8 mk8(u32 a, u32 b, u32 c, u32 d) {
  return __builtin_bit_cast(bf16x8, (u32x4){a, b, c, d});
}
__device__ __forceinline__ bf16x8 tr_frag(const char* p, int off2) {
  s16x4 b0 = tr_read(p);
  s16x4 b1 = tr_read(p + off2);
  bf16x8 r;
  r[0] = b0[0]; r[1] = b0[1]; r[2] = b0[2]; r[3] = b0[3];
  r[4] = b1[0]; r[5] = b1[1]; r[6] = b1[2]; r[7] = b1[3];
  return r;
}
#define SPIN_CAP 400000
#define MFMA(A, B, C) __builtin_amdgcn_mfma_f32_16x16x32_bf16(A, B, C, 0, 0, 0)

#define HSLOT 16384
#define XRING 98304
#define XSLOT 12288
#define LDS_TOTAL 163840
#define EXO 102400
#define HIM 148480
#define PST_OFF (EXO + 40960)
#define TAB_OFF 161280
#define ONES_OFF 162048
#define CTL_OFF 162176
#define WR_OFF 162304
#define REC_OFF 162816
#define ZERO_OFF 163456
#define HN_OFF 124928
#define HSL 1344
#define ASL 1056
#define TCH_OFF 162560
#define ACT_OFF 135168
#define KEY_OFF (ACT_OFF + 5120)
#define POST_OFF (KEY_OFF + 256)
#define DOWN_OFF 81920
#define WS_EPOCH 0
#define WS_HFLAG 4096
#define WS_VP 40960
#define WS_LN1 16384
#define WS_FILL 131072
#define WS_O WS_FILL
#define WS_STAT (WS_O + 2621440)
#define WS_CAND (WS_STAT + 425984)
#define WS_PART (WS_CAND + 133120)
#define WS_BXF (WS_PART + 122880)
#define WS_PC (WS_BXF + 26624)
#define WS_PART2 (WS_PC + 66560)
#define WS_PK (4u << 20)
#define SENT 0xFFFFFFFFu

#define GST4(VO, SB, O, V) asm volatile("global_store_dwordx4 %0, %1, %2 offset:%3 sc1\n\ts_nop 1" ::"v"(VO), "v"(V), "s"(SB), "n"(O) : "memory")
#define GST2(VO, SB, O, V) asm volatile("global_store_dwordx2 %0, %1, %2 offset:%3 sc1\n\ts_nop 1" ::"v"(VO), "v"(V), "s"(SB), "n"(O) : "memory")
#define GLD2(D, VO, SB, O) asm volatile("global_load_dwordx2 %0, %1, %2 offset:%3 sc1" : "=v"(D) : "v"(VO), "s"(SB), "n"(O) : "memory")

__device__ __forceinline__ void touch_line(const void* p) {
  asm volatile("s_mov_b32 m0, %1\n\ts_nop 0\n\tglobal_load_lds_dword %0, off" ::"v"(p), "s"(TCH_OFF) : "memory");
}
__device__ __forceinline__ u32 w_off(int i, int lane, int hg) {
  int cc = (lane & 31) ^ (((2 * i + (lane >> 5)) & 7) << 1);
  cc = cc > 29 ? 29 : cc;
  return (lane >> 5) * 3840 + ((cc >= 10 ? 1120 : 0) + (cc >= 20 ? 1120 : 0)) + hg * 160 + cc * 16;
}
#define W_ROW(I) (((((2 * (I)) & 0x13) | (((2 * (I)) & 4) << 1) | (((2 * (I)) & 8) >> 1))) * 3840)
__device__ __forceinline__ u32 x_off(int i, int lane, int r0) {
  const int xr = i * 8 + (lane >> 3);
  int grow = r0 - 16 + xr;
  grow = grow < 0 ? 0 : (grow > SEQ - 1 ? SEQ - 1 : grow);
  return grow * 1280 + (((lane & 7) ^ ((xr >> 1) & 7)) * 16);
}

#define ATT_ASM \
  "ds_read_b32 v28, %[aq] offset:64\n\tds_read_b32 v30, %[aka]\n\tds_read_b32 v31, %[akb]\n\tv_mov_b64 v[38:39], 0\n\tv_mov_b64 v[42:43], 0\n\t" \
  "s_lshl_b32 s41, %[kh], 5\n\ts_lshl_b64 s[46:47], 0xffff, s41\n\ts_lshl_b64 s[48:49], s[46:47], 16\n\ts_mov_b32 s50, 0xaaaaaaaa\n\t" \
  "s_mov_b32 s51, 0xaaaaaaaa\n\ts_setprio 1\n\ts_waitcnt lgkmcnt(0)\n\ts_barrier\n\tds_read_b64 v[36:37], %[eh] offset:512\n\t" \
  "ds_read_b64 v[44:45], %[eh] offset:15360\n\tds_read_b64 v[46:47], %[e4] offset:27648\n\tds_read_b64 v[48:49], %[eh] offset:15872\n\t" \
  "ds_read_b64 v[50:51], %[e4] offset:28160\n\ts_mov_b64 exec, s[46:47]\n\tds_read_b64 v[38:39], %[e4] offset:12800\n\ts_mov_b64 exec, s[48:49]\n\t" \
  "ds_read_b64 v[42:43], %[e4] offset:12800\n\ts_mov_b64 exec, -1\n\tds_read_b64 v[40:41], %[eh] offset:3584\n\t" \
  "ds_read_b64 v[52:53], %[eh] offset:18432\n\tds_read_b64 v[54:55], %[e4] offset:27648\n\tds_read_b64 v[56:57], %[eh] offset:18944\n\t" \
  "ds_read_b64 v[58:59], %[e4] offset:28160\n\tv_mul_f32 v28, 0x3ea52b5b, v28\n\tv_pk_mul_f32 v[32:33], v[28:29], v[30:31] op_sel_hi:[0,1]\n\t" \
  "s_waitcnt lgkmcnt(6)\n\tv_mfma_f32_16x16x32_bf16 v[44:47], v[44:47], v[36:39], 0\n\tds_read2st64_b64 v[60:63], %[eh] offset0:60 offset1:61\n\t" \
  "v_mfma_f32_16x16x32_bf16 v[48:51], v[48:51], v[36:39], 0\n\tds_read2st64_b64 v[64:67], %[eh] offset0:66 offset1:67\n\ts_waitcnt lgkmcnt(2)\n\t" \
  "v_mfma_f32_16x16x32_bf16 v[52:55], v[52:55], v[40:43], 0\n\tds_read2st64_b64 v[68:71], %[e4] offset0:84 offset1:85\n\t" \
  "v_mfma_f32_16x16x32_bf16 v[56:59], v[56:59], v[40:43], 0\n\ts_mov_b32 exec_lo, 0xfff0ffff\n\ts_mov_b32 exec_hi, 0xf000ff00\n\ts_nop 0\n\t" \
  "v_mov_b64 v[44:45], v[48:49]\n\ts_mov_b32 exec_lo, 0xffc0fffc\n\ts_mov_b32 exec_hi, 0xc000fc00\n\tv_mov_b64 v[46:47], v[50:51]\n\ts_nop 0\n\t" \
  "v_mov_b64 v[54:55], v[58:59]\n\ts_mov_b32 exec_lo, 0xfff0ffff\n\ts_mov_b32 exec_hi, 0xf000ff00\n\tv_mov_b64 v[52:53], v[56:57]\n\t" \
  "s_mov_b64 exec, -1\n\tv_cndmask_b32 v44, v44, v45, s[50:51]\n\tv_cndmask_b32 v46, v46, v47, s[50:51]\n\tv_cndmask_b32 v52, v52, v53, s[50:51]\n\t" \
  "v_cndmask_b32 v54, v54, v55, s[50:51]\n\tv_fma_f32 v72, v44, v32, %[b0]\n\tv_fma_f32 v73, v46, v33, %[b1]\n\tv_fma_f32 v74, v52, v32, %[b0]\n\t" \
  "v_fma_f32 v75, v54, v33, %[b1]\n\tv_max_f32 v34, v72, v73\n\tv_max_f32 v35, v74, v75\n\tds_read_b128 v[76:79], %[arec]\n\t" \
  "ds_read_b128 v[80:83], %[arec] offset:16\n\tv_permlane32_swap_b32 v34, v35\n\tv_max_f32 v34, v34, v35\n\tv_mov_b32 v35, v34\n\t" \
  "ds_read_b128 v[84:87], %[arec] offset:32\n\ts_nop 0\n\tv_permlane16_swap_b32 v34, v35\n\tv_max_f32 v34, v34, v35\n\tv_mov_b32 v35, v34\n\ts_nop 1\n\t" \
  "v_permlane32_swap_b32 v34, v35\n\tv_sub_f32 v72, v72, v34\n\tv_sub_f32 v73, v73, v34\n\tv_sub_f32 v74, v74, v35\n\tv_sub_f32 v75, v75, v35\n\t" \
  "v_exp_f32 v72, v72\n\tv_exp_f32 v73, v73\n\tv_exp_f32 v74, v74\n\tv_exp_f32 v75, v75\n\tv_add_f32 v34, v72, v73\n\tv_add_f32 v35, v74, v75\n\t" \
  "v_pk_mul_f32 v[72:73], v[72:73], v[30:31]\n\tv_pk_mul_f32 v[74:75], v[74:75], v[30:31]\n\tv_permlane32_swap_b32 v34, v35\n\t" \
  "v_add_f32 v34, v34, v35\n\tv_mov_b32 v35, v34\n\ts_nop 1\n\tv_permlane16_swap_b32 v34, v35\n\tv_add_f32 v34, v34, v35\n\tv_rcp_f32 v34, v34\n\t" \
  "s_nop 0\n\tv_mov_b32 v30, v34\n\ts_waitcnt lgkmcnt(0)\n\ts_nop 0\n\tv_permlane32_swap_b32 v34, v30\n\t" \
  "v_pk_mul_f32 v[72:73], v[34:35], v[72:73] op_sel_hi:[0,1]\n\tv_pk_mul_f32 v[74:75], v[30:31], v[74:75] op_sel_hi:[0,1]\n\t" \
  "v_cvt_pk_bf16_f32 v72, v72, v73\n\tv_cvt_pk_bf16_f32 v74, v74, v75\n\tv_perm_b32 v36, 0, v72, %[s0]\n\tv_perm_b32 v37, 0, v72, %[s1]\n\t" \
  "v_perm_b32 v38, 0, v72, %[s2]\n\tv_perm_b32 v39, 0, v72, %[s3]\n\tv_perm_b32 v40, 0, v74, %[s0]\n\tv_perm_b32 v41, 0, v74, %[s1]\n\t" \
  "v_mfma_f32_16x16x32_bf16 v[60:63], v[60:63], v[36:39], 0\n\tv_perm_b32 v42, 0, v74, %[s2]\n\tv_perm_b32 v43, 0, v74, %[s3]\n\t" \
  "v_mfma_f32_16x16x32_bf16 v[52:55], v[68:71], v[36:39], 0\n\ts_nop 0\n\tv_mfma_f32_16x16x32_bf16 v[64:67], v[64:67], v[40:43], 0\n\t" \
  "v_mfma_f32_16x16x32_bf16 v[56:59], v[68:71], v[40:43], 0\n\ts_nop 1\n\tv_cvt_pk_bf16_f32 v24, v60, v61\n\tv_cvt_pk_bf16_f32 v25, v62, v63\n\t" \
  "s_nop 0\n\tv_cvt_pk_bf16_f32 v52, v52, v53\n\tv_cvt_pk_bf16_f32 v53, v54, v55\n\tv_cvt_pk_bf16_f32 v26, v64, v65\n\t" \
  "v_cvt_pk_bf16_f32 v27, v66, v67\n\tglobal_store_dwordx4 %[l16], v[24:27], %[os] sc1\n\ts_mov_b64 exec, s[48:49]\n\tv_cvt_pk_bf16_f32 v52, v56, v57\n\t" \
  "v_cvt_pk_bf16_f32 v53, v58, v59\n\ts_or_b64 exec, s[46:47], s[48:49]\n\tglobal_store_dwordx2 %[l16], v[52:53], %[os4] sc1\n\ts_mov_b64 exec, -1\n\t" \
  "s_setprio 0\n\tv_mfma_f32_16x16x32_bf16 v[60:63], v[24:27], v[76:79], 0\n\tv_and_b32 v54, 0x7fff7fff, v52\n\tv_and_b32 v55, 0x7fff7fff, v53\n\t" \
  "v_and_b32 v64, 0x7fff7fff, v24\n\tv_and_b32 v65, 0x7fff7fff, v25\n\tv_and_b32 v66, 0x7fff7fff, v26\n\tv_and_b32 v67, 0x7fff7fff, v27\n\t" \
  "v_mfma_f32_16x16x32_bf16 v[60:63], v[52:55], v[80:83], v[60:63]\n\tds_write2st64_b64 %[eh], v[24:25], v[26:27] offset0:1 offset1:7\n\t" \
  "v_mfma_f32_16x16x32_bf16 v[60:63], v[64:67], v[84:87], v[60:63]\n\ts_mov_b32 exec_lo, 0x01010101\n\ts_mov_b32 exec_hi, 0x01010101\n\ts_nop 5\n\t" \
  "v_cvt_pk_bf16_f32 v60, v60, v61\n\tv_cvt_pk_bf16_f32 v61, v62, v63\n\tv_and_b32 v60, -2, v60\n\tglobal_store_dwordx2 %[vpp], v[60:61], %[sst] sc1\n\t" \
  "s_mov_b64 exec, -1\n\t" \
  ""
#define OP_ASM \
  "s_mov_b32 s43, %[cap]\n\ts_cmp_lt_u32 %[w], 4\n\ts_cbranch_scc1 LH_%=\n\ts_setprio 3\n\tglobal_load_dwordx4 v[124:127], %[vd], %[oa1] sc1\n\t" \
  "global_load_dwordx4 v[128:131], %[vk], %[oa1] sc1\n\tglobal_load_dwordx4 v[132:135], %[vd], %[oa2] sc1\n\t" \
  "global_load_dwordx4 v[136:139], %[vk], %[oa2] sc1\n\tv_add_u32 %[tk], %[sk], %[e4]\n\tv_add_u32 %[ta], %[so0], %[pm]\n\t" \
  "ds_read2st64_b64 v[104:107], %[tk] offset0:1 offset1:7\n\tv_xor_b32 %[tk], 512, %[tk]\n\tds_read2st64_b64 v[108:111], %[tk] offset0:1 offset1:7\n\t" \
  "ds_read_b64_tr_b16 v[64:65], %[ta] offset:0\n\tds_read_b64_tr_b16 v[66:67], %[ta] offset:3200\n\tds_read_b64_tr_b16 v[68:69], %[ta] offset:8\n\t" \
  "ds_read_b64_tr_b16 v[70:71], %[ta] offset:3208\n\tds_read_b64_tr_b16 v[72:73], %[ta] offset:16\n\tds_read_b64_tr_b16 v[74:75], %[ta] offset:3216\n\t" \
  "ds_read_b64_tr_b16 v[76:77], %[ta] offset:24\n\tds_read_b64_tr_b16 v[78:79], %[ta] offset:3224\n\tds_read_b64_tr_b16 v[80:81], %[ta] offset:32\n\t" \
  "ds_read_b64_tr_b16 v[82:83], %[ta] offset:3232\n\tds_read_b64 v[112:113], %[ra] offset:0\n\tds_read_b64 v[114:115], %[ra] offset:8\n\t" \
  "ds_read_b64 v[116:117], %[ra] offset:16\n\tds_read_b64 v[118:119], %[ra] offset:24\n\tds_read_b64 v[120:121], %[ra] offset:32\n\t" \
  "global_load_dwordx4 v[140:143], %[vd], %[oa3] sc1\n\tv_add_u32 %[ta], %[so1], %[pm]\n\ts_waitcnt lgkmcnt(5)\n\t" \
  "v_mfma_f32_16x16x32_bf16 v[44:47], v[64:67], v[108:111], 0\n\tds_read_b64_tr_b16 v[84:85], %[ta] offset:0\n\t" \
  "ds_read_b64_tr_b16 v[86:87], %[ta] offset:3200\n\tv_mfma_f32_16x16x32_bf16 v[48:51], v[68:71], v[108:111], 0\n\t" \
  "ds_read_b64_tr_b16 v[88:89], %[ta] offset:8\n\tds_read_b64_tr_b16 v[90:91], %[ta] offset:3208\n\t" \
  "v_mfma_f32_16x16x32_bf16 v[52:55], v[72:75], v[108:111], 0\n\tds_read_b64_tr_b16 v[92:93], %[ta] offset:16\n\t" \
  "ds_read_b64_tr_b16 v[94:95], %[ta] offset:3216\n\tv_mfma_f32_16x16x32_bf16 v[56:59], v[76:79], v[108:111], 0\n\t" \
  "ds_read_b64_tr_b16 v[96:97], %[ta] offset:24\n\tds_read_b64_tr_b16 v[98:99], %[ta] offset:3224\n\t" \
  "v_mfma_f32_16x16x32_bf16 v[60:63], v[80:83], v[108:111], 0\n\tds_read_b64_tr_b16 v[100:101], %[ta] offset:32\n\t" \
  "ds_read_b64_tr_b16 v[102:103], %[ta] offset:3232\n\ts_waitcnt lgkmcnt(10)\n\tv_lshlrev_b32 v24, 16, v112\n\tv_and_b32 v25, 0xffff0000, v112\n\t" \
  "v_lshlrev_b32 v26, 16, v113\n\tv_and_b32 v27, 0xffff0000, v113\n\tv_lshlrev_b32 v28, 16, v114\n\tv_and_b32 v29, 0xffff0000, v114\n\t" \
  "v_lshlrev_b32 v30, 16, v115\n\tv_and_b32 v31, 0xffff0000, v115\n\tv_mfma_f32_16x16x32_bf16 v[24:27], v[64:67], v[104:107], v[24:27]\n\t" \
  "v_lshlrev_b32 v32, 16, v116\n\tv_and_b32 v33, 0xffff0000, v116\n\tv_lshlrev_b32 v34, 16, v117\n\tv_and_b32 v35, 0xffff0000, v117\n\t" \
  "v_mfma_f32_16x16x32_bf16 v[28:31], v[68:71], v[104:107], v[28:31]\n\tv_lshlrev_b32 v36, 16, v118\n\tv_and_b32 v37, 0xffff0000, v118\n\t" \
  "v_lshlrev_b32 v38, 16, v119\n\tv_and_b32 v39, 0xffff0000, v119\n\tv_mfma_f32_16x16x32_bf16 v[32:35], v[72:75], v[104:107], v[32:35]\n\t" \
  "v_lshlrev_b32 v40, 16, v120\n\tv_and_b32 v41, 0xffff0000, v120\n\tv_lshlrev_b32 v42, 16, v121\n\tv_and_b32 v43, 0xffff0000, v121\n\t" \
  "v_mfma_f32_16x16x32_bf16 v[36:39], v[76:79], v[104:107], v[36:39]\n\ts_nop 0\n\tv_mfma_f32_16x16x32_bf16 v[40:43], v[80:83], v[104:107], v[40:43]\n\t" \
  "v_add_u32 %[ta], %[so2], %[pm]\n\tds_read_b64_tr_b16 v[104:105], %[ta] offset:0\n\tds_read_b64_tr_b16 v[106:107], %[ta] offset:3200\n\t" \
  "ds_read_b64_tr_b16 v[108:109], %[ta] offset:8\n\tds_read_b64_tr_b16 v[110:111], %[ta] offset:3208\n\t" \
  "ds_read_b64_tr_b16 v[112:113], %[ta] offset:16\n\tds_read_b64_tr_b16 v[114:115], %[ta] offset:3216\n\t" \
  "ds_read_b64_tr_b16 v[116:117], %[ta] offset:24\n\tds_read_b64_tr_b16 v[118:119], %[ta] offset:3224\n\t" \
  "ds_read_b64_tr_b16 v[120:121], %[ta] offset:32\n\tds_read_b64_tr_b16 v[122:123], %[ta] offset:3232\n\t" \
  "global_load_dwordx4 v[144:147], %[vk], %[oa3] sc1\n\tglobal_load_dwordx4 v[148:151], %[vd], %[oa4] sc1\n\ts_waitcnt vmcnt(5)\n\t" \
  "v_max_u32 %[tk], v124, v128\n\tv_cmp_eq_u32 vcc, -1, %[tk]\n\ts_cbranch_vccnz LSs1_%=\n\tLQs1_%=:\n\ts_waitcnt lgkmcnt(10)\n\t" \
  "v_mfma_f32_16x16x32_bf16 v[44:47], v[84:87], v[124:127], v[44:47]\n\tv_add_u32 %[ta], %[so3], %[pm]\n\tds_read_b64_tr_b16 v[64:65], %[ta] offset:0\n\t" \
  "v_mfma_f32_16x16x32_bf16 v[24:27], v[84:87], v[128:131], v[24:27]\n\tds_read_b64_tr_b16 v[66:67], %[ta] offset:3200\n\t" \
  "global_load_dwordx4 v[152:155], %[vk], %[oa4] sc1\n\tv_mfma_f32_16x16x32_bf16 v[48:51], v[88:91], v[124:127], v[48:51]\n\t" \
  "ds_read_b64_tr_b16 v[68:69], %[ta] offset:8\n\tv_mfma_f32_16x16x32_bf16 v[28:31], v[88:91], v[128:131], v[28:31]\n\t" \
  "ds_read_b64_tr_b16 v[70:71], %[ta] offset:3208\n\tglobal_load_dwordx4 v[156:159], %[vd], %[oa5] sc1\n\t" \
  "v_mfma_f32_16x16x32_bf16 v[52:55], v[92:95], v[124:127], v[52:55]\n\tds_read_b64_tr_b16 v[72:73], %[ta] offset:16\n\t" \
  "v_mfma_f32_16x16x32_bf16 v[32:35], v[92:95], v[128:131], v[32:35]\n\tds_read_b64_tr_b16 v[74:75], %[ta] offset:3216\n\t" \
  "v_mfma_f32_16x16x32_bf16 v[56:59], v[96:99], v[124:127], v[56:59]\n\tds_read_b64_tr_b16 v[76:77], %[ta] offset:24\n\t" \
  "v_mfma_f32_16x16x32_bf16 v[36:39], v[96:99], v[128:131], v[36:39]\n\tds_read_b64_tr_b16 v[78:79], %[ta] offset:3224\n\ts_waitcnt vmcnt(5)\n\t" \
  "v_max_u32 %[tk], v132, v136\n\tv_cmp_eq_u32 vcc, -1, %[tk]\n\tv_mfma_f32_16x16x32_bf16 v[60:63], v[100:103], v[124:127], v[60:63]\n\t" \
  "ds_read_b64_tr_b16 v[80:81], %[ta] offset:32\n\tv_mfma_f32_16x16x32_bf16 v[40:43], v[100:103], v[128:131], v[40:43]\n\t" \
  "ds_read_b64_tr_b16 v[82:83], %[ta] offset:3232\n\ts_cbranch_vccnz LSs2_%=\n\tLQs2_%=:\n\ts_waitcnt lgkmcnt(10)\n\t" \
  "v_mfma_f32_16x16x32_bf16 v[44:47], v[104:107], v[132:135], v[44:47]\n\tv_add_u32 %[ta], %[so4], %[pf]\n\t" \
  "ds_read_b64_tr_b16 v[84:85], %[ta] offset:0\n\tv_mfma_f32_16x16x32_bf16 v[24:27], v[104:107], v[136:139], v[24:27]\n\t" \
  "ds_read_b64_tr_b16 v[86:87], %[ta] offset:12800\n\tglobal_load_dwordx4 v[124:127], %[vk], %[oa5] sc1\n\t" \
  "v_mfma_f32_16x16x32_bf16 v[48:51], v[108:111], v[132:135], v[48:51]\n\tds_read_b64_tr_b16 v[88:89], %[ta] offset:8\n\t" \
  "v_mfma_f32_16x16x32_bf16 v[28:31], v[108:111], v[136:139], v[28:31]\n\tds_read_b64_tr_b16 v[90:91], %[ta] offset:12808\n\t" \
  "global_load_dwordx4 v[128:131], %[vd], %[oa6] sc1\n\tv_mfma_f32_16x16x32_bf16 v[52:55], v[112:115], v[132:135], v[52:55]\n\t" \
  "ds_read_b64_tr_b16 v[92:93], %[ta] offset:16\n\tv_mfma_f32_16x16x32_bf16 v[32:35], v[112:115], v[136:139], v[32:35]\n\t" \
  "ds_read_b64_tr_b16 v[94:95], %[ta] offset:12816\n\tv_mfma_f32_16x16x32_bf16 v[56:59], v[116:119], v[132:135], v[56:59]\n\t" \
  "ds_read_b64_tr_b16 v[96:97], %[ta] offset:24\n\tv_mfma_f32_16x16x32_bf16 v[36:39], v[116:119], v[136:139], v[36:39]\n\t" \
  "ds_read_b64_tr_b16 v[98:99], %[ta] offset:12824\n\ts_waitcnt vmcnt(5)\n\tv_max_u32 %[tk], v140, v144\n\tv_cmp_eq_u32 vcc, -1, %[tk]\n\t" \
  "v_mfma_f32_16x16x32_bf16 v[60:63], v[120:123], v[132:135], v[60:63]\n\tds_read_b64_tr_b16 v[100:101], %[ta] offset:32\n\t" \
  "v_mfma_f32_16x16x32_bf16 v[40:43], v[120:123], v[136:139], v[40:43]\n\tds_read_b64_tr_b16 v[102:103], %[ta] offset:12832\n\t" \
  "s_cbranch_vccnz LSs3_%=\n\tLQs3_%=:\n\ts_waitcnt lgkmcnt(10)\n\tv_mfma_f32_16x16x32_bf16 v[44:47], v[64:67], v[140:143], v[44:47]\n\t" \
  "v_add_u32 %[ta], %[so5], %[pm]\n\tds_read_b64_tr_b16 v[104:105], %[ta] offset:0\n\t" \
  "v_mfma_f32_16x16x32_bf16 v[24:27], v[64:67], v[144:147], v[24:27]\n\tds_read_b64_tr_b16 v[106:107], %[ta] offset:3200\n\t" \
  "global_load_dwordx4 v[132:135], %[vk], %[oa6] sc1\n\tv_mfma_f32_16x16x32_bf16 v[48:51], v[68:71], v[140:143], v[48:51]\n\t" \
  "ds_read_b64_tr_b16 v[108:109], %[ta] offset:8\n\tv_mfma_f32_16x16x32_bf16 v[28:31], v[68:71], v[144:147], v[28:31]\n\t" \
  "ds_read_b64_tr_b16 v[110:111], %[ta] offset:3208\n\tglobal_load_dwordx4 v[136:139], %[vd], %[oa7] sc1\n\t" \
  "v_mfma_f32_16x16x32_bf16 v[52:55], v[72:75], v[140:143], v[52:55]\n\tds_read_b64_tr_b16 v[112:113], %[ta] offset:16\n\t" \
  "v_mfma_f32_16x16x32_bf16 v[32:35], v[72:75], v[144:147], v[32:35]\n\tds_read_b64_tr_b16 v[114:115], %[ta] offset:3216\n\t" \
  "v_mfma_f32_16x16x32_bf16 v[56:59], v[76:79], v[140:143], v[56:59]\n\tds_read_b64_tr_b16 v[116:117], %[ta] offset:24\n\t" \
  "v_mfma_f32_16x16x32_bf16 v[36:39], v[76:79], v[144:147], v[36:39]\n\tds_read_b64_tr_b16 v[118:119], %[ta] offset:3224\n\ts_waitcnt vmcnt(5)\n\t" \
  "v_max3_u32 %[tk], v148, v150, v152\n\tv_max_u32 %[tk], %[tk], v154\n\tv_cmp_eq_u32 vcc, -1, %[tk]\n\t" \
  "v_mfma_f32_16x16x32_bf16 v[60:63], v[80:83], v[140:143], v[60:63]\n\tds_read_b64_tr_b16 v[120:121], %[ta] offset:32\n\t" \
  "v_mfma_f32_16x16x32_bf16 v[40:43], v[80:83], v[144:147], v[40:43]\n\tds_read_b64_tr_b16 v[122:123], %[ta] offset:3232\n\ts_cbranch_vccnz LSs4_%=\n\t" \
  "LQs4_%=:\n\ts_waitcnt lgkmcnt(10)\n\tv_mfma_f32_16x16x32_bf16 v[44:47], v[84:87], v[148:151], v[44:47]\n\tv_add_u32 %[ta], %[so6], %[pm]\n\t" \
  "ds_read_b64_tr_b16 v[64:65], %[ta] offset:0\n\tv_mfma_f32_16x16x32_bf16 v[24:27], v[84:87], v[152:155], v[24:27]\n\t" \
  "ds_read_b64_tr_b16 v[66:67], %[ta] offset:3200\n\tglobal_load_dwordx4 v[140:143], %[vd], %[oa8] sc1\n\t" \
  "v_mfma_f32_16x16x32_bf16 v[48:51], v[88:91], v[148:151], v[48:51]\n\tds_read_b64_tr_b16 v[68:69], %[ta] offset:8\n\t" \
  "v_mfma_f32_16x16x32_bf16 v[28:31], v[88:91], v[152:155], v[28:31]\n\tds_read_b64_tr_b16 v[70:71], %[ta] offset:3208\n\t" \
  "global_load_dwordx4 v[144:147], %[vd], %[oa9] sc1\n\tv_mfma_f32_16x16x32_bf16 v[52:55], v[92:95], v[148:151], v[52:55]\n\t" \
  "ds_read_b64_tr_b16 v[72:73], %[ta] offset:16\n\tv_mfma_f32_16x16x32_bf16 v[32:35], v[92:95], v[152:155], v[32:35]\n\t" \
  "ds_read_b64_tr_b16 v[74:75], %[ta] offset:3216\n\tv_mfma_f32_16x16x32_bf16 v[56:59], v[96:99], v[148:151], v[56:59]\n\t" \
  "ds_read_b64_tr_b16 v[76:77], %[ta] offset:24\n\tv_mfma_f32_16x16x32_bf16 v[36:39], v[96:99], v[152:155], v[36:39]\n\t" \
  "ds_read_b64_tr_b16 v[78:79], %[ta] offset:3224\n\ts_waitcnt vmcnt(5)\n\tv_max_u32 %[tk], v156, v124\n\tv_cmp_eq_u32 vcc, -1, %[tk]\n\t" \
  "v_mfma_f32_16x16x32_bf16 v[60:63], v[100:103], v[148:151], v[60:63]\n\tds_read_b64_tr_b16 v[80:81], %[ta] offset:32\n\t" \
  "v_mfma_f32_16x16x32_bf16 v[40:43], v[100:103], v[152:155], v[40:43]\n\tds_read_b64_tr_b16 v[82:83], %[ta] offset:3232\n\ts_cbranch_vccnz LSs5_%=\n\t" \
  "LQs5_%=:\n\ts_waitcnt lgkmcnt(10)\n\tv_mfma_f32_16x16x32_bf16 v[44:47], v[104:107], v[156:159], v[44:47]\n\tv_add_u32 %[ta], %[so7], %[pm]\n\t" \
  "ds_read_b64_tr_b16 v[84:85], %[ta] offset:0\n\tv_mfma_f32_16x16x32_bf16 v[24:27], v[104:107], v[124:127], v[24:27]\n\t" \
  "ds_read_b64_tr_b16 v[86:87], %[ta] offset:3200\n\tglobal_load_dwordx4 v[148:151], %[vk], %[oa7] sc1\n\t" \
  "v_mfma_f32_16x16x32_bf16 v[48:51], v[108:111], v[156:159], v[48:51]\n\tds_read_b64_tr_b16 v[88:89], %[ta] offset:8\n\t" \
  "v_mfma_f32_16x16x32_bf16 v[28:31], v[108:111], v[124:127], v[28:31]\n\tds_read_b64_tr_b16 v[90:91], %[ta] offset:3208\n\t" \
  "global_load_dwordx4 v[152:155], %[vk], %[oa8] sc1\n\tv_mfma_f32_16x16x32_bf16 v[52:55], v[112:115], v[156:159], v[52:55]\n\t" \
  "ds_read_b64_tr_b16 v[92:93], %[ta] offset:16\n\tv_mfma_f32_16x16x32_bf16 v[32:35], v[112:115], v[124:127], v[32:35]\n\t" \
  "ds_read_b64_tr_b16 v[94:95], %[ta] offset:3216\n\tv_mfma_f32_16x16x32_bf16 v[56:59], v[116:119], v[156:159], v[56:59]\n\t" \
  "ds_read_b64_tr_b16 v[96:97], %[ta] offset:24\n\tv_mfma_f32_16x16x32_bf16 v[36:39], v[116:119], v[124:127], v[36:39]\n\t" \
  "ds_read_b64_tr_b16 v[98:99], %[ta] offset:3224\n\ts_waitcnt vmcnt(5)\n\tv_max_u32 %[tk], v128, v132\n\tv_cmp_eq_u32 vcc, -1, %[tk]\n\t" \
  "v_mfma_f32_16x16x32_bf16 v[60:63], v[120:123], v[156:159], v[60:63]\n\tds_read_b64_tr_b16 v[100:101], %[ta] offset:32\n\t" \
  "v_mfma_f32_16x16x32_bf16 v[40:43], v[120:123], v[124:127], v[40:43]\n\tds_read_b64_tr_b16 v[102:103], %[ta] offset:3232\n\ts_cbranch_vccnz LSs6_%=\n\t" \
  "LQs6_%=:\n\ts_waitcnt lgkmcnt(10)\n\tv_mfma_f32_16x16x32_bf16 v[44:47], v[64:67], v[128:131], v[44:47]\n\tv_add_u32 %[ta], %[so8], %[pm]\n\t" \
  "ds_read_b64_tr_b16 v[104:105], %[ta] offset:0\n\tv_mfma_f32_16x16x32_bf16 v[24:27], v[64:67], v[132:135], v[24:27]\n\t" \
  "ds_read_b64_tr_b16 v[106:107], %[ta] offset:3200\n\tglobal_load_dwordx4 v[156:159], %[vk], %[oa9] sc1\n\t" \
  "v_mfma_f32_16x16x32_bf16 v[48:51], v[68:71], v[128:131], v[48:51]\n\tds_read_b64_tr_b16 v[108:109], %[ta] offset:8\n\t" \
  "v_mfma_f32_16x16x32_bf16 v[28:31], v[68:71], v[132:135], v[28:31]\n\tds_read_b64_tr_b16 v[110:111], %[ta] offset:3208\n\t" \
  "v_mfma_f32_16x16x32_bf16 v[52:55], v[72:75], v[128:131], v[52:55]\n\tds_read_b64_tr_b16 v[112:113], %[ta] offset:16\n\t" \
  "v_mfma_f32_16x16x32_bf16 v[32:35], v[72:75], v[132:135], v[32:35]\n\tds_read_b64_tr_b16 v[114:115], %[ta] offset:3216\n\t" \
  "v_mfma_f32_16x16x32_bf16 v[56:59], v[76:79], v[128:131], v[56:59]\n\tds_read_b64_tr_b16 v[116:117], %[ta] offset:24\n\t" \
  "v_mfma_f32_16x16x32_bf16 v[36:39], v[76:79], v[132:135], v[36:39]\n\tds_read_b64_tr_b16 v[118:119], %[ta] offset:3224\n\ts_waitcnt vmcnt(4)\n\t" \
  "v_max_u32 %[tk], v136, v140\n\tv_cmp_eq_u32 vcc, -1, %[tk]\n\tv_mfma_f32_16x16x32_bf16 v[60:63], v[80:83], v[128:131], v[60:63]\n\t" \
  "ds_read_b64_tr_b16 v[120:121], %[ta] offset:32\n\tv_mfma_f32_16x16x32_bf16 v[40:43], v[80:83], v[132:135], v[40:43]\n\t" \
  "ds_read_b64_tr_b16 v[122:123], %[ta] offset:3232\n\ts_cbranch_vccnz LSd7_%=\n\tLQd7_%=:\n\ts_waitcnt lgkmcnt(10)\n\t" \
  "v_mfma_f32_16x16x32_bf16 v[44:47], v[84:87], v[136:139], v[44:47]\n\tv_add_u32 %[ta], %[so9], %[pf]\n\tds_read_b64_tr_b16 v[64:65], %[ta] offset:0\n\t" \
  "ds_read_b64_tr_b16 v[66:67], %[ta] offset:12800\n\tv_mfma_f32_16x16x32_bf16 v[48:51], v[88:91], v[136:139], v[48:51]\n\t" \
  "ds_read_b64_tr_b16 v[68:69], %[ta] offset:8\n\tds_read_b64_tr_b16 v[70:71], %[ta] offset:12808\n\t" \
  "v_mfma_f32_16x16x32_bf16 v[52:55], v[92:95], v[136:139], v[52:55]\n\tds_read_b64_tr_b16 v[72:73], %[ta] offset:16\n\t" \
  "ds_read_b64_tr_b16 v[74:75], %[ta] offset:12816\n\tv_mfma_f32_16x16x32_bf16 v[56:59], v[96:99], v[136:139], v[56:59]\n\t" \
  "ds_read_b64_tr_b16 v[76:77], %[ta] offset:24\n\tds_read_b64_tr_b16 v[78:79], %[ta] offset:12824\n\t" \
  "v_mfma_f32_16x16x32_bf16 v[60:63], v[100:103], v[136:139], v[60:63]\n\tds_read_b64_tr_b16 v[80:81], %[ta] offset:32\n\t" \
  "ds_read_b64_tr_b16 v[82:83], %[ta] offset:12832\n\ts_waitcnt lgkmcnt(10)\n\tv_mfma_f32_16x16x32_bf16 v[44:47], v[104:107], v[140:143], v[44:47]\n\t" \
  "v_mfma_f32_16x16x32_bf16 v[48:51], v[108:111], v[140:143], v[48:51]\n\tv_mfma_f32_16x16x32_bf16 v[52:55], v[112:115], v[140:143], v[52:55]\n\t" \
  "s_waitcnt vmcnt(2)\n\tv_max3_u32 %[tk], v144, v146, v148\n\tv_cmp_eq_u32 vcc, -1, %[tk]\n\t" \
  "v_mfma_f32_16x16x32_bf16 v[56:59], v[116:119], v[140:143], v[56:59]\n\tv_mfma_f32_16x16x32_bf16 v[60:63], v[120:123], v[140:143], v[60:63]\n\t" \
  "s_cbranch_vccnz LSd9_%=\n\tLQd9_%=:\n\ts_waitcnt lgkmcnt(0)\n\tv_mfma_f32_16x16x32_bf16 v[44:47], v[64:67], v[144:147], v[44:47]\n\t" \
  "ds_read2_b64 v[124:127], %[aw] offset0:0 offset1:1\n\tds_read2_b64 v[128:131], %[aw] offset0:2 offset1:3\n\t" \
  "ds_read_b64 v[132:133], %[aw] offset:32\n\tv_mov_b64 v[134:135], 0\n\tv_mfma_f32_16x16x32_bf16 v[48:51], v[68:71], v[144:147], v[48:51]\n\t" \
  "v_mfma_f32_16x16x32_bf16 v[52:55], v[72:75], v[144:147], v[52:55]\n\tv_add_u32 %[ta], %[sd], %[l16]\n\t" \
  "v_mfma_f32_16x16x32_bf16 v[56:59], v[76:79], v[144:147], v[56:59]\n\tv_mfma_f32_16x16x32_bf16 v[60:63], v[80:83], v[144:147], v[60:63]\n\t" \
  "v_mov_b32 v137, %[sc]\n\tv_mov_b32 v136, 1\n\tv_mfma_f32_16x16x32_bf16 v[24:27], v[84:87], v[148:151], v[24:27]\n\t" \
  "ds_write_b128 %[ta], v[44:47] offset:0\n\tv_mfma_f32_16x16x32_bf16 v[28:31], v[88:91], v[148:151], v[28:31]\n\t" \
  "ds_write_b128 %[ta], v[48:51] offset:1024\n\tv_mfma_f32_16x16x32_bf16 v[32:35], v[92:95], v[148:151], v[32:35]\n\t" \
  "ds_write_b128 %[ta], v[52:55] offset:2048\n\ts_waitcnt vmcnt(0)\n\tv_max3_u32 %[tk], v152, v156, v158\n\tv_cmp_eq_u32 vcc, -1, %[tk]\n\t" \
  "v_mfma_f32_16x16x32_bf16 v[36:39], v[96:99], v[148:151], v[36:39]\n\tds_write_b128 %[ta], v[56:59] offset:3072\n\t" \
  "v_mfma_f32_16x16x32_bf16 v[40:43], v[100:103], v[148:151], v[40:43]\n\tds_write_b128 %[ta], v[60:63] offset:4096\n\ts_mov_b64 exec, 1\n\t" \
  "ds_add_u32 v137, v136\n\ts_mov_b64 exec, -1\n\ts_cbranch_vccnz LSk8_%=\n\tLQk8_%=:\n\t" \
  "v_mfma_f32_16x16x32_bf16 v[24:27], v[104:107], v[152:155], v[24:27]\n\tv_mov_b64 v[94:95], 0\n\t" \
  "v_mfma_f32_16x16x32_bf16 v[28:31], v[108:111], v[152:155], v[28:31]\n\tv_xor_b32 v96, 4, v137\n\t" \
  "v_mfma_f32_16x16x32_bf16 v[32:35], v[112:115], v[152:155], v[32:35]\n\tv_add_u32 %[tk], %[sr], %[l16]\n\t" \
  "v_mfma_f32_16x16x32_bf16 v[36:39], v[116:119], v[152:155], v[36:39]\n\tv_mfma_f32_16x16x32_bf16 v[40:43], v[120:123], v[152:155], v[40:43]\n\t" \
  "v_mfma_f32_16x16x32_bf16 v[24:27], v[64:67], v[156:159], v[24:27]\n\tds_read_b32 v97, v96\n\tds_read_b128 v[44:47], %[tk] offset:0\n\t" \
  "ds_read_b128 v[48:51], %[tk] offset:1024\n\tds_read_b128 v[52:55], %[tk] offset:2048\n\tds_read_b128 v[56:59], %[tk] offset:3072\n\t" \
  "ds_read_b128 v[60:63], %[tk] offset:4096\n\tv_mfma_f32_16x16x32_bf16 v[28:31], v[68:71], v[156:159], v[28:31]\n\t" \
  "v_mfma_f32_16x16x32_bf16 v[32:35], v[72:75], v[156:159], v[32:35]\n\tv_mfma_f32_16x16x32_bf16 v[36:39], v[76:79], v[156:159], v[36:39]\n\t" \
  "v_mfma_f32_16x16x32_bf16 v[40:43], v[80:83], v[156:159], v[40:43]\n\tLD1_%=:\n\ts_waitcnt lgkmcnt(0)\n\tv_cmp_eq_u32 vcc, 0, v97\n\t" \
  "s_cbranch_vccnz LD2_%=\n\tLT_%=:\n\tv_mov_b32 v137, %[sa]\n\tds_read_b32 v138, v137 offset:16\n\tv_add_f32 v24, v24, v44\n\t" \
  "v_add_f32 v25, v25, v45\n\tv_add_f32 v26, v26, v46\n\tv_add_f32 v27, v27, v47\n\tv_cvt_pk_bf16_f32 v84, v24, v25\n\t" \
  "v_cvt_pk_bf16_f32 v85, v26, v27\n\tv_add_f32 v28, v28, v48\n\tv_add_f32 v29, v29, v49\n\tv_add_f32 v30, v30, v50\n\tv_add_f32 v31, v31, v51\n\t" \
  "v_cvt_pk_bf16_f32 v86, v28, v29\n\tv_cvt_pk_bf16_f32 v87, v30, v31\n\tv_add_f32 v32, v32, v52\n\tv_add_f32 v33, v33, v53\n\t" \
  "v_add_f32 v34, v34, v54\n\tv_add_f32 v35, v35, v55\n\tv_cvt_pk_bf16_f32 v88, v32, v33\n\tv_cvt_pk_bf16_f32 v89, v34, v35\n\t" \
  "v_mfma_f32_16x16x32_bf16 v[100:103], v[84:87], v[84:87], 0\n\tv_mfma_f32_16x16x32_bf16 v[96:99], v[124:127], v[84:87], 0\n\t" \
  "v_add_f32 v36, v36, v56\n\tv_add_f32 v37, v37, v57\n\tv_add_f32 v38, v38, v58\n\tv_add_f32 v39, v39, v59\n\tv_cvt_pk_bf16_f32 v90, v36, v37\n\t" \
  "v_cvt_pk_bf16_f32 v91, v38, v39\n\tv_add_f32 v40, v40, v60\n\tv_add_f32 v41, v41, v61\n\tv_add_f32 v42, v42, v62\n\tv_add_f32 v43, v43, v63\n\t" \
  "v_cvt_pk_bf16_f32 v92, v40, v41\n\tv_cvt_pk_bf16_f32 v93, v42, v43\n\tv_mfma_f32_16x16x32_bf16 v[100:103], v[88:91], v[88:91], v[100:103]\n\t" \
  "v_mfma_f32_16x16x32_bf16 v[96:99], v[128:131], v[88:91], v[96:99]\n\tv_mfma_f32_16x16x32_bf16 v[100:103], v[92:95], v[92:95], v[100:103]\n\t" \
  "v_mfma_f32_16x16x32_bf16 v[96:99], v[132:135], v[92:95], v[96:99]\n\ts_waitcnt lgkmcnt(0)\n\tv_sub_u32 v138, %[ra], v138\n\t" \
  "v_cmp_gt_u32 vcc, 0xa0, v138\n\tv_and_b32 %[ta], 0xff, %[l16]\n\tv_add_u32 %[tk], %[sp], %[ta]\n\tv_mov_b32 v136, 1\n\ts_cbranch_vccz LP1_%=\n\t" \
  "s_mov_b64 exec, vcc\n\tglobal_store_dwordx4 v138, v[84:87], %[cp] sc1\n\tglobal_store_dwordx4 v138, v[88:91], %[cp] offset:16 sc1\n\t" \
  "global_store_dwordx2 v138, v[92:93], %[cp] offset:32 sc1\n\tds_write_b32 v137, v136 offset:24\n\tLP1_%=:\n\ts_mov_b32 exec_lo, 0x00200002\n\t" \
  "s_mov_b32 exec_hi, 0x20000200\n\tv_mov_b32 v100, v101\n\ts_lshl_b64 exec, exec, 1\n\tv_mov_b32 v100, v102\n\ts_lshl_b64 exec, exec, 1\n\t" \
  "v_mov_b32 v100, v103\n\ts_mov_b32 exec_lo, 0x00f0000f\n\ts_mov_b32 exec_hi, 0xf0000f00\n\tv_mov_b32 v98, v100\n\t" \
  "global_store_dwordx4 %[ta], v[96:99], %[sst] sc1\n\tds_write_b128 %[tk], v[96:99]\n\ts_mov_b64 exec, -1\n\tds_write_b64 %[ra], v[84:85] offset:0\n\t" \
  "ds_write_b64 %[ra], v[86:87] offset:8\n\tds_write_b64 %[ra], v[88:89] offset:16\n\tds_write_b64 %[ra], v[90:91] offset:24\n\t" \
  "ds_write_b64 %[ra], v[92:93] offset:32\n\ts_mov_b64 exec, 1\n\tds_add_u32 v137, v136\n\ts_mov_b64 exec, -1\n\ts_setprio 0\n\ts_branch LZ_%=\n\t" \
  "LD2_%=:\n\ts_sub_u32 s43, s43, 1\n\ts_cbranch_scc1 LT_%=\n\tds_read_b32 v97, v96\n\tds_read_b128 v[44:47], %[tk] offset:0\n\t" \
  "ds_read_b128 v[48:51], %[tk] offset:1024\n\tds_read_b128 v[52:55], %[tk] offset:2048\n\tds_read_b128 v[56:59], %[tk] offset:3072\n\t" \
  "ds_read_b128 v[60:63], %[tk] offset:4096\n\ts_branch LD1_%=\n\tLH_%=:\n\ts_sleep 3\n\tglobal_load_dwordx4 v[140:143], %[vk], %[oa1] sc1\n\t" \
  "global_load_dwordx4 v[144:147], %[vk], %[oa2] sc1\n\tglobal_load_dwordx4 v[148:151], %[vk], %[oa3] sc1\n\t" \
  "global_load_dwordx4 v[152:155], %[vk], %[oa4] sc1\n\tglobal_load_dwordx4 v[156:159], %[vk], %[oa0] sc1\n\ts_cmp_eq_u32 %[w], 3\n\t" \
  "s_cbranch_scc0 LG_%=\n\tds_read_b64 v[112:113], %[ra] offset:0\n\tds_read_b64 v[114:115], %[ra] offset:8\n\t" \
  "ds_read_b64 v[116:117], %[ra] offset:16\n\tds_read_b64 v[118:119], %[ra] offset:24\n\tds_read_b64 v[120:121], %[ra] offset:32\n\t" \
  "ds_read2_b64 v[124:127], %[aw] offset0:0 offset1:1\n\tds_read2_b64 v[128:131], %[aw] offset0:2 offset1:3\n\t" \
  "ds_read_b64 v[132:133], %[aw] offset:32\n\tv_mov_b64 v[134:135], 0\n\ts_waitcnt lgkmcnt(3)\n\tv_lshlrev_b32 v24, 16, v112\n\t" \
  "v_and_b32 v25, 0xffff0000, v112\n\tv_lshlrev_b32 v26, 16, v113\n\tv_and_b32 v27, 0xffff0000, v113\n\tv_lshlrev_b32 v28, 16, v114\n\t" \
  "v_and_b32 v29, 0xffff0000, v114\n\tv_lshlrev_b32 v30, 16, v115\n\tv_and_b32 v31, 0xffff0000, v115\n\tv_lshlrev_b32 v32, 16, v116\n\t" \
  "v_and_b32 v33, 0xffff0000, v116\n\tv_lshlrev_b32 v34, 16, v117\n\tv_and_b32 v35, 0xffff0000, v117\n\tv_lshlrev_b32 v36, 16, v118\n\t" \
  "v_and_b32 v37, 0xffff0000, v118\n\tv_lshlrev_b32 v38, 16, v119\n\tv_and_b32 v39, 0xffff0000, v119\n\tv_lshlrev_b32 v40, 16, v120\n\t" \
  "v_and_b32 v41, 0xffff0000, v120\n\tv_lshlrev_b32 v42, 16, v121\n\tv_and_b32 v43, 0xffff0000, v121\n\ts_branch LG2_%=\n\tLG_%=:\n\t" \
  "v_mov_b64 v[24:25], 0\n\tv_mov_b64 v[26:27], 0\n\tv_mov_b64 v[28:29], 0\n\tv_mov_b64 v[30:31], 0\n\tv_mov_b64 v[32:33], 0\n\tv_mov_b64 v[34:35], 0\n\t" \
  "v_mov_b64 v[36:37], 0\n\tv_mov_b64 v[38:39], 0\n\tv_mov_b64 v[40:41], 0\n\tv_mov_b64 v[42:43], 0\n\tLG2_%=:\n\tv_add_u32 %[ta], %[so1], %[pm]\n\t" \
  "ds_read_b64_tr_b16 v[64:65], %[ta] offset:0\n\tds_read_b64_tr_b16 v[66:67], %[ta] offset:3200\n\tds_read_b64_tr_b16 v[68:69], %[ta] offset:8\n\t" \
  "ds_read_b64_tr_b16 v[70:71], %[ta] offset:3208\n\tds_read_b64_tr_b16 v[72:73], %[ta] offset:16\n\tds_read_b64_tr_b16 v[74:75], %[ta] offset:3216\n\t" \
  "ds_read_b64_tr_b16 v[76:77], %[ta] offset:24\n\tds_read_b64_tr_b16 v[78:79], %[ta] offset:3224\n\tds_read_b64_tr_b16 v[80:81], %[ta] offset:32\n\t" \
  "ds_read_b64_tr_b16 v[82:83], %[ta] offset:3232\n\tv_add_u32 %[ta], %[so2], %[pm]\n\tds_read_b64_tr_b16 v[84:85], %[ta] offset:0\n\t" \
  "ds_read_b64_tr_b16 v[86:87], %[ta] offset:3200\n\tds_read_b64_tr_b16 v[88:89], %[ta] offset:8\n\tds_read_b64_tr_b16 v[90:91], %[ta] offset:3208\n\t" \
  "ds_read_b64_tr_b16 v[92:93], %[ta] offset:16\n\tds_read_b64_tr_b16 v[94:95], %[ta] offset:3216\n\tds_read_b64_tr_b16 v[96:97], %[ta] offset:24\n\t" \
  "ds_read_b64_tr_b16 v[98:99], %[ta] offset:3224\n\tds_read_b64_tr_b16 v[100:101], %[ta] offset:32\n\t" \
  "ds_read_b64_tr_b16 v[102:103], %[ta] offset:3232\n\ts_waitcnt vmcnt(4)\n\tv_cmp_eq_u32 vcc, -1, v140\n\ts_cbranch_vccnz LSh1_%=\n\tLQh1_%=:\n\t" \
  "s_waitcnt lgkmcnt(10)\n\tv_mfma_f32_16x16x32_bf16 v[24:27], v[64:67], v[140:143], v[24:27]\n\tv_add_u32 %[ta], %[so3], %[pm]\n\t" \
  "ds_read_b64_tr_b16 v[104:105], %[ta] offset:0\n\tds_read_b64_tr_b16 v[106:107], %[ta] offset:3200\n\t" \
  "v_mfma_f32_16x16x32_bf16 v[28:31], v[68:71], v[140:143], v[28:31]\n\tds_read_b64_tr_b16 v[108:109], %[ta] offset:8\n\t" \
  "ds_read_b64_tr_b16 v[110:111], %[ta] offset:3208\n\tv_mfma_f32_16x16x32_bf16 v[32:35], v[72:75], v[140:143], v[32:35]\n\t" \
  "ds_read_b64_tr_b16 v[112:113], %[ta] offset:16\n\tds_read_b64_tr_b16 v[114:115], %[ta] offset:3216\n\t" \
  "v_mfma_f32_16x16x32_bf16 v[36:39], v[76:79], v[140:143], v[36:39]\n\tds_read_b64_tr_b16 v[116:117], %[ta] offset:24\n\t" \
  "ds_read_b64_tr_b16 v[118:119], %[ta] offset:3224\n\ts_waitcnt vmcnt(3)\n\tv_cmp_eq_u32 vcc, -1, v144\n\t" \
  "v_mfma_f32_16x16x32_bf16 v[40:43], v[80:83], v[140:143], v[40:43]\n\tds_read_b64_tr_b16 v[120:121], %[ta] offset:32\n\t" \
  "ds_read_b64_tr_b16 v[122:123], %[ta] offset:3232\n\ts_cbranch_vccnz LSh2_%=\n\tLQh2_%=:\n\ts_waitcnt lgkmcnt(10)\n\t" \
  "v_mfma_f32_16x16x32_bf16 v[24:27], v[84:87], v[144:147], v[24:27]\n\tv_add_u32 %[ta], %[so4], %[pf]\n\tds_read_b64_tr_b16 v[64:65], %[ta] offset:0\n\t" \
  "ds_read_b64_tr_b16 v[66:67], %[ta] offset:12800\n\tv_mfma_f32_16x16x32_bf16 v[28:31], v[88:91], v[144:147], v[28:31]\n\t" \
  "ds_read_b64_tr_b16 v[68:69], %[ta] offset:8\n\tds_read_b64_tr_b16 v[70:71], %[ta] offset:12808\n\t" \
  "v_mfma_f32_16x16x32_bf16 v[32:35], v[92:95], v[144:147], v[32:35]\n\tds_read_b64_tr_b16 v[72:73], %[ta] offset:16\n\t" \
  "ds_read_b64_tr_b16 v[74:75], %[ta] offset:12816\n\tv_mfma_f32_16x16x32_bf16 v[36:39], v[96:99], v[144:147], v[36:39]\n\t" \
  "ds_read_b64_tr_b16 v[76:77], %[ta] offset:24\n\tds_read_b64_tr_b16 v[78:79], %[ta] offset:12824\n\ts_waitcnt vmcnt(2)\n\t" \
  "v_cmp_eq_u32 vcc, -1, v148\n\tv_mfma_f32_16x16x32_bf16 v[40:43], v[100:103], v[144:147], v[40:43]\n\tds_read_b64_tr_b16 v[80:81], %[ta] offset:32\n\t" \
  "ds_read_b64_tr_b16 v[82:83], %[ta] offset:12832\n\ts_cbranch_vccnz LSh3_%=\n\tLQh3_%=:\n\ts_waitcnt lgkmcnt(10)\n\t" \
  "v_mfma_f32_16x16x32_bf16 v[24:27], v[104:107], v[148:151], v[24:27]\n\tv_add_u32 %[ta], %[so0], %[pm]\n\t" \
  "ds_read_b64_tr_b16 v[84:85], %[ta] offset:0\n\tds_read_b64_tr_b16 v[86:87], %[ta] offset:3200\n\t" \
  "v_mfma_f32_16x16x32_bf16 v[28:31], v[108:111], v[148:151], v[28:31]\n\tds_read_b64_tr_b16 v[88:89], %[ta] offset:8\n\t" \
  "ds_read_b64_tr_b16 v[90:91], %[ta] offset:3208\n\tv_mfma_f32_16x16x32_bf16 v[32:35], v[112:115], v[148:151], v[32:35]\n\t" \
  "ds_read_b64_tr_b16 v[92:93], %[ta] offset:16\n\tds_read_b64_tr_b16 v[94:95], %[ta] offset:3216\n\t" \
  "v_mfma_f32_16x16x32_bf16 v[36:39], v[116:119], v[148:151], v[36:39]\n\tds_read_b64_tr_b16 v[96:97], %[ta] offset:24\n\t" \
  "ds_read_b64_tr_b16 v[98:99], %[ta] offset:3224\n\ts_waitcnt vmcnt(1)\n\tv_max_u32 %[tk], v152, v154\n\tv_cmp_eq_u32 vcc, -1, %[tk]\n\t" \
  "v_mfma_f32_16x16x32_bf16 v[40:43], v[120:123], v[148:151], v[40:43]\n\tds_read_b64_tr_b16 v[100:101], %[ta] offset:32\n\t" \
  "ds_read_b64_tr_b16 v[102:103], %[ta] offset:3232\n\ts_cbranch_vccnz LSh4_%=\n\tLQh4_%=:\n\ts_waitcnt lgkmcnt(10)\n\t" \
  "v_mfma_f32_16x16x32_bf16 v[24:27], v[64:67], v[152:155], v[24:27]\n\tv_mfma_f32_16x16x32_bf16 v[28:31], v[68:71], v[152:155], v[28:31]\n\t" \
  "v_mfma_f32_16x16x32_bf16 v[32:35], v[72:75], v[152:155], v[32:35]\n\tv_mfma_f32_16x16x32_bf16 v[36:39], v[76:79], v[152:155], v[36:39]\n\t" \
  "s_waitcnt vmcnt(0)\n\tv_cmp_eq_u32 vcc, -1, v156\n\tv_mfma_f32_16x16x32_bf16 v[40:43], v[80:83], v[152:155], v[40:43]\n\ts_cbranch_vccnz LSh0_%=\n\t" \
  "LQh0_%=:\n\ts_waitcnt lgkmcnt(0)\n\tv_mfma_f32_16x16x32_bf16 v[24:27], v[84:87], v[156:159], v[24:27]\n\t" \
  "v_mfma_f32_16x16x32_bf16 v[28:31], v[88:91], v[156:159], v[28:31]\n\tv_mfma_f32_16x16x32_bf16 v[32:35], v[92:95], v[156:159], v[32:35]\n\t" \
  "v_mfma_f32_16x16x32_bf16 v[36:39], v[96:99], v[156:159], v[36:39]\n\tv_mfma_f32_16x16x32_bf16 v[40:43], v[100:103], v[156:159], v[40:43]\n\t" \
  "s_cmp_eq_u32 %[w], 3\n\ts_cbranch_scc1 LF_%=\n\ts_setprio 3\n\tv_add_u32 %[ta], %[sd], %[l16]\n\tv_mov_b32 v137, %[sc]\n\tv_mov_b32 v136, 1\n\t" \
  "ds_write_b128 %[ta], v[24:27] offset:0\n\tds_write_b128 %[ta], v[28:31] offset:1024\n\tds_write_b128 %[ta], v[32:35] offset:2048\n\t" \
  "ds_write_b128 %[ta], v[36:39] offset:3072\n\tds_write_b128 %[ta], v[40:43] offset:4096\n\ts_mov_b64 exec, 1\n\tds_add_u32 v137, v136\n\t" \
  "s_mov_b64 exec, -1\n\ts_branch LZ_%=\n\tLF_%=:\n\ts_setprio 3\n\tv_mov_b64 v[94:95], 0\n\tv_mov_b32 v96, %[sc]\n\tv_add_u32 %[ta], %[sr], %[l16]\n\t" \
  "LF1_%=:\n\tds_read_b32 v97, v96\n\ts_waitcnt lgkmcnt(0)\n\tv_cmp_gt_u32 vcc, 3, v97\n\ts_cbranch_vccz LF2_%=\n\ts_sleep 1\n\ts_sub_u32 s43, s43, 1\n\t" \
  "s_cbranch_scc0 LF1_%=\n\tLF2_%=:\n\tds_read_b128 v[64:67], %[ta] offset:0\n\tds_read_b128 v[68:71], %[ta] offset:1024\n\t" \
  "ds_read_b128 v[72:75], %[ta] offset:2048\n\tds_read_b128 v[76:79], %[ta] offset:3072\n\tds_read_b128 v[80:83], %[ta] offset:4096\n\t" \
  "ds_read_b128 v[104:107], %[ta] offset:5120\n\tds_read_b128 v[108:111], %[ta] offset:6144\n\tds_read_b128 v[112:115], %[ta] offset:7168\n\t" \
  "ds_read_b128 v[116:119], %[ta] offset:8192\n\tds_read_b128 v[120:123], %[ta] offset:9216\n\tds_read_b128 v[44:47], %[ta] offset:10240\n\t" \
  "ds_read_b128 v[48:51], %[ta] offset:11264\n\tds_read_b128 v[52:55], %[ta] offset:12288\n\tds_read_b128 v[56:59], %[ta] offset:13312\n\t" \
  "ds_read_b128 v[60:63], %[ta] offset:14336\n\ts_waitcnt lgkmcnt(10)\n\tv_pk_add_f32 v[24:25], v[24:25], v[64:65]\n\t" \
  "v_pk_add_f32 v[26:27], v[26:27], v[66:67]\n\tv_pk_add_f32 v[28:29], v[28:29], v[68:69]\n\tv_pk_add_f32 v[30:31], v[30:31], v[70:71]\n\t" \
  "v_pk_add_f32 v[32:33], v[32:33], v[72:73]\n\tv_pk_add_f32 v[34:35], v[34:35], v[74:75]\n\tv_pk_add_f32 v[36:37], v[36:37], v[76:77]\n\t" \
  "v_pk_add_f32 v[38:39], v[38:39], v[78:79]\n\tv_pk_add_f32 v[40:41], v[40:41], v[80:81]\n\tv_pk_add_f32 v[42:43], v[42:43], v[82:83]\n\t" \
  "s_waitcnt lgkmcnt(5)\n\tv_pk_add_f32 v[24:25], v[24:25], v[104:105]\n\tv_pk_add_f32 v[26:27], v[26:27], v[106:107]\n\t" \
  "v_pk_add_f32 v[28:29], v[28:29], v[108:109]\n\tv_pk_add_f32 v[30:31], v[30:31], v[110:111]\n\tv_pk_add_f32 v[32:33], v[32:33], v[112:113]\n\t" \
  "v_pk_add_f32 v[34:35], v[34:35], v[114:115]\n\tv_pk_add_f32 v[36:37], v[36:37], v[116:117]\n\tv_pk_add_f32 v[38:39], v[38:39], v[118:119]\n\t" \
  "v_pk_add_f32 v[40:41], v[40:41], v[120:121]\n\tv_pk_add_f32 v[42:43], v[42:43], v[122:123]\n\ts_waitcnt lgkmcnt(0)\n\ts_branch LT_%=\n\tLSs1_%=:\n\t" \
  "s_sub_u32 s43, s43, 1\n\ts_cbranch_scc1 LQs1_%=\n\ts_sleep %[oback]\n\tglobal_load_dwordx4 v[124:127], %[vd], %[oa1] sc1\n\t" \
  "global_load_dwordx4 v[128:131], %[vk], %[oa1] sc1\n\ts_waitcnt vmcnt(0)\n\tv_max_u32 %[tk], v124, v128\n\tv_cmp_eq_u32 vcc, -1, %[tk]\n\t" \
  "s_cbranch_vccnz LSs1_%=\n\ts_branch LQs1_%=\n\tLSs2_%=:\n\ts_sub_u32 s43, s43, 1\n\ts_cbranch_scc1 LQs2_%=\n\ts_sleep %[oback]\n\t" \
  "global_load_dwordx4 v[132:135], %[vd], %[oa2] sc1\n\tglobal_load_dwordx4 v[136:139], %[vk], %[oa2] sc1\n\ts_waitcnt vmcnt(0)\n\t" \
  "v_max_u32 %[tk], v132, v136\n\tv_cmp_eq_u32 vcc, -1, %[tk]\n\ts_cbranch_vccnz LSs2_%=\n\ts_branch LQs2_%=\n\tLSs3_%=:\n\ts_sub_u32 s43, s43, 1\n\t" \
  "s_cbranch_scc1 LQs3_%=\n\ts_sleep %[oback]\n\tglobal_load_dwordx4 v[140:143], %[vd], %[oa3] sc1\n\t" \
  "global_load_dwordx4 v[144:147], %[vk], %[oa3] sc1\n\ts_waitcnt vmcnt(0)\n\tv_max_u32 %[tk], v140, v144\n\tv_cmp_eq_u32 vcc, -1, %[tk]\n\t" \
  "s_cbranch_vccnz LSs3_%=\n\ts_branch LQs3_%=\n\tLSs4_%=:\n\ts_sub_u32 s43, s43, 1\n\ts_cbranch_scc1 LQs4_%=\n\ts_sleep %[oback]\n\t" \
  "global_load_dwordx4 v[148:151], %[vd], %[oa4] sc1\n\tglobal_load_dwordx4 v[152:155], %[vk], %[oa4] sc1\n\ts_waitcnt vmcnt(0)\n\t" \
  "v_max3_u32 %[tk], v148, v150, v152\n\tv_max_u32 %[tk], %[tk], v154\n\tv_cmp_eq_u32 vcc, -1, %[tk]\n\ts_cbranch_vccnz LSs4_%=\n\ts_branch LQs4_%=\n\t" \
  "LSs5_%=:\n\ts_sub_u32 s43, s43, 1\n\ts_cbranch_scc1 LQs5_%=\n\ts_sleep %[oback]\n\tglobal_load_dwordx4 v[156:159], %[vd], %[oa5] sc1\n\t" \
  "global_load_dwordx4 v[124:127], %[vk], %[oa5] sc1\n\ts_waitcnt vmcnt(0)\n\tv_max_u32 %[tk], v156, v124\n\tv_cmp_eq_u32 vcc, -1, %[tk]\n\t" \
  "s_cbranch_vccnz LSs5_%=\n\ts_branch LQs5_%=\n\tLSs6_%=:\n\ts_sub_u32 s43, s43, 1\n\ts_cbranch_scc1 LQs6_%=\n\ts_sleep %[oback]\n\t" \
  "global_load_dwordx4 v[128:131], %[vd], %[oa6] sc1\n\tglobal_load_dwordx4 v[132:135], %[vk], %[oa6] sc1\n\ts_waitcnt vmcnt(0)\n\t" \
  "v_max_u32 %[tk], v128, v132\n\tv_cmp_eq_u32 vcc, -1, %[tk]\n\ts_cbranch_vccnz LSs6_%=\n\ts_branch LQs6_%=\n\tLSd7_%=:\n\ts_sub_u32 s43, s43, 1\n\t" \
  "s_cbranch_scc1 LQd7_%=\n\ts_sleep %[oback]\n\tglobal_load_dwordx4 v[136:139], %[vd], %[oa7] sc1\n\t" \
  "global_load_dwordx4 v[140:143], %[vd], %[oa8] sc1\n\ts_waitcnt vmcnt(0)\n\tv_max_u32 %[tk], v136, v140\n\tv_cmp_eq_u32 vcc, -1, %[tk]\n\t" \
  "s_cbranch_vccnz LSd7_%=\n\ts_branch LQd7_%=\n\tLSd9_%=:\n\ts_sub_u32 s43, s43, 1\n\ts_cbranch_scc1 LQd9_%=\n\ts_sleep %[oback]\n\t" \
  "global_load_dwordx4 v[144:147], %[vd], %[oa9] sc1\n\tglobal_load_dwordx4 v[148:151], %[vk], %[oa7] sc1\n\ts_waitcnt vmcnt(0)\n\t" \
  "v_max3_u32 %[tk], v144, v146, v148\n\tv_cmp_eq_u32 vcc, -1, %[tk]\n\ts_cbranch_vccnz LSd9_%=\n\ts_branch LQd9_%=\n\tLSk8_%=:\n\t" \
  "s_sub_u32 s43, s43, 1\n\ts_cbranch_scc1 LQk8_%=\n\ts_sleep %[oback]\n\tglobal_load_dwordx4 v[152:155], %[vk], %[oa8] sc1\n\t" \
  "global_load_dwordx4 v[156:159], %[vk], %[oa9] sc1\n\ts_waitcnt vmcnt(0)\n\tv_max3_u32 %[tk], v152, v156, v158\n\tv_cmp_eq_u32 vcc, -1, %[tk]\n\t" \
  "s_cbranch_vccnz LSk8_%=\n\ts_branch LQk8_%=\n\tLSh1_%=:\n\ts_sub_u32 s43, s43, 1\n\ts_cbranch_scc1 LQh1_%=\n\ts_sleep %[oback]\n\t" \
  "global_load_dwordx4 v[140:143], %[vk], %[oa1] sc1\n\ts_waitcnt vmcnt(0)\n\tv_cmp_eq_u32 vcc, -1, v140\n\ts_cbranch_vccnz LSh1_%=\n\t" \
  "s_branch LQh1_%=\n\tLSh2_%=:\n\ts_sub_u32 s43, s43, 1\n\ts_cbranch_scc1 LQh2_%=\n\ts_sleep %[oback]\n\t" \
  "global_load_dwordx4 v[144:147], %[vk], %[oa2] sc1\n\ts_waitcnt vmcnt(0)\n\tv_cmp_eq_u32 vcc, -1, v144\n\ts_cbranch_vccnz LSh2_%=\n\t" \
  "s_branch LQh2_%=\n\tLSh3_%=:\n\ts_sub_u32 s43, s43, 1\n\ts_cbranch_scc1 LQh3_%=\n\ts_sleep %[oback]\n\t" \
  "global_load_dwordx4 v[148:151], %[vk], %[oa3] sc1\n\ts_waitcnt vmcnt(0)\n\tv_cmp_eq_u32 vcc, -1, v148\n\ts_cbranch_vccnz LSh3_%=\n\t" \
  "s_branch LQh3_%=\n\tLSh4_%=:\n\ts_sub_u32 s43, s43, 1\n\ts_cbranch_scc1 LQh4_%=\n\ts_sleep %[oback]\n\t" \
  "global_load_dwordx4 v[152:155], %[vk], %[oa4] sc1\n\ts_waitcnt vmcnt(0)\n\tv_max_u32 %[tk], v152, v154\n\tv_cmp_eq_u32 vcc, -1, %[tk]\n\t" \
  "s_cbranch_vccnz LSh4_%=\n\ts_branch LQh4_%=\n\tLSh0_%=:\n\ts_sub_u32 s43, s43, 1\n\ts_cbranch_scc1 LQh0_%=\n\ts_sleep %[oback]\n\t" \
  "global_load_dwordx4 v[156:159], %[vk], %[oa0] sc1\n\ts_waitcnt vmcnt(0)\n\tv_cmp_eq_u32 vcc, -1, v156\n\ts_cbranch_vccnz LSh0_%=\n\t" \
  "s_branch LQh0_%=\n\tLZ_%=:\n\t" \
  ""
#define COL_ASM \
  "s_mov_b32 s43, %[cap]\n\tv_xor_b32 v101, 0x400, %[lq]\n\tv_xor_b32 v102, 0x800, %[lq]\n\tv_xor_b32 v103, 0xc00, %[lq]\n\t" \
  "v_xor_b32 v104, 0x1000, %[lq]\n\tv_xor_b32 v105, 0x1400, %[lq]\n\tv_xor_b32 v106, 0x1800, %[lq]\n\tv_xor_b32 v107, 0x1c00, %[lq]\n\tLC0_%=:\n\t" \
  "ds_read_b32 v96, %[ac]\n\ts_waitcnt lgkmcnt(0)\n\tv_cmp_gt_u32 vcc, %[nf], v96\n\ts_cbranch_vccz LC1_%=\n\ts_sleep 1\n\ts_sub_u32 s43, s43, 1\n\t" \
  "s_cbranch_scc0 LC0_%=\n\tLC1_%=:\n\ts_setprio 2\n\ts_sleep %[pdel]\n\tglobal_load_dwordx4 v[68:71], v101, %[sp] sc1\n\t" \
  "global_load_dwordx4 v[72:75], v102, %[sp] sc1\n\tglobal_load_dwordx4 v[76:79], v103, %[sp] sc1\n\tglobal_load_dwordx4 v[80:83], v104, %[sp] sc1\n\t" \
  "global_load_dwordx4 v[84:87], v105, %[sp] sc1\n\tglobal_load_dwordx4 v[88:91], v106, %[sp] sc1\n\tglobal_load_dwordx4 v[92:95], v107, %[sp] sc1\n\t" \
  "ds_read_b128 v[64:67], %[ao]\n\ts_waitcnt lgkmcnt(0)\n\tLC2_%=:\n\ts_waitcnt vmcnt(4)\n\tv_pk_add_f32 v[108:109], v[64:65], v[68:69]\n\t" \
  "v_add_f32 v110, v66, v70\n\tv_pk_add_f32 v[112:113], v[72:73], v[76:77]\n\tv_add_f32 v114, v74, v78\n\t" \
  "v_pk_add_f32 v[108:109], v[108:109], v[112:113]\n\tv_add_f32 v110, v110, v114\n\ts_waitcnt vmcnt(0)\n\tv_pk_add_f32 v[116:117], v[80:81], v[84:85]\n\t" \
  "v_add_f32 v118, v82, v86\n\tv_pk_add_f32 v[120:121], v[88:89], v[92:93]\n\tv_add_f32 v122, v90, v94\n\t" \
  "v_pk_add_f32 v[116:117], v[116:117], v[120:121]\n\tv_add_f32 v118, v118, v122\n\tv_pk_add_f32 v[108:109], v[108:109], v[116:117]\n\t" \
  "v_add_f32 v110, v110, v118\n\tv_mul_f32 v112, 0x3acccccd, v108\n\tv_add_f32 v111, v108, v109\n\tv_mul_f32 v113, v112, v112\n\t" \
  "v_cmp_u_f32 vcc, v111, v110\n\tv_fma_f32 v113, v110, %[rdm], -v113\n\ts_cbranch_vccnz LC5_%=\n\tLC3_%=:\n\tv_max_f32 v113, 0, v113\n\t" \
  "v_cvt_pk_bf16_f32 v113, 0, v113\n\tv_add_f32 v113, 0x3727c5ac, v113\n\tv_cvt_pk_bf16_f32 v113, 0, v113\n\tv_sqrt_f32 v113, v113\n\t" \
  "v_cvt_pk_bf16_f32 v112, 0, v112\n\tv_cvt_pk_bf16_f32 v113, 0, v113\n\tv_rcp_f32 v113, v113\n\tv_fma_f32 v114, -%[swr], v112, v109\n\t" \
  "v_mul_f32 v114, v114, v113\n\tv_ashrrev_i32 v115, 31, v114\n\tv_or_b32 v115, 0x80000000, v115\n\tv_xor_b32 v114, v114, v115\n\t" \
  "v_and_or_b32 v114, v114, %[msk], %[rid]\n\ts_nop 1\n\tv_max_u32_dpp v114, v114, v114 quad_perm:[1,0,3,2] row_mask:0xf bank_mask:0xf bound_ctrl:1\n\t" \
  "ds_write_b64 %[at], v[112:113]\n\ts_nop 0\n\tv_max_u32_dpp v114, v114, v114 quad_perm:[2,3,0,1] row_mask:0xf bank_mask:0xf bound_ctrl:1\n\ts_nop 1\n\t" \
  "v_max_u32_dpp v114, v114, v114 row_half_mirror row_mask:0xf bank_mask:0xf bound_ctrl:1\n\ts_nop 1\n\t" \
  "v_max_u32_dpp v114, v114, v114 row_mirror row_mask:0xf bank_mask:0xf bound_ctrl:1\n\ts_nop 0\n\tv_readlane_b32 s40, v114, 0\n\t" \
  "v_readlane_b32 s41, v114, 16\n\tv_readlane_b32 s42, v114, 32\n\ts_max_u32 s40, s40, s41\n\ts_max_u32 s40, s40, s42\n\tv_mov_b32 v98, s40\n\t" \
  "s_mov_b32 exec_lo, 0x01010101\n\ts_mov_b32 exec_hi, 0x0101\n\tglobal_store_dwordx2 %[zr], v[98:99], %[kp] sc1\n\tds_write_b32 %[ak], v98\n\t" \
  "s_mov_b64 exec, -1\n\ts_and_b32 s41, s40, 0x7ff\n\ts_sub_u32 s41, %[rb], s41\n\ts_mul_i32 s42, s41, 160\n\tv_add_u32 v115, s42, %[ah]\n\t" \
  "ds_read_b64 v[116:117], v115\n\tv_readlane_b32 s44, v112, s41\n\tv_readlane_b32 s46, v113, s41\n\ts_waitcnt lgkmcnt(0)\n\ts_mov_b32 exec_hi, 0\n\t" \
  "v_lshlrev_b32 v118, 16, v116\n\tv_and_b32 v119, 0xffff0000, v116\n\tv_lshlrev_b32 v120, 16, v117\n\tv_and_b32 v121, 0xffff0000, v117\n\t" \
  "v_pk_add_f32 v[122:123], v[118:119], s[44:45] op_sel_hi:[1,0] neg_lo:[0,1] neg_hi:[0,1]\n\t" \
  "v_pk_add_f32 v[114:115], v[120:121], s[44:45] op_sel_hi:[1,0] neg_lo:[0,1] neg_hi:[0,1]\n\t" \
  "v_pk_fma_f32 v[118:119], v[122:123], s[46:47], v[118:119] op_sel_hi:[1,0,1]\n\t" \
  "v_pk_fma_f32 v[120:121], v[114:115], s[46:47], v[120:121] op_sel_hi:[1,0,1]\n\tv_cvt_pk_bf16_f32 v116, v118, v119\n\t" \
  "v_cvt_pk_bf16_f32 v117, v120, v121\n\ts_mov_b32 exec_hi, -1\n\tglobal_store_dwordx2 %[co], v[116:117], %[cp] sc1\n\ts_branch LC9_%=\n\tLC5_%=:\n\t" \
  "s_sub_u32 s43, s43, 1\n\ts_cbranch_scc1 LC3_%=\n\ts_mov_b32 s40, 0\n\tv_max3_u32 v96, v68, v69, v70\n\tv_cmp_eq_u32 vcc, -1, v96\n\t" \
  "s_cbranch_vccz LC61_%=\n\tglobal_load_dwordx4 v[68:71], v101, %[sp] sc1\n\ts_mov_b32 s40, 1\n\tLC61_%=:\n\tv_max3_u32 v96, v72, v73, v74\n\t" \
  "v_cmp_eq_u32 vcc, -1, v96\n\ts_cbranch_vccz LC62_%=\n\tglobal_load_dwordx4 v[72:75], v102, %[sp] sc1\n\ts_mov_b32 s40, 1\n\tLC62_%=:\n\t" \
  "v_max3_u32 v96, v76, v77, v78\n\tv_cmp_eq_u32 vcc, -1, v96\n\ts_cbranch_vccz LC63_%=\n\tglobal_load_dwordx4 v[76:79], v103, %[sp] sc1\n\t" \
  "s_mov_b32 s40, 1\n\tLC63_%=:\n\tv_max3_u32 v96, v80, v81, v82\n\tv_cmp_eq_u32 vcc, -1, v96\n\ts_cbranch_vccz LC64_%=\n\t" \
  "global_load_dwordx4 v[80:83], v104, %[sp] sc1\n\ts_mov_b32 s40, 1\n\tLC64_%=:\n\tv_max3_u32 v96, v84, v85, v86\n\tv_cmp_eq_u32 vcc, -1, v96\n\t" \
  "s_cbranch_vccz LC65_%=\n\tglobal_load_dwordx4 v[84:87], v105, %[sp] sc1\n\ts_mov_b32 s40, 1\n\tLC65_%=:\n\tv_max3_u32 v96, v88, v89, v90\n\t" \
  "v_cmp_eq_u32 vcc, -1, v96\n\ts_cbranch_vccz LC66_%=\n\tglobal_load_dwordx4 v[88:91], v106, %[sp] sc1\n\ts_mov_b32 s40, 1\n\tLC66_%=:\n\t" \
  "v_max3_u32 v96, v92, v93, v94\n\tv_cmp_eq_u32 vcc, -1, v96\n\ts_cbranch_vccz LC67_%=\n\tglobal_load_dwordx4 v[92:95], v107, %[sp] sc1\n\t" \
  "s_mov_b32 s40, 1\n\tLC67_%=:\n\ts_cmp_eq_u32 s40, 0\n\ts_cbranch_scc1 LC3_%=\n\ts_waitcnt vmcnt(0)\n\ts_branch LC2_%=\n\tLC9_%=:\n\t" \
  ""
extern "C" __global__ __launch_bounds__(768, 3) void fused_block(const u16* x, const u16* wqkv, const u16* wo, const u16* w_r,
                                                   const u16* w_gate, const u16* w_up, const u16* w_down, u16* out,
                                                   char* ws) {
  extern __shared__ __attribute__((aligned(16))) char smem[];
  const int tid = threadIdx.x;
  const int lane = tid & 63;
  const int w = __builtin_amdgcn_readfirstlane(tid >> 6);
  const int g = lane >> 4, r16 = lane & 15, q4 = (lane & 15) >> 2, p4 = lane & 3;
  const int bid = blockIdx.x;
  const int xcd = bid & 7;
  const int bidx = bid >> 3;
  const u32 l16 = lane * 16;
  if (w == 11) {
    u64 pc;
    asm volatile("s_getpc_b64 %0" : "=s"(pc));
    if (lane < 10) touch_line((const void*)(pc + min((bidx * 10 + lane) * 128, CODE_BYTES)));
  }

  if (w == 0 && lane == 0) {
    const int q = bidx & 3;
    touch_line(((q & 2) ? (const char*)x : (const char*)wqkv) + (q & 1) * 2359296 + bidx * 128);
  }
  if (bidx >= 26) {
    const int fb = xcd * 6 + (bidx - 26);
    const u32 tstart = (u32)__builtin_amdgcn_s_memrealtime();
    const bool hurry = ((((u32)(u64)w_gate + fb * 64) >> 7) & 7u) == 3u;
#define WAIT_UNTIL(T)                                                                      \
  _Pragma("nounroll") for (int it = 0; it < 1000; ++it) {                                  \
    if ((int)((u32)__builtin_amdgcn_s_memrealtime() - tstart) >= (T)) break;               \
    __builtin_amdgcn_s_sleep(4);                                                           \
  }
    if (w < 3) {
      int li = (bidx - 26) * 160 + w * 64 + lane;
      if (w < 2 || lane < 32) touch_line((const char*)x + (size_t)(((xcd >> 2) * 13 + 6) * 80 - 16) * 1280 + li * 128);
    }
    u32x2 ep;
    u32 tag0;
    asm volatile("global_load_dwordx2 %0, %1, off sc1" : "=v"(ep) : "v"(ws + WS_EPOCH) : "memory");
    {
      int wi = (((xcd >> 1) & 1) * 6 + (bidx - 26)) * 12 + w;
      wi = wi > 129 ? 129 : wi;
      int row = (xcd >> 2) * 1040 + (xcd & 1) * 520 + wi * 4 + g;
      row = row > SEQ - 1 ? SEQ - 1 : row;
      const u32 vo = row * 1280 + r16 * 16;
      u32x4 xr[5];
#pragma unroll
      for (int i = 0; i < 5; ++i)
        asm volatile("global_load_dwordx4 %0, %1, %2 offset:%3" : "=v"(xr[i]) : "v"(vo), "s"(x), "n"(i * 256) : "memory");
      asm volatile("s_waitcnt vmcnt(0)" ::: "memory");
      asm volatile("" : "+v"(xr[0]), "+v"(xr[1]), "+v"(xr[2]), "+v"(xr[3]), "+v"(xr[4]), "+v"(ep)::"memory");
      tag0 = ep[0] + (ep[0] == SENT - 1u ? 2u : 1u);
      float sm = 0.f;
#pragma unroll
      for (int i = 0; i < 5; ++i)
#pragma unroll
        for (int e = 0; e < 4; ++e) sm = dot2b(xr[i][e], 0x3F803F80u, sm);
      const float mean = row16_sum(sm) * (1.0f / DM);
      float ss = 0.f;
#pragma unroll
      for (int i = 0; i < 5; ++i)
#pragma unroll
        for (int e = 0; e < 4; ++e) {
          const float a = lo_f(xr[i][e]) - mean, b = hi_f(xr[i][e]) - mean;
          ss = __builtin_fmaf(a, a, ss);
          ss = __builtin_fmaf(b, b, ss);
        }
      const float var = row16_sum(ss) * (1.0f / DM);
      const float den = rbf(__builtin_amdgcn_sqrtf(rbf(rbf(var) + 1e-5f)));
      if (r16 == 0) GST2((u32)(row * 8), ws + WS_LN1, 0, ((u32x2){cvt2((f32x2){mean, den}), tag0}));
    }
    u32x4 va[2][5], vb[5];
    const int vrk = (fb >> 4) * 14 + (int)(((((u32)(u64)w_gate + fb * 64) >> 6) - 8u) & 15u);
    const int vq0 = vrk * 4 + w * 2;
    u32x4 ra;
    u32 rb5;
    asm volatile("global_load_dwordx4 %0, %2, %4\n\tglobal_load_dword %1, %3, %4 offset:1024"
                   : "=&v"(ra), "=&v"(rb5) : "v"(l16), "v"((u32)(lane * 4)), "s"(w_r) : "memory");
    if (w >= 2) {
      WAIT_UNTIL(hurry ? 0 : PDELAY + 12 * (w - 2))
      const u32x4 s4 = (u32x4){SENT, SENT, SENT, SENT};
      const char* fp = ws + WS_FILL + (size_t)fb * 73728 + (w - 2) * 7168 + 4096;
      GST4(l16, fp, -4096, s4); GST4(l16, fp, -3072, s4); GST4(l16, fp, -2048, s4); GST4(l16, fp, -1024, s4);
      GST4(l16, fp, 0, s4); GST4(l16, fp, 1024, s4); GST4(l16, fp, 2048, s4);
      if (w < 4) GST4(l16, ws + WS_FILL + (size_t)fb * 73728 + 69632 + w * 1024, 0, s4);
      if (w >= 5) GST4((u32)(((w - 5) * 8 + (lane >> 3)) * 6144 + (lane & 7) * 16), ws + WS_PK + fb * 128, 0, s4);
    }
    if (w == 0) {
      const u64 e1 = (((u64)ep[1] << 32) | ep[0]) + (ep[0] == SENT - 1u ? 2ull : 1ull);
      if (lane == 0) *(u32x4*)(smem + CTL_OFF) = (u32x4){(u32)e1, (u32)(e1 >> 32), 0u, 0u};
      if (lane >= 1 && lane < 8) *(u32x4*)(smem + CTL_OFF + lane * 16) = (u32x4){0u, 0u, 0u, 0u};
      if (lane >= 32) *(u32x4*)(smem + KEY_OFF - 512 + lane * 16) = (u32x4){0u, 0u, 0u, 0u};
    }
    if (w >= 6) {
      const u32 o1 = tid >= 636 ? 0x3F803F80u : 0u;
      if (tid < 720) *(u32x4*)(smem + HN_OFF + (tid - 384) * 16) = (u32x4){o1, o1, o1, o1};
    }
    if (w < 2 && !hurry && vrk < 40) {
      WAIT_UNTIL(VDELAY)
      const u32 vo = (vq0 * 4 + g) * 1280 + r16 * 16;
#pragma unroll
      for (int i = 0; i < 5; ++i)
        asm volatile("global_load_dwordx4 %0, %3, %4 offset:%8\n\tglobal_load_dwordx4 %1, %3, %5 offset:%8\n\tglobal_load_dwordx4 %2, %6, %7 offset:%8"
                     : "=&v"(va[0][i]), "=&v"(va[1][i]), "=&v"(vb[i]) : "v"(vo), "s"(wo), "s"((const char*)wo + 5120), "v"((u32)(r16 * 16)), "s"(w_r), "n"(i * 256) : "memory");
      asm volatile("s_waitcnt vmcnt(0)" : "+v"(va[0][0]), "+v"(va[0][1]), "+v"(va[0][2]), "+v"(va[0][3]), "+v"(va[0][4]), "+v"(va[1][0]), "+v"(va[1][1]),
                   "+v"(va[1][2]), "+v"(va[1][3]), "+v"(va[1][4]), "+v"(vb[0]), "+v"(vb[1]), "+v"(vb[2]), "+v"(vb[3]), "+v"(vb[4])::"memory");
      float sb = 0.f;
#pragma unroll
      for (int i = 0; i < 20; ++i) sb = dot2b(vb[i >> 2][i & 3], 0x3F803F80u, sb);
      sb = row16_sum(sb) * (1.0f / DM);
      u32x4 pk2[2];
#pragma unroll
      for (int k = 0; k < 2; ++k) {
        float d = 0.f, sa = 0.f;
#pragma unroll
        for (int i = 0; i < 20; ++i) {
          d = dot2b(va[k][i >> 2][i & 3], vb[i >> 2][i & 3], d);
          sa = dot2b(va[k][i >> 2][i & 3], 0x3F803F80u, sa);
        }
        const u32 vp = __float_as_uint(row16_sum(d - sa * sb));
        pk2[k] = (u32x4){cvt2((f32x2){__uint_as_float(__builtin_amdgcn_readlane(vp, 0)), __uint_as_float(__builtin_amdgcn_readlane(vp, 16))}), tag0,
                          cvt2((f32x2){__uint_as_float(__builtin_amdgcn_readlane(vp, 32)), __uint_as_float(__builtin_amdgcn_readlane(vp, 48))}), tag0};
      }
      if (lane < 8) GST4(l16, ws + WS_VP + vq0 * 64, 0, ((lane & 1) ? pk2[1] : pk2[0]));
    } else
      asm volatile("s_waitcnt vmcnt(0)" ::: "memory");
    asm volatile("" : "+v"(ra), "+v"(rb5)::"memory");
    LDS_BARRIER();
    const u32x2 tg = *(const u32x2*)(smem + CTL_OFF);
    const u32 tag = tg[0];
    if (w == 2 && lane == 0) GST2(0u, ws + WS_HFLAG + fb * 128, 0, tg);
    if (w >= 3) {
      int li = (bidx - 26) * 534 + (w - 3) * 64 + lane;
      li = li > 3199 ? 3199 : li;
      const int rr = li / 5;
      if (w < 11 || lane < 22) touch_line((const char*)wo + rr * 1280 + ((xcd >> 1) & 1) * 640 + (li - rr * 5) * 128);
    }
    WAIT_UNTIL(hurry ? 0 : FDELAY)
    asm volatile("s_barrier" ::: "memory");
    if (w >= 2) {
      const int ch = (lane & 7) ^ (((lane >> 4) & 1) * 2 + ((w + fb) & 1) * 4);
      const u32 vg = (lane >> 3) * (FF * 2) + fb * 64 + (ch & 3) * 16;
      const u64 gsel = (ch & 4) ? (u64)w_up : (u64)w_gate;
#pragma nounroll
      for (int n0 = w - 2; n0 < 80; n0 += 10) {
        const int n = (fb & 1) ? 79 - n0 : n0;
        const u64 p = gsel + vg + (u32)(n * 8 * FF * 2);
        asm volatile("s_mov_b32 m0, %1\n\ts_nop 0\n\tglobal_load_lds_dwordx4 %0, off" ::"v"(p), "s"(n * 1024) : "memory");
      }
#pragma nounroll
      for (int n = 11 - w; n < 40; n += 10) {
        const u32 row = (u32)(n * 64 + lane) / 80u;
        asm volatile("s_mov_b32 m0, %2\n\ts_nop 0\n\tglobal_load_lds_dwordx4 %0, %1" " nt"
                     ::"v"(l16 ^ ((row & 3u) * 32u + (row & 8u) * 16u)), "s"((const char*)w_down + (size_t)fb * 40960 + n * 1024), "s"(DOWN_OFF + n * 1024) : "memory");
      }
      asm volatile("s_waitcnt vmcnt(0)" ::: "memory");
      if (lane == 0) asm volatile("ds_add_u32 %0, %1" ::"v"(CTL_OFF + 48), "v"(1u) : "memory");
    } else {
      if (w == 1 && lane < 4)
        touch_line(lane == 3 ? ws + WS_PK - 65536 + bid * 128 : (const char*)out + lane * 1277952 + fb * 1280);
      WAIT_UNTIL(KSTART)
    }
    if (w >= 4) {
      const int v = w - 4, kq = v & 3, m = v >> 2;
#pragma nounroll
      for (int it = 0; it < SPIN_CAP; ++it) {
        if (__builtin_amdgcn_readfirstlane(lds_ld32(CTL_OFF + 48)) >= 10u) break;
        __builtin_amdgcn_s_sleep(1);
      }
      bf16x8 gfr[5], ufr[5], dfr[5];
      {
        const int gx = (160 * kq + 8 * g + q4) * 128 + ((m ^ (q4 >> 1) ^ ((g & 1) * 2)) * 32) + p4 * 8;
        const char* db = smem + DOWN_OFF + (8 * g + q4) * 1280 + p4 * 8;
#pragma unroll
        for (int j = 0; j < 5; ++j) {
          gfr[j] = tr_frag(smem + gx + j * 4096, 512);
          ufr[j] = tr_frag(smem + (gx ^ 64) + j * 4096, 512);
          dfr[j] = tr_frag(db + ((v * 160 + j * 32) ^ (q4 * 32 + (g & 1) * 128)), 5120);
        }
#pragma unroll
        for (int j = 0; j < 5; ++j) asm volatile("" : "+v"(gfr[j]), "+v"(ufr[j]), "+v"(dfr[j]));
      }
      u32 ahn = HN_OFF + p4 * HSL + kq * 320 + g * 16, asw = ACT_OFF + p4 * ASL + kq * 256 + (q4 & 1) * 128 + m * 64 + g * 16, asr = ACT_OFF + (g * 8 + (lane & 7)) * 4;
      u32 vpo = v * 320 + r16 * 64 + g * 16, up3 = (lane & 8) ? 3u : 0u;
      asm volatile("" : "+v"(up3));
      const char* pdst = ws + WS_PART + fb * 2560;
      asm volatile("" : "+v"(ahn), "+v"(asw), "+v"(asr), "+v"(vpo));
      asm volatile("" : "+s"(pdst));
      int pass = 0;
      asm volatile("" : "+s"(pass));
#pragma nounroll
      for (; pass < 2; ++pass) {
        asm volatile("s_barrier" ::: "memory");
        u32x4 bs;
#define GU(J, C0, C1) "v_mfma_f32_16x16x32_bf16 v[120:123], %[g" #J "], v[100+4*" #J ":103+4*" #J "], " C0 "\n\t" \
                      "v_mfma_f32_16x16x32_bf16 v[124:127], %[u" #J "], v[100+4*" #J ":103+4*" #J "], " C1 "\n\t"
#define GU1(J) GU(J, "v[120:123]", "v[124:127]")
        asm volatile("ds_read_b128 v[100:103], %[ah]\n\tds_read_b128 v[104:107], %[ah] offset:64\n\tds_read_b128 v[108:111], %[ah] offset:128\n\t"
                     "ds_read_b128 v[112:115], %[ah] offset:192\n\tds_read_b128 v[116:119], %[ah] offset:256\n\t"
                     "s_waitcnt lgkmcnt(4)\n\t" GU(0, "0", "0") "s_waitcnt lgkmcnt(3)\n\t" GU1(1) "s_waitcnt lgkmcnt(2)\n\t" GU1(2)
                     "s_waitcnt lgkmcnt(1)\n\t" GU1(3) "s_waitcnt lgkmcnt(0)\n\t" GU1(4)
                     "ds_read_b128 %[bs], %[ab]\n\ts_mov_b32 exec_lo, 0x00f000f0\n\ts_mov_b32 exec_hi, 0x00f000f0\n\ts_nop 4\n\t"
                     "v_mov_b64 v[120:121], v[124:125]\n\tv_mov_b64 v[122:123], v[126:127]\n\t"
                     "s_mov_b32 exec_lo, 0x00ff00ff\n\ts_mov_b32 exec_hi, 0x00ff00ff\n\tds_write_b128 %[aw], v[120:123]\n\ts_mov_b64 exec, -1\n\t"
                     "s_setprio 3\n\ts_waitcnt lgkmcnt(0)\n\tv_cmp_eq_u32 vcc, 3, v129\n\ts_barrier\n\t"
                     "s_cbranch_vccnz 1f\n\tds_read_b128 %[bs], %[ab]\n\ts_waitcnt lgkmcnt(0)\n1:"
                     : [bs] "=&{v[128:131]}"(bs)
                     : [ah] "v"(ahn), [ab] "v"((u32)(CTL_OFF + 112)), [aw] "v"(asw), [g0] "v"(gfr[0]), [g1] "v"(gfr[1]), [g2] "v"(gfr[2]),
                       [g3] "v"(gfr[3]), [g4] "v"(gfr[4]), [u0] "v"(ufr[0]), [u1] "v"(ufr[1]), [u2] "v"(ufr[2]), [u3] "v"(ufr[3]), [u4] "v"(ufr[4])
                     : "memory", "vcc", "scc", "v100", "v101", "v102", "v103", "v104", "v105", "v106", "v107", "v108", "v109", "v110", "v111", "v112", "v113",
                       "v114", "v115", "v116", "v117", "v118", "v119", "v120", "v121", "v122", "v123", "v124", "v125", "v126", "v127");
        const u32x2 best = {bs[2], bs[3]};
        f32x2 s0, s1, s2, s3;
        asm volatile("ds_read2_b32 %0, %4 offset0:0 offset1:32\n\tds_read2_b32 %1, %4 offset0:64 offset1:96\n\t"
                     "ds_read2_b32 %2, %4 offset0:128 offset1:160\n\tds_read2_b32 %3, %4 offset0:192 offset1:224"
                     : "=&v"(s0), "=&v"(s1), "=&v"(s2), "=&v"(s3) : "v"(asr + ((best[1] & 3u) | up3) * ASL) : "memory");
        float rd = __builtin_amdgcn_rcpf(hi_f(best[0])), mb = lo_f(best[0]);
        asm volatile("s_waitcnt lgkmcnt(0)" : "+v"(s0), "+v"(s1), "+v"(s2), "+v"(s3), "+v"(rd), "+v"(mb)::"memory");
        const f32x2 sm = (s0 + s1) + (s2 + s3);
        const float gt = (sm[0] - mb * dppf<0x108>(sm[0])) * rd, up = (sm[1] - mb * dppf<0x108>(sm[1])) * rd;
        const float av = gt * up * __builtin_amdgcn_rcpf(1.0f + __expf(-gt));
        const u32 ap = cvt2((f32x2){av, dppf<0x101>(av)});
        const u32x4 bm = {dppu<0x150>(ap), dppu<0x152>(ap), dppu<0x154>(ap), dppu<0x156>(ap)};
#define DN(J) "v_mfma_f32_16x16x32_bf16 v[104+4*" #J ":107+4*" #J "], %[d" #J "], %[bm], 0\n\t"
#define MV(J) "s_lshl_b64 exec, exec, 1\n\tv_mov_b64 v[104:105], v[104+4*" #J ":105+4*" #J "]\n\tv_mov_b64 v[106:107], v[106+4*" #J ":107+4*" #J "]\n\t"
        asm volatile("s_nop 1\n\t" DN(0) DN(1) DN(2) DN(3) DN(4)
                     "s_mov_b32 exec_lo, 0x00010001\n\ts_mov_b32 exec_hi, 0x00010001\n\ts_nop 1\n\t" MV(1) MV(2) MV(3) MV(4)
                     "s_mov_b32 exec_lo, 0x001f001f\n\ts_mov_b32 exec_hi, 0x001f001f\n\tglobal_store_dwordx4 %[vp], v[104:107], %[pd] sc1\n\t"
                     "s_mov_b64 exec, -1\n\ts_setprio 0"
                     ::[bm] "v"(bm), [vp] "v"(vpo), [pd] "s"(pdst), [d0] "v"(dfr[0]), [d1] "v"(dfr[1]),
                       [d2] "v"(dfr[2]), [d3] "v"(dfr[3]), [d4] "v"(dfr[4])
                     : "memory", "scc", "v100", "v101", "v102", "v103", "v104", "v105", "v106", "v107", "v108", "v109", "v110", "v111", "v112", "v113",
                       "v114", "v115", "v116", "v117", "v118", "v119", "v120", "v121", "v122", "v123");
        if (pass) break;
        u32 fin = 0;
#pragma nounroll
        for (int it = 0; it < SPIN_CAP * 8; ++it) {
          fin = __builtin_amdgcn_readfirstlane(lds_ld32(CTL_OFF + 40));
          if (fin) break;
          __builtin_amdgcn_s_sleep(4);
        }
        if (fin != 2u) break;
        pdst += WS_PART2 - WS_PART;
      }
      return;
    }
    if (w == 0) {
      u32 pk6;
      asm volatile(
          "s_mov_b32 s41, %[cap]\n\ts_mov_b32 s40, 1\n\ts_mov_b64 s[46:47], 0\n\tv_mov_b32 v5, %[ln]\n\t"
          "s_mov_b32 s54, -1\n\ts_mov_b32 s55, 0x7ffff\n\tv_mov_b32 v12, 0\n\t"
#define PKLD(D) "s_mov_b64 exec, s[54:55]\n\tglobal_atomic_or_x2 " D ", %[vk], %[z], %[kb] sc0 sc1\n\ts_mov_b64 exec, -1\n\t"
          PKLD("v[8:9]") "s_sleep %[gap]\n\t" PKLD("v[10:11]") "s_sleep %[gap]\n\t" PKLD("v[14:15]")
#define MDPP(C) "s_nop 1\n\tv_max_u32_dpp v13, v13, v13 " C " row_mask:0xf bank_mask:0xf bound_ctrl:1\n\t"
#define PSTEP(L, D0, D1, DR)                                                                        \
          L "0:\n\t"                                                                               \
          "s_waitcnt vmcnt(2)\n\t"                                                                 \
          "v_cmp_eq_u32 s[42:43], " D1 ", %[tag]\n\t"                                              \
          "s_and_b64 exec, s[42:43], s[54:55]\n\ts_andn2_b64 s[54:55], s[54:55], s[42:43]\n\t"      \
          "v_and_or_b32 v12, " D0 ", %[msk], %[lid]\n\t"                                           \
          "v_mov_b32 v6, " D0 "\n\ts_mov_b64 exec, -1\n\t"                                         \
          "v_cmp_le_u32 vcc, s40, v12\n\t"                                                         \
          "s_cmp_eq_u64 vcc, s[46:47]\n\ts_cbranch_scc1 " L "1f\n\t"                              \
          "s_swappc_b64 s[50:51], s[48:49]\n"                                                      \
          L "1:\n\t"                                                                               \
          "s_cmp_eq_u64 s[54:55], 0\n\ts_cbranch_scc1 9f\n\t"                                     \
          PKLD(DR)
          "s_getpc_b64 s[48:49]\n\ts_add_u32 s48, s48, 12\n\ts_addc_u32 s49, s49, 0\n\ts_branch 10f\n\t"
          "v_mov_b32 v13, v12\n\t"
          MDPP("quad_perm:[1,0,3,2]") MDPP("quad_perm:[2,3,0,1]") MDPP("row_half_mirror") MDPP("row_mirror")
          "v_bfe_u32 v4, v6, 10, 6\n\tv_readlane_b32 s44, v13, 0\n\tv_readlane_b32 s45, v13, 16\n\tv_readlane_b32 s46, v13, 32\n\tv_readlane_b32 s47, v13, 48\n\t"
          "v_sub_u32 v4, %[rb], v4\n\ts_max_u32 s44, s44, s45\n\ts_max_u32 s46, s46, s47\n\ts_max_u32 s44, s44, s46\n\t"
          "s_and_b32 s45, s44, 0x7fffff\n\ts_bitset1_b32 s45, 23\n\ts_mul_hi_u32 s45, s45, %[shr]\n"
          "5:\n\ts_sub_u32 s40, s44, s45\n\ts_cselect_b32 s40, 1, s40\n\ts_max_u32 s40, s40, 1\n\tv_cmp_le_u32 s[46:47], s40, v12\n\ts_mul_i32 s45, s45, 3\n\ts_lshr_b32 s45, s45, 2\n\t"
          "s_bcnt1_i32_b64 s52, s[46:47]\n\ts_cmp_gt_u32 s52, 3\n\ts_cbranch_scc1 5b\n\t"
          "v_mbcnt_lo_u32_b32 v13, s46, 0\n\tv_mbcnt_hi_u32_b32 v13, s47, v13\n\tv_sub_u32 v7, %[ln], v13\n\tv_add_u32 v7, s52, v7\n\t"
          "v_cndmask_b32 v13, v7, v13, s[46:47]\n\tv_cndmask_b32 v7, 2, 1, s[46:47]\n\tv_cmp_gt_u32 s[44:45], 3, v13\n\t"
          "v_lshl_add_u32 v13, v13, 4, %[pa]\n\ts_mov_b64 exec, s[44:45]\n\tds_write_b128 v13, v[4:7]\n\ts_mov_b64 exec, -1\n\t"
          "s_setpc_b64 s[50:51]\n"
          PSTEP("1", "v8", "v9", "v[8:9]")
          PSTEP("2", "v10", "v11", "v[10:11]")
          PSTEP("3", "v14", "v15", "v[14:15]")
          "s_sub_u32 s41, s41, 1\n\ts_cbranch_scc0 10b\n\ts_swappc_b64 s[50:51], s[48:49]\n"
          "9:\n\t"  "v_or_b32 v7, 4, v7\n\ts_mov_b64 exec, s[44:45]\n\tds_write_b32 v13, v7 offset:12\n\ts_mov_b64 exec, -1\n\tds_write_b32 %[at], v6\n\ts_waitcnt lgkmcnt(0)\n\ts_barrier\n\ts_waitcnt vmcnt(0)"
          : "=&{v6}"(pk6) : [cap] "n"(SPIN_CAP), [gap] "n"(PGAP), [vk] "v"((u32)(lane * 6144)), [kb] "s"(ws + WS_PK + fb * 128), [tag] "v"(tag), [z] "v"(0ull), [ln] "v"(lane),
            [msk] "s"(~1023u), [lid] "v"(64u | lane), [rb] "v"(2047 - ((lane >> 1) * 80 + (lane & 1) * 48)),
            [shr] "s"((u32)((1.0 - PTHR) * 4294967296.0)), [pa] "s"(POST_OFF), [at] "v"((u32)(KEY_OFF - 256 + lane * 4))
          : "memory", "vcc", "scc", "s40", "s41", "s42", "s43", "s44", "s45", "s46", "s47", "s48", "s49", "s50", "s51", "s52", "s54", "s55",
            "v4", "v5", "v7", "v8", "v9", "v10", "v11", "v12", "v13", "v14", "v15");
      if (fb == 47 && lane < 8) GST4(l16, ws + WS_EPOCH, 0, ((u32x4){tg[0], tg[1], tg[0], tg[1]}));
      const u32 now = (u32)__builtin_amdgcn_s_memrealtime();
      const u32 nag = (pk6 - (now >> 1) - 1u) & 1023u;
      u32 kgo = __builtin_amdgcn_readfirstlane(now + KCD - 2 * (1023u - wave_max(lane < 51 ? nag : 0u)));
      u64 old = __ballot(lane < 51 && nag < 1023u - (KOLD - 8) / 2);
      asm volatile("" : "+s"(kgo), "+s"(old));
      asm volatile(
          "s_mov_b32 s41, %[cap]\n\ts_mov_b32 s54, -1\n\ts_mov_b32 s55, 0x7ffff\n\tv_mov_b32 v12, 0\n\t"
          "s_mov_b64 exec, %[old]\n\ts_cbranch_execz 3f\n\tglobal_load_dwordx2 v[14:15], %[vk], %[kb] sc1\n"
          "3:\n\ts_setprio 3\n\ts_barrier\n"
          "4:\n\ts_memrealtime s[42:43]\n\ts_waitcnt lgkmcnt(0)\n\ts_sub_u32 s42, s42, %[go]\n\ts_cmp_lt_i32 s42, 0\n\ts_cbranch_scc1 4b\n\t"
          "s_andn2_b64 exec, s[54:55], %[old]\n\ts_cbranch_execz 5f\n\tglobal_load_dwordx2 v[14:15], %[vk], %[kb] sc1\n"
          "5:\n\ts_mov_b64 exec, s[54:55]\n\tds_read_b32 v4, %[ctl] offset:92\n\ts_waitcnt lgkmcnt(0)\n\tv_and_b32 v4, 3, v4\n\t"
          "v_lshl_add_u32 v4, v4, 4, %[pa]\n\tds_read_b64 v[6:7], v4\n\ts_waitcnt lgkmcnt(0)\n\t"
          "v_readfirstlane_b32 s48, v6\n\tv_readfirstlane_b32 s49, v7\n\tv_mov_b32 v6, 1\n\ts_waitcnt vmcnt(0)\n\tv_cmp_ne_u32 s[54:55], v15, %[tag]\n\tv_mov_b32 v12, v14\n\ts_mov_b64 exec, -1\n\t"
          "s_cmp_eq_u64 s[54:55], 0\n\ts_cbranch_scc1 9f\n\t"
          PKLD("v[8:9]") "s_sleep %[gap]\n\t" PKLD("v[10:11]")
#define KSTEP(L, D0, D1, DR)                                                                        \
          L "0:\n\t"                                                                               \
          "s_waitcnt vmcnt(1)\n\t"                                                                 \
          "v_cmp_eq_u32 s[42:43], " D1 ", %[tag]\n\t"                                              \
          "s_and_b64 exec, s[42:43], s[54:55]\n\tv_mov_b32 v12, " D0 "\n\ts_mov_b64 exec, -1\n\t"   \
          "s_andn2_b64 s[54:55], s[54:55], s[42:43]\n\ts_cbranch_scc0 9f\n\t"                     \
          PKLD(DR)
          KSTEP("1", "v8", "v9", "v[8:9]")
          KSTEP("2", "v10", "v11", "v[10:11]")
          "s_sub_u32 s41, s41, 1\n\ts_cbranch_scc0 10b\n"
          "9:\n\t"
          "v_readlane_b32 s40, v12, s49\n\tds_write_b32 %[ak], v12\n\tv_mov_b32 v13, v12\n\t"
          "s_and_b32 s45, s40, 0x7ff\n\tv_cmp_lt_u32 vcc, s40, v12\n\ts_cmp_eq_u32 s45, s48\n\ts_cselect_b64 vcc, vcc, -1\n\ts_cbranch_vccz 8f\n\t"
          MDPP("quad_perm:[1,0,3,2]") MDPP("quad_perm:[2,3,0,1]") MDPP("row_half_mirror") MDPP("row_mirror")
          "s_nop 0\n\tv_readlane_b32 s44, v13, 0\n\tv_readlane_b32 s45, v13, 16\n\tv_readlane_b32 s46, v13, 32\n\tv_readlane_b32 s47, v13, 48\n\t"
          "s_max_u32 s44, s44, s45\n\ts_max_u32 s46, s46, s47\n\ts_max_u32 s40, s44, s46\n\t"
          "v_cmp_eq_u32 vcc, s40, v12\n\tv_mov_b32 v6, 2\n\ts_ff1_i32_b64 s44, vcc\n\tv_mov_b32 v7, s44\n"
          "8:\n\tv_mov_b32 v4, s40\n\ts_mov_b64 exec, 1\n\tds_write_b128 %[ctl], v[4:7]\n\ts_mov_b64 exec, -1\n\t"
          "s_waitcnt vmcnt(0) lgkmcnt(0)"
          :: [cap] "n"(SPIN_CAP), [gap] "n"(KGAP), [go] "s"(kgo), [old] "s"(old), [vk] "v"((u32)(lane * 6144)), [kb] "s"(ws + WS_PK + fb * 128 + 8), [tag] "v"(tag),
            [ctl] "v"((u32)(CTL_OFF + 32)), [ak] "v"((u32)(KEY_OFF + lane * 4)), [pa] "s"(POST_OFF), [z] "v"(0ull)
          : "memory", "vcc", "scc", "s40", "s41", "s42", "s43", "s44", "s45", "s46", "s47", "s48", "s49", "s54", "s55", "v4", "v5", "v6", "v7", "v8", "v9", "v10",
            "v11", "v12", "v13", "v14", "v15");
      return;
    }
    float swr = dot2b(rb5, 0x3F803F80u, 0.f);
#pragma unroll
    for (int e = 0; e < 4; ++e) swr = dot2b(ra[e], 0x3F803F80u, swr);
    swr = wave_sum(swr);
    u32 ah16 = HN_OFF + (w - 1) * HSL + l16, ah4 = HN_OFF + (w - 1) * HSL + 1024 + lane * 4, l4 = lane * 4;
    asm volatile("" : "+v"(swr), "+v"(ah16), "+v"(ah4), "+v"(l4));
#define DOT3(H, W) "v_dot2c_f32_bf16 v102, 0x3f803f80, " H "\n\tv_dot2c_f32_bf16 v103, " H ", " H "\n\tv_dot2c_f32_bf16 v104, " H ", " W "\n\t"
#define ADPP(C) "s_nop 1\n\tv_add_f32_dpp v102, v102, v102 " C " row_mask:0xf bank_mask:0xf bound_ctrl:1\n\t"
#define XDPP(C) "s_nop 1\n\tv_max_u32_dpp v102, v102, v102 " C " row_mask:0xf bank_mask:0xf bound_ctrl:1\n\t"
    const char* rp = (const char*)w_r;
    u32 second = 0;
#pragma nounroll
    for (;;) {
      asm volatile(
          "s_mov_b32 s47, %[cap]\n\ts_mov_b32 s43, -1\n\ts_sub_u32 s46, %[sec], 1\n"
          "7:\n\ts_mov_b64 s[48:49], %[rp]\n\ts_mov_b32 s45, 0\n\ts_branch 4f\n"
          "18:\n\ts_sleep 1\n"
          "1:\n\ts_sub_u32 s47, s47, 1\n\ts_cbranch_scc1 8f\n\ts_cmp_lg_u32 %[sec], 0\n\ts_cbranch_scc1 7b\n\t"
          "ds_read_b128 v[108:111], %[ap]\n\ts_memrealtime s[40:41]\n\ts_waitcnt lgkmcnt(0)\n\t"
          "v_readfirstlane_b32 s45, v111\n\tv_readfirstlane_b32 s46, v109\n\tv_readfirstlane_b32 s50, v110\n\ts_lshr_b32 s41, s40, 1\n\t"
          "s_cmp_eq_u32 s45, 6\n\ts_cbranch_scc1 8f\n\ts_bitcmp0_b32 s45, 0\n\ts_cbranch_scc1 18b\n\t"
          "s_cmp_eq_u32 s46, s42\n\ts_cbranch_scc1 6f\n\ts_cmp_eq_u32 s46, s43\n\ts_cbranch_scc1 2f\n\t"
          "s_sub_u32 s41, s41, s50\n\ts_and_b32 s41, s41, 0x3ff\n\ts_cmp_lt_u32 s41, %[tq]\n\ts_cbranch_scc1 18b\n\t"
          "s_mov_b32 s43, s46\n\ts_add_u32 s44, s50, 8\n\ts_cmp_ge_u32 s41, %[pc] - 20\n\ts_cbranch_scc1 1b\n\t"
          "s_mul_i32 s48, s46, 6144\n\tv_mov_b64 v[104:105], 0\n\tv_mov_b32 v101, s48\n\tv_mad_u32_u24 v101, %[l16], 48, v101\n\t"
          "s_mov_b64 exec, 0xff\n\tglobal_atomic_or_x2 v[102:103], v101, v[104:105], %[pk] sc0 sc1\n\ts_sub_u32 s50, s50, 512\n\ts_waitcnt vmcnt(0)\n\t"
          "v_cmp_eq_u32 vcc, v103, %[tag]\n\tv_subrev_u32 v102, s50, v102\n\tv_and_b32 v102, 0x3ff, v102\n\tv_cndmask_b32 v102, 0, v102, vcc\n\t"
          XDPP("quad_perm:[1,0,3,2]") XDPP("quad_perm:[2,3,0,1]") XDPP("row_half_mirror")
          "s_mov_b64 exec, -1\n\tv_readfirstlane_b32 s44, v102\n\ts_min_u32 s44, s44, 562\n\ts_add_u32 s44, s44, s50\n\ts_branch 1b\n"
          "2:\n\ts_sub_u32 s41, s41, s44\n\ts_and_b32 s41, s41, 0x3ff\n\ts_cmp_lt_u32 s41, %[pc] - 8\n\ts_cbranch_scc1 1b\n\t"
          "s_sub_u32 s41, %[pc], s41\n\ts_lshl1_add_u32 s51, s41, s40\n\ts_mul_i32 s48, s46, 1280\n\ts_add_u32 s48, s48, %[lo]\n\ts_addc_u32 s49, %[hi], 0\n\t"
          "3:\n\ts_memrealtime s[40:41]\n\ts_waitcnt lgkmcnt(0)\n\ts_sub_u32 s40, s40, s51\n\ts_cmp_lt_i32 s40, 0\n\ts_cbranch_scc1 3b\n"
          "4:\n\tglobal_load_dwordx4 v[96:99], %[l16], s[48:49] sc1\n\tglobal_load_dword v100, %[l4], s[48:49] offset:1024 sc1\n\t"
          "v_mov_b64 v[102:103], 0\n\tv_mov_b32 v104, 0\n\ts_mov_b32 s42, -2\n\ts_waitcnt vmcnt(0)\n\t"
          "ds_read_b128 v[108:111], %[ap]\n\tds_read_b32 v105, %[ab] offset:4\n\tds_write_b128 %[a16], v[96:99]\n\tds_write_b32 %[a4], v100\n\t"
          "v_max3_u32 v101, v96, v97, v98\n\tv_max3_u32 v101, v101, v99, v100\n\tv_cmp_eq_u32 vcc, -1, v101\n\t"
          DOT3("v96", "v92") DOT3("v97", "v93")
          "s_cbranch_vccnz 1b\n\ts_mov_b32 s42, s46\n\ts_waitcnt lgkmcnt(2)\n\tv_lshl_or_b32 v109, v111, 8, v109\n\tv_lshl_or_b32 v109, v105, 16, v109\n\t"
          "s_or_b32 s45, s46, 0x20500\n\tv_cmp_eq_u32 vcc, s45, v109\n\ts_cbranch_vccz 5f\n\ts_waitcnt lgkmcnt(0)\n\ts_barrier\n"
          "5:\n\t" DOT3("v98", "v94") DOT3("v99", "v95") DOT3("v100", "%[rb]")
          "s_nop 2\n\tv_mov_b32 v105, v104\n\tv_permlane32_swap_b32 v102, v103\n\tv_add_f32 v102, v102, v103\n\tv_permlane32_swap_b32 v104, v105\n\t"
          "v_add_f32 v104, v104, v105\n\ts_nop 1\n\tv_permlane16_swap_b32 v102, v104\n\tv_add_f32 v102, v102, v104\n\t"
          ADPP("quad_perm:[1,0,3,2]") ADPP("quad_perm:[2,3,0,1]") ADPP("row_half_mirror") ADPP("row_mirror")
          "v_mul_f32 v103, 0x3acccccd, v102\n\tv_readlane_b32 s41, v102, 16\n\tv_readlane_b32 s40, v103, 32\n\t"
          "s_nop 1\n\tv_fma_f32 v102, -v103, v103, s40\n\tv_max_f32 v102, 0, v102\n\tv_cvt_pk_bf16_f32 v102, 0, v102\n\t"
          "v_add_f32 v102, 0x3727c5ac, v102\n\tv_cvt_pk_bf16_f32 v102, 0, v102\n\tv_sqrt_f32 v102, v102\n\tv_cvt_pk_bf16_f32 v105, 0, v103\n\t"
          "v_fma_f32 v105, -%[swr], v105, s41\n\tv_cvt_pk_bf16_f32 v106, v103, v102\n\tv_and_b32 v102, 0xffff0000, v106\n\tv_rcp_f32 v102, v102\n\t"
          "v_mov_b32 v104, 1\n\tv_mul_f32 v102, v102, v105\n\tv_ashrrev_i32 v103, 31, v102\n\tv_or_b32 v103, 0x80000000, v103\n\t"
          "v_xor_b32 v102, v102, v103\n\tv_and_or_b32 v107, v102, -4, %[sl]\n\ts_cbranch_vccz 1b\n\t"
          "s_mov_b64 exec, 1\n\tds_max_u64 %[ab], v[106:107] offset:8\n\tds_add_u32 %[ab], v104 offset:4\n\ts_branch 9f\n"
          "6:\n\ts_bitcmp0_b32 s45, 2\n\ts_cbranch_scc1 1b\n\ts_mov_b64 exec, 1\n\tds_max_u64 %[ab], v[106:107] offset:8\n"
          "8:\n\tv_mov_b32 v104, 1\n\ts_mov_b64 exec, 1\n\tds_add_u32 %[ab], v104 offset:4\n\ts_barrier\n"
          "9:\n\ts_mov_b64 exec, -1\n\ts_waitcnt lgkmcnt(0)\n\ts_barrier"
          :
          : [cap] "n"(SPIN_CAP * 8), [rp] "s"(rp), [sec] "s"(second), [ap] "v"((u32)(POST_OFF + (w - 1) * 16)), [tq] "n"(TQD / 2), [pc] "n"(PCD / 2),
            [pk] "s"(ws + WS_PK + (bidx - 26) * 128), [tag] "v"(tag), [lo] "s"((u32)(u64)(ws + WS_PC)), [hi] "s"((u32)((u64)(ws + WS_PC) >> 32)),
            [l16] "v"(l16), [l4] "v"(l4), [a16] "v"(ah16), [a4] "v"(ah4), "{v[92:95]}"(ra), [rb] "v"(rb5), [swr] "v"(swr), [sl] "s"(w - 1),
            [ab] "v"((u32)(CTL_OFF + 112))
          : "memory", "vcc", "scc", "s40", "s41", "s42", "s43", "s44", "s45", "s46", "s47", "s48", "s49", "s50", "s51", "v96", "v97", "v98", "v99",
            "v100", "v101", "v102", "v103", "v104", "v105", "v106", "v107", "v108", "v109", "v110", "v111");
      if (w != 1 || second) break;
      u32x4 wst;
#pragma nounroll
      for (int it = 0; it < SPIN_CAP * 8; ++it) {
        wst = lds_ld128(CTL_OFF + 32);
        if (__builtin_amdgcn_readfirstlane(wst[2])) break;
        __builtin_amdgcn_s_sleep(1);
      }
      if (__builtin_amdgcn_readfirstlane(wst[2]) != 2u) break;
      if (lane < 2) *(u32x4*)(smem + (lane ? POST_OFF : CTL_OFF + 112)) = lane ? (u32x4){0u, 0u, 0u, 5u} : (u32x4){0u, 2u, 0x3F800000u, 0u};
      rp = ws + WS_CAND + __builtin_amdgcn_readfirstlane(wst[3]) * 2560;
      second = 1;
    }
    if (w == 1) {
    } else if (w == 3 || fb >= 40) {
      const int c = w == 3 ? fb : fb + 8;
      if (c < 51) {
        const u32 now = (u32)__builtin_amdgcn_s_memrealtime();
        const u32 age = ((now >> 1) - __builtin_amdgcn_readfirstlane(*(const u32*)(smem + KEY_OFF - 256 + c * 4))) & 1023u;
        asm volatile("s_mov_b32 s42, %[cap]\n"
                     "3:\n\ts_memrealtime s[40:41]\n\ts_waitcnt lgkmcnt(0)\n\ts_sub_u32 s40, s40, %[go]\n\ts_cmp_lt_i32 s40, 0\n\ts_cbranch_scc1 3b\n"
                     "4:\n\tglobal_load_dwordx4 v[96:99], %[l16], %[cp] sc1\n\tglobal_load_dword v103, %[l4], %[cp] offset:1024 sc1\n\t"
                     "s_sub_u32 s42, s42, 8\n\ts_waitcnt vmcnt(0)\n\ts_cbranch_scc1 2f\n\t"
                     "v_max3_u32 v101, v96, v98, v103\n\tv_cmp_eq_u32 vcc, -1, v101\n\ts_cbranch_vccnz 4b\n"
                     "1:\n\tds_read_b64 v[100:101], %[af]\n\tds_read_b32 v102, %[ak]\n\ts_sub_u32 s42, s42, 1\n\ts_cbranch_scc1 2f\n\ts_waitcnt lgkmcnt(0)\n\t"
                     "v_cmp_eq_u32 vcc, 0, v100\n\ts_cbranch_vccnz 1b\n\t"
                     "v_cmp_eq_u32 vcc, %[c], v101\n\tv_bfi_b32 v102, v102, 0, %[m]\n\ts_cbranch_vccnz 2f\n\t"
                     "v_mad_u32_u24 v100, v102, %[rs], %[l16]\n\tv_mad_u32_u24 v101, v102, %[rs], %[l4]\n\t"
                     "global_store_dwordx4 v100, v[96:99], %[out]" "\n\tglobal_store_dword v101, v103, %[out] offset:1024" "\n"
                     "2:\n\ts_waitcnt lgkmcnt(0)"
                     ::[cap] "n"(SPIN_CAP * 8), [go] "s"(now + CCD - 2 * age), [cp] "s"(ws + WS_CAND + c * 2560 + 1280), [af] "v"((u32)(CTL_OFF + 40)),
                       [ak] "v"((u32)(KEY_OFF + c * 4)), [c] "s"(c), [m] "s"(2047), [rs] "s"(1280), [l16] "v"(l16), [l4] "v"(l4), [out] "s"(out)
                     : "memory", "vcc", "scc", "s40", "s41", "s42", "v96", "v97", "v98", "v99", "v100", "v101", "v102", "v103");
      }
    } else {
      const char* pp = ws + WS_PART + fb * 64;
      int pass = 0;
      asm volatile("" : "+s"(pass));
#pragma nounroll
      for (; pass < 2; ++pass) {
        const u32x2 best = *(const u32x2*)(smem + CTL_OFF + 120);
        const u32 slot = best[1] & 3u;
        const u32 wkey = __builtin_amdgcn_readfirstlane(*(const u32*)(smem + (pass ? CTL_OFF + 32 : POST_OFF + slot * 16)));
        const float rr = rbf(__uint_as_float((best[1] & ~3u) ^ ((u32)((int)~best[1] >> 31) | 0x80000000u)));
        float rs = rbf(__builtin_amdgcn_rcpf(1.0f + __expf(-rr)));
        const int ccol = (g & 1) * 2 + (g >> 1);
        float hv = bf2f(*(const u16*)(smem + HN_OFF + slot * HSL + fb * 32 + p4 * 8 + ccol * 2));
        hv = __builtin_fmaf(hv - lo_f(best[0]), __builtin_amdgcn_rcpf(hi_f(best[0])), hv);
        const char* op_ = (const char*)out + (size_t)(2047 - (int)(wkey & 2047u)) * 1280 + fb * 32;
        u32 vo_ = p4 * 8 + ccol * 2;
        asm volatile("" : "+v"(vo_), "+v"(hv), "+v"(rs));
        asm volatile("" : "+s"(op_));
        const u32 vp = (lane >> 2) * 2560 + p4 * 16;
        typedef __attribute__((ext_vector_type(16))) unsigned int u32x16;
        u32x16 keepa, keepb;
        f32x4 ys;
        u32 fin;
        asm volatile(
#define PLD(B0, B1, B2)                                                                             \
            "global_load_dwordx4 " B0 ", %[vp], %[p0] sc1\n\t"                                       \
            "global_load_dwordx4 " B1 ", %[vp], %[p1] sc1\n\tglobal_load_dwordx4 " B2 ", %[vp], %[p2] sc1\n\t"
#define PCHK(A0, A1, B0, B1, C0, C1)                                                                \
            "s_waitcnt vmcnt(3)\n\t"                                                                 \
            "v_pk_add_f32 v[96:97], " A0 ", " B0 "\n\tv_pk_add_f32 v[98:99], " A1 ", " B1 "\n\t"    \
            "v_pk_add_f32 v[96:97], v[96:97], " C0 "\n\tv_pk_add_f32 v[98:99], v[98:99], " C1 "\n\t" \
            "v_pk_add_f32 v[102:103], v[96:97], v[98:99]\n\t"                                        \
            "v_cmp_u_f32 vcc, v102, v103\n\ts_cbranch_vccz 9f\n\t"
            "s_mov_b32 s42, %[cap]\n\ts_sleep %[cdel]\n\t"
            PLD("v[64:67]", "v[68:71]", "v[72:75]")
            "s_sleep %[gap]\n\t"
            PLD("v[80:83]", "v[84:87]", "v[88:91]")
            "1:\n\t"
            PCHK("v[64:65]", "v[66:67]", "v[68:69]", "v[70:71]", "v[72:73]", "v[74:75]")
            PLD("v[64:67]", "v[68:71]", "v[72:75]")
            PCHK("v[80:81]", "v[82:83]", "v[84:85]", "v[86:87]", "v[88:89]", "v[90:91]")
            PLD("v[80:83]", "v[84:87]", "v[88:91]")
            "s_sub_u32 s42, s42, 1\n\ts_cbranch_scc0 1b\n"
            "9:\n\tds_read_b32 %[fin], %[af]"
            : "=&{v[96:99]}"(ys), "=&{v[64:79]}"(keepa), "=&{v[80:95]}"(keepb), [fin] "=&v"(fin)
            : [cap] "n"(SPIN_CAP), [gap] "n"(CGAP), [cdel] "n"(CDEL), [vp] "v"(vp), [p0] "s"(pp),
              [p1] "s"(pp + 40960), [p2] "s"(pp + 81920), [af] "v"((u32)(CTL_OFF + 40))
            : "memory", "vcc", "scc", "s42", "v102", "v103");
        float y = tr_sum(ys);
        y += dppf<0x124>(y);
        y += dppf<0x128>(y);
        const u32 res = cvt2((f32x2){__builtin_fmaf(y, rs, hv), 0.f});
        asm volatile("s_mov_b32 s42, %[cap]\n"
                     "1:\n\ts_waitcnt lgkmcnt(0)\n\tv_cmp_eq_u32 vcc, %[ok], %[fin]\n\ts_cbranch_vccz 2f\n\t"
                     "s_mov_b32 exec_lo, 0x000f000f\n\ts_mov_b32 exec_hi, 0x000f000f\n\tglobal_store_short %[vo], %[res], %[op]" "\n\ts_mov_b64 exec, -1\n"
                     "2:\n\tv_cmp_eq_u32 vcc, 0, %[fin]\n\ts_cbranch_vccz 3f\n\tds_read_b32 %[fin], %[af]\n\ts_sub_u32 s42, s42, 1\n\ts_cbranch_scc0 1b\n"
                     "3:\n\ts_waitcnt lgkmcnt(0)\n\tv_cmp_lt_u32 vcc, %[ok], %[fin]\n\ts_cbranch_vccz 4f\n\ts_waitcnt vmcnt(0)\n4:"
                     : [fin] "+v"(fin) : [cap] "n"(SPIN_CAP * 8), [ok] "s"(pass + 1), [vo] "v"(vo_), [res] "v"(res), [op] "s"(op_),
                       [af] "v"((u32)(CTL_OFF + 40)), "{v[64:79]}"(keepa), "{v[80:95]}"(keepb) : "memory", "vcc", "scc", "s42");
        if (pass || __builtin_amdgcn_readfirstlane(fin) != 2u) break;
        asm volatile("s_barrier\n\ts_barrier" ::: "memory");
        pp += WS_PART2 - WS_PART;
      }
    }
    return;
  }

  const int idx2 = (xcd & 1) * 26 + bidx;
  const int hg = ((xcd >> 1) & 1) * 4 + (idx2 & 3);
  const int mt = (xcd >> 2) * 13 + (idx2 >> 2);
#define ROTF (((idx2 >> 2) * 3 / 2 + (idx2 & 3) * 3) % 10)
  const int kb0 = ROTF;
  const int r0 = mt * 80;
  if (w >= 4) {
    const int cw = w - 4;
    u32 vo = w_off(cw, lane, hg), vx = x_off(cw, lane, r0), vx2 = x_off(8 + (cw & 3), lane, r0);
    asm volatile("" : "+v"(vo), "+v"(vx), "+v"(vx2));
    __builtin_amdgcn_sched_barrier(0);
    const char* wb = (const char*)wqkv + (size_t)kb0 * 245760 + W_ROW(cw);
    dma_m0<0>(vo, wb, cw * 1024);
    dma_m0<0>(vx, (const char*)x + kb0 * 128, XRING + cw * 1024);
    dma_m0<0>(vo, wb + 61440, 8192 + cw * 1024);
    if (cw < 4) dma_m0<0>(vx2, (const char*)x + kb0 * 128, XRING + 8192 + cw * 1024);
    asm volatile("s_barrier" ::: "memory");
  }
  const int rt = w >= 4 ? (w & 3) : 4, kh = w >= 4 ? (w - 4) >> 2 : (w & 1);
  float bias[2];
  u32 sel[4], e4, eh, aq, aka, akb, pm, pf, hi, arec, vpp;
  const char *os, *os4, *sst, *oa[10];
  int so_[10];
  int lane_ = lane, mt_ = mt, hg_ = hg;
  u32 tgo = 0;
  auto prepv = [&](const int lane) __attribute__((always_inline)) {
    const int g = lane >> 4, r16 = lane & 15, q4 = (lane & 15) >> 2, p4 = lane & 3;
    const int par = lane & 1;
    const bool ca = r16 >= 4 * g, cb = r16 >= 4 * g + 2;
    bias[0] = (ca || r0 + rt > 0) ? 0.f : -__builtin_inff();
    bias[1] = (cb || r0 + rt > 0) ? 0.f : -__builtin_inff();
    const u32 sa = par ? 0x01000C0Cu : 0x0C0C0100u, sb = par ? 0x03020C0Cu : 0x0C0C0302u;
    sel[0] = ca ? 0x0C0C0C0Cu : sa;
    sel[1] = cb ? 0x0C0C0C0Cu : sb;
    sel[2] = ca ? sa : 0x0C0C0C0Cu;
    sel[3] = cb ? sb : 0x0C0C0C0Cu;
    e4 = EXO + rt * 512 + lane * 8;
    eh = e4 + kh * 6144;
    aq = TAB_OFF + 384 + (rt * 16 + r16) * 4;
    aka = TAB_OFF + 384 + (rt * 16 + 4 * g + par + (ca ? 16 : 0)) * 4;
    akb = TAB_OFF + 384 + (rt * 16 + 4 * g + par + 2 + (cb ? 16 : 0)) * 4;
    pm = (4 * g + q4) * 160 + p4 * 40;
    pf = (20 * g + 16 + q4) * 160 + p4 * 40;
    hi = HIM + (rt * 16 + r16) * 160 + g * 40;
    arec = REC_OFF + (r16 == 0 ? kh * 192 + g * 48 : (r16 == 8 ? ((g >> 1) == kh ? 480 : 384) : 432));
    vpp = lane + kh * 64 + (rt == 0 ? 768 : (rt == 2 ? 8192 : (rt == 4 ? 512 : 640)));
    asm volatile("" : "+v"(arec), "+v"(vpp));
    asm volatile("" : "+v"(bias[0]), "+v"(bias[1]), "+v"(sel[0]), "+v"(sel[1]), "+v"(sel[2]), "+v"(sel[3]), "+v"(e4), "+v"(eh),
                 "+v"(aq), "+v"(aka), "+v"(akb), "+v"(pm), "+v"(pf), "+v"(hi));
  };
  auto preps = [&](const int mt, const int hg) __attribute__((always_inline)) {
    const char* ob = ws + WS_O + (size_t)mt * 102400;
    os = ob + rt * 20480 + (2 * hg + kh) * 1024;
    os4 = ob + rt * 20480 + (16 + (hg >> 1)) * 1024 + (hg & 1) * 8;
    sst = ws + WS_STAT + (size_t)(2 * mt + (rt < 3 ? 0 : 1)) * 8192 + hg * 1024 + (rt < 3 ? rt : rt - 3) * 256;
    asm volatile("" : "+s"(os), "+s"(os4), "+s"(sst));
    const int k2 = w & 1;
#pragma unroll
    for (int j = 0; j < 10; ++j) {
      const bool oth = j >= 5 || w < 2;
      const int mo[8] = {OORD};
      const int ch = j % 5 < 4 ? (2 * hg + k2) ^ (oth ? 2 * mo[4 + j % 5] : 2 * mo[j % 5]) : (hg >> 1) ^ ((oth != ((hg & 1) != 0)) ? (k2 ? 0 : 3) : 1 + k2);
      oa[j] = ob + (j % 5 < 4 ? ch : 16 + ch) * 1024;
      so_[j] = ch * (j % 5 < 4 ? 6400 : 25600);
      asm volatile("" : "+s"(oa[j]), "+s"(so_[j]));
    }
  };
#define WO_OFFS                                                                                    \
  u32 woff[5];                                                                                     \
  _Pragma("unroll") for (int i = 0; i < 5; ++i) {                                                  \
    const int c = i * 64 + lane;                                                                   \
    const int row = c / 10;                                                                        \
    woff[i] = row * 1120 + hg * 160 + c * 16 + (4 - i) * 1024;                                     \
  }
#define WO_RUN(RHO)                                                                                \
  {                                                                                                \
    const char* wq_ = (const char*)wo + (RHO) * 40960; \
    dma_m0<-4096>(woff[0], wq_, (RHO) * 5120 + 4096);                                               \
    dma_nx<-3072>(woff[1], wq_); dma_nx<-2048>(woff[2], wq_); dma_nx<-1024>(woff[3], wq_); dma_nx<0>(woff[4], wq_); \
  }

#define BAR asm volatile("s_barrier" ::: "memory");
#define PREP_V __builtin_amdgcn_sched_barrier(0); asm volatile("" : "+v"(lane_)); prepv(lane_); __builtin_amdgcn_sched_barrier(0);
#define PREP_S __builtin_amdgcn_sched_barrier(0); asm volatile("" : "+s"(mt_), "+s"(hg_)); preps(mt_, hg_); __builtin_amdgcn_sched_barrier(0);
  if (w < 2) {
    const int l = w;
    __builtin_amdgcn_s_setprio(3);
    u32 vw[4];
#pragma unroll
    for (int i = 0; i < 4; ++i) vw[i] = w_off(i, lane, hg) + 4096;
    u32 go = (u32)kb0 * 245760u + 122880u + l * 61440u;
    u32 so = HSLOT + l * 8192 + 4096;
#define W1(I) dma_nx<((I) - 4) * 1024>(vw[(I) & 3], wb + (W_ROW(I) - (I) * 1024));
#define ISSUE_W                                                                                    \
  {                                                                                                \
    const char* wb = (const char*)wqkv + go;                                                       \
    dma_m0<-4096>(vw[0], wb, so);                                                                  \
    W1(1) W1(2) W1(3) W1(4) W1(5) W1(6) W1(7)                                                      \
    go += 122880u;                                                                                 \
    if (go >= 2457600u) go -= 2457600u;                                                            \
    so = so >= 5 * HSLOT ? so - 5 * HSLOT : so + HSLOT;                                            \
  }
    asm volatile("" : "+v"(vw[0]), "+v"(vw[1]), "+v"(vw[2]), "+v"(vw[3]), "+s"(go));
    BAR
    ISSUE_W
    __builtin_amdgcn_sched_barrier(0);
    WO_OFFS
    asm volatile("" : "+v"(woff[0]), "+v"(woff[1]), "+v"(woff[2]), "+v"(woff[3]), "+v"(woff[4]));
    BAR
    ISSUE_W
    asm volatile("s_waitcnt vmcnt(8)\n\ts_barrier" ::: "memory");
#pragma unroll 1
    for (int h = 0; h < 17; ++h) {
      ISSUE_W
      asm volatile("s_waitcnt vmcnt(8)\n\ts_barrier" ::: "memory");
    }
    asm volatile("s_waitcnt vmcnt(0)\n\ts_barrier" ::: "memory");
    WO_RUN(12 + l)
    const u32 tagw = lds_ld32(CTL_OFF);
    const char* bxf = ws + WS_BXF + mt * 1024;
    BAR
    const u32 town = (u32)__builtin_amdgcn_s_memrealtime();
    tgo = town;
    if (l == 0) {
      if (lane == 0) GST2((u32)(hg * 128), bxf, 0, ((u32x2){town, tagw}));
    }
    WO_RUN(18 + l)
    WO_RUN(8 + l)
    WO_RUN(l)
    const u32 vfl = (((lane & 7) == hg ? hg ^ 1 : (lane & 7))) * 128;
    u32x2 f;
    asm volatile("" : "=v"(f));
    BAR
    int gate = (int)((u32)__builtin_amdgcn_s_memrealtime() - town) + GADD;
    gate = gate > 240 ? 240 : gate;
    WO_RUN(2 + l)
    WO_RUN(4 + l)
    WO_RUN(6 + l)
    if (l == 1) asm volatile("global_load_dwordx2 %0, %1, %2 sc1" : "+v"(f) : "v"(vfl), "s"(bxf) : "memory");
    __builtin_amdgcn_s_setprio(0);
    pm = (4 * g + q4) * 160 + p4 * 40;
    pf = (20 * g + 16 + q4) * 160 + p4 * 40;
    asm volatile("" : "=v"(bias[0]), "=v"(bias[1]), "=v"(sel[0]), "=v"(sel[1]), "=v"(sel[2]), "=v"(sel[3]), "=v"(e4), "=v"(eh),
                 "=v"(aq), "=v"(aka), "=v"(akb), "+v"(pm), "+v"(pf), "=v"(hi), "=v"(arec), "=v"(vpp));
    PREP_S
    if (l == 1) {
      asm volatile("s_waitcnt vmcnt(0)" : "+v"(f)::"memory");
#pragma nounroll
      for (int it = 0; it < SPIN_CAP; ++it) {
        if (__all(f[1] == tagw)) break;
        asm volatile("global_load_dwordx2 %0, %1, %2 sc1\n\ts_waitcnt vmcnt(0)" : "=&v"(f) : "v"(vfl), "s"(bxf) : "memory");
      }
      u32 d = min(max((int)(f[0] - town), 0), 200);
      d = max(d, dppu<0xB1>(d)); d = max(d, dppu<0x4E>(d)); d = max(d, dppu<0x141>(d));
      tgo = town + gate + __builtin_amdgcn_readfirstlane(d);
    }
    __builtin_amdgcn_s_setprio(0);
  } else if (w < 4) {
    __builtin_amdgcn_s_setprio(3);
    if (w == 2) {
      u32 vx[12];
#pragma unroll
      for (int i = 0; i < 12; ++i) vx[i] = x_off(i, lane, r0) + (4 - (i % 6)) * 1024;
      int kb = kb0 == 9 ? 0 : kb0 + 1;
      u32 xo = XRING + XSLOT + 4096;
#define ISSUE_X0                                                                                   \
  {                                                                                                \
    const char* xb = (const char*)x + kb * 128;                                                    \
    dma_m0<-4096>(vx[0], xb, xo);                                                                  \
    dma_nx<-3072>(vx[1], xb); dma_nx<-2048>(vx[2], xb); dma_nx<-1024>(vx[3], xb); dma_nx<0>(vx[4], xb); dma_nx<1024>(vx[5], xb); \
  }
#define ISSUE_X1                                                                                   \
  {                                                                                                \
    const char* xb = (const char*)x + kb * 128;                                                    \
    dma_m0<-4096>(vx[6], xb, xo + 6144);                                                           \
    dma_nx<-3072>(vx[7], xb); dma_nx<-2048>(vx[8], xb); dma_nx<-1024>(vx[9], xb); dma_nx<0>(vx[10], xb); dma_nx<1024>(vx[11], xb); \
    kb = kb == 9 ? 0 : kb + 1;                                                                     \
    xo = xo >= XRING + 3 * XSLOT ? xo - 3 * XSLOT : xo + XSLOT;                                    \
  }
      asm volatile("" : "+v"(vx[0]), "+v"(vx[1]), "+v"(vx[2]), "+v"(vx[3]), "+v"(vx[4]), "+v"(vx[5]));
      BAR
      __builtin_amdgcn_s_sleep(4);
      ISSUE_X0
      __builtin_amdgcn_sched_barrier(0);
      WO_OFFS
      asm volatile("" : "+v"(woff[0]), "+v"(woff[1]), "+v"(woff[2]), "+v"(woff[3]), "+v"(woff[4]));
      BAR
      ISSUE_X1
      ISSUE_X0
      BAR
      ISSUE_X1
      asm volatile("s_waitcnt vmcnt(12)\n\ts_barrier" ::: "memory");
#pragma unroll 1
      for (int j = 3; j < 10; ++j) {
        ISSUE_X0
        BAR
        ISSUE_X1
        asm volatile("s_waitcnt vmcnt(12)\n\ts_barrier" ::: "memory");
      }
      const char* xq = (const char*)x + (size_t)r0 * 1280;
      const int nv = mt == 25 ? 480 : 800;
#define RES1(I) if ((I) < 7 || (I) * 64 + lane < nv) dma_m0<(((I) % 5) - 4) * 1024>(woff[(I) % 5], xq + ((I) / 5) * 40960, HIM + ((I) / 5) * 5120 + 4096);
      PREP_V
      BAR
      PREP_S
      asm volatile("s_waitcnt vmcnt(0)\n\ts_barrier" ::: "memory");
      RES1(0) RES1(1) RES1(2) RES1(3) RES1(4) RES1(5) RES1(6)
      BAR
      RES1(7) RES1(8) RES1(9) RES1(10) RES1(11) RES1(12)
      if (lane < 10) dma_m0<0>(l16, (const char*)w_r + hg * 160, WR_OFF);
      BAR
    } else {
      u32x2 ep;
      u32x4 ra;
      u32 rb5;
      BAR
      if (lane < 8) {
        *(u32x4*)(smem + ONES_OFF + lane * 16) = (u32x4){0x3F803F80u, 0x3F803F80u, 0x3F803F80u, 0x3F803F80u};
        *(u32x4*)(smem + CTL_OFF + lane * 16) = (u32x4){0u, 0u, 0u, 0u};
        *(u32x4*)(smem + ZERO_OFF + lane * 16) = (u32x4){0u, 0u, 0u, 0u};
      }
      {
        const u32 o1 = lane == 26 || lane == 32 ? 0x3F803F80u : 0u;
        const u32 o2 = lane == 31 ? 0x3F803F80u : o1;
        if (lane < 33) *(u32x4*)(smem + REC_OFF + lane * 16) = (u32x4){o1, o1, o2, o2};
      }
      WO_OFFS
      asm volatile("" : "+v"(woff[0]), "+v"(woff[1]), "+v"(woff[2]), "+v"(woff[3]), "+v"(woff[4]));
      __builtin_amdgcn_s_sleep(TWS);
      if (lane < 5)
        touch_line(lane == 4 ? (const char*)wo + hg * 160
                             : (lane >= 2 ? ws + WS_PK - 65536 + (lane - 2) * 524288 + bid * 128
                                          : (const char*)out + (size_t)(r0 + lane * 47) * 1280 + hg * 160));
      LDS_BARRIER();
      BAR BAR
      asm volatile("global_load_dwordx2 %0, %1, off sc1" : "=v"(ep) : "v"(ws + WS_EPOCH) : "memory");
      asm volatile("global_load_dwordx4 %0, %2, %4\n\tglobal_load_dword %1, %3, %4 offset:1024"
                   : "=&v"(ra), "=&v"(rb5) : "v"(l16), "v"((u32)(lane * 4)), "s"(w_r) : "memory");
      BAR BAR BAR
      PREP_V
      BAR
      PREP_S
      BAR
      asm volatile("s_waitcnt vmcnt(0)" : "+v"(ep), "+v"(ra), "+v"(rb5)::"memory");
      const u64 e1 = (((u64)ep[1] << 32) | ep[0]) + (ep[0] == SENT - 1u ? 2ull : 1ull);
      float swr = lo_f(rb5) + hi_f(rb5);
#pragma unroll
      for (int e = 0; e < 4; ++e) swr += lo_f(ra[e]) + hi_f(ra[e]);
      swr = wave_sum(swr);
      if (lane == 0) {
        *(u32x2*)(smem + CTL_OFF) = (u32x2){(u32)e1, (u32)(e1 >> 32)};
        *(float*)(smem + CTL_OFF + 40) = swr;
      }
      int ra0 = r0 - 16 + lane, ra1 = r0 + 48 + (lane & 31);
      ra0 = ra0 < 0 ? 0 : (ra0 > SEQ - 1 ? SEQ - 1 : ra0);
      ra1 = ra1 > SEQ - 1 ? SEQ - 1 : ra1;
      const int hd = lane / 5, pt = lane - hd * 5;
      u32 avp = REC_OFF + (hd >> 1) * 192 + (pt < 4 ? pt * 48 + (hd & 1) * 8 : hd * 48 + 16), vvp = lane < 20 ? (lane >> 1) * 128 + (lane & 1) * 16 : 0;
      u32 vf = (lane < 48 ? lane : 0) * 128, va0 = ra0 * 8, va1 = ra1 * 8, vt0 = TAB_OFF + lane * 4, vt1 = TAB_OFF + 256 + (lane & 31) * 4;
      asm volatile("" : "+v"(avp), "+v"(vvp), "+v"(vf), "+v"(va0), "+v"(va1), "+v"(vt0), "+v"(vt1));
#pragma nounroll
      for (int h = 7; h <= HK0; ++h) BAR
      u32x2 f, t0, t1;
      u32x4 vp4;
      GLD2(f, vf, ws + WS_HFLAG, 0);
      GLD2(t0, va0, ws + WS_LN1, 0);
      GLD2(t1, va1, ws + WS_LN1, 0);
      asm volatile("global_load_dwordx4 %0, %1, %2 sc1" : "=v"(vp4) : "v"(vvp), "s"(ws + WS_VP + hg * 1280) : "memory");
#pragma unroll
      for (int h = HK0 + 1; h <= 17; ++h) BAR
      WO_RUN(10)
      WO_RUN(11)
      BAR
      asm volatile("s_waitcnt vmcnt(10)" : "+v"(t0), "+v"(t1), "+v"(f), "+v"(vp4)::"memory");
#pragma nounroll
      for (int it = 0; it < SPIN_CAP; ++it) {
        if (__all(t0[1] == (u32)e1 && t1[1] == (u32)e1 && f[0] == (u32)e1 && f[1] == (u32)(e1 >> 32) && vp4[1] == (u32)e1 && vp4[3] == (u32)e1)) break;
        asm volatile("global_load_dwordx2 %2, %6, %9 sc1\n\tglobal_load_dwordx2 %0, %4, %8 sc1\n\tglobal_load_dwordx2 %1, %5, %8 sc1\n\t"
                     "global_load_dwordx4 %3, %7, %10 sc1\n\ts_waitcnt vmcnt(0)"
                     : "=&v"(t0), "=&v"(t1), "=&v"(f), "=&v"(vp4)
                     : "v"(va0), "v"(va1), "v"(vf), "v"(vvp), "s"(ws + WS_LN1), "s"(ws + WS_HFLAG), "s"(ws + WS_VP + hg * 1280) : "memory");
      }
      *(u32*)(smem + vt0) = t0[0] << 16;
      *(u32*)(smem + vt1) = t1[0] << 16;
      *(float*)(smem + vt0 + 384) = __builtin_amdgcn_rcpf(hi_f(t0[0]));
      *(float*)(smem + vt1 + 384) = __builtin_amdgcn_rcpf(hi_f(t1[0]));
      if (lane < 20) *(u32x2*)(smem + avp) = (u32x2){vp4[0], vp4[2]};
      asm volatile("" ::: "memory");
      WO_RUN(14)
      WO_RUN(15)
      LDS_BARRIER();
      WO_RUN(16)
      WO_RUN(17)
    }
    __builtin_amdgcn_s_setprio(0);
  } else {
    const int cw = w - 4;
    prepv(lane);
    preps(mt, hg);
    if (cw >= 4) __builtin_amdgcn_s_setprio(1);
    const int xa0 = XRING + r16 * 128 + ((g ^ ((r16 >> 1) & 7)) * 16);
    const bool zr = cw < 2;
    const u32 m1 = r16 == 0 || zr ? 0u : 1u;
    const u32 xz0 = r16 == 0 ? (u32)ONES_OFF + g * 16 : (zr ? (u32)ZERO_OFF : (u32)xa0);
    int wa[2];
#pragma unroll
    for (int c = 0; c < 2; ++c) {
      const int ct = cw < 7 ? 2 * cw + c : 14;
      const int seg = ct / 5, tt = ct - seg * 5;
      const int G = tt < 4 ? 5 * tt + p4 : 5 * p4 + 4;
      wa[c] = (16 * (g >> 1) + 4 * (g & 1) + q4) * 512 + ((seg * 160 + G * 8) ^ ((4 * (g & 1) + q4) << 5));
    }
    auto run = [&](auto tyc) __attribute__((always_inline)) {
      constexpr int TY = decltype(tyc)::value;
      constexpr int NW = TY == 2 ? 1 : 2;
      f32x4 acc[2][6];
#pragma unroll
      for (int c = 0; c < 2; ++c)
#pragma unroll
        for (int r = 0; r < 6; ++r) acc[c][r] = (f32x4){0.f, 0.f, 0.f, 0.f};
      u32x4 xA[6], xB[6];
      u32x2 wA[4], wB[4];
#define RX(D, A, O) asm volatile("ds_read_b128 %0, %1 offset:%2" : "=v"(D) : "v"(A), "n"(O) : "memory");
#define RW(D, A, O) asm volatile("ds_read_b64_tr_b16 %0, %1 offset:%2" : "=v"(D) : "v"(A), "n"(O) : "memory");
#define MM(C, R, XF, WF)                                                                           \
  if ((C) < NW) {                                                                                    \
    const bf16x8 wf_ = mk8(WF[2 * (C)][0], WF[2 * (C)][1], WF[2 * (C) + 1][0], WF[2 * (C) + 1][1]); \
    const bf16x8 xf_ = __builtin_bit_cast(bf16x8, XF[R]);                                          \
    acc[C][R] = TY == 0 ? MFMA(wf_, xf_, acc[C][R]) : MFMA(xf_, wf_, acc[C][R]);                   \
    __builtin_amdgcn_sched_barrier(0);                                                             \
  }
#define READS(QX, QW)                                                                              \
  RW(QW[0], aw0, 0) RW(QW[1], aw0, 4096) RX(QX[1], ax, 2048)                                       \
  if (NW == 2) { RW(QW[2], aw1, 0) RW(QW[3], aw1, 4096) }                                          \
  RX(QX[2], ax, 4096) RX(QX[3], ax, 6144) RX(QX[4], ax, 8192) RX(QX[5], ax, 10240) RX(QX[0], az, 0)
#define ARRIVED(PX, PW)                                                                            \
  if (NW == 2)                                                                                     \
    asm volatile("s_waitcnt lgkmcnt(0)" : "+v"(PX[0]), "+v"(PX[1]), "+v"(PX[2]), "+v"(PX[3]), "+v"(PX[4]), "+v"(PX[5]), \
                 "+v"(PW[0]), "+v"(PW[1]), "+v"(PW[2]), "+v"(PW[3])::"memory");                    \
  else                                                                                             \
    asm volatile("s_waitcnt lgkmcnt(0)" : "+v"(PX[0]), "+v"(PX[1]), "+v"(PX[2]), "+v"(PX[3]), "+v"(PX[4]), "+v"(PX[5]), \
                 "+v"(PW[0]), "+v"(PW[1])::"memory");                                              \
  __builtin_amdgcn_sched_barrier(0);
#define SB __builtin_amdgcn_sched_barrier(0);
#define WT(N, ...) asm volatile("s_waitcnt lgkmcnt(" #N ")" : __VA_ARGS__::"memory"); SB
#define HSTAGE2(PX, PW, QX, QW, NX)                                                                \
  BAR                                                                                              \
  WT(7, "+v"(PW[0]), "+v"(PW[1]), "+v"(PX[1]))                                                     \
  MM(0, 1, PX, PW) RW(QW[0], aw0, 0)                                                               \
  WT(6, "+v"(PW[2]), "+v"(PW[3]))                                                                  \
  MM(1, 1, PX, PW) RW(QW[1], aw0, 4096)                                                            \
  WT(6, "+v"(PX[2]))                                                                               \
  MM(0, 2, PX, PW) RX(QX[1], ax, 2048) SB                                                          \
  MM(1, 2, PX, PW) RW(QW[2], aw1, 0)                                                               \
  WT(7, "+v"(PX[3]))                                                                               \
  MM(0, 3, PX, PW) RW(QW[3], aw1, 4096) SB                                                         \
  MM(1, 3, PX, PW) RX(QX[2], ax, 4096)                                                             \
  WT(6, "+v"(PX[4]), "+v"(PX[5]), "+v"(PX[0]))                                                     \
  MM(0, 4, PX, PW) RX(QX[3], ax, 6144) SB                                                          \
  MM(1, 4, PX, PW) RX(QX[4], ax, 8192) SB                                                          \
  MM(0, 5, PX, PW) RX(QX[5], ax, 10240) SB                                                         \
  MM(1, 5, PX, PW) RX(QX[0], az, 0) SB                                                             \
  MM(0, 0, PX, PW)                                                                                 \
  NX asm volatile("" : "+v"(aw0), "+v"(aw1), "+v"(ax), "+v"(az), "+s"(so), "+s"(xo)); SB           \
  MM(1, 0, PX, PW)
#define HSTAGE1(PX, PW, QX, QW, NX)                                                                \
  BAR                                                                                              \
  ARRIVED(PX, PW)                                                                                  \
  MM(0, 1, PX, PW) RW(QW[0], aw0, 0) RW(QW[1], aw0, 4096) SB                                       \
  MM(0, 2, PX, PW) RX(QX[1], ax, 2048) RX(QX[2], ax, 4096) SB                                      \
  MM(0, 3, PX, PW) RX(QX[3], ax, 6144) RX(QX[4], ax, 8192) SB                                      \
  MM(0, 4, PX, PW) RX(QX[5], ax, 10240) RX(QX[0], az, 0) SB                                        \
  MM(0, 5, PX, PW)                                                                                 \
  NX asm volatile("" : "+v"(aw0), "+v"(ax), "+v"(az), "+s"(so), "+s"(xo)); SB                      \
  MM(0, 0, PX, PW)
#define HSTAGE(...) if (NW == 2) { HSTAGE2(__VA_ARGS__) } else { HSTAGE1(__VA_ARGS__) }
      u32 aw0 = wa[0], aw1 = wa[1], ax = xa0, az = xz0;
      asm volatile("s_waitcnt vmcnt(0)\n\ts_barrier" ::: "memory");
      READS(xA, wA)
      aw0 += HSLOT; aw1 += HSLOT; ax ^= 64; az ^= 64;
      int so = 2 * HSLOT, xo = XSLOT;
#pragma unroll 1
      for (int s = 0; s < 9; ++s) {
        HSTAGE(xA, wA, xB, wB, aw0 = wa[0] + so; aw1 = wa[1] + so; ax = xa0 + xo; az = xz0 + xo * m1;
               so = so >= 4 * HSLOT ? 0 : so + 2 * HSLOT; xo = xo >= 3 * XSLOT ? 0 : xo + XSLOT;)
        HSTAGE(xB, wB, xA, wA, aw0 += HSLOT; aw1 += HSLOT; ax ^= 64; az ^= 64;)
      }
      {
        HSTAGE(xA, wA, xB, wB, )
        ARRIVED(xB, wB)
        BAR
        SB
      }
      char* ex = smem + EXO + cw * 6144 + lane * 8;
      if (TY == 0) {
        float st[6];
        const u32 ta = TAB_OFF + r16 * 4;
#define RT(R) asm volatile("ds_read_b32 %0, %1 offset:%2" : "=v"(st[R]) : "v"(ta), "n"(64 * (R)) : "memory");
        RT(1) RT(2) RT(3) RT(4) RT(5) RT(0)
        float cs[2][4];
#define FMS(A, M, CS) ((A) - (M) * (CS))
#define EP0(C, R)                                                                                  \
  {                                                                                                \
    const u32 a = cvt2((f32x2){FMS(acc[C][R][0], st[R], cs[C][0]), FMS(acc[C][R][1], st[R], cs[C][1])}); \
    const u32 b = cvt2((f32x2){FMS(acc[C][R][2], st[R], cs[C][2]), FMS(acc[C][R][3], st[R], cs[C][3])}); \
    *(u32x2*)(ex + (C) * 3072 + (R) * 512) = (u32x2){a, b};                                        \
    SB                                                                                             \
  }
        MM(0, 0, xB, wB) MM(1, 0, xB, wB) MM(0, 1, xB, wB) MM(1, 1, xB, wB)
#pragma unroll
        for (int i = 0; i < 4; ++i) cs[0][i] = dppf<0x150>(acc[0][0][i]);
        SB
        MM(0, 2, xB, wB)
#pragma unroll
        for (int i = 0; i < 4; ++i) cs[1][i] = dppf<0x150>(acc[1][0][i]);
        WT(0, "+v"(st[0]), "+v"(st[1]), "+v"(st[2]), "+v"(st[3]), "+v"(st[4]), "+v"(st[5]))
        MM(1, 2, xB, wB) EP0(0, 1)
        MM(0, 3, xB, wB) EP0(1, 1)
        MM(1, 3, xB, wB) EP0(0, 2)
        MM(0, 4, xB, wB) EP0(1, 2)
        MM(1, 4, xB, wB) EP0(0, 3)
        MM(0, 5, xB, wB) EP0(1, 3)
        MM(1, 5, xB, wB)
        if (cw >= 2) {
          EP0(1, 0)
          if (cw >= 3) EP0(0, 0)
        }
        EP0(0, 4) EP0(1, 4) EP0(0, 5) EP0(1, 5)
      } else {
        f32x4 mbv[6];
        const u32 ta = TAB_OFF + g * 16;
#define RV(R) RX(mbv[R], ta, 64 * (R))
        RV(1) RV(2) RV(3) RV(4) RV(5) RV(0)
        float cs[2];
#define CS1(C)                                                                                     \
  if ((C) < NW) {                                                                                  \
    const u32 v0 = __float_as_uint(acc[C][0][0]);                                                  \
    const u32 v1 = __builtin_amdgcn_permlane16_swap(v0, v0, false, false)[0];                      \
    cs[C] = __uint_as_float(__builtin_amdgcn_permlane32_swap(v1, v1, false, false)[0]);            \
    SB                                                                                             \
  }
#define EP1(C, R)                                                                                  \
  if ((C) < NW) {                                                                                  \
    const f32x4 v = (f32x4){FMS(acc[C][R][0], mbv[R][0], cs[C]), FMS(acc[C][R][1], mbv[R][1], cs[C]), FMS(acc[C][R][2], mbv[R][2], cs[C]), FMS(acc[C][R][3], mbv[R][3], cs[C])}; \
    *(u32x2*)(ex + (C) * 3072 + (R) * 512) = (u32x2){cvt2((f32x2){v[0], v[1]}), cvt2((f32x2){v[2], v[3]})}; \
    SB                                                                                             \
  }
        MM(0, 0, xB, wB) MM(1, 0, xB, wB) MM(0, 1, xB, wB) MM(1, 1, xB, wB)
        CS1(0)
        MM(0, 2, xB, wB)
        CS1(1)
        WT(0, "+v"(mbv[0]), "+v"(mbv[1]), "+v"(mbv[2]), "+v"(mbv[3]), "+v"(mbv[4]), "+v"(mbv[5]))
        MM(1, 2, xB, wB) EP1(0, 1)
        MM(0, 3, xB, wB) EP1(1, 1)
        MM(1, 3, xB, wB) EP1(0, 2)
        MM(0, 4, xB, wB) EP1(1, 2)
        MM(1, 4, xB, wB) EP1(0, 3)
        MM(0, 5, xB, wB) EP1(1, 3)
        MM(1, 5, xB, wB)
        EP1(0, 0) EP1(1, 0) EP1(0, 4) EP1(1, 4) EP1(0, 5) EP1(1, 5)
      }
    };
    if (cw < 5) run(icst<0>{});
    else if (cw < 7) run(icst<1>{});
    else run(icst<2>{});
  }
  __builtin_amdgcn_s_setprio(0);
  const bool live = r0 + rt * 16 < SEQ;
  if (!live) {
    if (w >= 2) LDS_BARRIER();
    if (w != 7) {
      asm volatile("s_waitcnt vmcnt(0)" ::: "memory");
      return;
    }
  } else if (w >= 2)
    asm volatile(ATT_ASM
                 :: [kh] "s"(kh), [l16] "v"(l16), [os] "s"(os), [os4] "s"(os4), [b0] "v"(bias[0]), [b1] "v"(bias[1]),
                    [s0] "v"(sel[0]), [s1] "v"(sel[1]), [s2] "v"(sel[2]), [s3] "v"(sel[3]), [e4] "v"(e4), [eh] "v"(eh), [aq] "v"(aq),
                    [aka] "v"(aka), [akb] "v"(akb), [arec] "v"(arec), [vpp] "v"(vpp), [sst] "s"(sst), [w] "s"(w)
                 : "memory", "s41", "s46", "s47", "s48", "s49", "s50", "s51", "scc", "v24", "v25", "v26", "v27", "v28", "v29", "v30", "v31", "v32", "v33", "v34", "v35", "v36", "v37", "v38", "v39", "v40", "v41", "v42", "v43", "v44", "v45", "v46", "v47", "v48", "v49", "v50", "v51", "v52", "v53", "v54", "v55", "v56", "v57", "v58", "v59", "v60", "v61", "v62", "v63", "v64", "v65", "v66", "v67", "v68", "v69", "v70", "v71", "v72", "v73", "v74", "v75", "v76", "v77", "v78", "v79", "v80", "v81", "v82", "v83", "v84", "v85", "v86", "v87");
  u32x4 hwa, hwb;
  u32x2 hwc;
  const int grp = rt < 3 ? 0 : 1;
  if (w < 8) {
    const int rmax = mt == 25 ? 2 : 4;
    const int rk = rt < rmax ? rt : rmax, rd = (rt ^ 1) < rmax ? (rt ^ 1) : rmax;
    u32 ta, tk;
    if (w == 0) asm volatile("s_waitcnt vmcnt(0)" ::: "memory");
    if (w == 2 || w == 3) asm volatile("s_waitcnt vmcnt(3)" ::: "memory");
    asm volatile("s_cmp_lg_u32 %[w], 1\n\ts_cbranch_scc1 2f\n\ts_setprio 3\n"
                 "1:\n\ts_memrealtime vcc\n\ts_waitcnt lgkmcnt(0)\n\ts_sub_u32 vcc_lo, vcc_lo, %[go]\n\ts_cmp_lt_i32 vcc_lo, 0\n\ts_cbranch_scc1 1b\n\ts_setprio 0\n"
                 "2:\n\ts_waitcnt lgkmcnt(0)\n\ts_barrier\n\t" OP_ASM
                 : "=&{v[84:87]}"(hwa), "=&{v[88:91]}"(hwb), "=&{v[92:93]}"(hwc), [ta] "=&v"(ta), [tk] "=&v"(tk)
                 : [w] "s"(w), [go] "s"(tgo), [l16] "v"(l16), [oa0] "s"(oa[0]), [so0] "s"(so_[0]), [oa1] "s"(oa[1]), [so1] "s"(so_[1]), [oa2] "s"(oa[2]), [so2] "s"(so_[2]), [oa3] "s"(oa[3]), [so3] "s"(so_[3]), [oa4] "s"(oa[4]), [so4] "s"(so_[4]), [oa5] "s"(oa[5]), [so5] "s"(so_[5]), [oa6] "s"(oa[6]), [so6] "s"(so_[6]), [oa7] "s"(oa[7]), [so7] "s"(so_[7]), [oa8] "s"(oa[8]), [so8] "s"(so_[8]), [oa9] "s"(oa[9]), [so9] "s"(so_[9]),
                   [pm] "v"(pm), [pf] "v"(pf), [ra] "v"(w < 3 ? (u32)(CTL_OFF + 72) : hi), [e4] "v"(e4),
                   [aw] "v"((lane & 1) ? (u32)(WR_OFF + g * 40) : (u32)ONES_OFF),
                   [vk] "v"(l16 + rk * 20480), [vd] "v"(l16 + rd * 20480), [sk] "s"((w & 1) * 6144),
                   [sd] "s"(EXO + (w < 4 ? 4 + w : rt ^ 1) * 5120), [sr] "s"(EXO + rt * 5120),
                   [sc] "s"(CTL_OFF + 48 + (w < 4 ? 4 : rt ^ 1) * 4), [sp] "s"(PST_OFF + rt * 256), [sa] "s"(CTL_OFF + 8 + grp * 4),
                   [sst] "s"(sst), [cp] "s"(ws + WS_PC + (2 * mt + grp) * 1280 + hg * 160), [oback] "n"(OBACK), [cap] "n"(SPIN_CAP)
                 : "memory", "vcc", "scc", "s43", "v24", "v25", "v26", "v27", "v28", "v29", "v30", "v31", "v32", "v33", "v34", "v35", "v36", "v37", "v38", "v39", "v40", "v41", "v42", "v43", "v44", "v45", "v46", "v47", "v48", "v49", "v50", "v51", "v52", "v53", "v54", "v55", "v56", "v57", "v58", "v59", "v60", "v61", "v62", "v63", "v64", "v65", "v66", "v67", "v68", "v69", "v70", "v71", "v72", "v73", "v74", "v75", "v76", "v77", "v78", "v79", "v80", "v81", "v82", "v83", "v94", "v95", "v96", "v97", "v98", "v99", "v100", "v101", "v102", "v103", "v104", "v105", "v106", "v107", "v108", "v109", "v110", "v111", "v112", "v113", "v114", "v115", "v116", "v117", "v118", "v119", "v120", "v121", "v122", "v123", "v124", "v125", "v126", "v127", "v128", "v129", "v130", "v131", "v132", "v133", "v134", "v135", "v136", "v137", "v138", "v139", "v140", "v141", "v142", "v143", "v144", "v145", "v146", "v147", "v148", "v149", "v150", "v151", "v152", "v153", "v154", "v155", "v156", "v157", "v158", "v159");
    if (!live || w < 3) return;
  } else {
    u64 tb;
    asm volatile("s_setprio 3\n\ts_waitcnt lgkmcnt(0)\n\ts_barrier\n\ts_memrealtime %0\n\ts_waitcnt lgkmcnt(0)" : "=s"(tb)::"memory");
    if (w == 8 || w == 11) {
      const int cg = w == 11, gid = 2 * mt + cg;
      const char* sp = sst - hg * 1024;
      const char* sp2 = sp + (cg ? 0 : 7936);
      const u32 vq = (lane >> 3) * 1024 + (lane & 7) * 16 + (cg ? 640 : 768);
      u32x4 pq[3];
      u64 t2;
#define PLOADS "global_load_dwordx4 %0, %4, %5 sc1\n\tglobal_load_dwordx4 %1, %4, %5 offset:128 sc1\n\tglobal_load_dwordx4 %2, %4, %6 sc1\n\t"
      asm volatile("1:\n\ts_memrealtime %3\n\ts_waitcnt lgkmcnt(0)\n\ts_sub_u32 s40, %7, s40\n\ts_cmp_gt_i32 s40, 0\n\ts_cbranch_scc1 1b\n\t" PLOADS "s_setprio 1"
                   : "=&v"(pq[0]), "=&v"(pq[1]), "=&v"(pq[2]), "=&{s[40:41]}"(t2) : "v"(vq), "s"(sp), "s"(sp2), "s"((u32)tb + PDT) : "memory", "scc");
      int lane2 = lane;
      asm volatile("" : "+v"(lane2));
      const int n16 = lane2 & 15;
      u32x4 sl;
      u32x2 km;
#pragma unroll
      for (int e = 0; e < 4; ++e) sl[e] = n16 == 2 * e ? 0x3F80u : (n16 == 2 * e + 1 ? 0x3F800000u : 0u);
      u32 rid = (u32)((((lane2 >> 4) & 1) * 2 + (lane2 >> 5)) * 4 + (lane2 & 3)) << 10;
      u32 rid1 = rid + (16 << 10), rid2 = rid + (cg ? 0 : 32 << 10), msk = n16 < 4 ? 0xFFFF0000u : 0u;
      u32 ah = HIM + cg * 7680 + (lane2 < 10 ? lane2 : 9) * 16;
      km[1] = *(const u32*)(smem + CTL_OFF);
      const char* kp = ws + WS_PK + gid * 6144 + hg * 768;
      const char* cp = ws + WS_PC + gid * 1280 + hg * 160;
      asm volatile("" : "+s"(kp), "+s"(cp));
      km[0] = ((u32)tb >> 1) & 1023u;
      asm volatile("s_setprio " "3" "\n\ts_waitcnt vmcnt(0)" : "+v"(pq[0]), "+v"(pq[1]), "+v"(pq[2]), "+v"(sl), "+v"(rid), "+v"(rid1), "+v"(rid2), "+v"(msk), "+v"(ah), "+v"(km)::"memory");
#define PMISS __any(max(max(max(pq[0][0], pq[0][2]), max(pq[1][0], pq[1][2])), max(pq[2][0], pq[2][2])) == SENT)
      if (__builtin_expect(PMISS, 0)) {
#pragma nounroll
        for (int it = 0; it < SPIN_CAP; ++it) {
          asm volatile(PLOADS "s_waitcnt vmcnt(0)" : "=&v"(pq[0]), "=&v"(pq[1]), "=&v"(pq[2]), "=&s"(t2) : "v"(vq), "s"(sp), "s"(sp2) : "memory");
          if (!PMISS) break;
          __builtin_amdgcn_s_sleep(2);
        }
      }
      u32 pkey = 0;
#pragma unroll
      for (int t = 0; t < 3; ++t) {
        const f32x4 ys = MFMA(__builtin_bit_cast(bf16x8, pq[t]), __builtin_bit_cast(bf16x8, sl), ((f32x4){0.f, 0.f, 0.f, 0.f}));
        const float y = tr_sum(ys);
        pkey = max(pkey, (mono(y * __builtin_amdgcn_rcpf(dppf<0x104>(y))) & msk) | (t == 0 ? rid : (t == 1 ? rid1 : rid2)));
      }
      pkey = max(pkey, dppu<0xB1>(pkey));
      pkey = max(pkey, dppu<0x4E>(pkey));
      pkey = max(max((u32)__builtin_amdgcn_readlane(pkey, 0), (u32)__builtin_amdgcn_readlane(pkey, 16)),
                 max((u32)__builtin_amdgcn_readlane(pkey, 32), (u32)__builtin_amdgcn_readlane(pkey, 48)));
      km[0] |= pkey;
      asm volatile("s_mov_b32 exec_lo, 0x01010101\n\ts_mov_b32 exec_hi, 0x0101\n\tglobal_store_dwordx2 %0, %1, %2 sc1\n\ts_mov_b64 exec, -1\n\ts_setprio 2" ::"v"(l16), "v"(km), "s"(kp) : "memory");
      ah += ((pkey >> 10) & 63u) * 160;
      if (lane == 0) *(u32*)(smem + CTL_OFF + 24 + cg * 4) = ah;
      __builtin_amdgcn_s_setprio(0);
#pragma nounroll
      for (int it = 0; it < 32; ++it) {
        if ((int)((u32)__builtin_amdgcn_s_memrealtime() - (u32)tb) >= PSL) break;
        __builtin_amdgcn_s_sleep(6);
      }
      __builtin_amdgcn_s_setprio(2);
      asm volatile("s_mov_b32 s40, %[cap]\n"
                   "1:\n\tds_read_b32 v100, %[ac]\n\tds_read_b32 v101, %[ac] offset:24\n\tds_read_b128 v[96:99], %[ah]\n\ts_sub_u32 s40, s40, 1\n\ts_cbranch_scc1 2f\n\ts_waitcnt lgkmcnt(0)\n\t"
                   "v_cmp_gt_u32 vcc, %[nf], v100\n\ts_cbranch_vccz 2f\n\ts_sleep 2\n\ts_branch 1b\n"
                   "2:\n\ts_waitcnt lgkmcnt(0)\n\tv_cmp_eq_u32 vcc, 0, v101\n\ts_cbranch_vccz 3f\n\t"
                   "s_mov_b64 exec, 0x3ff\n\tglobal_store_dwordx4 %[l16], v[96:99], %[cp] sc1\n\ts_mov_b64 exec, -1\n3:"
                   ::[cap] "n"(SPIN_CAP * 8), [ac] "v"((u32)(CTL_OFF + 8 + cg * 4)), [ah] "v"(ah), [nf] "s"(3 - cg), [l16] "v"(l16), [cp] "s"(cp)
                   : "memory", "vcc", "scc", "s40", "v96", "v97", "v98", "v99", "v100", "v101");
      return;
    }
    __builtin_amdgcn_s_setprio(0);
    if (w == 10 && mt == 25) return;
    const int cg = w - 9, gid = 2 * mt + cg;
    const int nr = cg ? 31 : 47;
    const int lr = lane < nr ? lane : nr;
    const int lrow = cg * 48 + lr;
    const int li = (lane & 31) < 20 ? (lane & 31) : 19;
    asm volatile(COL_ASM
                 :
                 : [lq] "v"((u32)(lr * 16 + hg * 1024)), [ac] "v"((u32)(CTL_OFF + 8 + cg * 4)), [nf] "s"(3 - cg),
                   [sp] "s"(ws + WS_STAT + (size_t)gid * 8192), [ao] "v"((u32)(PST_OFF + lrow * 16)), [rdm] "s"(1.0f / DM),
                   [swr] "v"(*(const float*)(smem + CTL_OFF + 40)), [msk] "s"(0xFFFFF000u), [rid] "v"(0x800u | (u32)(2047 - (r0 + lrow))),
                   [at] "v"((u32)(TAB_OFF + lrow * 8)), [zr] "v"(l16), "{v99}"(*(const u32*)(smem + CTL_OFF)),
                   [kp] "s"(ws + WS_PK + gid * 6144 + hg * 768 + 8), [ak] "v"((u32)(CTL_OFF + 16 + cg * 4)), [rb] "s"(2047 - r0 - cg * 48),
                   [ah] "v"((u32)(HIM + cg * 7680 + li * 8)), [co] "v"((u32)((lane < 32 ? 1280 : 0) + li * 8)),
                   [cp] "s"(ws + WS_CAND + gid * 2560 + hg * 160), [pdel] "n"(PDEL2), [cap] "n"(SPIN_CAP)
                 : "memory", "vcc", "scc", "s40", "s41", "s42", "s43", "s44", "s45", "s46", "s47", "v64", "v65", "v66", "v67", "v68", "v69", "v70", "v71", "v72", "v73", "v74", "v75", "v76", "v77", "v78", "v79", "v80", "v81", "v82", "v83", "v84", "v85", "v86", "v87", "v88", "v89", "v90", "v91", "v92", "v93", "v94", "v95", "v96", "v97", "v98", "v100", "v101", "v102", "v103", "v104", "v105", "v106", "v107", "v108", "v109", "v110", "v111", "v112", "v113", "v114", "v115", "v116", "v117", "v118", "v119", "v120", "v121", "v122", "v123");
    return;
  }
  const u32 hw[10] = {hwa[0], hwa[1], hwa[2], hwa[3], hwb[0], hwb[1], hwb[2], hwb[3], hwc[0], hwc[1]};
  f32x2 hf[10];
#pragma unroll
  for (int e = 0; e < 10; ++e) {
    hf[e] = unpk(hw[e]);
    asm volatile("" : "+v"(hf[e]));
  }
  const char* obase = (const char*)out + (size_t)r0 * 1280 + hg * 160;
  u32 tilekey;
  asm volatile("" : "+s"(obase));
  u32 aow = EXO + rt * 5120 + r16 * 160 + g * 40, aor = EXO + rt * 5120 + l16, vo3[3], rr3[3];
#pragma unroll
  for (int i = 0; i < 3; ++i) {
    const u32 c = i * 64 + lane, r = c / 10u;
    vo3[i] = (rt * 16 + r) * 1120 + rt * 2560 + c * 16;
    rr3[i] = c < 160u ? 2047 - (r0 + rt * 16 + r) : 4096u;
    asm volatile("" : "+v"(vo3[i]), "+v"(rr3[i]));
  }
  asm volatile("" : "+v"(aow), "+v"(aor));
  f32x2 st2;
  asm volatile("s_mov_b32 s40, %[cap]\n"
               "1:\n\ts_sleep 1\n\tds_read_b32 %[k], %[ak]\n\tds_read_b64 %[s], %[at]\n\ts_sub_u32 s40, s40, 1\n\ts_waitcnt lgkmcnt(0)\n\ts_cbranch_scc1 2f\n\t"
               "v_cmp_eq_u32 vcc, 0, %[k]\n\ts_cbranch_vccnz 1b\n"
               "2:"
               : [k] "=&v"(tilekey), [s] "=&v"(st2) : [cap] "n"(SPIN_CAP * 8), [ak] "v"((u32)(CTL_OFF + 16 + grp * 4)), [at] "v"((u32)(TAB_OFF + (rt * 16 + r16) * 8))
               : "memory", "vcc", "scc", "s40");
  u32 ok[10];
#pragma unroll
  for (int e = 0; e < 10; ++e) ok[e] = cvt2(__builtin_elementwise_fma(hf[e] - (f32x2){st2[0], st2[0]}, (f32x2){st2[1], st2[1]}, hf[e]));
  {
#pragma unroll
    for (int e = 0; e < 5; ++e) asm volatile("ds_write_b64 %0, %1 offset:%2" ::"v"(aow), "v"((u32x2){ok[2 * e], ok[2 * e + 1]}), "n"(e * 8) : "memory");
    u32x4 o3[3];
    asm volatile("ds_read_b128 %0, %3\n\tds_read_b128 %1, %3 offset:1024\n\tds_read_b128 %2, %3 offset:2048\n\ts_waitcnt lgkmcnt(0)"
                 : "=&v"(o3[0]), "=&v"(o3[1]), "=&v"(o3[2]) : "v"(aor) : "memory");
#pragma unroll
    for (int i = 0; i < 3; ++i)
      if (rr3[i] != (tilekey & 2047u) && rr3[i] != 4096u)
        asm volatile("global_store_dwordx4 %0, %1, %2" " sc1" "\n\ts_nop 1" ::"v"(vo3[i]), "v"(o3[i]), "s"(obase) : "memory");
  }
}

extern "C" void kernel_launch(void* const* d_in, const int* in_sizes, int n_in,
                              void* d_out, int out_size, void* d_ws, size_t ws_size,
                              hipStream_t stream) {
  const u16* x = (const u16*)d_in[0];
  const u16* wqkv = (const u16*)d_in[1];
  const u16* wo = (const u16*)d_in[2];
  const u16* w_r = (const u16*)d_in[4];
  const u16* w_gate = (const u16*)d_in[5];
  const u16* w_up = (const u16*)d_in[6];
  const u16* w_down = (const u16*)d_in[7];
  (void)hipFuncSetAttribute((const void*)fused_block, hipFuncAttributeMaxDynamicSharedMemorySize, LDS_TOTAL);
  fused_block<<<256, 768, LDS_TOTAL, stream>>>(x, wqkv, wo, w_r, w_gate, w_up, w_down, (u16*)d_out, (char*)d_ws);
}
```

```cpp
#include <hip/hip_runtime.h>
#include <hip/hip_bf16.h>
#include <stdint.h>


#define SEQ 2048
#define DM 640
#define FF 1536

typedef __attribute__((ext_vector_type(8))) short bf16x8;
typedef __attribute__((ext_vector_type(4))) short s16x4;
typedef __attribute__((ext_vector_type(4))) float f32x4;
typedef __attribute__((ext_vector_type(2))) float f32x2;
typedef __attribute__((ext_vector_type(2))) __bf16 bf16x2_t;
typedef __attribute__((address_space(3))) s16x4* lds_s16x4_p;
typedef unsigned short u16;
typedef unsigned int u32;
typedef unsigned long long u64;
typedef __attribute__((ext_vector_type(4))) unsigned int u32x4;
typedef __attribute__((ext_vector_type(2))) unsigned int u32x2;

#define HK0 15
#define CGAP 6
#define KGAP 7
#define CDEL 10
#define PDEL2 7
#define OBACK 2
#define PDELAY 220
#define FDELAY 450
#define KSTART 1050
#define PGAP 5
#define PDT 20
#define PSL 185
#define VDELAY 150
#define CCD 312
#define TQD 160
#define KCD 306
#define KOLD 316
#define TWS 6
#define XSL 2
#define PCD 230
#define PTHR 0.91f
#define GADD 80
#define OORD 0, 4, 5, 6, 7, 1, 2, 3
#define CODE_BYTES 35176

#define LDS_BARRIER() do { __builtin_amdgcn_sched_barrier(0); asm volatile("s_waitcnt lgkmcnt(0)\n\ts_barrier" ::: "memory"); __builtin_amdgcn_sched_barrier(0); } while (0)
__device__ __forceinline__ float bf2f(u16 v) { return __uint_as_float(((u32)v) << 16); }
__device__ __forceinline__ u16 f2bf(float f) { return __builtin_bit_cast(u16, (__bf16)f); }
__device__ __forceinline__ float rbf(float f) { return bf2f(f2bf(f)); }
__device__ __forceinline__ float lo_f(u32 w) { return __uint_as_float(w << 16); }
__device__ __forceinline__ float hi_f(u32 w) { return __uint_as_float(w & 0xFFFF0000u); }
__device__ __forceinline__ u32 cvt2(f32x2 v) { return __builtin_bit_cast(u32, __builtin_convertvector(v, bf16x2_t)); }
__device__ __forceinline__ f32x2 unpk(u32 w) { return (f32x2){lo_f(w), hi_f(w)}; }
__device__ __forceinline__ s16x4 tr_read(const char* p) {
  return __builtin_amdgcn_ds_read_tr16_b64_v4i16((lds_s16x4_p)(p));
}
template <int V> struct icst { static constexpr int value = V; };
template <int CTRL>
__device__ __forceinline__ float dppf(float v) {
  return __builtin_bit_cast(float, __builtin_amdgcn_update_dpp(0, __builtin_bit_cast(int, v), CTRL, 0xf, 0xf, true));
}
__device__ __forceinline__ float row8_sum(float v) {
  v += dppf<0xB1>(v);
  v += dppf<0x4E>(v);
  v += dppf<0x141>(v);
  return v;
}
__device__ __forceinline__ float row16_sum(float v) {
  v = row8_sum(v);
  v += dppf<0x140>(v);
  return v;
}
__device__ __forceinline__ float xg_sum(float v) {
  u32 a = __float_as_uint(v);
  auto r = __builtin_amdgcn_permlane32_swap(a, a, false, false);
  v = __uint_as_float(r[0]) + __uint_as_float(r[1]);
  a = __float_as_uint(v);
  auto r2 = __builtin_amdgcn_permlane16_swap(a, a, false, false);
  return __uint_as_float(r2[0]) + __uint_as_float(r2[1]);
}
__device__ __forceinline__ float tr_sum(f32x4 ys) {
  auto r01 = __builtin_amdgcn_permlane32_swap(__float_as_uint(ys[0]), __float_as_uint(ys[1]), false, false);
  auto r23 = __builtin_amdgcn_permlane32_swap(__float_as_uint(ys[2]), __float_as_uint(ys[3]), false, false);
  const u32 u = __float_as_uint(__uint_as_float(r01[0]) + __uint_as_float(r01[1]));
  const u32 vv = __float_as_uint(__uint_as_float(r23[0]) + __uint_as_float(r23[1]));
  auto r2 = __builtin_amdgcn_permlane16_swap(u, vv, false, false);
  return __uint_as_float(r2[0]) + __uint_as_float(r2[1]);
}
__device__ __forceinline__ float wave_sum(float v) { return xg_sum(row16_sum(v)); }
template <int CTRL>
__device__ __forceinline__ u32 dppu(u32 v) { return (u32)__builtin_amdgcn_update_dpp(0, (int)v, CTRL, 0xf, 0xf, true); }
__device__ __forceinline__ u32 wave_max(u32 v) {
  v = max(v, dppu<0xB1>(v)); v = max(v, dppu<0x4E>(v)); v = max(v, dppu<0x141>(v)); v = max(v, dppu<0x140>(v));
  return max(max((u32)__builtin_amdgcn_readlane(v, 0), (u32)__builtin_amdgcn_readlane(v, 16)),
             max((u32)__builtin_amdgcn_readlane(v, 32), (u32)__builtin_amdgcn_readlane(v, 48)));
}
__device__ __forceinline__ u32 mono(float f) {
  const u32 b = __float_as_uint(f);
  return b ^ ((u32)((int)b >> 31) | 0x80000000u);
}
__device__ __forceinline__ float dot2b(u32 a, u32 b, float c) {
  return __builtin_amdgcn_fdot2_f32_bf16(__builtin_bit_cast(bf16x2_t, a), __builtin_bit_cast(bf16x2_t, b), c, false);
}
template <int OFF>
__device__ __forceinline__ void dma_m0(u32 voff, const char* sbase, u32 dst) {
  asm volatile("s_mov_b32 m0, %2\n\ts_nop 0\n\tglobal_load_lds_dwordx4 %0, %1 offset:%3"
               :: "v"(voff), "s"(sbase), "s"(dst), "n"(OFF) : "memory");
}
template <int OFF>
__device__ __forceinline__ void dma_nx(u32 voff, const char* sbase) {
  asm volatile("global_load_lds_dwordx4 %0, %1 offset:%2" :: "v"(voff), "s"(sbase), "n"(OFF) : "memory");
}
__device__ __forceinline__ u32 lds_ld32(u32 addr) {
  u32 v;
  asm volatile("ds_read_b32 %0, %1\n\ts_waitcnt lgkmcnt(0)" : "=v"(v) : "v"(addr) : "memory");
  return v;
}
__device__ __forceinline__ u32x4 lds_ld128(u32 addr) {
  u32x4 v;
  asm volatile("ds_read_b128 %0, %1\n\ts_waitcnt lgkmcnt(0)" : "=v"(v) : "v"(addr) : "memory");
  return v;
}
__device__ __forceinline__ bf16x8 mk8(u32 a, u32 b, u32 c, u32 d) {
  return __builtin_bit_cast(bf16x8, (u32x4){a, b, c, d});
}
__device__ __forceinline__ bf16x8 tr_frag(const char* p, int off2) {
  s16x4 b0 = tr_read(p);
  s16x4 b1 = tr_read(p + off2);
  bf16x8 r;
  r[0] = b0[0]; r[1] = b0[1]; r[2] = b0[2]; r[3] = b0[3];
  r[4] = b1[0]; r[5] = b1[1]; r[6] = b1[2]; r[7] = b1[3];
  return r;
}
#define SPIN_CAP 400000
#define MFMA(A, B, C) __builtin_amdgcn_mfma_f32_16x16x32_bf16(A, B, C, 0, 0, 0)

#define HSLOT 16384
#define XRING 98304
#define XSLOT 12288
#define LDS_TOTAL 163840
#define EXO 102400
#define HIM 148480
#define PST_OFF (EXO + 40960)
#define TAB_OFF 161280
#define ONES_OFF 162048
#define CTL_OFF 162176
#define WR_OFF 162304
#define REC_OFF 162816
#define ZERO_OFF 163456
#define HN_OFF 124928
#define HSL 1344
#define ASL 1056
#define TCH_OFF 162560
#define ACT_OFF 135168
#define KEY_OFF (ACT_OFF + 5120)
#define POST_OFF (KEY_OFF + 256)
#define DOWN_OFF 81920
#define WS_EPOCH 0
#define WS_HFLAG 4096
#define WS_VP 40960
#define WS_LN1 16384
#define WS_FILL 131072
#define WS_O WS_FILL
#define WS_STAT (WS_O + 2621440)
#define WS_CAND (WS_STAT + 425984)
#define WS_PART (WS_CAND + 133120)
#define WS_BXF (WS_PART + 122880)
#define WS_PC (WS_BXF + 26624)
#define WS_PART2 (WS_PC + 66560)
#define WS_PK (4u << 20)
#define SENT 0xFFFFFFFFu

#define GST4(VO, SB, O, V) asm volatile("global_store_dwordx4 %0, %1, %2 offset:%3 sc1\n\ts_nop 1" ::"v"(VO), "v"(V), "s"(SB), "n"(O) : "memory")
#define GST2(VO, SB, O, V) asm volatile("global_store_dwordx2 %0, %1, %2 offset:%3 sc1\n\ts_nop 1" ::"v"(VO), "v"(V), "s"(SB), "n"(O) : "memory")
#define GLD2(D, VO, SB, O) asm volatile("global_load_dwordx2 %0, %1, %2 offset:%3 sc1" : "=v"(D) : "v"(VO), "s"(SB), "n"(O) : "memory")

__device__ __forceinline__ void touch_line(const void* p) {
  asm volatile("s_mov_b32 m0, %1\n\ts_nop 0\n\tglobal_load_lds_dword %0, off" ::"v"(p), "s"(TCH_OFF) : "memory");
}
__device__ __forceinline__ u32 w_off(int i, int lane, int hg) {
  int cc = (lane & 31) ^ (((2 * i + (lane >> 5)) & 7) << 1);
  cc = cc > 29 ? 29 : cc;
  return (lane >> 5) * 3840 + ((cc >= 10 ? 1120 : 0) + (cc >= 20 ? 1120 : 0)) + hg * 160 + cc * 16;
}
#define W_ROW(I) (((((2 * (I)) & 0x13) | (((2 * (I)) & 4) << 1) | (((2 * (I)) & 8) >> 1))) * 3840)
__device__ __forceinline__ u32 x_off(int i, int lane, int r0) {
  const int xr = i * 8 + (lane >> 3);
  int grow = r0 - 16 + xr;
  grow = grow < 0 ? 0 : (grow > SEQ - 1 ? SEQ - 1 : grow);
  return grow * 1280 + (((lane & 7) ^ ((xr >> 1) & 7)) * 16);
}

#define ATT_ASM \
  "ds_read_b32 v28, %[aq] offset:64\n\tds_read_b32 v30, %[aka]\n\tds_read_b32 v31, %[akb]\n\tv_mov_b64 v[38:39], 0\n\tv_mov_b64 v[42:43], 0\n\t" \
  "s_lshl_b32 s41, %[kh], 5\n\ts_lshl_b64 s[46:47], 0xffff, s41\n\ts_lshl_b64 s[48:49], s[46:47], 16\n\ts_mov_b32 s50, 0xaaaaaaaa\n\t" \
  "s_mov_b32 s51, 0xaaaaaaaa\n\ts_setprio 1\n\ts_waitcnt lgkmcnt(0)\n\ts_barrier\n\tds_read_b64 v[36:37], %[eh] offset:512\n\t" \
  "ds_read_b64 v[44:45], %[eh] offset:15360\n\tds_read_b64 v[46:47], %[e4] offset:27648\n\tds_read_b64 v[48:49], %[eh] offset:15872\n\t" \
  "ds_read_b64 v[50:51], %[e4] offset:28160\n\ts_mov_b64 exec, s[46:47]\n\tds_read_b64 v[38:39], %[e4] offset:12800\n\ts_mov_b64 exec, s[48:49]\n\t" \
  "ds_read_b64 v[42:43], %[e4] offset:12800\n\ts_mov_b64 exec, -1\n\tds_read_b64 v[40:41], %[eh] offset:3584\n\t" \
  "ds_read_b64 v[52:53], %[eh] offset:18432\n\tds_read_b64 v[54:55], %[e4] offset:27648\n\tds_read_b64 v[56:57], %[eh] offset:18944\n\t" \
  "ds_read_b64 v[58:59], %[e4] offset:28160\n\tv_mul_f32 v28, 0x3ea52b5b, v28\n\tv_pk_mul_f32 v[32:33], v[28:29], v[30:31] op_sel_hi:[0,1]\n\t" \
  "s_waitcnt lgkmcnt(6)\n\tv_mfma_f32_16x16x32_bf16 v[44:47], v[44:47], v[36:39], 0\n\tds_read2st64_b64 v[60:63], %[eh] offset0:60 offset1:61\n\t" \
  "v_mfma_f32_16x16x32_bf16 v[48:51], v[48:51], v[36:39], 0\n\tds_read2st64_b64 v[64:67], %[eh] offset0:66 offset1:67\n\ts_waitcnt lgkmcnt(2)\n\t" \
  "v_mfma_f32_16x16x32_bf16 v[52:55], v[52:55], v[40:43], 0\n\tds_read2st64_b64 v[68:71], %[e4] offset0:84 offset1:85\n\t" \
  "v_mfma_f32_16x16x32_bf16 v[56:59], v[56:59], v[40:43], 0\n\ts_mov_b32 exec_lo, 0xfff0ffff\n\ts_mov_b32 exec_hi, 0xf000ff00\n\ts_nop 0\n\t" \
  "v_mov_b64 v[44:45], v[48:49]\n\ts_mov_b32 exec_lo, 0xffc0fffc\n\ts_mov_b32 exec_hi, 0xc000fc00\n\tv_mov_b64 v[46:47], v[50:51]\n\ts_nop 0\n\t" \
  "v_mov_b64 v[54:55], v[58:59]\n\ts_mov_b32 exec_lo, 0xfff0ffff\n\ts_mov_b32 exec_hi, 0xf000ff00\n\tv_mov_b64 v[52:53], v[56:57]\n\t" \
  "s_mov_b64 exec, -1\n\tv_cndmask_b32 v44, v44, v45, s[50:51]\n\tv_cndmask_b32 v46, v46, v47, s[50:51]\n\tv_cndmask_b32 v52, v52, v53, s[50:51]\n\t" \
  "v_cndmask_b32 v54, v54, v55, s[50:51]\n\tv_fma_f32 v72, v44, v32, %[b0]\n\tv_fma_f32 v73, v46, v33, %[b1]\n\tv_fma_f32 v74, v52, v32, %[b0]\n\t" \
  "v_fma_f32 v75, v54, v33, %[b1]\n\tv_max_f32 v34, v72, v73\n\tv_max_f32 v35, v74, v75\n\tds_read_b128 v[76:79], %[arec]\n\t" \
  "ds_read_b128 v[80:83], %[arec] offset:16\n\tv_permlane32_swap_b32 v34, v35\n\tv_max_f32 v34, v34, v35\n\tv_mov_b32 v35, v34\n\t" \
  "ds_read_b128 v[84:87], %[arec] offset:32\n\ts_nop 0\n\tv_permlane16_swap_b32 v34, v35\n\tv_max_f32 v34, v34, v35\n\tv_mov_b32 v35, v34\n\ts_nop 1\n\t" \
  "v_permlane32_swap_b32 v34, v35\n\tv_sub_f32 v72, v72, v34\n\tv_sub_f32 v73, v73, v34\n\tv_sub_f32 v74, v74, v35\n\tv_sub_f32 v75, v75, v35\n\t" \
  "v_exp_f32 v72, v72\n\tv_exp_f32 v73, v73\n\tv_exp_f32 v74, v74\n\tv_exp_f32 v75, v75\n\tv_add_f32 v34, v72, v73\n\tv_add_f32 v35, v74, v75\n\t" \
  "v_pk_mul_f32 v[72:73], v[72:73], v[30:31]\n\tv_pk_mul_f32 v[74:75], v[74:75], v[30:31]\n\tv_permlane32_swap_b32 v34, v35\n\t" \
  "v_add_f32 v34, v34, v35\n\tv_mov_b32 v35, v34\n\ts_nop 1\n\tv_permlane16_swap_b32 v34, v35\n\tv_add_f32 v34, v34, v35\n\tv_rcp_f32 v34, v34\n\t" \
  "s_nop 0\n\tv_mov_b32 v30, v34\n\ts_waitcnt lgkmcnt(0)\n\ts_nop 0\n\tv_permlane32_swap_b32 v34, v30\n\t" \
  "v_pk_mul_f32 v[72:73], v[34:35], v[72:73] op_sel_hi:[0,1]\n\tv_pk_mul_f32 v[74:75], v[30:31], v[74:75] op_sel_hi:[0,1]\n\t" \
  "v_cvt_pk_bf16_f32 v72, v72, v73\n\tv_cvt_pk_bf16_f32 v74, v74, v75\n\tv_perm_b32 v36, 0, v72, %[s0]\n\tv_perm_b32 v37, 0, v72, %[s1]\n\t" \
  "v_perm_b32 v38, 0, v72, %[s2]\n\tv_perm_b32 v39, 0, v72, %[s3]\n\tv_perm_b32 v40, 0, v74, %[s0]\n\tv_perm_b32 v41, 0, v74, %[s1]\n\t" \
  "v_mfma_f32_16x16x32_bf16 v[60:63], v[60:63], v[36:39], 0\n\tv_perm_b32 v42, 0, v74, %[s2]\n\tv_perm_b32 v43, 0, v74, %[s3]\n\t" \
  "v_mfma_f32_16x16x32_bf16 v[52:55], v[68:71], v[36:39], 0\n\ts_nop 0\n\tv_mfma_f32_16x16x32_bf16 v[64:67], v[64:67], v[40:43], 0\n\t" \
  "v_mfma_f32_16x16x32_bf16 v[56:59], v[68:71], v[40:43], 0\n\ts_nop 1\n\tv_cvt_pk_bf16_f32 v24, v60, v61\n\tv_cvt_pk_bf16_f32 v25, v62, v63\n\t" \
  "s_nop 0\n\tv_cvt_pk_bf16_f32 v52, v52, v53\n\tv_cvt_pk_bf16_f32 v53, v54, v55\n\tv_cvt_pk_bf16_f32 v26, v64, v65\n\t" \
  "v_cvt_pk_bf16_f32 v27, v66, v67\n\tglobal_store_dwordx4 %[l16], v[24:27], %[os] sc1\n\ts_mov_b64 exec, s[48:49]\n\tv_cvt_pk_bf16_f32 v52, v56, v57\n\t" \
  "v_cvt_pk_bf16_f32 v53, v58, v59\n\ts_or_b64 exec, s[46:47], s[48:49]\n\tglobal_store_dwordx2 %[l16], v[52:53], %[os4] sc1\n\ts_mov_b64 exec, -1\n\t" \
  "s_setprio 0\n\tv_mfma_f32_16x16x32_bf16 v[60:63], v[24:27], v[76:79], 0\n\tv_and_b32 v54, 0x7fff7fff, v52\n\tv_and_b32 v55, 0x7fff7fff, v53\n\t" \
  "v_and_b32 v64, 0x7fff7fff, v24\n\tv_and_b32 v65, 0x7fff7fff, v25\n\tv_and_b32 v66, 0x7fff7fff, v26\n\tv_and_b32 v67, 0x7fff7fff, v27\n\t" \
  "v_mfma_f32_16x16x32_bf16 v[60:63], v[52:55], v[80:83], v[60:63]\n\tds_write2st64_b64 %[eh], v[24:25], v[26:27] offset0:1 offset1:7\n\t" \
  "v_mfma_f32_16x16x32_bf16 v[60:63], v[64:67], v[84:87], v[60:63]\n\ts_mov_b32 exec_lo, 0x01010101\n\ts_mov_b32 exec_hi, 0x01010101\n\ts_nop 5\n\t" \
  "v_cvt_pk_bf16_f32 v60, v60, v61\n\tv_cvt_pk_bf16_f32 v61, v62, v63\n\tv_and_b32 v60, -2, v60\n\tglobal_store_dwordx2 %[vpp], v[60:61], %[sst] sc1\n\t" \
  "s_mov_b64 exec, -1\n\t" \
  ""
#define OP_ASM \
  "s_mov_b32 s43, %[cap]\n\ts_cmp_lt_u32 %[w], 4\n\ts_cbranch_scc1 LH_%=\n\ts_setprio 3\n\tglobal_load_dwordx4 v[124:127], %[vd], %[oa1] sc1\n\t" \
  "global_load_dwordx4 v[128:131], %[vk], %[oa1] sc1\n\tglobal_load_dwordx4 v[132:135], %[vd], %[oa2] sc1\n\t" \
  "global_load_dwordx4 v[136:139], %[vk], %[oa2] sc1\n\tv_add_u32 %[tk], %[sk], %[e4]\n\tv_add_u32 %[ta], %[so0], %[pm]\n\t" \
  "ds_read2st64_b64 v[104:107], %[tk] offset0:1 offset1:7\n\tv_xor_b32 %[tk], 512, %[tk]\n\tds_read2st64_b64 v[108:111], %[tk] offset0:1 offset1:7\n\t" \
  "ds_read_b64_tr_b16 v[64:65], %[ta] offset:0\n\tds_read_b64_tr_b16 v[66:67], %[ta] offset:3200\n\tds_read_b64_tr_b16 v[68:69], %[ta] offset:8\n\t" \
  "ds_read_b64_tr_b16 v[70:71], %[ta] offset:3208\n\tds_read_b64_tr_b16 v[72:73], %[ta] offset:16\n\tds_read_b64_tr_b16 v[74:75], %[ta] offset:3216\n\t" \
  "ds_read_b64_tr_b16 v[76:77], %[ta] offset:24\n\tds_read_b64_tr_b16 v[78:79], %[ta] offset:3224\n\tds_read_b64_tr_b16 v[80:81], %[ta] offset:32\n\t" \
  "ds_read_b64_tr_b16 v[82:83], %[ta] offset:3232\n\tds_read_b64 v[112:113], %[ra] offset:0\n\tds_read_b64 v[114:115], %[ra] offset:8\n\t" \
  "ds_read_b64 v[116:117], %[ra] offset:16\n\tds_read_b64 v[118:119], %[ra] offset:24\n\tds_read_b64 v[120:121], %[ra] offset:32\n\t" \
  "global_load_dwordx4 v[140:143], %[vd], %[oa3] sc1\n\tv_add_u32 %[ta], %[so1], %[pm]\n\ts_waitcnt lgkmcnt(5)\n\t" \
  "v_mfma_f32_16x16x32_bf16 v[44:47], v[64:67], v[108:111], 0\n\tds_read_b64_tr_b16 v[84:85], %[ta] offset:0\n\t" \
  "ds_read_b64_tr_b16 v[86:87], %[ta] offset:3200\n\tv_mfma_f32_16x16x32_bf16 v[48:51], v[68:71], v[108:111], 0\n\t" \
  "ds_read_b64_tr_b16 v[88:89], %[ta] offset:8\n\tds_read_b64_tr_b16 v[90:91], %[ta] offset:3208\n\t" \
  "v_mfma_f32_16x16x32_bf16 v[52:55], v[72:75], v[108:111], 0\n\tds_read_b64_tr_b16 v[92:93], %[ta] offset:16\n\t" \
  "ds_read_b64_tr_b16 v[94:95], %[ta] offset:3216\n\tv_mfma_f32_16x16x32_bf16 v[56:59], v[76:79], v[108:111], 0\n\t" \
  "ds_read_b64_tr_b16 v[96:97], %[ta] offset:24\n\tds_read_b64_tr_b16 v[98:99], %[ta] offset:3224\n\t" \
  "v_mfma_f32_16x16x32_bf16 v[60:63], v[80:83], v[108:111], 0\n\tds_read_b64_tr_b16 v[100:101], %[ta] offset:32\n\t" \
  "ds_read_b64_tr_b16 v[102:103], %[ta] offset:3232\n\ts_waitcnt lgkmcnt(10)\n\tv_lshlrev_b32 v24, 16, v112\n\tv_and_b32 v25, 0xffff0000, v112\n\t" \
  "v_lshlrev_b32 v26, 16, v113\n\tv_and_b32 v27, 0xffff0000, v113\n\tv_lshlrev_b32 v28, 16, v114\n\tv_and_b32 v29, 0xffff0000, v114\n\t" \
  "v_lshlrev_b32 v30, 16, v115\n\tv_and_b32 v31, 0xffff0000, v115\n\tv_mfma_f32_16x16x32_bf16 v[24:27], v[64:67], v[104:107], v[24:27]\n\t" \
  "v_lshlrev_b32 v32, 16, v116\n\tv_and_b32 v33, 0xffff0000, v116\n\tv_lshlrev_b32 v34, 16, v117\n\tv_and_b32 v35, 0xffff0000, v117\n\t" \
  "v_mfma_f32_16x16x32_bf16 v[28:31], v[68:71], v[104:107], v[28:31]\n\tv_lshlrev_b32 v36, 16, v118\n\tv_and_b32 v37, 0xffff0000, v118\n\t" \
  "v_lshlrev_b32 v38, 16, v119\n\tv_and_b32 v39, 0xffff0000, v119\n\tv_mfma_f32_16x16x32_bf16 v[32:35], v[72:75], v[104:107], v[32:35]\n\t" \
  "v_lshlrev_b32 v40, 16, v120\n\tv_and_b32 v41, 0xffff0000, v120\n\tv_lshlrev_b32 v42, 16, v121\n\tv_and_b32 v43, 0xffff0000, v121\n\t" \
  "v_mfma_f32_16x16x32_bf16 v[36:39], v[76:79], v[104:107], v[36:39]\n\ts_nop 0\n\tv_mfma_f32_16x16x32_bf16 v[40:43], v[80:83], v[104:107], v[40:43]\n\t" \
  "v_add_u32 %[ta], %[so2], %[pm]\n\tds_read_b64_tr_b16 v[104:105], %[ta] offset:0\n\tds_read_b64_tr_b16 v[106:107], %[ta] offset:3200\n\t" \
  "ds_read_b64_tr_b16 v[108:109], %[ta] offset:8\n\tds_read_b64_tr_b16 v[110:111], %[ta] offset:3208\n\t" \
  "ds_read_b64_tr_b16 v[112:113], %[ta] offset:16\n\tds_read_b64_tr_b16 v[114:115], %[ta] offset:3216\n\t" \
  "ds_read_b64_tr_b16 v[116:117], %[ta] offset:24\n\tds_read_b64_tr_b16 v[118:119], %[ta] offset:3224\n\t" \
  "ds_read_b64_tr_b16 v[120:121], %[ta] offset:32\n\tds_read_b64_tr_b16 v[122:123], %[ta] offset:3232\n\t" \
  "global_load_dwordx4 v[144:147], %[vk], %[oa3] sc1\n\tglobal_load_dwordx4 v[148:151], %[vd], %[oa4] sc1\n\ts_waitcnt vmcnt(5)\n\t" \
  "v_max_u32 %[tk], v124, v128\n\tv_cmp_eq_u32 vcc, -1, %[tk]\n\ts_cbranch_vccnz LSs1_%=\n\tLQs1_%=:\n\ts_waitcnt lgkmcnt(10)\n\t" \
  "v_mfma_f32_16x16x32_bf16 v[44:47], v[84:87], v[124:127], v[44:47]\n\tv_add_u32 %[ta], %[so3], %[pm]\n\tds_read_b64_tr_b16 v[64:65], %[ta] offset:0\n\t" \
  "v_mfma_f32_16x16x32_bf16 v[24:27], v[84:87], v[128:131], v[24:27]\n\tds_read_b64_tr_b16 v[66:67], %[ta] offset:3200\n\t" \
  "global_load_dwordx4 v[152:155], %[vk], %[oa4] sc1\n\tv_mfma_f32_16x16x32_bf16 v[48:51], v[88:91], v[124:127], v[48:51]\n\t" \
  "ds_read_b64_tr_b16 v[68:69], %[ta] offset:8\n\tv_mfma_f32_16x16x32_bf16 v[28:31], v[88:91], v[128:131], v[28:31]\n\t" \
  "ds_read_b64_tr_b16 v[70:71], %[ta] offset:3208\n\tglobal_load_dwordx4 v[156:159], %[vd], %[oa5] sc1\n\t" \
  "v_mfma_f32_16x16x32_bf16 v[52:55], v[92:95], v[124:127], v[52:55]\n\tds_read_b64_tr_b16 v[72:73], %[ta] offset:16\n\t" \
  "v_mfma_f32_16x16x32_bf16 v[32:35], v[92:95], v[128:131], v[32:35]\n\tds_read_b64_tr_b16 v[74:75], %[ta] offset:3216\n\t" \
  "v_mfma_f32_16x16x32_bf16 v[56:59], v[96:99], v[124:127], v[56:59]\n\tds_read_b64_tr_b16 v[76:77], %[ta] offset:24\n\t" \
  "v_mfma_f32_16x16x32_bf16 v[36:39], v[96:99], v[128:131], v[36:39]\n\tds_read_b64_tr_b16 v[78:79], %[ta] offset:3224\n\ts_waitcnt vmcnt(5)\n\t" \
  "v_max_u32 %[tk], v132, v136\n\tv_cmp_eq_u32 vcc, -1, %[tk]\n\tv_mfma_f32_16x16x32_bf16 v[60:63], v[100:103], v[124:127], v[60:63]\n\t" \
  "ds_read_b64_tr_b16 v[80:81], %[ta] offset:32\n\tv_mfma_f32_16x16x32_bf16 v[40:43], v[100:103], v[128:131], v[40:43]\n\t" \
  "ds_read_b64_tr_b16 v[82:83], %[ta] offset:3232\n\ts_cbranch_vccnz LSs2_%=\n\tLQs2_%=:\n\ts_waitcnt lgkmcnt(10)\n\t" \
  "v_mfma_f32_16x16x32_bf16 v[44:47], v[104:107], v[132:135], v[44:47]\n\tv_add_u32 %[ta], %[so4], %[pf]\n\t" \
  "ds_read_b64_tr_b16 v[84:85], %[ta] offset:0\n\tv_mfma_f32_16x16x32_bf16 v[24:27], v[104:107], v[136:139], v[24:27]\n\t" \
  "ds_read_b64_tr_b16 v[86:87], %[ta] offset:12800\n\tglobal_load_dwordx4 v[124:127], %[vk], %[oa5] sc1\n\t" \
  "v_mfma_f32_16x16x32_bf16 v[48:51], v[108:111], v[132:135], v[48:51]\n\tds_read_b64_tr_b16 v[88:89], %[ta] offset:8\n\t" \
  "v_mfma_f32_16x16x32_bf16 v[28:31], v[108:111], v[136:139], v[28:31]\n\tds_read_b64_tr_b16 v[90:91], %[ta] offset:12808\n\t" \
  "global_load_dwordx4 v[128:131], %[vd], %[oa6] sc1\n\tv_mfma_f32_16x16x32_bf16 v[52:55], v[112:115], v[132:135], v[52:55]\n\t" \
  "ds_read_b64_tr_b16 v[92:93], %[ta] offset:16\n\tv_mfma_f32_16x16x32_bf16 v[32:35], v[112:115], v[136:139], v[32:35]\n\t" \
  "ds_read_b64_tr_b16 v[94:95], %[ta] offset:12816\n\tv_mfma_f32_16x16x32_bf16 v[56:59], v[116:119], v[132:135], v[56:59]\n\t" \
  "ds_read_b64_tr_b16 v[96:97], %[ta] offset:24\n\tv_mfma_f32_16x16x32_bf16 v[36:39], v[116:119], v[136:139], v[36:39]\n\t" \
  "ds_read_b64_tr_b16 v[98:99], %[ta] offset:12824\n\ts_waitcnt vmcnt(5)\n\tv_max_u32 %[tk], v140, v144\n\tv_cmp_eq_u32 vcc, -1, %[tk]\n\t" \
  "v_mfma_f32_16x16x32_bf16 v[60:63], v[120:123], v[132:135], v[60:63]\n\tds_read_b64_tr_b16 v[100:101], %[ta] offset:32\n\t" \
  "v_mfma_f32_16x16x32_bf16 v[40:43], v[120:123], v[136:139], v[40:43]\n\tds_read_b64_tr_b16 v[102:103], %[ta] offset:12832\n\t" \
  "s_cbranch_vccnz LSs3_%=\n\tLQs3_%=:\n\ts_waitcnt lgkmcnt(10)\n\tv_mfma_f32_16x16x32_bf16 v[44:47], v[64:67], v[140:143], v[44:47]\n\t" \
  "v_add_u32 %[ta], %[so5], %[pm]\n\tds_read_b64_tr_b16 v[104:105], %[ta] offset:0\n\t" \
  "v_mfma_f32_16x16x32_bf16 v[24:27], v[64:67], v[144:147], v[24:27]\n\tds_read_b64_tr_b16 v[106:107], %[ta] offset:3200\n\t" \
  "global_load_dwordx4 v[132:135], %[vk], %[oa6] sc1\n\tv_mfma_f32_16x16x32_bf16 v[48:51], v[68:71], v[140:143], v[48:51]\n\t" \
  "ds_read_b64_tr_b16 v[108:109], %[ta] offset:8\n\tv_mfma_f32_16x16x32_bf16 v[28:31], v[68:71], v[144:147], v[28:31]\n\t" \
  "ds_read_b64_tr_b16 v[110:111], %[ta] offset:3208\n\tglobal_load_dwordx4 v[136:139], %[vd], %[oa7] sc1\n\t" \
  "v_mfma_f32_16x16x32_bf16 v[52:55], v[72:75], v[140:143], v[52:55]\n\tds_read_b64_tr_b16 v[112:113], %[ta] offset:16\n\t" \
  "v_mfma_f32_16x16x32_bf16 v[32:35], v[72:75], v[144:147], v[32:35]\n\tds_read_b64_tr_b16 v[114:115], %[ta] offset:3216\n\t" \
  "v_mfma_f32_16x16x32_bf16 v[56:59], v[76:79], v[140:143], v[56:59]\n\tds_read_b64_tr_b16 v[116:117], %[ta] offset:24\n\t" \
  "v_mfma_f32_16x16x32_bf16 v[36:39], v[76:79], v[144:147], v[36:39]\n\tds_read_b64_tr_b16 v[118:119], %[ta] offset:3224\n\ts_waitcnt vmcnt(5)\n\t" \
  "v_max3_u32 %[tk], v148, v150, v152\n\tv_max_u32 %[tk], %[tk], v154\n\tv_cmp_eq_u32 vcc, -1, %[tk]\n\t" \
  "v_mfma_f32_16x16x32_bf16 v[60:63], v[80:83], v[140:143], v[60:63]\n\tds_read_b64_tr_b16 v[120:121], %[ta] offset:32\n\t" \
  "v_mfma_f32_16x16x32_bf16 v[40:43], v[80:83], v[144:147], v[40:43]\n\tds_read_b64_tr_b16 v[122:123], %[ta] offset:3232\n\ts_cbranch_vccnz LSs4_%=\n\t" \
  "LQs4_%=:\n\ts_waitcnt lgkmcnt(10)\n\tv_mfma_f32_16x16x32_bf16 v[44:47], v[84:87], v[148:151], v[44:47]\n\tv_add_u32 %[ta], %[so6], %[pm]\n\t" \
  "ds_read_b64_tr_b16 v[64:65], %[ta] offset:0\n\tv_mfma_f32_16x16x32_bf16 v[24:27], v[84:87], v[152:155], v[24:27]\n\t" \
  "ds_read_b64_tr_b16 v[66:67], %[ta] offset:3200\n\tglobal_load_dwordx4 v[140:143], %[vd], %[oa8] sc1\n\t" \
  "v_mfma_f32_16x16x32_bf16 v[48:51], v[88:91], v[148:151], v[48:51]\n\tds_read_b64_tr_b16 v[68:69], %[ta] offset:8\n\t" \
  "v_mfma_f32_16x16x32_bf16 v[28:31], v[88:91], v[152:155], v[28:31]\n\tds_read_b64_tr_b16 v[70:71], %[ta] offset:3208\n\t" \
  "global_load_dwordx4 v[144:147], %[vd], %[oa9] sc1\n\tv_mfma_f32_16x16x32_bf16 v[52:55], v[92:95], v[148:151], v[52:55]\n\t" \
  "ds_read_b64_tr_b16 v[72:73], %[ta] offset:16\n\tv_mfma_f32_16x16x32_bf16 v[32:35], v[92:95], v[152:155], v[32:35]\n\t" \
  "ds_read_b64_tr_b16 v[74:75], %[ta] offset:3216\n\tv_mfma_f32_16x16x32_bf16 v[56:59], v[96:99], v[148:151], v[56:59]\n\t" \
  "ds_read_b64_tr_b16 v[76:77], %[ta] offset:24\n\tv_mfma_f32_16x16x32_bf16 v[36:39], v[96:99], v[152:155], v[36:39]\n\t" \
  "ds_read_b64_tr_b16 v[78:79], %[ta] offset:3224\n\ts_waitcnt vmcnt(5)\n\tv_max_u32 %[tk], v156, v124\n\tv_cmp_eq_u32 vcc, -1, %[tk]\n\t" \
  "v_mfma_f32_16x16x32_bf16 v[60:63], v[100:103], v[148:151], v[60:63]\n\tds_read_b64_tr_b16 v[80:81], %[ta] offset:32\n\t" \
  "v_mfma_f32_16x16x32_bf16 v[40:43], v[100:103], v[152:155], v[40:43]\n\tds_read_b64_tr_b16 v[82:83], %[ta] offset:3232\n\ts_cbranch_vccnz LSs5_%=\n\t" \
  "LQs5_%=:\n\ts_waitcnt lgkmcnt(10)\n\tv_mfma_f32_16x16x32_bf16 v[44:47], v[104:107], v[156:159], v[44:47]\n\tv_add_u32 %[ta], %[so7], %[pm]\n\t" \
  "ds_read_b64_tr_b16 v[84:85], %[ta] offset:0\n\tv_mfma_f32_16x16x32_bf16 v[24:27], v[104:107], v[124:127], v[24:27]\n\t" \
  "ds_read_b64_tr_b16 v[86:87], %[ta] offset:3200\n\tglobal_load_dwordx4 v[148:151], %[vk], %[oa7] sc1\n\t" \
  "v_mfma_f32_16x16x32_bf16 v[48:51], v[108:111], v[156:159], v[48:51]\n\tds_read_b64_tr_b16 v[88:89], %[ta] offset:8\n\t" \
  "v_mfma_f32_16x16x32_bf16 v[28:31], v[108:111], v[124:127], v[28:31]\n\tds_read_b64_tr_b16 v[90:91], %[ta] offset:3208\n\t" \
  "global_load_dwordx4 v[152:155], %[vk], %[oa8] sc1\n\tv_mfma_f32_16x16x32_bf16 v[52:55], v[112:115], v[156:159], v[52:55]\n\t" \
  "ds_read_b64_tr_b16 v[92:93], %[ta] offset:16\n\tv_mfma_f32_16x16x32_bf16 v[32:35], v[112:115], v[124:127], v[32:35]\n\t" \
  "ds_read_b64_tr_b16 v[94:95], %[ta] offset:3216\n\tv_mfma_f32_16x16x32_bf16 v[56:59], v[116:119], v[156:159], v[56:59]\n\t" \
  "ds_read_b64_tr_b16 v[96:97], %[ta] offset:24\n\tv_mfma_f32_16x16x32_bf16 v[36:39], v[116:119], v[124:127], v[36:39]\n\t" \
  "ds_read_b64_tr_b16 v[98:99], %[ta] offset:3224\n\ts_waitcnt vmcnt(5)\n\tv_max_u32 %[tk], v128, v132\n\tv_cmp_eq_u32 vcc, -1, %[tk]\n\t" \
  "v_mfma_f32_16x16x32_bf16 v[60:63], v[120:123], v[156:159], v[60:63]\n\tds_read_b64_tr_b16 v[100:101], %[ta] offset:32\n\t" \
  "v_mfma_f32_16x16x32_bf16 v[40:43], v[120:123], v[124:127], v[40:43]\n\tds_read_b64_tr_b16 v[102:103], %[ta] offset:3232\n\ts_cbranch_vccnz LSs6_%=\n\t" \
  "LQs6_%=:\n\ts_waitcnt lgkmcnt(10)\n\tv_mfma_f32_16x16x32_bf16 v[44:47], v[64:67], v[128:131], v[44:47]\n\tv_add_u32 %[ta], %[so8], %[pm]\n\t" \
  "ds_read_b64_tr_b16 v[104:105], %[ta] offset:0\n\tv_mfma_f32_16x16x32_bf16 v[24:27], v[64:67], v[132:135], v[24:27]\n\t" \
  "ds_read_b64_tr_b16 v[106:107], %[ta] offset:3200\n\tglobal_load_dwordx4 v[156:159], %[vk], %[oa9] sc1\n\t" \
  "v_mfma_f32_16x16x32_bf16 v[48:51], v[68:71], v[128:131], v[48:51]\n\tds_read_b64_tr_b16 v[108:109], %[ta] offset:8\n\t" \
  "v_mfma_f32_16x16x32_bf16 v[28:31], v[68:71], v[132:135], v[28:31]\n\tds_read_b64_tr_b16 v[110:111], %[ta] offset:3208\n\t" \
  "v_mfma_f32_16x16x32_bf16 v[52:55], v[72:75], v[128:131], v[52:55]\n\tds_read_b64_tr_b16 v[112:113], %[ta] offset:16\n\t" \
  "v_mfma_f32_16x16x32_bf16 v[32:35], v[72:75], v[132:135], v[32:35]\n\tds_read_b64_tr_b16 v[114:115], %[ta] offset:3216\n\t" \
  "v_mfma_f32_16x16x32_bf16 v[56:59], v[76:79], v[128:131], v[56:59]\n\tds_read_b64_tr_b16 v[116:117], %[ta] offset:24\n\t" \
  "v_mfma_f32_16x16x32_bf16 v[36:39], v[76:79], v[132:135], v[36:39]\n\tds_read_b64_tr_b16 v[118:119], %[ta] offset:3224\n\ts_waitcnt vmcnt(4)\n\t" \
  "v_max_u32 %[tk], v136, v140\n\tv_cmp_eq_u32 vcc, -1, %[tk]\n\tv_mfma_f32_16x16x32_bf16 v[60:63], v[80:83], v[128:131], v[60:63]\n\t" \
  "ds_read_b64_tr_b16 v[120:121], %[ta] offset:32\n\tv_mfma_f32_16x16x32_bf16 v[40:43], v[80:83], v[132:135], v[40:43]\n\t" \
  "ds_read_b64_tr_b16 v[122:123], %[ta] offset:3232\n\ts_cbranch_vccnz LSd7_%=\n\tLQd7_%=:\n\ts_waitcnt lgkmcnt(10)\n\t" \
  "v_mfma_f32_16x16x32_bf16 v[44:47], v[84:87], v[136:139], v[44:47]\n\tv_add_u32 %[ta], %[so9], %[pf]\n\tds_read_b64_tr_b16 v[64:65], %[ta] offset:0\n\t" \
  "ds_read_b64_tr_b16 v[66:67], %[ta] offset:12800\n\tv_mfma_f32_16x16x32_bf16 v[48:51], v[88:91], v[136:139], v[48:51]\n\t" \
  "ds_read_b64_tr_b16 v[68:69], %[ta] offset:8\n\tds_read_b64_tr_b16 v[70:71], %[ta] offset:12808\n\t" \
  "v_mfma_f32_16x16x32_bf16 v[52:55], v[92:95], v[136:139], v[52:55]\n\tds_read_b64_tr_b16 v[72:73], %[ta] offset:16\n\t" \
  "ds_read_b64_tr_b16 v[74:75], %[ta] offset:12816\n\tv_mfma_f32_16x16x32_bf16 v[56:59], v[96:99], v[136:139], v[56:59]\n\t" \
  "ds_read_b64_tr_b16 v[76:77], %[ta] offset:24\n\tds_read_b64_tr_b16 v[78:79], %[ta] offset:12824\n\t" \
  "v_mfma_f32_16x16x32_bf16 v[60:63], v[100:103], v[136:139], v[60:63]\n\tds_read_b64_tr_b16 v[80:81], %[ta] offset:32\n\t" \
  "ds_read_b64_tr_b16 v[82:83], %[ta] offset:12832\n\ts_waitcnt lgkmcnt(10)\n\tv_mfma_f32_16x16x32_bf16 v[44:47], v[104:107], v[140:143], v[44:47]\n\t" \
  "v_mfma_f32_16x16x32_bf16 v[48:51], v[108:111], v[140:143], v[48:51]\n\tv_mfma_f32_16x16x32_bf16 v[52:55], v[112:115], v[140:143], v[52:55]\n\t" \
  "s_waitcnt vmcnt(2)\n\tv_max3_u32 %[tk], v144, v146, v148\n\tv_cmp_eq_u32 vcc, -1, %[tk]\n\t" \
  "v_mfma_f32_16x16x32_bf16 v[56:59], v[116:119], v[140:143], v[56:59]\n\tv_mfma_f32_16x16x32_bf16 v[60:63], v[120:123], v[140:143], v[60:63]\n\t" \
  "s_cbranch_vccnz LSd9_%=\n\tLQd9_%=:\n\ts_waitcnt lgkmcnt(0)\n\tv_mfma_f32_16x16x32_bf16 v[44:47], v[64:67], v[144:147], v[44:47]\n\t" \
  "ds_read2_b64 v[124:127], %[aw] offset0:0 offset1:1\n\tds_read2_b64 v[128:131], %[aw] offset0:2 offset1:3\n\t" \
  "ds_read_b64 v[132:133], %[aw] offset:32\n\tv_mov_b64 v[134:135], 0\n\tv_mfma_f32_16x16x32_bf16 v[48:51], v[68:71], v[144:147], v[48:51]\n\t" \
  "v_mfma_f32_16x16x32_bf16 v[52:55], v[72:75], v[144:147], v[52:55]\n\tv_add_u32 %[ta], %[sd], %[l16]\n\t" \
  "v_mfma_f32_16x16x32_bf16 v[56:59], v[76:79], v[144:147], v[56:59]\n\tv_mfma_f32_16x16x32_bf16 v[60:63], v[80:83], v[144:147], v[60:63]\n\t" \
  "v_mov_b32 v137, %[sc]\n\tv_mov_b32 v136, 1\n\tv_mfma_f32_16x16x32_bf16 v[24:27], v[84:87], v[148:151], v[24:27]\n\t" \
  "ds_write_b128 %[ta], v[44:47] offset:0\n\tv_mfma_f32_16x16x32_bf16 v[28:31], v[88:91], v[148:151], v[28:31]\n\t" \
  "ds_write_b128 %[ta], v[48:51] offset:1024\n\tv_mfma_f32_16x16x32_bf16 v[32:35], v[92:95], v[148:151], v[32:35]\n\t" \
  "ds_write_b128 %[ta], v[52:55] offset:2048\n\ts_waitcnt vmcnt(0)\n\tv_max3_u32 %[tk], v152, v156, v158\n\tv_cmp_eq_u32 vcc, -1, %[tk]\n\t" \
  "v_mfma_f32_16x16x32_bf16 v[36:39], v[96:99], v[148:151], v[36:39]\n\tds_write_b128 %[ta], v[56:59] offset:3072\n\t" \
  "v_mfma_f32_16x16x32_bf16 v[40:43], v[100:103], v[148:151], v[40:43]\n\tds_write_b128 %[ta], v[60:63] offset:4096\n\ts_mov_b64 exec, 1\n\t" \
  "ds_add_u32 v137, v136\n\ts_mov_b64 exec, -1\n\ts_cbranch_vccnz LSk8_%=\n\tLQk8_%=:\n\t" \
  "v_mfma_f32_16x16x32_bf16 v[24:27], v[104:107], v[152:155], v[24:27]\n\tv_mov_b64 v[94:95], 0\n\t" \
  "v_mfma_f32_16x16x32_bf16 v[28:31], v[108:111], v[152:155], v[28:31]\n\tv_xor_b32 v96, 4, v137\n\t" \
  "v_mfma_f32_16x16x32_bf16 v[32:35], v[112:115], v[152:155], v[32:35]\n\tv_add_u32 %[tk], %[sr], %[l16]\n\t" \
  "v_mfma_f32_16x16x32_bf16 v[36:39], v[116:119], v[152:155], v[36:39]\n\tv_mfma_f32_16x16x32_bf16 v[40:43], v[120:123], v[152:155], v[40:43]\n\t" \
  "v_mfma_f32_16x16x32_bf16 v[24:27], v[64:67], v[156:159], v[24:27]\n\tds_read_b32 v97, v96\n\tds_read_b128 v[44:47], %[tk] offset:0\n\t" \
  "ds_read_b128 v[48:51], %[tk] offset:1024\n\tds_read_b128 v[52:55], %[tk] offset:2048\n\tds_read_b128 v[56:59], %[tk] offset:3072\n\t" \
  "ds_read_b128 v[60:63], %[tk] offset:4096\n\tv_mfma_f32_16x16x32_bf16 v[28:31], v[68:71], v[156:159], v[28:31]\n\t" \
  "v_mfma_f32_16x16x32_bf16 v[32:35], v[72:75], v[156:159], v[32:35]\n\tv_mfma_f32_16x16x32_bf16 v[36:39], v[76:79], v[156:159], v[36:39]\n\t" \
  "v_mfma_f32_16x16x32_bf16 v[40:43], v[80:83], v[156:159], v[40:43]\n\tLD1_%=:\n\ts_waitcnt lgkmcnt(0)\n\tv_cmp_eq_u32 vcc, 0, v97\n\t" \
  "s_cbranch_vccnz LD2_%=\n\tLT_%=:\n\tv_mov_b32 v137, %[sa]\n\tds_read_b32 v138, v137 offset:16\n\tv_add_f32 v24, v24, v44\n\t" \
  "v_add_f32 v25, v25, v45\n\tv_add_f32 v26, v26, v46\n\tv_add_f32 v27, v27, v47\n\tv_cvt_pk_bf16_f32 v84, v24, v25\n\t" \
  "v_cvt_pk_bf16_f32 v85, v26, v27\n\tv_add_f32 v28, v28, v48\n\tv_add_f32 v29, v29, v49\n\tv_add_f32 v30, v30, v50\n\tv_add_f32 v31, v31, v51\n\t" \
  "v_cvt_pk_bf16_f32 v86, v28, v29\n\tv_cvt_pk_bf16_f32 v87, v30, v31\n\tv_add_f32 v32, v32, v52\n\tv_add_f32 v33, v33, v53\n\t" \
  "v_add_f32 v34, v34, v54\n\tv_add_f32 v35, v35, v55\n\tv_cvt_pk_bf16_f32 v88, v32, v33\n\tv_cvt_pk_bf16_f32 v89, v34, v35\n\t" \
  "v_mfma_f32_16x16x32_bf16 v[100:103], v[84:87], v[84:87], 0\n\tv_mfma_f32_16x16x32_bf16 v[96:99], v[124:127], v[84:87], 0\n\t" \
  "v_add_f32 v36, v36, v56\n\tv_add_f32 v37, v37, v57\n\tv_add_f32 v38, v38, v58\n\tv_add_f32 v39, v39, v59\n\tv_cvt_pk_bf16_f32 v90, v36, v37\n\t" \
  "v_cvt_pk_bf16_f32 v91, v38, v39\n\tv_add_f32 v40, v40, v60\n\tv_add_f32 v41, v41, v61\n\tv_add_f32 v42, v42, v62\n\tv_add_f32 v43, v43, v63\n\t" \
  "v_cvt_pk_bf16_f32 v92, v40, v41\n\tv_cvt_pk_bf16_f32 v93, v42, v43\n\tv_mfma_f32_16x16x32_bf16 v[100:103], v[88:91], v[88:91], v[100:103]\n\t" \
  "v_mfma_f32_16x16x32_bf16 v[96:99], v[128:131], v[88:91], v[96:99]\n\tv_mfma_f32_16x16x32_bf16 v[100:103], v[92:95], v[92:95], v[100:103]\n\t" \
  "v_mfma_f32_16x16x32_bf16 v[96:99], v[132:135], v[92:95], v[96:99]\n\ts_waitcnt lgkmcnt(0)\n\tv_sub_u32 v138, %[ra], v138\n\t" \
  "v_cmp_gt_u32 vcc, 0xa0, v138\n\tv_and_b32 %[ta], 0xff, %[l16]\n\tv_add_u32 %[tk], %[sp], %[ta]\n\tv_mov_b32 v136, 1\n\ts_cbranch_vccz LP1_%=\n\t" \
  "s_mov_b64 exec, vcc\n\tglobal_store_dwordx4 v138, v[84:87], %[cp] sc1\n\tglobal_store_dwordx4 v138, v[88:91], %[cp] offset:16 sc1\n\t" \
  "global_store_dwordx2 v138, v[92:93], %[cp] offset:32 sc1\n\tds_write_b32 v137, v136 offset:24\n\tLP1_%=:\n\ts_mov_b32 exec_lo, 0x00200002\n\t" \
  "s_mov_b32 exec_hi, 0x20000200\n\tv_mov_b32 v100, v101\n\ts_lshl_b64 exec, exec, 1\n\tv_mov_b32 v100, v102\n\ts_lshl_b64 exec, exec, 1\n\t" \
  "v_mov_b32 v100, v103\n\ts_mov_b32 exec_lo, 0x00f0000f\n\ts_mov_b32 exec_hi, 0xf0000f00\n\tv_mov_b32 v98, v100\n\t" \
  "global_store_dwordx4 %[ta], v[96:99], %[sst] sc1\n\tds_write_b128 %[tk], v[96:99]\n\ts_mov_b64 exec, -1\n\tds_write_b64 %[ra], v[84:85] offset:0\n\t" \
  "ds_write_b64 %[ra], v[86:87] offset:8\n\tds_write_b64 %[ra], v[88:89] offset:16\n\tds_write_b64 %[ra], v[90:91] offset:24\n\t" \
  "ds_write_b64 %[ra], v[92:93] offset:32\n\ts_mov_b64 exec, 1\n\tds_add_u32 v137, v136\n\ts_mov_b64 exec, -1\n\ts_setprio 0\n\ts_branch LZ_%=\n\t" \
  "LD2_%=:\n\ts_sub_u32 s43, s43, 1\n\ts_cbranch_scc1 LT_%=\n\tds_read_b32 v97, v96\n\tds_read_b128 v[44:47], %[tk] offset:0\n\t" \
  "ds_read_b128 v[48:51], %[tk] offset:1024\n\tds_read_b128 v[52:55], %[tk] offset:2048\n\tds_read_b128 v[56:59], %[tk] offset:3072\n\t" \
  "ds_read_b128 v[60:63], %[tk] offset:4096\n\ts_branch LD1_%=\n\tLH_%=:\n\ts_sleep 3\n\tglobal_load_dwordx4 v[140:143], %[vk], %[oa1] sc1\n\t" \
  "global_load_dwordx4 v[144:147], %[vk], %[oa2] sc1\n\tglobal_load_dwordx4 v[148:151], %[vk], %[oa3] sc1\n\t" \
  "global_load_dwordx4 v[152:155], %[vk], %[oa4] sc1\n\tglobal_load_dwordx4 v[156:159], %[vk], %[oa0] sc1\n\ts_cmp_eq_u32 %[w], 3\n\t" \
  "s_cbranch_scc0 LG_%=\n\tds_read_b64 v[112:113], %[ra] offset:0\n\tds_read_b64 v[114:115], %[ra] offset:8\n\t" \
  "ds_read_b64 v[116:117], %[ra] offset:16\n\tds_read_b64 v[118:119], %[ra] offset:24\n\tds_read_b64 v[120:121], %[ra] offset:32\n\t" \
  "ds_read2_b64 v[124:127], %[aw] offset0:0 offset1:1\n\tds_read2_b64 v[128:131], %[aw] offset0:2 offset1:3\n\t" \
  "ds_read_b64 v[132:133], %[aw] offset:32\n\tv_mov_b64 v[134:135], 0\n\ts_waitcnt lgkmcnt(3)\n\tv_lshlrev_b32 v24, 16, v112\n\t" \
  "v_and_b32 v25, 0xffff0000, v112\n\tv_lshlrev_b32 v26, 16, v113\n\tv_and_b32 v27, 0xffff0000, v113\n\tv_lshlrev_b32 v28, 16, v114\n\t" \
  "v_and_b32 v29, 0xffff0000, v114\n\tv_lshlrev_b32 v30, 16, v115\n\tv_and_b32 v31, 0xffff0000, v115\n\tv_lshlrev_b32 v32, 16, v116\n\t" \
  "v_and_b32 v33, 0xffff0000, v116\n\tv_lshlrev_b32 v34, 16, v117\n\tv_and_b32 v35, 0xffff0000, v117\n\tv_lshlrev_b32 v36, 16, v118\n\t" \
  "v_and_b32 v37, 0xffff0000, v118\n\tv_lshlrev_b32 v38, 16, v119\n\tv_and_b32 v39, 0xffff0000, v119\n\tv_lshlrev_b32 v40, 16, v120\n\t" \
  "v_and_b32 v41, 0xffff0000, v120\n\tv_lshlrev_b32 v42, 16, v121\n\tv_and_b32 v43, 0xffff0000, v121\n\ts_branch LG2_%=\n\tLG_%=:\n\t" \
  "v_mov_b64 v[24:25], 0\n\tv_mov_b64 v[26:27], 0\n\tv_mov_b64 v[28:29], 0\n\tv_mov_b64 v[30:31], 0\n\tv_mov_b64 v[32:33], 0\n\tv_mov_b64 v[34:35], 0\n\t" \
  "v_mov_b64 v[36:37], 0\n\tv_mov_b64 v[38:39], 0\n\tv_mov_b64 v[40:41], 0\n\tv_mov_b64 v[42:43], 0\n\tLG2_%=:\n\tv_add_u32 %[ta], %[so1], %[pm]\n\t" \
  "ds_read_b64_tr_b16 v[64:65], %[ta] offset:0\n\tds_read_b64_tr_b16 v[66:67], %[ta] offset:3200\n\tds_read_b64_tr_b16 v[68:69], %[ta] offset:8\n\t" \
  "ds_read_b64_tr_b16 v[70:71], %[ta] offset:3208\n\tds_read_b64_tr_b16 v[72:73], %[ta] offset:16\n\tds_read_b64_tr_b16 v[74:75], %[ta] offset:3216\n\t" \
  "ds_read_b64_tr_b16 v[76:77], %[ta] offset:24\n\tds_read_b64_tr_b16 v[78:79], %[ta] offset:3224\n\tds_read_b64_tr_b16 v[80:81], %[ta] offset:32\n\t" \
  "ds_read_b64_tr_b16 v[82:83], %[ta] offset:3232\n\tv_add_u32 %[ta], %[so2], %[pm]\n\tds_read_b64_tr_b16 v[84:85], %[ta] offset:0\n\t" \
  "ds_read_b64_tr_b16 v[86:87], %[ta] offset:3200\n\tds_read_b64_tr_b16 v[88:89], %[ta] offset:8\n\tds_read_b64_tr_b16 v[90:91], %[ta] offset:3208\n\t" \
  "ds_read_b64_tr_b16 v[92:93], %[ta] offset:16\n\tds_read_b64_tr_b16 v[94:95], %[ta] offset:3216\n\tds_read_b64_tr_b16 v[96:97], %[ta] offset:24\n\t" \
  "ds_read_b64_tr_b16 v[98:99], %[ta] offset:3224\n\tds_read_b64_tr_b16 v[100:101], %[ta] offset:32\n\t" \
  "ds_read_b64_tr_b16 v[102:103], %[ta] offset:3232\n\ts_waitcnt vmcnt(4)\n\tv_cmp_eq_u32 vcc, -1, v140\n\ts_cbranch_vccnz LSh1_%=\n\tLQh1_%=:\n\t" \
  "s_waitcnt lgkmcnt(10)\n\tv_mfma_f32_16x16x32_bf16 v[24:27], v[64:67], v[140:143], v[24:27]\n\tv_add_u32 %[ta], %[so3], %[pm]\n\t" \
  "ds_read_b64_tr_b16 v[104:105], %[ta] offset:0\n\tds_read_b64_tr_b16 v[106:107], %[ta] offset:3200\n\t" \
  "v_mfma_f32_16x16x32_bf16 v[28:31], v[68:71], v[140:143], v[28:31]\n\tds_read_b64_tr_b16 v[108:109], %[ta] offset:8\n\t" \
  "ds_read_b64_tr_b16 v[110:111], %[ta] offset:3208\n\tv_mfma_f32_16x16x32_bf16 v[32:35], v[72:75], v[140:143], v[32:35]\n\t" \
  "ds_read_b64_tr_b16 v[112:113], %[ta] offset:16\n\tds_read_b64_tr_b16 v[114:115], %[ta] offset:3216\n\t" \
  "v_mfma_f32_16x16x32_bf16 v[36:39], v[76:79], v[140:143], v[36:39]\n\tds_read_b64_tr_b16 v[116:117], %[ta] offset:24\n\t" \
  "ds_read_b64_tr_b16 v[118:119], %[ta] offset:3224\n\ts_waitcnt vmcnt(3)\n\tv_cmp_eq_u32 vcc, -1, v144\n\t" \
  "v_mfma_f32_16x16x32_bf16 v[40:43], v[80:83], v[140:143], v[40:43]\n\tds_read_b64_tr_b16 v[120:121], %[ta] offset:32\n\t" \
  "ds_read_b64_tr_b16 v[122:123], %[ta] offset:3232\n\ts_cbranch_vccnz LSh2_%=\n\tLQh2_%=:\n\ts_waitcnt lgkmcnt(10)\n\t" \
  "v_mfma_f32_16x16x32_bf16 v[24:27], v[84:87], v[144:147], v[24:27]\n\tv_add_u32 %[ta], %[so4], %[pf]\n\tds_read_b64_tr_b16 v[64:65], %[ta] offset:0\n\t" \
  "ds_read_b64_tr_b16 v[66:67], %[ta] offset:12800\n\tv_mfma_f32_16x16x32_bf16 v[28:31], v[88:91], v[144:147], v[28:31]\n\t" \
  "ds_read_b64_tr_b16 v[68:69], %[ta] offset:8\n\tds_read_b64_tr_b16 v[70:71], %[ta] offset:12808\n\t" \
  "v_mfma_f32_16x16x32_bf16 v[32:35], v[92:95], v[144:147], v[32:35]\n\tds_read_b64_tr_b16 v[72:73], %[ta] offset:16\n\t" \
  "ds_read_b64_tr_b16 v[74:75], %[ta] offset:12816\n\tv_mfma_f32_16x16x32_bf16 v[36:39], v[96:99], v[144:147], v[36:39]\n\t" \
  "ds_read_b64_tr_b16 v[76:77], %[ta] offset:24\n\tds_read_b64_tr_b16 v[78:79], %[ta] offset:12824\n\ts_waitcnt vmcnt(2)\n\t" \
  "v_cmp_eq_u32 vcc, -1, v148\n\tv_mfma_f32_16x16x32_bf16 v[40:43], v[100:103], v[144:147], v[40:43]\n\tds_read_b64_tr_b16 v[80:81], %[ta] offset:32\n\t" \
  "ds_read_b64_tr_b16 v[82:83], %[ta] offset:12832\n\ts_cbranch_vccnz LSh3_%=\n\tLQh3_%=:\n\ts_waitcnt lgkmcnt(10)\n\t" \
  "v_mfma_f32_16x16x32_bf16 v[24:27], v[104:107], v[148:151], v[24:27]\n\tv_add_u32 %[ta], %[so0], %[pm]\n\t" \
  "ds_read_b64_tr_b16 v[84:85], %[ta] offset:0\n\tds_read_b64_tr_b16 v[86:87], %[ta] offset:3200\n\t" \
  "v_mfma_f32_16x16x32_bf16 v[28:31], v[108:111], v[148:151], v[28:31]\n\tds_read_b64_tr_b16 v[88:89], %[ta] offset:8\n\t" \
  "ds_read_b64_tr_b16 v[90:91], %[ta] offset:3208\n\tv_mfma_f32_16x16x32_bf16 v[32:35], v[112:115], v[148:151], v[32:35]\n\t" \
  "ds_read_b64_tr_b16 v[92:93], %[ta] offset:16\n\tds_read_b64_tr_b16 v[94:95], %[ta] offset:3216\n\t" \
  "v_mfma_f32_16x16x32_bf16 v[36:39], v[116:119], v[148:151], v[36:39]\n\tds_read_b64_tr_b16 v[96:97], %[ta] offset:24\n\t" \
  "ds_read_b64_tr_b16 v[98:99], %[ta] offset:3224\n\ts_waitcnt vmcnt(1)\n\tv_max_u32 %[tk], v152, v154\n\tv_cmp_eq_u32 vcc, -1, %[tk]\n\t" \
  "v_mfma_f32_16x16x32_bf16 v[40:43], v[120:123], v[148:151], v[40:43]\n\tds_read_b64_tr_b16 v[100:101], %[ta] offset:32\n\t" \
  "ds_read_b64_tr_b16 v[102:103], %[ta] offset:3232\n\ts_cbranch_vccnz LSh4_%=\n\tLQh4_%=:\n\ts_waitcnt lgkmcnt(10)\n\t" \
  "v_mfma_f32_16x16x32_bf16 v[24:27], v[64:67], v[152:155], v[24:27]\n\tv_mfma_f32_16x16x32_bf16 v[28:31], v[68:71], v[152:155], v[28:31]\n\t" \
  "v_mfma_f32_16x16x32_bf16 v[32:35], v[72:75], v[152:155], v[32:35]\n\tv_mfma_f32_16x16x32_bf16 v[36:39], v[76:79], v[152:155], v[36:39]\n\t" \
  "s_waitcnt vmcnt(0)\n\tv_cmp_eq_u32 vcc, -1, v156\n\tv_mfma_f32_16x16x32_bf16 v[40:43], v[80:83], v[152:155], v[40:43]\n\ts_cbranch_vccnz LSh0_%=\n\t" \
  "LQh0_%=:\n\ts_waitcnt lgkmcnt(0)\n\tv_mfma_f32_16x16x32_bf16 v[24:27], v[84:87], v[156:159], v[24:27]\n\t" \
  "v_mfma_f32_16x16x32_bf16 v[28:31], v[88:91], v[156:159], v[28:31]\n\tv_mfma_f32_16x16x32_bf16 v[32:35], v[92:95], v[156:159], v[32:35]\n\t" \
  "v_mfma_f32_16x16x32_bf16 v[36:39], v[96:99], v[156:159], v[36:39]\n\tv_mfma_f32_16x16x32_bf16 v[40:43], v[100:103], v[156:159], v[40:43]\n\t" \
  "s_cmp_eq_u32 %[w], 3\n\ts_cbranch_scc1 LF_%=\n\ts_setprio 3\n\tv_add_u32 %[ta], %[sd], %[l16]\n\tv_mov_b32 v137, %[sc]\n\tv_mov_b32 v136, 1\n\t" \
  "ds_write_b128 %[ta], v[24:27] offset:0\n\tds_write_b128 %[ta], v[28:31] offset:1024\n\tds_write_b128 %[ta], v[32:35] offset:2048\n\t" \
  "ds_write_b128 %[ta], v[36:39] offset:3072\n\tds_write_b128 %[ta], v[40:43] offset:4096\n\ts_mov_b64 exec, 1\n\tds_add_u32 v137, v136\n\t" \
  "s_mov_b64 exec, -1\n\ts_branch LZ_%=\n\tLF_%=:\n\ts_setprio 3\n\tv_mov_b64 v[94:95], 0\n\tv_mov_b32 v96, %[sc]\n\tv_add_u32 %[ta], %[sr], %[l16]\n\t" \
  "LF1_%=:\n\tds_read_b32 v97, v96\n\ts_waitcnt lgkmcnt(0)\n\tv_cmp_gt_u32 vcc, 3, v97\n\ts_cbranch_vccz LF2_%=\n\ts_sleep 1\n\ts_sub_u32 s43, s43, 1\n\t" \
  "s_cbranch_scc0 LF1_%=\n\tLF2_%=:\n\tds_read_b128 v[64:67], %[ta] offset:0\n\tds_read_b128 v[68:71], %[ta] offset:1024\n\t" \
  "ds_read_b128 v[72:75], %[ta] offset:2048\n\tds_read_b128 v[76:79], %[ta] offset:3072\n\tds_read_b128 v[80:83], %[ta] offset:4096\n\t" \
  "ds_read_b128 v[104:107], %[ta] offset:5120\n\tds_read_b128 v[108:111], %[ta] offset:6144\n\tds_read_b128 v[112:115], %[ta] offset:7168\n\t" \
  "ds_read_b128 v[116:119], %[ta] offset:8192\n\tds_read_b128 v[120:123], %[ta] offset:9216\n\tds_read_b128 v[44:47], %[ta] offset:10240\n\t" \
  "ds_read_b128 v[48:51], %[ta] offset:11264\n\tds_read_b128 v[52:55], %[ta] offset:12288\n\tds_read_b128 v[56:59], %[ta] offset:13312\n\t" \
  "ds_read_b128 v[60:63], %[ta] offset:14336\n\ts_waitcnt lgkmcnt(10)\n\tv_pk_add_f32 v[24:25], v[24:25], v[64:65]\n\t" \
  "v_pk_add_f32 v[26:27], v[26:27], v[66:67]\n\tv_pk_add_f32 v[28:29], v[28:29], v[68:69]\n\tv_pk_add_f32 v[30:31], v[30:31], v[70:71]\n\t" \
  "v_pk_add_f32 v[32:33], v[32:33], v[72:73]\n\tv_pk_add_f32 v[34:35], v[34:35], v[74:75]\n\tv_pk_add_f32 v[36:37], v[36:37], v[76:77]\n\t" \
  "v_pk_add_f32 v[38:39], v[38:39], v[78:79]\n\tv_pk_add_f32 v[40:41], v[40:41], v[80:81]\n\tv_pk_add_f32 v[42:43], v[42:43], v[82:83]\n\t" \
  "s_waitcnt lgkmcnt(5)\n\tv_pk_add_f32 v[24:25], v[24:25], v[104:105]\n\tv_pk_add_f32 v[26:27], v[26:27], v[106:107]\n\t" \
  "v_pk_add_f32 v[28:29], v[28:29], v[108:109]\n\tv_pk_add_f32 v[30:31], v[30:31], v[110:111]\n\tv_pk_add_f32 v[32:33], v[32:33], v[112:113]\n\t" \
  "v_pk_add_f32 v[34:35], v[34:35], v[114:115]\n\tv_pk_add_f32 v[36:37], v[36:37], v[116:117]\n\tv_pk_add_f32 v[38:39], v[38:39], v[118:119]\n\t" \
  "v_pk_add_f32 v[40:41], v[40:41], v[120:121]\n\tv_pk_add_f32 v[42:43], v[42:43], v[122:123]\n\ts_waitcnt lgkmcnt(0)\n\ts_branch LT_%=\n\tLSs1_%=:\n\t" \
  "s_sub_u32 s43, s43, 1\n\ts_cbranch_scc1 LQs1_%=\n\ts_sleep %[oback]\n\tglobal_load_dwordx4 v[124:127], %[vd], %[oa1] sc1\n\t" \
  "global_load_dwordx4 v[128:131], %[vk], %[oa1] sc1\n\ts_waitcnt vmcnt(0)\n\tv_max_u32 %[tk], v124, v128\n\tv_cmp_eq_u32 vcc, -1, %[tk]\n\t" \
  "s_cbranch_vccnz LSs1_%=\n\ts_branch LQs1_%=\n\tLSs2_%=:\n\ts_sub_u32 s43, s43, 1\n\ts_cbranch_scc1 LQs2_%=\n\ts_sleep %[oback]\n\t" \
  "global_load_dwordx4 v[132:135], %[vd], %[oa2] sc1\n\tglobal_load_dwordx4 v[136:139], %[vk], %[oa2] sc1\n\ts_waitcnt vmcnt(0)\n\t" \
  "v_max_u32 %[tk], v132, v136\n\tv_cmp_eq_u32 vcc, -1, %[tk]\n\ts_cbranch_vccnz LSs2_%=\n\ts_branch LQs2_%=\n\tLSs3_%=:\n\ts_sub_u32 s43, s43, 1\n\t" \
  "s_cbranch_scc1 LQs3_%=\n\ts_sleep %[oback]\n\tglobal_load_dwordx4 v[140:143], %[vd], %[oa3] sc1\n\t" \
  "global_load_dwordx4 v[144:147], %[vk], %[oa3] sc1\n\ts_waitcnt vmcnt(0)\n\tv_max_u32 %[tk], v140, v144\n\tv_cmp_eq_u32 vcc, -1, %[tk]\n\t" \
  "s_cbranch_vccnz LSs3_%=\n\ts_branch LQs3_%=\n\tLSs4_%=:\n\ts_sub_u32 s43, s43, 1\n\ts_cbranch_scc1 LQs4_%=\n\ts_sleep %[oback]\n\t" \
  "global_load_dwordx4 v[148:151], %[vd], %[oa4] sc1\n\tglobal_load_dwordx4 v[152:155], %[vk], %[oa4] sc1\n\ts_waitcnt vmcnt(0)\n\t" \
  "v_max3_u32 %[tk], v148, v150, v152\n\tv_max_u32 %[tk], %[tk], v154\n\tv_cmp_eq_u32 vcc, -1, %[tk]\n\ts_cbranch_vccnz LSs4_%=\n\ts_branch LQs4_%=\n\t" \
  "LSs5_%=:\n\ts_sub_u32 s43, s43, 1\n\ts_cbranch_scc1 LQs5_%=\n\ts_sleep %[oback]\n\tglobal_load_dwordx4 v[156:159], %[vd], %[oa5] sc1\n\t" \
  "global_load_dwordx4 v[124:127], %[vk], %[oa5] sc1\n\ts_waitcnt vmcnt(0)\n\tv_max_u32 %[tk], v156, v124\n\tv_cmp_eq_u32 vcc, -1, %[tk]\n\t" \
  "s_cbranch_vccnz LSs5_%=\n\ts_branch LQs5_%=\n\tLSs6_%=:\n\ts_sub_u32 s43, s43, 1\n\ts_cbranch_scc1 LQs6_%=\n\ts_sleep %[oback]\n\t" \
  "global_load_dwordx4 v[128:131], %[vd], %[oa6] sc1\n\tglobal_load_dwordx4 v[132:135], %[vk], %[oa6] sc1\n\ts_waitcnt vmcnt(0)\n\t" \
  "v_max_u32 %[tk], v128, v132\n\tv_cmp_eq_u32 vcc, -1, %[tk]\n\ts_cbranch_vccnz LSs6_%=\n\ts_branch LQs6_%=\n\tLSd7_%=:\n\ts_sub_u32 s43, s43, 1\n\t" \
  "s_cbranch_scc1 LQd7_%=\n\ts_sleep %[oback]\n\tglobal_load_dwordx4 v[136:139], %[vd], %[oa7] sc1\n\t" \
  "global_load_dwordx4 v[140:143], %[vd], %[oa8] sc1\n\ts_waitcnt vmcnt(0)\n\tv_max_u32 %[tk], v136, v140\n\tv_cmp_eq_u32 vcc, -1, %[tk]\n\t" \
  "s_cbranch_vccnz LSd7_%=\n\ts_branch LQd7_%=\n\tLSd9_%=:\n\ts_sub_u32 s43, s43, 1\n\ts_cbranch_scc1 LQd9_%=\n\ts_sleep %[oback]\n\t" \
  "global_load_dwordx4 v[144:147], %[vd], %[oa9] sc1\n\tglobal_load_dwordx4 v[148:151], %[vk], %[oa7] sc1\n\ts_waitcnt vmcnt(0)\n\t" \
  "v_max3_u32 %[tk], v144, v146, v148\n\tv_cmp_eq_u32 vcc, -1, %[tk]\n\ts_cbranch_vccnz LSd9_%=\n\ts_branch LQd9_%=\n\tLSk8_%=:\n\t" \
  "s_sub_u32 s43, s43, 1\n\ts_cbranch_scc1 LQk8_%=\n\ts_sleep %[oback]\n\tglobal_load_dwordx4 v[152:155], %[vk], %[oa8] sc1\n\t" \
  "global_load_dwordx4 v[156:159], %[vk], %[oa9] sc1\n\ts_waitcnt vmcnt(0)\n\tv_max3_u32 %[tk], v152, v156, v158\n\tv_cmp_eq_u32 vcc, -1, %[tk]\n\t" \
  "s_cbranch_vccnz LSk8_%=\n\ts_branch LQk8_%=\n\tLSh1_%=:\n\ts_sub_u32 s43, s43, 1\n\ts_cbranch_scc1 LQh1_%=\n\ts_sleep %[oback]\n\t" \
  "global_load_dwordx4 v[140:143], %[vk], %[oa1] sc1\n\ts_waitcnt vmcnt(0)\n\tv_cmp_eq_u32 vcc, -1, v140\n\ts_cbranch_vccnz LSh1_%=\n\t" \
  "s_branch LQh1_%=\n\tLSh2_%=:\n\ts_sub_u32 s43, s43, 1\n\ts_cbranch_scc1 LQh2_%=\n\ts_sleep %[oback]\n\t" \
  "global_load_dwordx4 v[144:147], %[vk], %[oa2] sc1\n\ts_waitcnt vmcnt(0)\n\tv_cmp_eq_u32 vcc, -1, v144\n\ts_cbranch_vccnz LSh2_%=\n\t" \
  "s_branch LQh2_%=\n\tLSh3_%=:\n\ts_sub_u32 s43, s43, 1\n\ts_cbranch_scc1 LQh3_%=\n\ts_sleep %[oback]\n\t" \
  "global_load_dwordx4 v[148:151], %[vk], %[oa3] sc1\n\ts_waitcnt vmcnt(0)\n\tv_cmp_eq_u32 vcc, -1, v148\n\ts_cbranch_vccnz LSh3_%=\n\t" \
  "s_branch LQh3_%=\n\tLSh4_%=:\n\ts_sub_u32 s43, s43, 1\n\ts_cbranch_scc1 LQh4_%=\n\ts_sleep %[oback]\n\t" \
  "global_load_dwordx4 v[152:155], %[vk], %[oa4] sc1\n\ts_waitcnt vmcnt(0)\n\tv_max_u32 %[tk], v152, v154\n\tv_cmp_eq_u32 vcc, -1, %[tk]\n\t" \
  "s_cbranch_vccnz LSh4_%=\n\ts_branch LQh4_%=\n\tLSh0_%=:\n\ts_sub_u32 s43, s43, 1\n\ts_cbranch_scc1 LQh0_%=\n\ts_sleep %[oback]\n\t" \
  "global_load_dwordx4 v[156:159], %[vk], %[oa0] sc1\n\ts_waitcnt vmcnt(0)\n\tv_cmp_eq_u32 vcc, -1, v156\n\ts_cbranch_vccnz LSh0_%=\n\t" \
  "s_branch LQh0_%=\n\tLZ_%=:\n\t" \
  ""
#define COL_ASM \
  "s_mov_b32 s43, %[cap]\n\tv_xor_b32 v101, 0x400, %[lq]\n\tv_xor_b32 v102, 0x800, %[lq]\n\tv_xor_b32 v103, 0xc00, %[lq]\n\t" \
  "v_xor_b32 v104, 0x1000, %[lq]\n\tv_xor_b32 v105, 0x1400, %[lq]\n\tv_xor_b32 v106, 0x1800, %[lq]\n\tv_xor_b32 v107, 0x1c00, %[lq]\n\tLC0_%=:\n\t" \
  "ds_read_b32 v96, %[ac]\n\ts_waitcnt lgkmcnt(0)\n\tv_cmp_gt_u32 vcc, %[nf], v96\n\ts_cbranch_vccz LC1_%=\n\ts_sleep 1\n\ts_sub_u32 s43, s43, 1\n\t" \
  "s_cbranch_scc0 LC0_%=\n\tLC1_%=:\n\ts_setprio 2\n\ts_sleep %[pdel]\n\tglobal_load_dwordx4 v[68:71], v101, %[sp] sc1\n\t" \
  "global_load_dwordx4 v[72:75], v102, %[sp] sc1\n\tglobal_load_dwordx4 v[76:79], v103, %[sp] sc1\n\tglobal_load_dwordx4 v[80:83], v104, %[sp] sc1\n\t" \
  "global_load_dwordx4 v[84:87], v105, %[sp] sc1\n\tglobal_load_dwordx4 v[88:91], v106, %[sp] sc1\n\tglobal_load_dwordx4 v[92:95], v107, %[sp] sc1\n\t" \
  "ds_read_b128 v[64:67], %[ao]\n\ts_waitcnt lgkmcnt(0)\n\tLC2_%=:\n\ts_waitcnt vmcnt(4)\n\tv_pk_add_f32 v[108:109], v[64:65], v[68:69]\n\t" \
  "v_add_f32 v110, v66, v70\n\tv_pk_add_f32 v[112:113], v[72:73], v[76:77]\n\tv_add_f32 v114, v74, v78\n\t" \
  "v_pk_add_f32 v[108:109], v[108:109], v[112:113]\n\tv_add_f32 v110, v110, v114\n\ts_waitcnt vmcnt(0)\n\tv_pk_add_f32 v[116:117], v[80:81], v[84:85]\n\t" \
  "v_add_f32 v118, v82, v86\n\tv_pk_add_f32 v[120:121], v[88:89], v[92:93]\n\tv_add_f32 v122, v90, v94\n\t" \
  "v_pk_add_f32 v[116:117], v[116:117], v[120:121]\n\tv_add_f32 v118, v118, v122\n\tv_pk_add_f32 v[108:109], v[108:109], v[116:117]\n\t" \
  "v_add_f32 v110, v110, v118\n\tv_mul_f32 v112, 0x3acccccd, v108\n\tv_add_f32 v111, v108, v109\n\tv_mul_f32 v113, v112, v112\n\t" \
  "v_cmp_u_f32 vcc, v111, v110\n\tv_fma_f32 v113, v110, %[rdm], -v113\n\ts_cbranch_vccnz LC5_%=\n\tLC3_%=:\n\tv_max_f32 v113, 0, v113\n\t" \
  "v_cvt_pk_bf16_f32 v113, 0, v113\n\tv_add_f32 v113, 0x3727c5ac, v113\n\tv_cvt_pk_bf16_f32 v113, 0, v113\n\tv_sqrt_f32 v113, v113\n\t" \
  "v_cvt_pk_bf16_f32 v112, 0, v112\n\tv_cvt_pk_bf16_f32 v113, 0, v113\n\tv_rcp_f32 v113, v113\n\tv_fma_f32 v114, -%[swr], v112, v109\n\t" \
  "v_mul_f32 v114, v114, v113\n\tv_ashrrev_i32 v115, 31, v114\n\tv_or_b32 v115, 0x80000000, v115\n\tv_xor_b32 v114, v114, v115\n\t" \
  "v_and_or_b32 v114, v114, %[msk], %[rid]\n\ts_nop 1\n\tv_max_u32_dpp v114, v114, v114 quad_perm:[1,0,3,2] row_mask:0xf bank_mask:0xf bound_ctrl:1\n\t" \
  "ds_write_b64 %[at], v[112:113]\n\ts_nop 0\n\tv_max_u32_dpp v114, v114, v114 quad_perm:[2,3,0,1] row_mask:0xf bank_mask:0xf bound_ctrl:1\n\ts_nop 1\n\t" \
  "v_max_u32_dpp v114, v114, v114 row_half_mirror row_mask:0xf bank_mask:0xf bound_ctrl:1\n\ts_nop 1\n\t" \
  "v_max_u32_dpp v114, v114, v114 row_mirror row_mask:0xf bank_mask:0xf bound_ctrl:1\n\ts_nop 0\n\tv_readlane_b32 s40, v114, 0\n\t" \
  "v_readlane_b32 s41, v114, 16\n\tv_readlane_b32 s42, v114, 32\n\ts_max_u32 s40, s40, s41\n\ts_max_u32 s40, s40, s42\n\tv_mov_b32 v98, s40\n\t" \
  "s_mov_b32 exec_lo, 0x01010101\n\ts_mov_b32 exec_hi, 0x0101\n\tglobal_store_dwordx2 %[zr], v[98:99], %[kp] sc1\n\tds_write_b32 %[ak], v98\n\t" \
  "s_mov_b64 exec, -1\n\ts_and_b32 s41, s40, 0x7ff\n\ts_sub_u32 s41, %[rb], s41\n\ts_mul_i32 s42, s41, 160\n\tv_add_u32 v115, s42, %[ah]\n\t" \
  "ds_read_b64 v[116:117], v115\n\tv_readlane_b32 s44, v112, s41\n\tv_readlane_b32 s46, v113, s41\n\ts_waitcnt lgkmcnt(0)\n\ts_mov_b32 exec_hi, 0\n\t" \
  "v_lshlrev_b32 v118, 16, v116\n\tv_and_b32 v119, 0xffff0000, v116\n\tv_lshlrev_b32 v120, 16, v117\n\tv_and_b32 v121, 0xffff0000, v117\n\t" \
  "v_pk_add_f32 v[122:123], v[118:119], s[44:45] op_sel_hi:[1,0] neg_lo:[0,1] neg_hi:[0,1]\n\t" \
  "v_pk_add_f32 v[114:115], v[120:121], s[44:45] op_sel_hi:[1,0] neg_lo:[0,1] neg_hi:[0,1]\n\t" \
  "v_pk_fma_f32 v[118:119], v[122:123], s[46:47], v[118:119] op_sel_hi:[1,0,1]\n\t" \
  "v_pk_fma_f32 v[120:121], v[114:115], s[46:47], v[120:121] op_sel_hi:[1,0,1]\n\tv_cvt_pk_bf16_f32 v116, v118, v119\n\t" \
  "v_cvt_pk_bf16_f32 v117, v120, v121\n\ts_mov_b32 exec_hi, -1\n\tglobal_store_dwordx2 %[co], v[116:117], %[cp] sc1\n\ts_branch LC9_%=\n\tLC5_%=:\n\t" \
  "s_sub_u32 s43, s43, 1\n\ts_cbranch_scc1 LC3_%=\n\ts_mov_b32 s40, 0\n\tv_max3_u32 v96, v68, v69, v70\n\tv_cmp_eq_u32 vcc, -1, v96\n\t" \
  "s_cbranch_vccz LC61_%=\n\tglobal_load_dwordx4 v[68:71], v101, %[sp] sc1\n\ts_mov_b32 s40, 1\n\tLC61_%=:\n\tv_max3_u32 v96, v72, v73, v74\n\t" \
  "v_cmp_eq_u32 vcc, -1, v96\n\ts_cbranch_vccz LC62_%=\n\tglobal_load_dwordx4 v[72:75], v102, %[sp] sc1\n\ts_mov_b32 s40, 1\n\tLC62_%=:\n\t" \
  "v_max3_u32 v96, v76, v77, v78\n\tv_cmp_eq_u32 vcc, -1, v96\n\ts_cbranch_vccz LC63_%=\n\tglobal_load_dwordx4 v[76:79], v103, %[sp] sc1\n\t" \
  "s_mov_b32 s40, 1\n\tLC63_%=:\n\tv_max3_u32 v96, v80, v81, v82\n\tv_cmp_eq_u32 vcc, -1, v96\n\ts_cbranch_vccz LC64_%=\n\t" \
  "global_load_dwordx4 v[80:83], v104, %[sp] sc1\n\ts_mov_b32 s40, 1\n\tLC64_%=:\n\tv_max3_u32 v96, v84, v85, v86\n\tv_cmp_eq_u32 vcc, -1, v96\n\t" \
  "s_cbranch_vccz LC65_%=\n\tglobal_load_dwordx4 v[84:87], v105, %[sp] sc1\n\ts_mov_b32 s40, 1\n\tLC65_%=:\n\tv_max3_u32 v96, v88, v89, v90\n\t" \
  "v_cmp_eq_u32 vcc, -1, v96\n\ts_cbranch_vccz LC66_%=\n\tglobal_load_dwordx4 v[88:91], v106, %[sp] sc1\n\ts_mov_b32 s40, 1\n\tLC66_%=:\n\t" \
  "v_max3_u32 v96, v92, v93, v94\n\tv_cmp_eq_u32 vcc, -1, v96\n\ts_cbranch_vccz LC67_%=\n\tglobal_load_dwordx4 v[92:95], v107, %[sp] sc1\n\t" \
  "s_mov_b32 s40, 1\n\tLC67_%=:\n\ts_cmp_eq_u32 s40, 0\n\ts_cbranch_scc1 LC3_%=\n\ts_waitcnt vmcnt(0)\n\ts_branch LC2_%=\n\tLC9_%=:\n\t" \
  ""
extern "C" __global__ __launch_bounds__(768, 3) void fused_block(const u16* x, const u16* wqkv, const u16* wo, const u16* w_r,
                                                   const u16* w_gate, const u16* w_up, const u16* w_down, u16* out,
                                                   char* ws) {
  extern __shared__ __attribute__((aligned(16))) char smem[];
  asm volatile(
      "v_readfirstlane_b32 s44, %[tid]\n\tv_and_b32 v4, 63, %[tid]\n\ts_lshr_b32 s45, %[bid], 3\n\ts_lshr_b32 s44, s44, 6\n\t"
      "s_cmp_eq_u32 s44, 0\n\ts_cbranch_scc0 1f\n\t"
      "s_getpc_b64 s[46:47]\n\ts_mul_i32 s48, s45, 10\n\tv_add_lshl_u32 v5, v4, s48, 7\n\tv_min_u32 v5, %[cb], v5\n\ts_mov_b32 m0, %[tch]\n\t"
      "s_mov_b64 exec, 0x3ff\n\tglobal_load_lds_dword v5, s[46:47]\n\ts_mov_b64 exec, -1\n"
      "1:\n\ts_cmp_ge_u32 s45, 26\n\ts_cbranch_scc1 2f\n\ts_load_dwordx4 s[40:43], %[ka], 0\n\ts_load_dword s50, %[ka], 64\n\t"
      "s_sub_u32 s44, s44, 4\n\ts_cbranch_scc1 3f\n\t"
      "v_lshrrev_b32 v5, 5, v4\n\ts_bitcmp1_b32 %[bid], 0\n\ts_cselect_b32 s46, 26, 0\n\tv_lshl_add_u32 v6, s44, 1, v5\n\ts_add_u32 s45, s45, s46\n\t"
      "v_and_b32 v6, 7, v6\n\ts_and_b32 s46, s45, 3\n\ts_lshr_b32 s45, s45, 2\n\tv_and_b32 v7, 31, v4\n\ts_lshr_b32 s47, s45, 1\n\t"
      "v_lshlrev_b32 v6, 1, v6\n\ts_mul_i32 s48, s46, 3\n\ts_add_u32 s47, s47, s45\n\tv_xor_b32 v6, v6, v7\n\ts_add_u32 s47, s47, s48\n\t"
      "v_min_u32 v6, 29, v6\n\ts_mul_i32 s48, s47, 26\n\ts_lshr_b32 s48, s48, 8\n\tv_mul_u32_u24 v7, 26, v6\n\ts_mul_i32 s48, s48, 10\n\t"
      "v_lshrrev_b32 v7, 8, v7\n\ts_sub_u32 s47, s47, s48\n\ts_bitcmp1_b32 %[bid], 2\n\ts_cselect_b32 s48, 13, 0\n\t"
      "v_mul_u32_u24 v7, 70, v7\n\ts_add_u32 s45, s45, s48\n\ts_and_b32 s48, %[bid], 2\n\ts_lshl1_add_u32 s46, s48, s46\n\t"
      "v_mul_u32_u24 v5, 0xf0, v5\n\ts_lshl_b32 s48, s44, 2\n\ts_lshr_b32 s48, 0xec64a820, s48\n\ts_and_b32 s48, s48, 15\n\t"
      "v_add3_u32 v5, v5, v6, v7\n\ts_lshl_b32 s49, s47, 6\n\ts_add_u32 s48, s48, s49\n\ts_mul_i32 s48, s48, 24\n\ts_add_u32 s48, s48, s46\n\t"
      "v_lshrrev_b32 v6, 3, v4\n\ts_mul_i32 s48, s48, 10\n\tv_lshl_add_u32 v6, s44, 3, v6\n\tv_add_lshl_u32 v5, v5, s48, 4\n\t"
      "v_lshrrev_b32 v7, 1, v6\n\ts_mul_i32 s45, s45, 80\n\tv_xor_b32 v7, v7, v4\n\ts_sub_u32 s45, s45, 16\n\tv_and_b32 v7, 7, v7\n\t"
      "v_add_u32 v6, s45, v6\n\tv_lshl_add_u32 v7, s47, 3, v7\n\tv_add_u32 v8, 64, v6\n\tv_lshlrev_b32 v7, 4, v7\n\ts_movk_i32 s45, 0x7ff\n\t"
      "v_med3_i32 v6, v6, 0, s45\n\tv_min_u32 v8, s45, v8\n\ts_movk_i32 s45, 0x500\n\ts_lshl_b32 s46, s44, 10\n\t"
      "v_mad_u32_u24 v6, v6, s45, v7\n\tv_mad_u32_u24 v8, v8, s45, v7\n\tv_add_u32 v7, 0xf000, v5\n\ts_mov_b32 m0, s46\n\t"
      "s_waitcnt lgkmcnt(0)\n\tglobal_load_lds_dwordx4 v5, s[42:43]\n\ts_add_u32 m0, s46, %[xr]\n\ts_cmp_lt_u32 s44, 4\n\t"
      "global_load_lds_dwordx4 v6, s[40:41]\n\ts_or_b32 m0, s46, 0x2000\n\ts_cmp_lt_u32 s44, 4\n\tglobal_load_lds_dwordx4 v7, s[42:43]\n\t"
      "s_cbranch_scc0 3f\n\ts_add_u32 m0, s46, %[xr] + 0x2000\n\ts_nop 0\n\tglobal_load_lds_dwordx4 v8, s[40:41]\n"
      "3:\n\ts_waitcnt lgkmcnt(0)\n\ts_barrier\n"
      "2:"
      :: [ka] "s"(__builtin_amdgcn_kernarg_segment_ptr()), [tid] "v"(threadIdx.x), [bid] "s"(blockIdx.x), [cb] "n"(CODE_BYTES), [tch] "n"(TCH_OFF),
        [xr] "n"(XRING)
      : "memory", "scc", "s40", "s41", "s42", "s43", "s44", "s45", "s46", "s47", "s48", "s49", "s50", "v4", "v5", "v6", "v7", "v8");
  const int tid = threadIdx.x;
  const int lane = tid & 63;
  const int w = __builtin_amdgcn_readfirstlane(tid >> 6);
  const int g = lane >> 4, r16 = lane & 15, q4 = (lane & 15) >> 2, p4 = lane & 3;
  const int bid = blockIdx.x;
  const int xcd = bid & 7;
  const int bidx = bid >> 3;
  const u32 l16 = lane * 16;
  if (bidx >= 26) {
    const int fb = xcd * 6 + (bidx - 26);
    const u32 tstart = (u32)__builtin_amdgcn_s_memrealtime();
    const bool hurry = ((((u32)(u64)w_gate + fb * 64) >> 7) & 7u) == 3u;
#define WAIT_UNTIL(T)                                                                      \
  _Pragma("nounroll") for (int it = 0; it < 1000; ++it) {                                  \
    if ((int)((u32)__builtin_amdgcn_s_memrealtime() - tstart) >= (T)) break;               \
    __builtin_amdgcn_s_sleep(4);                                                           \
  }
    if (w < 3) {
      int li = (bidx - 26) * 160 + w * 64 + lane;
      if (w < 2 || lane < 32) touch_line((const char*)x + (size_t)(((xcd >> 2) * 13 + 6) * 80 - 16) * 1280 + li * 128);
    }
    u32x2 ep;
    u32 tag0;
    asm volatile("global_load_dwordx2 %0, %1, off sc1" : "=v"(ep) : "v"(ws + WS_EPOCH) : "memory");
    {
      int wi = (((xcd >> 1) & 1) * 6 + (bidx - 26)) * 12 + w;
      wi = wi > 129 ? 129 : wi;
      int row = (xcd >> 2) * 1040 + (xcd & 1) * 520 + wi * 4 + g;
      row = row > SEQ - 1 ? SEQ - 1 : row;
      const u32 vo = row * 1280 + r16 * 16;
      u32x4 xr[5];
#pragma unroll
      for (int i = 0; i < 5; ++i)
        asm volatile("global_load_dwordx4 %0, %1, %2 offset:%3" : "=v"(xr[i]) : "v"(vo), "s"(x), "n"(i * 256) : "memory");
      asm volatile("s_waitcnt vmcnt(0)" ::: "memory");
      asm volatile("" : "+v"(xr[0]), "+v"(xr[1]), "+v"(xr[2]), "+v"(xr[3]), "+v"(xr[4]), "+v"(ep)::"memory");
      tag0 = ep[0] + (ep[0] == SENT - 1u ? 2u : 1u);
      float sm = 0.f;
#pragma unroll
      for (int i = 0; i < 5; ++i)
#pragma unroll
        for (int e = 0; e < 4; ++e) sm = dot2b(xr[i][e], 0x3F803F80u, sm);
      const float mean = row16_sum(sm) * (1.0f / DM);
      float ss = 0.f;
#pragma unroll
      for (int i = 0; i < 5; ++i)
#pragma unroll
        for (int e = 0; e < 4; ++e) {
          const float a = lo_f(xr[i][e]) - mean, b = hi_f(xr[i][e]) - mean;
          ss = __builtin_fmaf(a, a, ss);
          ss = __builtin_fmaf(b, b, ss);
        }
      const float var = row16_sum(ss) * (1.0f / DM);
      const float den = rbf(__builtin_amdgcn_sqrtf(rbf(rbf(var) + 1e-5f)));
      if (r16 == 0) GST2((u32)(row * 8), ws + WS_LN1, 0, ((u32x2){cvt2((f32x2){mean, den}), tag0}));
    }
    u32x4 va[2][5], vb[5];
    const int vrk = (fb >> 4) * 14 + (int)(((((u32)(u64)w_gate + fb * 64) >> 6) - 8u) & 15u);
    const int vq0 = vrk * 4 + w * 2;
    u32x4 ra;
    u32 rb5;
    asm volatile("global_load_dwordx4 %0, %2, %4\n\tglobal_load_dword %1, %3, %4 offset:1024"
                   : "=&v"(ra), "=&v"(rb5) : "v"(l16), "v"((u32)(lane * 4)), "s"(w_r) : "memory");
    if (w >= 2) {
      WAIT_UNTIL(hurry ? 0 : PDELAY + 12 * (w - 2))
      const u32x4 s4 = (u32x4){SENT, SENT, SENT, SENT};
      const char* fp = ws + WS_FILL + (size_t)fb * 73728 + (w - 2) * 7168 + 4096;
      GST4(l16, fp, -4096, s4); GST4(l16, fp, -3072, s4); GST4(l16, fp, -2048, s4); GST4(l16, fp, -1024, s4);
      GST4(l16, fp, 0, s4); GST4(l16, fp, 1024, s4); GST4(l16, fp, 2048, s4);
      if (w < 4) GST4(l16, ws + WS_FILL + (size_t)fb * 73728 + 69632 + w * 1024, 0, s4);
      if (w >= 5) GST4((u32)(((w - 5) * 8 + (lane >> 3)) * 6144 + (lane & 7) * 16), ws + WS_PK + fb * 128, 0, s4);
    }
    if (w == 0) {
      const u64 e1 = (((u64)ep[1] << 32) | ep[0]) + (ep[0] == SENT - 1u ? 2ull : 1ull);
      if (lane == 0) *(u32x4*)(smem + CTL_OFF) = (u32x4){(u32)e1, (u32)(e1 >> 32), 0u, 0u};
      if (lane >= 1 && lane < 8) *(u32x4*)(smem + CTL_OFF + lane * 16) = (u32x4){0u, 0u, 0u, 0u};
      if (lane >= 32) *(u32x4*)(smem + KEY_OFF - 512 + lane * 16) = (u32x4){0u, 0u, 0u, 0u};
    }
    if (w >= 6) {
      const u32 o1 = tid >= 636 ? 0x3F803F80u : 0u;
      if (tid < 720) *(u32x4*)(smem + HN_OFF + (tid - 384) * 16) = (u32x4){o1, o1, o1, o1};
    }
    if (w < 2 && !hurry && vrk < 40) {
      WAIT_UNTIL(VDELAY)
      const u32 vo = (vq0 * 4 + g) * 1280 + r16 * 16;
#pragma unroll
      for (int i = 0; i < 5; ++i)
        asm volatile("global_load_dwordx4 %0, %3, %4 offset:%8\n\tglobal_load_dwordx4 %1, %3, %5 offset:%8\n\tglobal_load_dwordx4 %2, %6, %7 offset:%8"
                     : "=&v"(va[0][i]), "=&v"(va[1][i]), "=&v"(vb[i]) : "v"(vo), "s"(wo), "s"((const char*)wo + 5120), "v"((u32)(r16 * 16)), "s"(w_r), "n"(i * 256) : "memory");
      asm volatile("s_waitcnt vmcnt(0)" : "+v"(va[0][0]), "+v"(va[0][1]), "+v"(va[0][2]), "+v"(va[0][3]), "+v"(va[0][4]), "+v"(va[1][0]), "+v"(va[1][1]),
                   "+v"(va[1][2]), "+v"(va[1][3]), "+v"(va[1][4]), "+v"(vb[0]), "+v"(vb[1]), "+v"(vb[2]), "+v"(vb[3]), "+v"(vb[4])::"memory");
      float sb = 0.f;
#pragma unroll
      for (int i = 0; i < 20; ++i) sb = dot2b(vb[i >> 2][i & 3], 0x3F803F80u, sb);
      sb = row16_sum(sb) * (1.0f / DM);
      u32x4 pk2[2];
#pragma unroll
      for (int k = 0; k < 2; ++k) {
        float d = 0.f, sa = 0.f;
#pragma unroll
        for (int i = 0; i < 20; ++i) {
          d = dot2b(va[k][i >> 2][i & 3], vb[i >> 2][i & 3], d);
          sa = dot2b(va[k][i >> 2][i & 3], 0x3F803F80u, sa);
        }
        const u32 vp = __float_as_uint(row16_sum(d - sa * sb));
        pk2[k] = (u32x4){cvt2((f32x2){__uint_as_float(__builtin_amdgcn_readlane(vp, 0)), __uint_as_float(__builtin_amdgcn_readlane(vp, 16))}), tag0,
                          cvt2((f32x2){__uint_as_float(__builtin_amdgcn_readlane(vp, 32)), __uint_as_float(__builtin_amdgcn_readlane(vp, 48))}), tag0};
      }
      if (lane < 8) GST4(l16, ws + WS_VP + vq0 * 64, 0, ((lane & 1) ? pk2[1] : pk2[0]));
    } else
      asm volatile("s_waitcnt vmcnt(0)" ::: "memory");
    asm volatile("" : "+v"(ra), "+v"(rb5)::"memory");
    LDS_BARRIER();
    const u32x2 tg = *(const u32x2*)(smem + CTL_OFF);
    const u32 tag = tg[0];
    if (w == 2 && lane == 0) GST2(0u, ws + WS_HFLAG + fb * 128, 0, tg);
    if (w >= 3) {
      int li = (bidx - 26) * 534 + (w - 3) * 64 + lane;
      li = li > 3199 ? 3199 : li;
      const int rr = li / 5;
      if (w < 11 || lane < 22) touch_line((const char*)wo + rr * 1280 + ((xcd >> 1) & 1) * 640 + (li - rr * 5) * 128);
    }
    WAIT_UNTIL(hurry ? 0 : FDELAY)
    asm volatile("s_barrier" ::: "memory");
    if (w >= 2) {
      const int ch = (lane & 7) ^ (((lane >> 4) & 1) * 2 + ((w + fb) & 1) * 4);
      const u32 vg = (lane >> 3) * (FF * 2) + fb * 64 + (ch & 3) * 16;
      const u64 gsel = (ch & 4) ? (u64)w_up : (u64)w_gate;
#pragma nounroll
      for (int n0 = w - 2; n0 < 80; n0 += 10) {
        const int n = (fb & 1) ? 79 - n0 : n0;
        const u64 p = gsel + vg + (u32)(n * 8 * FF * 2);
        asm volatile("s_mov_b32 m0, %1\n\ts_nop 0\n\tglobal_load_lds_dwordx4 %0, off" ::"v"(p), "s"(n * 1024) : "memory");
      }
#pragma nounroll
      for (int n = 11 - w; n < 40; n += 10) {
        const u32 row = (u32)(n * 64 + lane) / 80u;
        asm volatile("s_mov_b32 m0, %2\n\ts_nop 0\n\tglobal_load_lds_dwordx4 %0, %1" " nt"
                     ::"v"(l16 ^ ((row & 3u) * 32u + (row & 8u) * 16u)), "s"((const char*)w_down + (size_t)fb * 40960 + n * 1024), "s"(DOWN_OFF + n * 1024) : "memory");
      }
      asm volatile("s_waitcnt vmcnt(0)" ::: "memory");
      if (lane == 0) asm volatile("ds_add_u32 %0, %1" ::"v"(CTL_OFF + 48), "v"(1u) : "memory");
    } else {
      if (w == 1 && lane < 4)
        touch_line(lane == 3 ? ws + WS_PK - 65536 + bid * 128 : (const char*)out + lane * 1277952 + fb * 1280);
      WAIT_UNTIL(KSTART)
    }
    if (w >= 4) {
      const int v = w - 4, kq = v & 3, m = v >> 2;
#pragma nounroll
      for (int it = 0; it < SPIN_CAP; ++it) {
        if (__builtin_amdgcn_readfirstlane(lds_ld32(CTL_OFF + 48)) >= 10u) break;
        __builtin_amdgcn_s_sleep(1);
      }
      bf16x8 gfr[5], ufr[5], dfr[5];
      {
        const int gx = (160 * kq + 8 * g + q4) * 128 + ((m ^ (q4 >> 1) ^ ((g & 1) * 2)) * 32) + p4 * 8;
        const char* db = smem + DOWN_OFF + (8 * g + q4) * 1280 + p4 * 8;
#pragma unroll
        for (int j = 0; j < 5; ++j) {
          gfr[j] = tr_frag(smem + gx + j * 4096, 512);
          ufr[j] = tr_frag(smem + (gx ^ 64) + j * 4096, 512);
          dfr[j] = tr_frag(db + ((v * 160 + j * 32) ^ (q4 * 32 + (g & 1) * 128)), 5120);
        }
#pragma unroll
        for (int j = 0; j < 5; ++j) asm volatile("" : "+v"(gfr[j]), "+v"(ufr[j]), "+v"(dfr[j]));
      }
      u32 ahn = HN_OFF + p4 * HSL + kq * 320 + g * 16, asw = ACT_OFF + p4 * ASL + kq * 256 + (q4 & 1) * 128 + m * 64 + g * 16, asr = ACT_OFF + (g * 8 + (lane & 7)) * 4;
      u32 vpo = v * 320 + r16 * 64 + g * 16, up3 = (lane & 8) ? 3u : 0u;
      asm volatile("" : "+v"(up3));
      const char* pdst = ws + WS_PART + fb * 2560;
      asm volatile("" : "+v"(ahn), "+v"(asw), "+v"(asr), "+v"(vpo));
      asm volatile("" : "+s"(pdst));
      int pass = 0;
      asm volatile("" : "+s"(pass));
#pragma nounroll
      for (; pass < 2; ++pass) {
        asm volatile("s_barrier" ::: "memory");
        u32x4 bs;
#define GU(J, C0, C1) "v_mfma_f32_16x16x32_bf16 v[120:123], %[g" #J "], v[100+4*" #J ":103+4*" #J "], " C0 "\n\t" \
                      "v_mfma_f32_16x16x32_bf16 v[124:127], %[u" #J "], v[100+4*" #J ":103+4*" #J "], " C1 "\n\t"
#define GU1(J) GU(J, "v[120:123]", "v[124:127]")
        asm volatile("ds_read_b128 v[100:103], %[ah]\n\tds_read_b128 v[104:107], %[ah] offset:64\n\tds_read_b128 v[108:111], %[ah] offset:128\n\t"
                     "ds_read_b128 v[112:115], %[ah] offset:192\n\tds_read_b128 v[116:119], %[ah] offset:256\n\t"
                     "s_waitcnt lgkmcnt(4)\n\t" GU(0, "0", "0") "s_waitcnt lgkmcnt(3)\n\t" GU1(1) "s_waitcnt lgkmcnt(2)\n\t" GU1(2)
                     "s_waitcnt lgkmcnt(1)\n\t" GU1(3) "s_waitcnt lgkmcnt(0)\n\t" GU1(4)
                     "ds_read_b128 %[bs], %[ab]\n\ts_mov_b32 exec_lo, 0x00f000f0\n\ts_mov_b32 exec_hi, 0x00f000f0\n\ts_nop 4\n\t"
                     "v_mov_b64 v[120:121], v[124:125]\n\tv_mov_b64 v[122:123], v[126:127]\n\t"
                     "s_mov_b32 exec_lo, 0x00ff00ff\n\ts_mov_b32 exec_hi, 0x00ff00ff\n\tds_write_b128 %[aw], v[120:123]\n\ts_mov_b64 exec, -1\n\t"
                     "s_setprio 3\n\ts_waitcnt lgkmcnt(0)\n\tv_cmp_eq_u32 vcc, 3, v129\n\ts_barrier\n\t"
                     "s_cbranch_vccnz 1f\n\tds_read_b128 %[bs], %[ab]\n\ts_waitcnt lgkmcnt(0)\n1:"
                     : [bs] "=&{v[128:131]}"(bs)
                     : [ah] "v"(ahn), [ab] "v"((u32)(CTL_OFF + 112)), [aw] "v"(asw), [g0] "v"(gfr[0]), [g1] "v"(gfr[1]), [g2] "v"(gfr[2]),
                       [g3] "v"(gfr[3]), [g4] "v"(gfr[4]), [u0] "v"(ufr[0]), [u1] "v"(ufr[1]), [u2] "v"(ufr[2]), [u3] "v"(ufr[3]), [u4] "v"(ufr[4])
                     : "memory", "vcc", "scc", "v100", "v101", "v102", "v103", "v104", "v105", "v106", "v107", "v108", "v109", "v110", "v111", "v112", "v113",
                       "v114", "v115", "v116", "v117", "v118", "v119", "v120", "v121", "v122", "v123", "v124", "v125", "v126", "v127");
        const u32x2 best = {bs[2], bs[3]};
        f32x2 s0, s1, s2, s3;
        asm volatile("ds_read2_b32 %0, %4 offset0:0 offset1:32\n\tds_read2_b32 %1, %4 offset0:64 offset1:96\n\t"
                     "ds_read2_b32 %2, %4 offset0:128 offset1:160\n\tds_read2_b32 %3, %4 offset0:192 offset1:224"
                     : "=&v"(s0), "=&v"(s1), "=&v"(s2), "=&v"(s3) : "v"(asr + ((best[1] & 3u) | up3) * ASL) : "memory");
        float rd = __builtin_amdgcn_rcpf(hi_f(best[0])), mb = lo_f(best[0]);
        asm volatile("s_waitcnt lgkmcnt(0)" : "+v"(s0), "+v"(s1), "+v"(s2), "+v"(s3), "+v"(rd), "+v"(mb)::"memory");
        const f32x2 sm = (s0 + s1) + (s2 + s3);
        const float gt = (sm[0] - mb * dppf<0x108>(sm[0])) * rd, up = (sm[1] - mb * dppf<0x108>(sm[1])) * rd;
        const float av = gt * up * __builtin_amdgcn_rcpf(1.0f + __expf(-gt));
        const u32 ap = cvt2((f32x2){av, dppf<0x101>(av)});
        const u32x4 bm = {dppu<0x150>(ap), dppu<0x152>(ap), dppu<0x154>(ap), dppu<0x156>(ap)};
#define DN(J) "v_mfma_f32_16x16x32_bf16 v[104+4*" #J ":107+4*" #J "], %[d" #J "], %[bm], 0\n\t"
#define MV(J) "s_lshl_b64 exec, exec, 1\n\tv_mov_b64 v[104:105], v[104+4*" #J ":105+4*" #J "]\n\tv_mov_b64 v[106:107], v[106+4*" #J ":107+4*" #J "]\n\t"
        asm volatile("s_nop 1\n\t" DN(0) DN(1) DN(2) DN(3) DN(4)
                     "s_mov_b32 exec_lo, 0x00010001\n\ts_mov_b32 exec_hi, 0x00010001\n\ts_nop 1\n\t" MV(1) MV(2) MV(3) MV(4)
                     "s_mov_b32 exec_lo, 0x001f001f\n\ts_mov_b32 exec_hi, 0x001f001f\n\tglobal_store_dwordx4 %[vp], v[104:107], %[pd] sc1\n\t"
                     "s_mov_b64 exec, -1\n\ts_setprio 0"
                     ::[bm] "v"(bm), [vp] "v"(vpo), [pd] "s"(pdst), [d0] "v"(dfr[0]), [d1] "v"(dfr[1]),
                       [d2] "v"(dfr[2]), [d3] "v"(dfr[3]), [d4] "v"(dfr[4])
                     : "memory", "scc", "v100", "v101", "v102", "v103", "v104", "v105", "v106", "v107", "v108", "v109", "v110", "v111", "v112", "v113",
                       "v114", "v115", "v116", "v117", "v118", "v119", "v120", "v121", "v122", "v123");
        if (pass) break;
        u32 fin = 0;
#pragma nounroll
        for (int it = 0; it < SPIN_CAP * 8; ++it) {
          fin = __builtin_amdgcn_readfirstlane(lds_ld32(CTL_OFF + 40));
          if (fin) break;
          __builtin_amdgcn_s_sleep(4);
        }
        if (fin != 2u) break;
        pdst += WS_PART2 - WS_PART;
      }
      return;
    }
    if (w == 0) {
      u32 pk6;
      asm volatile(
          "s_mov_b32 s41, %[cap]\n\ts_mov_b32 s40, 1\n\ts_mov_b64 s[46:47], 0\n\tv_mov_b32 v5, %[ln]\n\t"
          "s_mov_b32 s54, -1\n\ts_mov_b32 s55, 0x7ffff\n\tv_mov_b32 v12, 0\n\t"
#define PKLD(D) "s_mov_b64 exec, s[54:55]\n\tglobal_atomic_or_x2 " D ", %[vk], %[z], %[kb] sc0 sc1\n\ts_mov_b64 exec, -1\n\t"
          PKLD("v[8:9]") "s_sleep %[gap]\n\t" PKLD("v[10:11]") "s_sleep %[gap]\n\t" PKLD("v[14:15]")
#define MDPP(C) "s_nop 1\n\tv_max_u32_dpp v13, v13, v13 " C " row_mask:0xf bank_mask:0xf bound_ctrl:1\n\t"
#define PSTEP(L, D0, D1, DR)                                                                        \
          L "0:\n\t"                                                                               \
          "s_waitcnt vmcnt(2)\n\t"                                                                 \
          "v_cmp_eq_u32 s[42:43], " D1 ", %[tag]\n\t"                                              \
          "s_and_b64 exec, s[42:43], s[54:55]\n\ts_andn2_b64 s[54:55], s[54:55], s[42:43]\n\t"      \
          "v_and_or_b32 v12, " D0 ", %[msk], %[lid]\n\t"                                           \
          "v_mov_b32 v6, " D0 "\n\ts_mov_b64 exec, -1\n\t"                                         \
          "v_cmp_le_u32 vcc, s40, v12\n\t"                                                         \
          "s_cmp_eq_u64 vcc, s[46:47]\n\ts_cbranch_scc1 " L "1f\n\t"                              \
          "s_swappc_b64 s[50:51], s[48:49]\n"                                                      \
          L "1:\n\t"                                                                               \
          "s_cmp_eq_u64 s[54:55], 0\n\ts_cbranch_scc1 9f\n\t"                                     \
          PKLD(DR)
          "s_getpc_b64 s[48:49]\n\ts_add_u32 s48, s48, 12\n\ts_addc_u32 s49, s49, 0\n\ts_branch 10f\n\t"
          "v_mov_b32 v13, v12\n\t"
          MDPP("quad_perm:[1,0,3,2]") MDPP("quad_perm:[2,3,0,1]") MDPP("row_half_mirror") MDPP("row_mirror")
          "v_bfe_u32 v4, v6, 10, 6\n\tv_readlane_b32 s44, v13, 0\n\tv_readlane_b32 s45, v13, 16\n\tv_readlane_b32 s46, v13, 32\n\tv_readlane_b32 s47, v13, 48\n\t"
          "v_sub_u32 v4, %[rb], v4\n\ts_max_u32 s44, s44, s45\n\ts_max_u32 s46, s46, s47\n\ts_max_u32 s44, s44, s46\n\t"
          "s_and_b32 s45, s44, 0x7fffff\n\ts_bitset1_b32 s45, 23\n\ts_mul_hi_u32 s45, s45, %[shr]\n"
          "5:\n\ts_sub_u32 s40, s44, s45\n\ts_cselect_b32 s40, 1, s40\n\ts_max_u32 s40, s40, 1\n\tv_cmp_le_u32 s[46:47], s40, v12\n\ts_mul_i32 s45, s45, 3\n\ts_lshr_b32 s45, s45, 2\n\t"
          "s_bcnt1_i32_b64 s52, s[46:47]\n\ts_cmp_gt_u32 s52, 3\n\ts_cbranch_scc1 5b\n\t"
          "v_mbcnt_lo_u32_b32 v13, s46, 0\n\tv_mbcnt_hi_u32_b32 v13, s47, v13\n\tv_sub_u32 v7, %[ln], v13\n\tv_add_u32 v7, s52, v7\n\t"
          "v_cndmask_b32 v13, v7, v13, s[46:47]\n\tv_cndmask_b32 v7, 2, 1, s[46:47]\n\tv_cmp_gt_u32 s[44:45], 3, v13\n\t"
          "v_lshl_add_u32 v13, v13, 4, %[pa]\n\ts_mov_b64 exec, s[44:45]\n\tds_write_b128 v13, v[4:7]\n\ts_mov_b64 exec, -1\n\t"
          "s_setpc_b64 s[50:51]\n"
          PSTEP("1", "v8", "v9", "v[8:9]")
          PSTEP("2", "v10", "v11", "v[10:11]")
          PSTEP("3", "v14", "v15", "v[14:15]")
          "s_sub_u32 s41, s41, 1\n\ts_cbranch_scc0 10b\n\ts_swappc_b64 s[50:51], s[48:49]\n"
          "9:\n\t"  "v_or_b32 v7, 4, v7\n\ts_mov_b64 exec, s[44:45]\n\tds_write_b32 v13, v7 offset:12\n\ts_mov_b64 exec, -1\n\tds_write_b32 %[at], v6\n\ts_waitcnt lgkmcnt(0)\n\ts_barrier\n\ts_waitcnt vmcnt(0)"
          : "=&{v6}"(pk6) : [cap] "n"(SPIN_CAP), [gap] "n"(PGAP), [vk] "v"((u32)(lane * 6144)), [kb] "s"(ws + WS_PK + fb * 128), [tag] "v"(tag), [z] "v"(0ull), [ln] "v"(lane),
            [msk] "s"(~1023u), [lid] "v"(64u | lane), [rb] "v"(2047 - ((lane >> 1) * 80 + (lane & 1) * 48)),
            [shr] "s"((u32)((1.0 - PTHR) * 4294967296.0)), [pa] "s"(POST_OFF), [at] "v"((u32)(KEY_OFF - 256 + lane * 4))
          : "memory", "vcc", "scc", "s40", "s41", "s42", "s43", "s44", "s45", "s46", "s47", "s48", "s49", "s50", "s51", "s52", "s54", "s55",
            "v4", "v5", "v7", "v8", "v9", "v10", "v11", "v12", "v13", "v14", "v15");
      if (fb == 47 && lane < 8) GST4(l16, ws + WS_EPOCH, 0, ((u32x4){tg[0], tg[1], tg[0], tg[1]}));
      const u32 now = (u32)__builtin_amdgcn_s_memrealtime();
      const u32 nag = (pk6 - (now >> 1) - 1u) & 1023u;
      u32 kgo = __builtin_amdgcn_readfirstlane(now + KCD - 2 * (1023u - wave_max(lane < 51 ? nag : 0u)));
      u64 old = __ballot(lane < 51 && nag < 1023u - (KOLD - 8) / 2);
      asm volatile("" : "+s"(kgo), "+s"(old));
      asm volatile(
          "s_mov_b32 s41, %[cap]\n\ts_mov_b32 s54, -1\n\ts_mov_b32 s55, 0x7ffff\n\tv_mov_b32 v12, 0\n\t"
          "s_mov_b64 exec, %[old]\n\ts_cbranch_execz 3f\n\tglobal_load_dwordx2 v[14:15], %[vk], %[kb] sc1\n"
          "3:\n\ts_setprio 3\n\ts_barrier\n"
          "4:\n\ts_memrealtime s[42:43]\n\ts_waitcnt lgkmcnt(0)\n\ts_sub_u32 s42, s42, %[go]\n\ts_cmp_lt_i32 s42, 0\n\ts_cbranch_scc1 4b\n\t"
          "s_andn2_b64 exec, s[54:55], %[old]\n\ts_cbranch_execz 5f\n\tglobal_load_dwordx2 v[14:15], %[vk], %[kb] sc1\n"
          "5:\n\ts_mov_b64 exec, s[54:55]\n\tds_read_b32 v4, %[ctl] offset:92\n\ts_waitcnt lgkmcnt(0)\n\tv_and_b32 v4, 3, v4\n\t"
          "v_lshl_add_u32 v4, v4, 4, %[pa]\n\tds_read_b64 v[6:7], v4\n\ts_waitcnt lgkmcnt(0)\n\t"
          "v_readfirstlane_b32 s48, v6\n\tv_readfirstlane_b32 s49, v7\n\tv_mov_b32 v6, 1\n\ts_waitcnt vmcnt(0)\n\tv_cmp_ne_u32 s[54:55], v15, %[tag]\n\tv_mov_b32 v12, v14\n\ts_mov_b64 exec, -1\n\t"
          "s_cmp_eq_u64 s[54:55], 0\n\ts_cbranch_scc1 9f\n\t"
          PKLD("v[8:9]") "s_sleep %[gap]\n\t" PKLD("v[10:11]")
#define KSTEP(L, D0, D1, DR)                                                                        \
          L "0:\n\t"                                                                               \
          "s_waitcnt vmcnt(1)\n\t"                                                                 \
          "v_cmp_eq_u32 s[42:43], " D1 ", %[tag]\n\t"                                              \
          "s_and_b64 exec, s[42:43], s[54:55]\n\tv_mov_b32 v12, " D0 "\n\ts_mov_b64 exec, -1\n\t"   \
          "s_andn2_b64 s[54:55], s[54:55], s[42:43]\n\ts_cbranch_scc0 9f\n\t"                     \
          PKLD(DR)
          KSTEP("1", "v8", "v9", "v[8:9]")
          KSTEP("2", "v10", "v11", "v[10:11]")
          "s_sub_u32 s41, s41, 1\n\ts_cbranch_scc0 10b\n"
          "9:\n\t"
          "v_readlane_b32 s40, v12, s49\n\tds_write_b32 %[ak], v12\n\tv_mov_b32 v13, v12\n\t"
          "s_and_b32 s45, s40, 0x7ff\n\tv_cmp_lt_u32 vcc, s40, v12\n\ts_cmp_eq_u32 s45, s48\n\ts_cselect_b64 vcc, vcc, -1\n\ts_cbranch_vccz 8f\n\t"
          MDPP("quad_perm:[1,0,3,2]") MDPP("quad_perm:[2,3,0,1]") MDPP("row_half_mirror") MDPP("row_mirror")
          "s_nop 0\n\tv_readlane_b32 s44, v13, 0\n\tv_readlane_b32 s45, v13, 16\n\tv_readlane_b32 s46, v13, 32\n\tv_readlane_b32 s47, v13, 48\n\t"
          "s_max_u32 s44, s44, s45\n\ts_max_u32 s46, s46, s47\n\ts_max_u32 s40, s44, s46\n\t"
          "v_cmp_eq_u32 vcc, s40, v12\n\tv_mov_b32 v6, 2\n\ts_ff1_i32_b64 s44, vcc\n\tv_mov_b32 v7, s44\n"
          "8:\n\tv_mov_b32 v4, s40\n\ts_mov_b64 exec, 1\n\tds_write_b128 %[ctl], v[4:7]\n\ts_mov_b64 exec, -1\n\t"
          "s_waitcnt vmcnt(0) lgkmcnt(0)"
          :: [cap] "n"(SPIN_CAP), [gap] "n"(KGAP), [go] "s"(kgo), [old] "s"(old), [vk] "v"((u32)(lane * 6144)), [kb] "s"(ws + WS_PK + fb * 128 + 8), [tag] "v"(tag),
            [ctl] "v"((u32)(CTL_OFF + 32)), [ak] "v"((u32)(KEY_OFF + lane * 4)), [pa] "s"(POST_OFF), [z] "v"(0ull)
          : "memory", "vcc", "scc", "s40", "s41", "s42", "s43", "s44", "s45", "s46", "s47", "s48", "s49", "s54", "s55", "v4", "v5", "v6", "v7", "v8", "v9", "v10",
            "v11", "v12", "v13", "v14", "v15");
      return;
    }
    float swr = dot2b(rb5, 0x3F803F80u, 0.f);
#pragma unroll
    for (int e = 0; e < 4; ++e) swr = dot2b(ra[e], 0x3F803F80u, swr);
    swr = wave_sum(swr);
    u32 ah16 = HN_OFF + (w - 1) * HSL + l16, ah4 = HN_OFF + (w - 1) * HSL + 1024 + lane * 4, l4 = lane * 4;
    asm volatile("" : "+v"(swr), "+v"(ah16), "+v"(ah4), "+v"(l4));
#define DOT3(H, W) "v_dot2c_f32_bf16 v102, 0x3f803f80, " H "\n\tv_dot2c_f32_bf16 v103, " H ", " H "\n\tv_dot2c_f32_bf16 v104, " H ", " W "\n\t"
#define ADPP(C) "s_nop 1\n\tv_add_f32_dpp v102, v102, v102 " C " row_mask:0xf bank_mask:0xf bound_ctrl:1\n\t"
#define XDPP(C) "s_nop 1\n\tv_max_u32_dpp v102, v102, v102 " C " row_mask:0xf bank_mask:0xf bound_ctrl:1\n\t"
    const char* rp = (const char*)w_r;
    u32 second = 0;
#pragma nounroll
    for (;;) {
      asm volatile(
          "s_mov_b32 s47, %[cap]\n\ts_mov_b32 s43, -1\n\ts_sub_u32 s46, %[sec], 1\n"
          "7:\n\ts_mov_b64 s[48:49], %[rp]\n\ts_mov_b32 s45, 0\n\ts_branch 4f\n"
          "18:\n\ts_sleep 1\n"
          "1:\n\ts_sub_u32 s47, s47, 1\n\ts_cbranch_scc1 8f\n\ts_cmp_lg_u32 %[sec], 0\n\ts_cbranch_scc1 7b\n\t"
          "ds_read_b128 v[108:111], %[ap]\n\ts_memrealtime s[40:41]\n\ts_waitcnt lgkmcnt(0)\n\t"
          "v_readfirstlane_b32 s45, v111\n\tv_readfirstlane_b32 s46, v109\n\tv_readfirstlane_b32 s50, v110\n\ts_lshr_b32 s41, s40, 1\n\t"
          "s_cmp_eq_u32 s45, 6\n\ts_cbranch_scc1 8f\n\ts_bitcmp0_b32 s45, 0\n\ts_cbranch_scc1 18b\n\t"
          "s_cmp_eq_u32 s46, s42\n\ts_cbranch_scc1 6f\n\ts_cmp_eq_u32 s46, s43\n\ts_cbranch_scc1 2f\n\t"
          "s_sub_u32 s41, s41, s50\n\ts_and_b32 s41, s41, 0x3ff\n\ts_cmp_lt_u32 s41, %[tq]\n\ts_cbranch_scc1 18b\n\t"
          "s_mov_b32 s43, s46\n\ts_add_u32 s44, s50, 8\n\ts_cmp_ge_u32 s41, %[pc] - 20\n\ts_cbranch_scc1 1b\n\t"
          "s_mul_i32 s48, s46, 6144\n\tv_mov_b64 v[104:105], 0\n\tv_mov_b32 v101, s48\n\tv_mad_u32_u24 v101, %[l16], 48, v101\n\t"
          "s_mov_b64 exec, 0xff\n\tglobal_atomic_or_x2 v[102:103], v101, v[104:105], %[pk] sc0 sc1\n\ts_sub_u32 s50, s50, 512\n\ts_waitcnt vmcnt(0)\n\t"
          "v_cmp_eq_u32 vcc, v103, %[tag]\n\tv_subrev_u32 v102, s50, v102\n\tv_and_b32 v102, 0x3ff, v102\n\tv_cndmask_b32 v102, 0, v102, vcc\n\t"
          XDPP("quad_perm:[1,0,3,2]") XDPP("quad_perm:[2,3,0,1]") XDPP("row_half_mirror")
          "s_mov_b64 exec, -1\n\tv_readfirstlane_b32 s44, v102\n\ts_min_u32 s44, s44, 562\n\ts_add_u32 s44, s44, s50\n\ts_branch 1b\n"
          "2:\n\ts_sub_u32 s41, s41, s44\n\ts_and_b32 s41, s41, 0x3ff\n\ts_cmp_lt_u32 s41, %[pc] - 8\n\ts_cbranch_scc1 1b\n\t"
          "s_sub_u32 s41, %[pc], s41\n\ts_lshl1_add_u32 s51, s41, s40\n\ts_mul_i32 s48, s46, 1280\n\ts_add_u32 s48, s48, %[lo]\n\ts_addc_u32 s49, %[hi], 0\n\t"
          "3:\n\ts_memrealtime s[40:41]\n\ts_waitcnt lgkmcnt(0)\n\ts_sub_u32 s40, s40, s51\n\ts_cmp_lt_i32 s40, 0\n\ts_cbranch_scc1 3b\n"
          "4:\n\tglobal_load_dwordx4 v[96:99], %[l16], s[48:49] sc1\n\tglobal_load_dword v100, %[l4], s[48:49] offset:1024 sc1\n\t"
          "v_mov_b64 v[102:103], 0\n\tv_mov_b32 v104, 0\n\ts_mov_b32 s42, -2\n\ts_waitcnt vmcnt(0)\n\t"
          "ds_read_b128 v[108:111], %[ap]\n\tds_read_b32 v105, %[ab] offset:4\n\tds_write_b128 %[a16], v[96:99]\n\tds_write_b32 %[a4], v100\n\t"
          "v_max3_u32 v101, v96, v97, v98\n\tv_max3_u32 v101, v101, v99, v100\n\tv_cmp_eq_u32 vcc, -1, v101\n\t"
          DOT3("v96", "v92") DOT3("v97", "v93")
          "s_cbranch_vccnz 1b\n\ts_mov_b32 s42, s46\n\ts_waitcnt lgkmcnt(2)\n\tv_lshl_or_b32 v109, v111, 8, v109\n\tv_lshl_or_b32 v109, v105, 16, v109\n\t"
          "s_or_b32 s45, s46, 0x20500\n\tv_cmp_eq_u32 vcc, s45, v109\n\ts_cbranch_vccz 5f\n\ts_waitcnt lgkmcnt(0)\n\ts_barrier\n"
          "5:\n\t" DOT3("v98", "v94") DOT3("v99", "v95") DOT3("v100", "%[rb]")
          "s_nop 2\n\tv_mov_b32 v105, v104\n\tv_permlane32_swap_b32 v102, v103\n\tv_add_f32 v102, v102, v103\n\tv_permlane32_swap_b32 v104, v105\n\t"
          "v_add_f32 v104, v104, v105\n\ts_nop 1\n\tv_permlane16_swap_b32 v102, v104\n\tv_add_f32 v102, v102, v104\n\t"
          ADPP("quad_perm:[1,0,3,2]") ADPP("quad_perm:[2,3,0,1]") ADPP("row_half_mirror") ADPP("row_mirror")
          "v_mul_f32 v103, 0x3acccccd, v102\n\tv_readlane_b32 s41, v102, 16\n\tv_readlane_b32 s40, v103, 32\n\t"
          "s_nop 1\n\tv_fma_f32 v102, -v103, v103, s40\n\tv_max_f32 v102, 0, v102\n\tv_cvt_pk_bf16_f32 v102, 0, v102\n\t"
          "v_add_f32 v102, 0x3727c5ac, v102\n\tv_cvt_pk_bf16_f32 v102, 0, v102\n\tv_sqrt_f32 v102, v102\n\tv_cvt_pk_bf16_f32 v105, 0, v103\n\t"
          "v_fma_f32 v105, -%[swr], v105, s41\n\tv_cvt_pk_bf16_f32 v106, v103, v102\n\tv_and_b32 v102, 0xffff0000, v106\n\tv_rcp_f32 v102, v102\n\t"
          "v_mov_b32 v104, 1\n\tv_mul_f32 v102, v102, v105\n\tv_ashrrev_i32 v103, 31, v102\n\tv_or_b32 v103, 0x80000000, v103\n\t"
          "v_xor_b32 v102, v102, v103\n\tv_and_or_b32 v107, v102, -4, %[sl]\n\ts_cbranch_vccz 1b\n\t"
          "s_mov_b64 exec, 1\n\tds_max_u64 %[ab], v[106:107] offset:8\n\tds_add_u32 %[ab], v104 offset:4\n\ts_branch 9f\n"
          "6:\n\ts_bitcmp0_b32 s45, 2\n\ts_cbranch_scc1 1b\n\ts_mov_b64 exec, 1\n\tds_max_u64 %[ab], v[106:107] offset:8\n"
          "8:\n\tv_mov_b32 v104, 1\n\ts_mov_b64 exec, 1\n\tds_add_u32 %[ab], v104 offset:4\n\ts_barrier\n"
          "9:\n\ts_mov_b64 exec, -1\n\ts_waitcnt lgkmcnt(0)\n\ts_barrier"
          :
          : [cap] "n"(SPIN_CAP * 8), [rp] "s"(rp), [sec] "s"(second), [ap] "v"((u32)(POST_OFF + (w - 1) * 16)), [tq] "n"(TQD / 2), [pc] "n"(PCD / 2),
            [pk] "s"(ws + WS_PK + (bidx - 26) * 128), [tag] "v"(tag), [lo] "s"((u32)(u64)(ws + WS_PC)), [hi] "s"((u32)((u64)(ws + WS_PC) >> 32)),
            [l16] "v"(l16), [l4] "v"(l4), [a16] "v"(ah16), [a4] "v"(ah4), "{v[92:95]}"(ra), [rb] "v"(rb5), [swr] "v"(swr), [sl] "s"(w - 1),
            [ab] "v"((u32)(CTL_OFF + 112))
          : "memory", "vcc", "scc", "s40", "s41", "s42", "s43", "s44", "s45", "s46", "s47", "s48", "s49", "s50", "s51", "v96", "v97", "v98", "v99",
            "v100", "v101", "v102", "v103", "v104", "v105", "v106", "v107", "v108", "v109", "v110", "v111");
      if (w != 1 || second) break;
      u32x4 wst;
#pragma nounroll
      for (int it = 0; it < SPIN_CAP * 8; ++it) {
        wst = lds_ld128(CTL_OFF + 32);
        if (__builtin_amdgcn_readfirstlane(wst[2])) break;
        __builtin_amdgcn_s_sleep(1);
      }
      if (__builtin_amdgcn_readfirstlane(wst[2]) != 2u) break;
      if (lane < 2) *(u32x4*)(smem + (lane ? POST_OFF : CTL_OFF + 112)) = lane ? (u32x4){0u, 0u, 0u, 5u} : (u32x4){0u, 2u, 0x3F800000u, 0u};
      rp = ws + WS_CAND + __builtin_amdgcn_readfirstlane(wst[3]) * 2560;
      second = 1;
    }
    if (w == 1) {
    } else if (w == 3 || fb >= 40) {
      const int c = w == 3 ? fb : fb + 8;
      if (c < 51) {
        const u32 now = (u32)__builtin_amdgcn_s_memrealtime();
        const u32 age = ((now >> 1) - __builtin_amdgcn_readfirstlane(*(const u32*)(smem + KEY_OFF - 256 + c * 4))) & 1023u;
        asm volatile("s_mov_b32 s42, %[cap]\n"
                     "3:\n\ts_memrealtime s[40:41]\n\ts_waitcnt lgkmcnt(0)\n\ts_sub_u32 s40, s40, %[go]\n\ts_cmp_lt_i32 s40, 0\n\ts_cbranch_scc1 3b\n"
                     "4:\n\tglobal_load_dwordx4 v[96:99], %[l16], %[cp] sc1\n\tglobal_load_dword v103, %[l4], %[cp] offset:1024 sc1\n\t"
                     "s_sub_u32 s42, s42, 8\n\ts_waitcnt vmcnt(0)\n\ts_cbranch_scc1 2f\n\t"
                     "v_max3_u32 v101, v96, v98, v103\n\tv_cmp_eq_u32 vcc, -1, v101\n\ts_cbranch_vccnz 4b\n"
                     "1:\n\tds_read_b64 v[100:101], %[af]\n\tds_read_b32 v102, %[ak]\n\ts_sub_u32 s42, s42, 1\n\ts_cbranch_scc1 2f\n\ts_waitcnt lgkmcnt(0)\n\t"
                     "v_cmp_eq_u32 vcc, 0, v100\n\ts_cbranch_vccnz 1b\n\t"
                     "v_cmp_eq_u32 vcc, %[c], v101\n\tv_bfi_b32 v102, v102, 0, %[m]\n\ts_cbranch_vccnz 2f\n\t"
                     "v_mad_u32_u24 v100, v102, %[rs], %[l16]\n\tv_mad_u32_u24 v101, v102, %[rs], %[l4]\n\t"
                     "global_store_dwordx4 v100, v[96:99], %[out]" "\n\tglobal_store_dword v101, v103, %[out] offset:1024" "\n"
                     "2:\n\ts_waitcnt lgkmcnt(0)"
                     ::[cap] "n"(SPIN_CAP * 8), [go] "s"(now + CCD - 2 * age), [cp] "s"(ws + WS_CAND + c * 2560 + 1280), [af] "v"((u32)(CTL_OFF + 40)),
                       [ak] "v"((u32)(KEY_OFF + c * 4)), [c] "s"(c), [m] "s"(2047), [rs] "s"(1280), [l16] "v"(l16), [l4] "v"(l4), [out] "s"(out)
                     : "memory", "vcc", "scc", "s40", "s41", "s42", "v96", "v97", "v98", "v99", "v100", "v101", "v102", "v103");
      }
    } else {
      const char* pp = ws + WS_PART + fb * 64;
      int pass = 0;
      asm volatile("" : "+s"(pass));
#pragma nounroll
      for (; pass < 2; ++pass) {
        const u32x2 best = *(const u32x2*)(smem + CTL_OFF + 120);
        const u32 slot = best[1] & 3u;
        const u32 wkey = __builtin_amdgcn_readfirstlane(*(const u32*)(smem + (pass ? CTL_OFF + 32 : POST_OFF + slot * 16)));
        const float rr = rbf(__uint_as_float((best[1] & ~3u) ^ ((u32)((int)~best[1] >> 31) | 0x80000000u)));
        float rs = rbf(__builtin_amdgcn_rcpf(1.0f + __expf(-rr)));
        const int ccol = (g & 1) * 2 + (g >> 1);
        float hv = bf2f(*(const u16*)(smem + HN_OFF + slot * HSL + fb * 32 + p4 * 8 + ccol * 2));
        hv = __builtin_fmaf(hv - lo_f(best[0]), __builtin_amdgcn_rcpf(hi_f(best[0])), hv);
        const char* op_ = (const char*)out + (size_t)(2047 - (int)(wkey & 2047u)) * 1280 + fb * 32;
        u32 vo_ = p4 * 8 + ccol * 2;
        asm volatile("" : "+v"(vo_), "+v"(hv), "+v"(rs));
        asm volatile("" : "+s"(op_));
        const u32 vp = (lane >> 2) * 2560 + p4 * 16;
        typedef __attribute__((ext_vector_type(16))) unsigned int u32x16;
        u32x16 keepa, keepb;
        f32x4 ys;
        u32 fin;
        asm volatile(
#define PLD(B0, B1, B2)                                                                             \
            "global_load_dwordx4 " B0 ", %[vp], %[p0] sc1\n\t"                                       \
            "global_load_dwordx4 " B1 ", %[vp], %[p1] sc1\n\tglobal_load_dwordx4 " B2 ", %[vp], %[p2] sc1\n\t"
#define PCHK(A0, A1, B0, B1, C0, C1)                                                                \
            "s_waitcnt vmcnt(3)\n\t"                                                                 \
            "v_pk_add_f32 v[96:97], " A0 ", " B0 "\n\tv_pk_add_f32 v[98:99], " A1 ", " B1 "\n\t"    \
            "v_pk_add_f32 v[96:97], v[96:97], " C0 "\n\tv_pk_add_f32 v[98:99], v[98:99], " C1 "\n\t" \
            "v_pk_add_f32 v[102:103], v[96:97], v[98:99]\n\t"                                        \
            "v_cmp_u_f32 vcc, v102, v103\n\ts_cbranch_vccz 9f\n\t"
            "s_mov_b32 s42, %[cap]\n\ts_sleep %[cdel]\n\t"
            PLD("v[64:67]", "v[68:71]", "v[72:75]")
            "s_sleep %[gap]\n\t"
            PLD("v[80:83]", "v[84:87]", "v[88:91]")
            "1:\n\t"
            PCHK("v[64:65]", "v[66:67]", "v[68:69]", "v[70:71]", "v[72:73]", "v[74:75]")
            PLD("v[64:67]", "v[68:71]", "v[72:75]")
            PCHK("v[80:81]", "v[82:83]", "v[84:85]", "v[86:87]", "v[88:89]", "v[90:91]")
            PLD("v[80:83]", "v[84:87]", "v[88:91]")
            "s_sub_u32 s42, s42, 1\n\ts_cbranch_scc0 1b\n"
            "9:\n\tds_read_b32 %[fin], %[af]"
            : "=&{v[96:99]}"(ys), "=&{v[64:79]}"(keepa), "=&{v[80:95]}"(keepb), [fin] "=&v"(fin)
            : [cap] "n"(SPIN_CAP), [gap] "n"(CGAP), [cdel] "n"(CDEL), [vp] "v"(vp), [p0] "s"(pp),
              [p1] "s"(pp + 40960), [p2] "s"(pp + 81920), [af] "v"((u32)(CTL_OFF + 40))
            : "memory", "vcc", "scc", "s42", "v102", "v103");
        float y = tr_sum(ys);
        y += dppf<0x124>(y);
        y += dppf<0x128>(y);
        const u32 res = cvt2((f32x2){__builtin_fmaf(y, rs, hv), 0.f});
        asm volatile("s_mov_b32 s42, %[cap]\n"
                     "1:\n\ts_waitcnt lgkmcnt(0)\n\tv_cmp_eq_u32 vcc, %[ok], %[fin]\n\ts_cbranch_vccz 2f\n\t"
                     "s_mov_b32 exec_lo, 0x000f000f\n\ts_mov_b32 exec_hi, 0x000f000f\n\tglobal_store_short %[vo], %[res], %[op]" "\n\ts_mov_b64 exec, -1\n"
                     "2:\n\tv_cmp_eq_u32 vcc, 0, %[fin]\n\ts_cbranch_vccz 3f\n\tds_read_b32 %[fin], %[af]\n\ts_sub_u32 s42, s42, 1\n\ts_cbranch_scc0 1b\n"
                     "3:\n\ts_waitcnt lgkmcnt(0)\n\tv_cmp_lt_u32 vcc, %[ok], %[fin]\n\ts_cbranch_vccz 4f\n\ts_waitcnt vmcnt(0)\n4:"
                     : [fin] "+v"(fin) : [cap] "n"(SPIN_CAP * 8), [ok] "s"(pass + 1), [vo] "v"(vo_), [res] "v"(res), [op] "s"(op_),
                       [af] "v"((u32)(CTL_OFF + 40)), "{v[64:79]}"(keepa), "{v[80:95]}"(keepb) : "memory", "vcc", "scc", "s42");
        if (pass || __builtin_amdgcn_readfirstlane(fin) != 2u) break;
        asm volatile("s_barrier\n\ts_barrier" ::: "memory");
        pp += WS_PART2 - WS_PART;
      }
    }
    return;
  }

  const int idx2 = (xcd & 1) * 26 + bidx;
  const int hg = ((xcd >> 1) & 1) * 4 + (idx2 & 3);
  const int mt = (xcd >> 2) * 13 + (idx2 >> 2);
#define ROTF (((idx2 >> 2) * 3 / 2 + (idx2 & 3) * 3) % 10)
  const int kb0 = ROTF;
  const int r0 = mt * 80;
  const int rt = w >= 4 ? (w & 3) : 4, kh = w >= 4 ? (w - 4) >> 2 : (w & 1);
  float bias[2];
  u32 sel[4], e4, eh, aq, aka, akb, pm, pf, hi, arec, vpp;
  const char *os, *os4, *sst, *oa[10];
  int so_[10];
  int lane_ = lane, mt_ = mt, hg_ = hg;
  u32 tgo = 0;
  auto prepv = [&](const int lane) __attribute__((always_inline)) {
    const int g = lane >> 4, r16 = lane & 15, q4 = (lane & 15) >> 2, p4 = lane & 3;
    const int par = lane & 1;
    const bool ca = r16 >= 4 * g, cb = r16 >= 4 * g + 2;
    bias[0] = (ca || r0 + rt > 0) ? 0.f : -__builtin_inff();
    bias[1] = (cb || r0 + rt > 0) ? 0.f : -__builtin_inff();
    const u32 sa = par ? 0x01000C0Cu : 0x0C0C0100u, sb = par ? 0x03020C0Cu : 0x0C0C0302u;
    sel[0] = ca ? 0x0C0C0C0Cu : sa;
    sel[1] = cb ? 0x0C0C0C0Cu : sb;
    sel[2] = ca ? sa : 0x0C0C0C0Cu;
    sel[3] = cb ? sb : 0x0C0C0C0Cu;
    e4 = EXO + rt * 512 + lane * 8;
    eh = e4 + kh * 6144;
    aq = TAB_OFF + 384 + (rt * 16 + r16) * 4;
    aka = TAB_OFF + 384 + (rt * 16 + 4 * g + par + (ca ? 16 : 0)) * 4;
    akb = TAB_OFF + 384 + (rt * 16 + 4 * g + par + 2 + (cb ? 16 : 0)) * 4;
    pm = (4 * g + q4) * 160 + p4 * 40;
    pf = (20 * g + 16 + q4) * 160 + p4 * 40;
    hi = HIM + (rt * 16 + r16) * 160 + g * 40;
    arec = REC_OFF + (r16 == 0 ? kh * 192 + g * 48 : (r16 == 8 ? ((g >> 1) == kh ? 480 : 384) : 432));
    vpp = lane + kh * 64 + (rt == 0 ? 768 : (rt == 2 ? 8192 : (rt == 4 ? 512 : 640)));
    asm volatile("" : "+v"(arec), "+v"(vpp));
    asm volatile("" : "+v"(bias[0]), "+v"(bias[1]), "+v"(sel[0]), "+v"(sel[1]), "+v"(sel[2]), "+v"(sel[3]), "+v"(e4), "+v"(eh),
                 "+v"(aq), "+v"(aka), "+v"(akb), "+v"(pm), "+v"(pf), "+v"(hi));
  };
  auto preps = [&](const int mt, const int hg) __attribute__((always_inline)) {
    const char* ob = ws + WS_O + (size_t)mt * 102400;
    os = ob + rt * 20480 + (2 * hg + kh) * 1024;
    os4 = ob + rt * 20480 + (16 + (hg >> 1)) * 1024 + (hg & 1) * 8;
    sst = ws + WS_STAT + (size_t)(2 * mt + (rt < 3 ? 0 : 1)) * 8192 + hg * 1024 + (rt < 3 ? rt : rt - 3) * 256;
    asm volatile("" : "+s"(os), "+s"(os4), "+s"(sst));
    const int k2 = w & 1;
    if (w >= 8) {
#pragma unroll
      for (int j = 0; j < 10; ++j) asm volatile("" : "=s"(oa[j]), "=s"(so_[j]));
      return;
    }
#pragma unroll
    for (int j = 0; j < 10; ++j) {
      const bool oth = j >= 5 || w < 2;
      const int mo[8] = {OORD};
      const int ch = j % 5 < 4 ? (2 * hg + k2) ^ (oth ? 2 * mo[4 + j % 5] : 2 * mo[j % 5]) : (hg >> 1) ^ ((oth != ((hg & 1) != 0)) ? (k2 ? 0 : 3) : 1 + k2);
      oa[j] = ob + (j % 5 < 4 ? ch : 16 + ch) * 1024;
      so_[j] = ch * (j % 5 < 4 ? 6400 : 25600);
      asm volatile("" : "+s"(oa[j]), "+s"(so_[j]));
    }
  };
#define WO_OFFS                                                                                    \
  u32 woff[5];                                                                                     \
  _Pragma("unroll") for (int i = 0; i < 5; ++i) {                                                  \
    const int c = i * 64 + lane;                                                                   \
    const int row = c / 10;                                                                        \
    woff[i] = row * 1120 + hg * 160 + c * 16 + (4 - i) * 1024;                                     \
  }
#define WO_RUN(RHO)                                                                                \
  {                                                                                                \
    const char* wq_ = (const char*)wo + (RHO) * 40960; \
    dma_m0<-4096>(woff[0], wq_, (RHO) * 5120 + 4096);                                               \
    dma_nx<-3072>(woff[1], wq_); dma_nx<-2048>(woff[2], wq_); dma_nx<-1024>(woff[3], wq_); dma_nx<0>(woff[4], wq_); \
  }

#define BAR asm volatile("s_barrier" ::: "memory");
#define PREP_V __builtin_amdgcn_sched_barrier(0); asm volatile("" : "+v"(lane_)); prepv(lane_); __builtin_amdgcn_sched_barrier(0);
#define PREP_S __builtin_amdgcn_sched_barrier(0); asm volatile("" : "+s"(mt_), "+s"(hg_)); preps(mt_, hg_); __builtin_amdgcn_sched_barrier(0);
  if (w < 2) {
    const int l = w;
    __builtin_amdgcn_s_setprio(3);
    u32 vw[4];
#pragma unroll
    for (int i = 0; i < 4; ++i) vw[i] = w_off(i, lane, hg) + 4096;
    u32 go = (u32)kb0 * 245760u + 122880u + l * 61440u;
    u32 so = HSLOT + l * 8192 + 4096;
#define W1(I) dma_nx<((I) - 4) * 1024>(vw[(I) & 3], wb + (W_ROW(I) - (I) * 1024));
#define ISSUE_W                                                                                    \
  {                                                                                                \
    const char* wb = (const char*)wqkv + go;                                                       \
    dma_m0<-4096>(vw[0], wb, so);                                                                  \
    W1(1) W1(2) W1(3) W1(4) W1(5) W1(6) W1(7)                                                      \
    go += 122880u;                                                                                 \
    if (go >= 2457600u) go -= 2457600u;                                                            \
    so = so >= 5 * HSLOT ? so - 5 * HSLOT : so + HSLOT;                                            \
  }
    ISSUE_W
    __builtin_amdgcn_sched_barrier(0);
    WO_OFFS
    asm volatile("" : "+v"(woff[0]), "+v"(woff[1]), "+v"(woff[2]), "+v"(woff[3]), "+v"(woff[4]));
    BAR
    ISSUE_W
    asm volatile("s_waitcnt vmcnt(8)\n\ts_barrier" ::: "memory");
#pragma unroll 1
    for (int h = 0; h < 17; ++h) {
      ISSUE_W
      asm volatile("s_waitcnt vmcnt(8)\n\ts_barrier" ::: "memory");
    }
    asm volatile("s_waitcnt vmcnt(0)\n\ts_barrier" ::: "memory");
    WO_RUN(12 + l)
    const u32 tagw = lds_ld32(CTL_OFF);
    const char* bxf = ws + WS_BXF + mt * 1024;
    BAR
    const u32 town = (u32)__builtin_amdgcn_s_memrealtime();
    tgo = town;
    if (l == 0) {
      if (lane == 0) GST2((u32)(hg * 128), bxf, 0, ((u32x2){town, tagw}));
    }
    WO_RUN(18 + l)
    WO_RUN(8 + l)
    WO_RUN(l)
    const u32 vfl = (((lane & 7) == hg ? hg ^ 1 : (lane & 7))) * 128;
    u32x2 f;
    asm volatile("" : "=v"(f));
    BAR
    int gate = (int)((u32)__builtin_amdgcn_s_memrealtime() - town) + GADD;
    gate = gate > 240 ? 240 : gate;
    WO_RUN(2 + l)
    WO_RUN(4 + l)
    WO_RUN(6 + l)
    if (l == 1) asm volatile("global_load_dwordx2 %0, %1, %2 sc1" : "+v"(f) : "v"(vfl), "s"(bxf) : "memory");
    __builtin_amdgcn_s_setprio(0);
    pm = (4 * g + q4) * 160 + p4 * 40;
    pf = (20 * g + 16 + q4) * 160 + p4 * 40;
    asm volatile("" : "=v"(bias[0]), "=v"(bias[1]), "=v"(sel[0]), "=v"(sel[1]), "=v"(sel[2]), "=v"(sel[3]), "=v"(e4), "=v"(eh),
                 "=v"(aq), "=v"(aka), "=v"(akb), "+v"(pm), "+v"(pf), "=v"(hi), "=v"(arec), "=v"(vpp));
    PREP_S
    if (l == 1) {
      asm volatile("s_waitcnt vmcnt(0)" : "+v"(f)::"memory");
#pragma nounroll
      for (int it = 0; it < SPIN_CAP; ++it) {
        if (__all(f[1] == tagw)) break;
        asm volatile("global_load_dwordx2 %0, %1, %2 sc1\n\ts_waitcnt vmcnt(0)" : "=&v"(f) : "v"(vfl), "s"(bxf) : "memory");
      }
      u32 d = min(max((int)(f[0] - town), 0), 200);
      d = max(d, dppu<0xB1>(d)); d = max(d, dppu<0x4E>(d)); d = max(d, dppu<0x141>(d));
      tgo = town + gate + __builtin_amdgcn_readfirstlane(d);
    }
    __builtin_amdgcn_s_setprio(0);
  } else if (w < 4) {
    __builtin_amdgcn_s_setprio(3);
    if (w == 2) {
      u32 vx[12];
#pragma unroll
      for (int i = 0; i < 12; ++i) vx[i] = x_off(i, lane, r0) + (4 - (i % 6)) * 1024;
      int kb = kb0 == 9 ? 0 : kb0 + 1;
      u32 xo = XRING + XSLOT + 4096;
#define ISSUE_X0                                                                                   \
  {                                                                                                \
    const char* xb = (const char*)x + kb * 128;                                                    \
    dma_m0<-4096>(vx[0], xb, xo);                                                                  \
    dma_nx<-3072>(vx[1], xb); dma_nx<-2048>(vx[2], xb); dma_nx<-1024>(vx[3], xb); dma_nx<0>(vx[4], xb); dma_nx<1024>(vx[5], xb); \
  }
#define ISSUE_X1                                                                                   \
  {                                                                                                \
    const char* xb = (const char*)x + kb * 128;                                                    \
    dma_m0<-4096>(vx[6], xb, xo + 6144);                                                           \
    dma_nx<-3072>(vx[7], xb); dma_nx<-2048>(vx[8], xb); dma_nx<-1024>(vx[9], xb); dma_nx<0>(vx[10], xb); dma_nx<1024>(vx[11], xb); \
    kb = kb == 9 ? 0 : kb + 1;                                                                     \
    xo = xo >= XRING + 3 * XSLOT ? xo - 3 * XSLOT : xo + XSLOT;                                    \
  }
      __builtin_amdgcn_s_sleep(XSL);
      ISSUE_X0
      __builtin_amdgcn_sched_barrier(0);
      WO_OFFS
      asm volatile("" : "+v"(woff[0]), "+v"(woff[1]), "+v"(woff[2]), "+v"(woff[3]), "+v"(woff[4]));
      BAR
      ISSUE_X1
      ISSUE_X0
      BAR
      ISSUE_X1
      asm volatile("s_waitcnt vmcnt(12)\n\ts_barrier" ::: "memory");
#pragma unroll 1
      for (int j = 3; j < 10; ++j) {
        ISSUE_X0
        BAR
        ISSUE_X1
        asm volatile("s_waitcnt vmcnt(12)\n\ts_barrier" ::: "memory");
      }
      const char* xq = (const char*)x + (size_t)r0 * 1280;
      const int nv = mt == 25 ? 480 : 800;
#define RES1(I) if ((I) < 7 || (I) * 64 + lane < nv) dma_m0<(((I) % 5) - 4) * 1024>(woff[(I) % 5], xq + ((I) / 5) * 40960, HIM + ((I) / 5) * 5120 + 4096);
      PREP_V
      BAR
      PREP_S
      asm volatile("s_waitcnt vmcnt(0)\n\ts_barrier" ::: "memory");
      RES1(0) RES1(1) RES1(2) RES1(3) RES1(4) RES1(5) RES1(6)
      BAR
      RES1(7) RES1(8) RES1(9) RES1(10) RES1(11) RES1(12)
      if (lane < 10) dma_m0<0>(l16, (const char*)w_r + hg * 160, WR_OFF);
      BAR
    } else {
      u32x2 ep;
      u32x4 ra;
      u32 rb5;
      if (lane < 8) {
        *(u32x4*)(smem + ONES_OFF + lane * 16) = (u32x4){0x3F803F80u, 0x3F803F80u, 0x3F803F80u, 0x3F803F80u};
        *(u32x4*)(smem + CTL_OFF + lane * 16) = (u32x4){0u, 0u, 0u, 0u};
        *(u32x4*)(smem + ZERO_OFF + lane * 16) = (u32x4){0u, 0u, 0u, 0u};
      }
      {
        const u32 o1 = lane == 26 || lane == 32 ? 0x3F803F80u : 0u;
        const u32 o2 = lane == 31 ? 0x3F803F80u : o1;
        if (lane < 33) *(u32x4*)(smem + REC_OFF + lane * 16) = (u32x4){o1, o1, o2, o2};
      }
      WO_OFFS
      asm volatile("" : "+v"(woff[0]), "+v"(woff[1]), "+v"(woff[2]), "+v"(woff[3]), "+v"(woff[4]));
      __builtin_amdgcn_s_sleep(TWS);
      if (lane < 5)
        touch_line(lane == 4 ? (const char*)wo + hg * 160
                             : (lane >= 2 ? ws + WS_PK - 65536 + (lane - 2) * 524288 + bid * 128
                                          : (const char*)out + (size_t)(r0 + lane * 47) * 1280 + hg * 160));
      LDS_BARRIER();
      BAR BAR
      asm volatile("global_load_dwordx2 %0, %1, off sc1" : "=v"(ep) : "v"(ws + WS_EPOCH) : "memory");
      asm volatile("global_load_dwordx4 %0, %2, %4\n\tglobal_load_dword %1, %3, %4 offset:1024"
                   : "=&v"(ra), "=&v"(rb5) : "v"(l16), "v"((u32)(lane * 4)), "s"(w_r) : "memory");
      BAR BAR BAR
      PREP_V
      BAR
      PREP_S
      BAR
      asm volatile("s_waitcnt vmcnt(0)" : "+v"(ep), "+v"(ra), "+v"(rb5)::"memory");
      const u64 e1 = (((u64)ep[1] << 32) | ep[0]) + (ep[0] == SENT - 1u ? 2ull : 1ull);
      float swr = lo_f(rb5) + hi_f(rb5);
#pragma unroll
      for (int e = 0; e < 4; ++e) swr += lo_f(ra[e]) + hi_f(ra[e]);
      swr = wave_sum(swr);
      if (lane == 0) {
        *(u32x2*)(smem + CTL_OFF) = (u32x2){(u32)e1, (u32)(e1 >> 32)};
        *(float*)(smem + CTL_OFF + 40) = swr;
      }
      int ra0 = r0 - 16 + lane, ra1 = r0 + 48 + (lane & 31);
      ra0 = ra0 < 0 ? 0 : (ra0 > SEQ - 1 ? SEQ - 1 : ra0);
      ra1 = ra1 > SEQ - 1 ? SEQ - 1 : ra1;
      const int hd = lane / 5, pt = lane - hd * 5;
      u32 avp = REC_OFF + (hd >> 1) * 192 + (pt < 4 ? pt * 48 + (hd & 1) * 8 : hd * 48 + 16), vvp = lane < 20 ? (lane >> 1) * 128 + (lane & 1) * 16 : 0;
      u32 vf = (lane < 48 ? lane : 0) * 128, va0 = ra0 * 8, va1 = ra1 * 8, vt0 = TAB_OFF + lane * 4, vt1 = TAB_OFF + 256 + (lane & 31) * 4;
      asm volatile("" : "+v"(avp), "+v"(vvp), "+v"(vf), "+v"(va0), "+v"(va1), "+v"(vt0), "+v"(vt1));
#pragma nounroll
      for (int h = 7; h <= HK0; ++h) BAR
      u32x2 f, t0, t1;
      u32x4 vp4;
      GLD2(f, vf, ws + WS_HFLAG, 0);
      GLD2(t0, va0, ws + WS_LN1, 0);
      GLD2(t1, va1, ws + WS_LN1, 0);
      asm volatile("global_load_dwordx4 %0, %1, %2 sc1" : "=v"(vp4) : "v"(vvp), "s"(ws + WS_VP + hg * 1280) : "memory");
#pragma unroll
      for (int h = HK0 + 1; h <= 17; ++h) BAR
      WO_RUN(10)
      WO_RUN(11)
      BAR
      asm volatile("s_waitcnt vmcnt(10)" : "+v"(t0), "+v"(t1), "+v"(f), "+v"(vp4)::"memory");
#pragma nounroll
      for (int it = 0; it < SPIN_CAP; ++it) {
        if (__all(t0[1] == (u32)e1 && t1[1] == (u32)e1 && f[0] == (u32)e1 && f[1] == (u32)(e1 >> 32) && vp4[1] == (u32)e1 && vp4[3] == (u32)e1)) break;
        asm volatile("global_load_dwordx2 %2, %6, %9 sc1\n\tglobal_load_dwordx2 %0, %4, %8 sc1\n\tglobal_load_dwordx2 %1, %5, %8 sc1\n\t"
                     "global_load_dwordx4 %3, %7, %10 sc1\n\ts_waitcnt vmcnt(0)"
                     : "=&v"(t0), "=&v"(t1), "=&v"(f), "=&v"(vp4)
                     : "v"(va0), "v"(va1), "v"(vf), "v"(vvp), "s"(ws + WS_LN1), "s"(ws + WS_HFLAG), "s"(ws + WS_VP + hg * 1280) : "memory");
      }
      *(u32*)(smem + vt0) = t0[0] << 16;
      *(u32*)(smem + vt1) = t1[0] << 16;
      *(float*)(smem + vt0 + 384) = __builtin_amdgcn_rcpf(hi_f(t0[0]));
      *(float*)(smem + vt1 + 384) = __builtin_amdgcn_rcpf(hi_f(t1[0]));
      if (lane < 20) *(u32x2*)(smem + avp) = (u32x2){vp4[0], vp4[2]};
      asm volatile("" ::: "memory");
      WO_RUN(14)
      WO_RUN(15)
      LDS_BARRIER();
      WO_RUN(16)
      WO_RUN(17)
    }
    __builtin_amdgcn_s_setprio(0);
  } else {
    const int cw = w - 4;
    if (cw >= 4) __builtin_amdgcn_s_setprio(1);
    prepv(lane);
    preps(mt, hg);
    const int xa0 = XRING + r16 * 128 + ((g ^ ((r16 >> 1) & 7)) * 16);
    const bool zr = cw < 2;
    const u32 m1 = r16 == 0 || zr ? 0u : 1u;
    const u32 xz0 = r16 == 0 ? (u32)ONES_OFF + g * 16 : (zr ? (u32)ZERO_OFF : (u32)xa0);
    int wa[2];
#pragma unroll
    for (int c = 0; c < 2; ++c) {
      const int ct = cw < 7 ? 2 * cw + c : 14;
      const int seg = ct / 5, tt = ct - seg * 5;
      const int G = tt < 4 ? 5 * tt + p4 : 5 * p4 + 4;
      wa[c] = (16 * (g >> 1) + 4 * (g & 1) + q4) * 512 + ((seg * 160 + G * 8) ^ ((4 * (g & 1) + q4) << 5));
    }
    auto run = [&](auto tyc) __attribute__((always_inline)) {
      constexpr int TY = decltype(tyc)::value;
      constexpr int NW = TY == 2 ? 1 : 2;
      f32x4 acc[2][6];
#pragma unroll
      for (int c = 0; c < 2; ++c)
#pragma unroll
        for (int r = 0; r < 6; ++r) acc[c][r] = (f32x4){0.f, 0.f, 0.f, 0.f};
      u32x4 xA[6], xB[6];
      u32x2 wA[4], wB[4];
#define RX(D, A, O) asm volatile("ds_read_b128 %0, %1 offset:%2" : "=v"(D) : "v"(A), "n"(O) : "memory");
#define RW(D, A, O) asm volatile("ds_read_b64_tr_b16 %0, %1 offset:%2" : "=v"(D) : "v"(A), "n"(O) : "memory");
#define MM(C, R, XF, WF)                                                                           \
  if ((C) < NW) {                                                                                    \
    const bf16x8 wf_ = mk8(WF[2 * (C)][0], WF[2 * (C)][1], WF[2 * (C) + 1][0], WF[2 * (C) + 1][1]); \
    const bf16x8 xf_ = __builtin_bit_cast(bf16x8, XF[R]);                                          \
    acc[C][R] = TY == 0 ? MFMA(wf_, xf_, acc[C][R]) : MFMA(xf_, wf_, acc[C][R]);                   \
    __builtin_amdgcn_sched_barrier(0);                                                             \
  }
#define READS(QX, QW)                                                                              \
  RW(QW[0], aw0, 0) RW(QW[1], aw0, 4096) RX(QX[1], ax, 2048)                                       \
  if (NW == 2) { RW(QW[2], aw1, 0) RW(QW[3], aw1, 4096) }                                          \
  RX(QX[2], ax, 4096) RX(QX[3], ax, 6144) RX(QX[4], ax, 8192) RX(QX[5], ax, 10240) RX(QX[0], az, 0)
#define ARRIVED(PX, PW)                                                                            \
  if (NW == 2)                                                                                     \
    asm volatile("s_waitcnt lgkmcnt(0)" : "+v"(PX[0]), "+v"(PX[1]), "+v"(PX[2]), "+v"(PX[3]), "+v"(PX[4]), "+v"(PX[5]), \
                 "+v"(PW[0]), "+v"(PW[1]), "+v"(PW[2]), "+v"(PW[3])::"memory");                    \
  else                                                                                             \
    asm volatile("s_waitcnt lgkmcnt(0)" : "+v"(PX[0]), "+v"(PX[1]), "+v"(PX[2]), "+v"(PX[3]), "+v"(PX[4]), "+v"(PX[5]), \
                 "+v"(PW[0]), "+v"(PW[1])::"memory");                                              \
  __builtin_amdgcn_sched_barrier(0);
#define SB __builtin_amdgcn_sched_barrier(0);
#define WT(N, ...) asm volatile("s_waitcnt lgkmcnt(" #N ")" : __VA_ARGS__::"memory"); SB
#define HSTAGE2(PX, PW, QX, QW, NX)                                                                \
  BAR                                                                                              \
  WT(7, "+v"(PW[0]), "+v"(PW[1]), "+v"(PX[1]))                                                     \
  MM(0, 1, PX, PW) RW(QW[0], aw0, 0)                                                               \
  WT(6, "+v"(PW[2]), "+v"(PW[3]))                                                                  \
  MM(1, 1, PX, PW) RW(QW[1], aw0, 4096)                                                            \
  WT(6, "+v"(PX[2]))                                                                               \
  MM(0, 2, PX, PW) RX(QX[1], ax, 2048) SB                                                          \
  MM(1, 2, PX, PW) RW(QW[2], aw1, 0)                                                               \
  WT(7, "+v"(PX[3]))                                                                               \
  MM(0, 3, PX, PW) RW(QW[3], aw1, 4096) SB                                                         \
  MM(1, 3, PX, PW) RX(QX[2], ax, 4096)                                                             \
  WT(6, "+v"(PX[4]), "+v"(PX[5]), "+v"(PX[0]))                                                     \
  MM(0, 4, PX, PW) RX(QX[3], ax, 6144) SB                                                          \
  MM(1, 4, PX, PW) RX(QX[4], ax, 8192) SB                                                          \
  MM(0, 5, PX, PW) RX(QX[5], ax, 10240) SB                                                         \
  MM(1, 5, PX, PW) RX(QX[0], az, 0) SB                                                             \
  MM(0, 0, PX, PW)                                                                                 \
  NX asm volatile("" : "+v"(aw0), "+v"(aw1), "+v"(ax), "+v"(az), "+s"(so), "+s"(xo)); SB           \
  MM(1, 0, PX, PW)
#define HSTAGE1(PX, PW, QX, QW, NX)                                                                \
  BAR                                                                                              \
  ARRIVED(PX, PW)                                                                                  \
  MM(0, 1, PX, PW) RW(QW[0], aw0, 0) RW(QW[1], aw0, 4096) SB                                       \
  MM(0, 2, PX, PW) RX(QX[1], ax, 2048) RX(QX[2], ax, 4096) SB                                      \
  MM(0, 3, PX, PW) RX(QX[3], ax, 6144) RX(QX[4], ax, 8192) SB                                      \
  MM(0, 4, PX, PW) RX(QX[5], ax, 10240) RX(QX[0], az, 0) SB                                        \
  MM(0, 5, PX, PW)                                                                                 \
  NX asm volatile("" : "+v"(aw0), "+v"(ax), "+v"(az), "+s"(so), "+s"(xo)); SB                      \
  MM(0, 0, PX, PW)
#define HSTAGE(...) if (NW == 2) { HSTAGE2(__VA_ARGS__) } else { HSTAGE1(__VA_ARGS__) }
      u32 aw0 = wa[0], aw1 = wa[1], ax = xa0, az = xz0;
      asm volatile("s_waitcnt vmcnt(0)\n\ts_barrier" ::: "memory");
      READS(xA, wA)
      aw0 += HSLOT; aw1 += HSLOT; ax ^= 64; az ^= 64;
      int so = 2 * HSLOT, xo = XSLOT;
#pragma unroll 1
      for (int s = 0; s < 9; ++s) {
        HSTAGE(xA, wA, xB, wB, aw0 = wa[0] + so; aw1 = wa[1] + so; ax = xa0 + xo; az = xz0 + xo * m1;
               so = so >= 4 * HSLOT ? 0 : so + 2 * HSLOT; xo = xo >= 3 * XSLOT ? 0 : xo + XSLOT;)
        HSTAGE(xB, wB, xA, wA, aw0 += HSLOT; aw1 += HSLOT; ax ^= 64; az ^= 64;)
      }
      {
        HSTAGE(xA, wA, xB, wB, )
        ARRIVED(xB, wB)
        BAR
        SB
      }
      char* ex = smem + EXO + cw * 6144 + lane * 8;
      if (TY == 0) {
        float st[6];
        const u32 ta = TAB_OFF + r16 * 4;
#define RT(R) asm volatile("ds_read_b32 %0, %1 offset:%2" : "=v"(st[R]) : "v"(ta), "n"(64 * (R)) : "memory");
        RT(1) RT(2) RT(3) RT(4) RT(5) RT(0)
        float cs[2][4];
#define FMS(A, M, CS) ((A) - (M) * (CS))
#define EP0(C, R)                                                                                  \
  {                                                                                                \
    const u32 a = cvt2((f32x2){FMS(acc[C][R][0], st[R], cs[C][0]), FMS(acc[C][R][1], st[R], cs[C][1])}); \
    const u32 b = cvt2((f32x2){FMS(acc[C][R][2], st[R], cs[C][2]), FMS(acc[C][R][3], st[R], cs[C][3])}); \
    *(u32x2*)(ex + (C) * 3072 + (R) * 512) = (u32x2){a, b};                                        \
    SB                                                                                             \
  }
        MM(0, 0, xB, wB) MM(1, 0, xB, wB) MM(0, 1, xB, wB) MM(1, 1, xB, wB)
#pragma unroll
        for (int i = 0; i < 4; ++i) cs[0][i] = dppf<0x150>(acc[0][0][i]);
        SB
        MM(0, 2, xB, wB)
#pragma unroll
        for (int i = 0; i < 4; ++i) cs[1][i] = dppf<0x150>(acc[1][0][i]);
        WT(0, "+v"(st[0]), "+v"(st[1]), "+v"(st[2]), "+v"(st[3]), "+v"(st[4]), "+v"(st[5]))
        MM(1, 2, xB, wB) EP0(0, 1)
        MM(0, 3, xB, wB) EP0(1, 1)
        MM(1, 3, xB, wB) EP0(0, 2)
        MM(0, 4, xB, wB) EP0(1, 2)
        MM(1, 4, xB, wB) EP0(0, 3)
        MM(0, 5, xB, wB) EP0(1, 3)
        MM(1, 5, xB, wB)
        if (cw >= 2) {
          EP0(1, 0)
          if (cw >= 3) EP0(0, 0)
        }
        EP0(0, 4) EP0(1, 4) EP0(0, 5) EP0(1, 5)
      } else {
        f32x4 mbv[6];
        const u32 ta = TAB_OFF + g * 16;
#define RV(R) RX(mbv[R], ta, 64 * (R))
        RV(1) RV(2) RV(3) RV(4) RV(5) RV(0)
        float cs[2];
#define CS1(C)                                                                                     \
  if ((C) < NW) {                                                                                  \
    const u32 v0 = __float_as_uint(acc[C][0][0]);                                                  \
    const u32 v1 = __builtin_amdgcn_permlane16_swap(v0, v0, false, false)[0];                      \
    cs[C] = __uint_as_float(__builtin_amdgcn_permlane32_swap(v1, v1, false, false)[0]);            \
    SB                                                                                             \
  }
#define EP1(C, R)                                                                                  \
  if ((C) < NW) {                                                                                  \
    const f32x4 v = (f32x4){FMS(acc[C][R][0], mbv[R][0], cs[C]), FMS(acc[C][R][1], mbv[R][1], cs[C]), FMS(acc[C][R][2], mbv[R][2], cs[C]), FMS(acc[C][R][3], mbv[R][3], cs[C])}; \
    *(u32x2*)(ex + (C) * 3072 + (R) * 512) = (u32x2){cvt2((f32x2){v[0], v[1]}), cvt2((f32x2){v[2], v[3]})}; \
    SB                                                                                             \
  }
        MM(0, 0, xB, wB) MM(1, 0, xB, wB) MM(0, 1, xB, wB) MM(1, 1, xB, wB)
        CS1(0)
        MM(0, 2, xB, wB)
        CS1(1)
        WT(0, "+v"(mbv[0]), "+v"(mbv[1]), "+v"(mbv[2]), "+v"(mbv[3]), "+v"(mbv[4]), "+v"(mbv[5]))
        MM(1, 2, xB, wB) EP1(0, 1)
        MM(0, 3, xB, wB) EP1(1, 1)
        MM(1, 3, xB, wB) EP1(0, 2)
        MM(0, 4, xB, wB) EP1(1, 2)
        MM(1, 4, xB, wB) EP1(0, 3)
        MM(0, 5, xB, wB) EP1(1, 3)
        MM(1, 5, xB, wB)
        EP1(0, 0) EP1(1, 0) EP1(0, 4) EP1(1, 4) EP1(0, 5) EP1(1, 5)
      }
    };
    if (cw < 5) run(icst<0>{});
    else if (cw < 7) run(icst<1>{});
    else run(icst<2>{});
  }
  __builtin_amdgcn_s_setprio(0);
  const bool live = r0 + rt * 16 < SEQ;
  if (!live) {
    if (w >= 2) LDS_BARRIER();
    if (w != 7) {
      asm volatile("s_waitcnt vmcnt(0)" ::: "memory");
      return;
    }
  } else if (w >= 2)
    asm volatile(ATT_ASM
                 :: [kh] "s"(kh), [l16] "v"(l16), [os] "s"(os), [os4] "s"(os4), [b0] "v"(bias[0]), [b1] "v"(bias[1]),
                    [s0] "v"(sel[0]), [s1] "v"(sel[1]), [s2] "v"(sel[2]), [s3] "v"(sel[3]), [e4] "v"(e4), [eh] "v"(eh), [aq] "v"(aq),
                    [aka] "v"(aka), [akb] "v"(akb), [arec] "v"(arec), [vpp] "v"(vpp), [sst] "s"(sst), [w] "s"(w)
                 : "memory", "s41", "s46", "s47", "s48", "s49", "s50", "s51", "scc", "v24", "v25", "v26", "v27", "v28", "v29", "v30", "v31", "v32", "v33", "v34", "v35", "v36", "v37", "v38", "v39", "v40", "v41", "v42", "v43", "v44", "v45", "v46", "v47", "v48", "v49", "v50", "v51", "v52", "v53", "v54", "v55", "v56", "v57", "v58", "v59", "v60", "v61", "v62", "v63", "v64", "v65", "v66", "v67", "v68", "v69", "v70", "v71", "v72", "v73", "v74", "v75", "v76", "v77", "v78", "v79", "v80", "v81", "v82", "v83", "v84", "v85", "v86", "v87");
  u32x4 hwa, hwb;
  u32x2 hwc;
  const int grp = rt < 3 ? 0 : 1;
  if (w < 8) {
    const int rmax = mt == 25 ? 2 : 4;
    const int rk = rt < rmax ? rt : rmax, rd = (rt ^ 1) < rmax ? (rt ^ 1) : rmax;
    u32 ta, tk;
    if (w == 0) asm volatile("s_waitcnt vmcnt(0)" ::: "memory");
    if (w == 2 || w == 3) asm volatile("s_waitcnt vmcnt(3)" ::: "memory");
    asm volatile("s_cmp_lg_u32 %[w], 1\n\ts_cbranch_scc1 2f\n\ts_setprio 3\n"
                 "1:\n\ts_memrealtime vcc\n\ts_waitcnt lgkmcnt(0)\n\ts_sub_u32 vcc_lo, vcc_lo, %[go]\n\ts_cmp_lt_i32 vcc_lo, 0\n\ts_cbranch_scc1 1b\n\ts_setprio 0\n"
                 "2:\n\ts_waitcnt lgkmcnt(0)\n\ts_barrier\n\t" OP_ASM
                 : "=&{v[84:87]}"(hwa), "=&{v[88:91]}"(hwb), "=&{v[92:93]}"(hwc), [ta] "=&v"(ta), [tk] "=&v"(tk)
                 : [w] "s"(w), [go] "s"(tgo), [l16] "v"(l16), [oa0] "s"(oa[0]), [so0] "s"(so_[0]), [oa1] "s"(oa[1]), [so1] "s"(so_[1]), [oa2] "s"(oa[2]), [so2] "s"(so_[2]), [oa3] "s"(oa[3]), [so3] "s"(so_[3]), [oa4] "s"(oa[4]), [so4] "s"(so_[4]), [oa5] "s"(oa[5]), [so5] "s"(so_[5]), [oa6] "s"(oa[6]), [so6] "s"(so_[6]), [oa7] "s"(oa[7]), [so7] "s"(so_[7]), [oa8] "s"(oa[8]), [so8] "s"(so_[8]), [oa9] "s"(oa[9]), [so9] "s"(so_[9]),
                   [pm] "v"(pm), [pf] "v"(pf), [ra] "v"(w < 3 ? (u32)(CTL_OFF + 72) : hi), [e4] "v"(e4),
                   [aw] "v"((lane & 1) ? (u32)(WR_OFF + g * 40) : (u32)ONES_OFF),
                   [vk] "v"(l16 + rk * 20480), [vd] "v"(l16 + rd * 20480), [sk] "s"((w & 1) * 6144),
                   [sd] "s"(EXO + (w < 4 ? 4 + w : rt ^ 1) * 5120), [sr] "s"(EXO + rt * 5120),
                   [sc] "s"(CTL_OFF + 48 + (w < 4 ? 4 : rt ^ 1) * 4), [sp] "s"(PST_OFF + rt * 256), [sa] "s"(CTL_OFF + 8 + grp * 4),
                   [sst] "s"(sst), [cp] "s"(ws + WS_PC + (2 * mt + grp) * 1280 + hg * 160), [oback] "n"(OBACK), [cap] "n"(SPIN_CAP)
                 : "memory", "vcc", "scc", "s43", "v24", "v25", "v26", "v27", "v28", "v29", "v30", "v31", "v32", "v33", "v34", "v35", "v36", "v37", "v38", "v39", "v40", "v41", "v42", "v43", "v44", "v45", "v46", "v47", "v48", "v49", "v50", "v51", "v52", "v53", "v54", "v55", "v56", "v57", "v58", "v59", "v60", "v61", "v62", "v63", "v64", "v65", "v66", "v67", "v68", "v69", "v70", "v71", "v72", "v73", "v74", "v75", "v76", "v77", "v78", "v79", "v80", "v81", "v82", "v83", "v94", "v95", "v96", "v97", "v98", "v99", "v100", "v101", "v102", "v103", "v104", "v105", "v106", "v107", "v108", "v109", "v110", "v111", "v112", "v113", "v114", "v115", "v116", "v117", "v118", "v119", "v120", "v121", "v122", "v123", "v124", "v125", "v126", "v127", "v128", "v129", "v130", "v131", "v132", "v133", "v134", "v135", "v136", "v137", "v138", "v139", "v140", "v141", "v142", "v143", "v144", "v145", "v146", "v147", "v148", "v149", "v150", "v151", "v152", "v153", "v154", "v155", "v156", "v157", "v158", "v159");
    if (!live || w < 3) return;
  } else {
    u64 tb;
    asm volatile("s_setprio 3\n\ts_waitcnt lgkmcnt(0)\n\ts_barrier\n\ts_memrealtime %0\n\ts_waitcnt lgkmcnt(0)" : "=s"(tb)::"memory");
    if (w == 8 || w == 11) {
      const int cg = w == 11, gid = 2 * mt + cg;
      const char* sp = sst - hg * 1024;
      const char* sp2 = sp + (cg ? 0 : 7936);
      const u32 vq = (lane >> 3) * 1024 + (lane & 7) * 16 + (cg ? 640 : 768);
      u32x4 pq[3];
      u64 t2;
#define PLOADS "global_load_dwordx4 %0, %4, %5 sc1\n\tglobal_load_dwordx4 %1, %4, %5 offset:128 sc1\n\tglobal_load_dwordx4 %2, %4, %6 sc1\n\t"
      asm volatile("1:\n\ts_memrealtime %3\n\ts_waitcnt lgkmcnt(0)\n\ts_sub_u32 s40, %7, s40\n\ts_cmp_gt_i32 s40, 0\n\ts_cbranch_scc1 1b\n\t" PLOADS "s_setprio 1"
                   : "=&v"(pq[0]), "=&v"(pq[1]), "=&v"(pq[2]), "=&{s[40:41]}"(t2) : "v"(vq), "s"(sp), "s"(sp2), "s"((u32)tb + PDT) : "memory", "scc");
      int lane2 = lane;
      asm volatile("" : "+v"(lane2));
      const int n16 = lane2 & 15;
      u32x4 sl;
      u32x2 km;
#pragma unroll
      for (int e = 0; e < 4; ++e) sl[e] = n16 == 2 * e ? 0x3F80u : (n16 == 2 * e + 1 ? 0x3F800000u : 0u);
      u32 rid = (u32)((((lane2 >> 4) & 1) * 2 + (lane2 >> 5)) * 4 + (lane2 & 3)) << 10;
      u32 rid1 = rid + (16 << 10), rid2 = rid + (cg ? 0 : 32 << 10), msk = n16 < 4 ? 0xFFFF0000u : 0u;
      u32 ah = HIM + cg * 7680 + (lane2 < 10 ? lane2 : 9) * 16;
      km[1] = *(const u32*)(smem + CTL_OFF);
      const char* kp = ws + WS_PK + gid * 6144 + hg * 768;
      const char* cp = ws + WS_PC + gid * 1280 + hg * 160;
      asm volatile("" : "+s"(kp), "+s"(cp));
      km[0] = ((u32)tb >> 1) & 1023u;
      asm volatile("s_setprio " "3" "\n\ts_waitcnt vmcnt(0)" : "+v"(pq[0]), "+v"(pq[1]), "+v"(pq[2]), "+v"(sl), "+v"(rid), "+v"(rid1), "+v"(rid2), "+v"(msk), "+v"(ah), "+v"(km)::"memory");
#define PMISS __any(max(max(max(pq[0][0], pq[0][2]), max(pq[1][0], pq[1][2])), max(pq[2][0], pq[2][2])) == SENT)
      if (__builtin_expect(PMISS, 0)) {
#pragma nounroll
        for (int it = 0; it < SPIN_CAP; ++it) {
          asm volatile(PLOADS "s_waitcnt vmcnt(0)" : "=&v"(pq[0]), "=&v"(pq[1]), "=&v"(pq[2]), "=&s"(t2) : "v"(vq), "s"(sp), "s"(sp2) : "memory");
          if (!PMISS) break;
          __builtin_amdgcn_s_sleep(2);
        }
      }
      u32 pkey = 0;
#pragma unroll
      for (int t = 0; t < 3; ++t) {
        const f32x4 ys = MFMA(__builtin_bit_cast(bf16x8, pq[t]), __builtin_bit_cast(bf16x8, sl), ((f32x4){0.f, 0.f, 0.f, 0.f}));
        const float y = tr_sum(ys);
        pkey = max(pkey, (mono(y * __builtin_amdgcn_rcpf(dppf<0x104>(y))) & msk) | (t == 0 ? rid : (t == 1 ? rid1 : rid2)));
      }
      pkey = max(pkey, dppu<0xB1>(pkey));
      pkey = max(pkey, dppu<0x4E>(pkey));
      pkey = max(max((u32)__builtin_amdgcn_readlane(pkey, 0), (u32)__builtin_amdgcn_readlane(pkey, 16)),
                 max((u32)__builtin_amdgcn_readlane(pkey, 32), (u32)__builtin_amdgcn_readlane(pkey, 48)));
      km[0] |= pkey;
      asm volatile("s_mov_b32 exec_lo, 0x01010101\n\ts_mov_b32 exec_hi, 0x0101\n\tglobal_store_dwordx2 %0, %1, %2 sc1\n\ts_mov_b64 exec, -1\n\ts_setprio 2" ::"v"(l16), "v"(km), "s"(kp) : "memory");
      ah += ((pkey >> 10) & 63u) * 160;
      if (lane == 0) *(u32*)(smem + CTL_OFF + 24 + cg * 4) = ah;
      __builtin_amdgcn_s_setprio(0);
#pragma nounroll
      for (int it = 0; it < 32; ++it) {
        if ((int)((u32)__builtin_amdgcn_s_memrealtime() - (u32)tb) >= PSL) break;
        __builtin_amdgcn_s_sleep(6);
      }
      __builtin_amdgcn_s_setprio(2);
      asm volatile("s_mov_b32 s40, %[cap]\n"
                   "1:\n\tds_read_b32 v100, %[ac]\n\tds_read_b32 v101, %[ac] offset:24\n\tds_read_b128 v[96:99], %[ah]\n\ts_sub_u32 s40, s40, 1\n\ts_cbranch_scc1 2f\n\ts_waitcnt lgkmcnt(0)\n\t"
                   "v_cmp_gt_u32 vcc, %[nf], v100\n\ts_cbranch_vccz 2f\n\ts_sleep 2\n\ts_branch 1b\n"
                   "2:\n\ts_waitcnt lgkmcnt(0)\n\tv_cmp_eq_u32 vcc, 0, v101\n\ts_cbranch_vccz 3f\n\t"
                   "s_mov_b64 exec, 0x3ff\n\tglobal_store_dwordx4 %[l16], v[96:99], %[cp] sc1\n\ts_mov_b64 exec, -1\n3:"
                   ::[cap] "n"(SPIN_CAP * 8), [ac] "v"((u32)(CTL_OFF + 8 + cg * 4)), [ah] "v"(ah), [nf] "s"(3 - cg), [l16] "v"(l16), [cp] "s"(cp)
                   : "memory", "vcc", "scc", "s40", "v96", "v97", "v98", "v99", "v100", "v101");
      return;
    }
    __builtin_amdgcn_s_setprio(0);
    if (w == 10 && mt == 25) return;
    const int cg = w - 9, gid = 2 * mt + cg;
    const int nr = cg ? 31 : 47;
    const int lr = lane < nr ? lane : nr;
    const int lrow = cg * 48 + lr;
    const int li = (lane & 31) < 20 ? (lane & 31) : 19;
    asm volatile(COL_ASM
                 :
                 : [lq] "v"((u32)(lr * 16 + hg * 1024)), [ac] "v"((u32)(CTL_OFF + 8 + cg * 4)), [nf] "s"(3 - cg),
                   [sp] "s"(ws + WS_STAT + (size_t)gid * 8192), [ao] "v"((u32)(PST_OFF + lrow * 16)), [rdm] "s"(1.0f / DM),
                   [swr] "v"(*(const float*)(smem + CTL_OFF + 40)), [msk] "s"(0xFFFFF000u), [rid] "v"(0x800u | (u32)(2047 - (r0 + lrow))),
                   [at] "v"((u32)(TAB_OFF + lrow * 8)), [zr] "v"(l16), "{v99}"(*(const u32*)(smem + CTL_OFF)),
                   [kp] "s"(ws + WS_PK + gid * 6144 + hg * 768 + 8), [ak] "v"((u32)(CTL_OFF + 16 + cg * 4)), [rb] "s"(2047 - r0 - cg * 48),
                   [ah] "v"((u32)(HIM + cg * 7680 + li * 8)), [co] "v"((u32)((lane < 32 ? 1280 : 0) + li * 8)),
                   [cp] "s"(ws + WS_CAND + gid * 2560 + hg * 160), [pdel] "n"(PDEL2), [cap] "n"(SPIN_CAP)
                 : "memory", "vcc", "scc", "s40", "s41", "s42", "s43", "s44", "s45", "s46", "s47", "v64", "v65", "v66", "v67", "v68", "v69", "v70", "v71", "v72", "v73", "v74", "v75", "v76", "v77", "v78", "v79", "v80", "v81", "v82", "v83", "v84", "v85", "v86", "v87", "v88", "v89", "v90", "v91", "v92", "v93", "v94", "v95", "v96", "v97", "v98", "v100", "v101", "v102", "v103", "v104", "v105", "v106", "v107", "v108", "v109", "v110", "v111", "v112", "v113", "v114", "v115", "v116", "v117", "v118", "v119", "v120", "v121", "v122", "v123");
    return;
  }
  const u32 hw[10] = {hwa[0], hwa[1], hwa[2], hwa[3], hwb[0], hwb[1], hwb[2], hwb[3], hwc[0], hwc[1]};
  f32x2 hf[10];
#pragma unroll
  for (int e = 0; e < 10; ++e) {
    hf[e] = unpk(hw[e]);
    asm volatile("" : "+v"(hf[e]));
  }
  const char* obase = (const char*)out + (size_t)r0 * 1280 + hg * 160;
  u32 tilekey;
  asm volatile("" : "+s"(obase));
  u32 aow = EXO + rt * 5120 + r16 * 160 + g * 40, aor = EXO + rt * 5120 + l16, vo3[3], rr3[3];
#pragma unroll
  for (int i = 0; i < 3; ++i) {
    const u32 c = i * 64 + lane, r = c / 10u;
    vo3[i] = (rt * 16 + r) * 1120 + rt * 2560 + c * 16;
    rr3[i] = c < 160u ? 2047 - (r0 + rt * 16 + r) : 4096u;
    asm volatile("" : "+v"(vo3[i]), "+v"(rr3[i]));
  }
  asm volatile("" : "+v"(aow), "+v"(aor));
  f32x2 st2;
  asm volatile("s_mov_b32 s40, %[cap]\n"
               "1:\n\ts_sleep 1\n\tds_read_b32 %[k], %[ak]\n\tds_read_b64 %[s], %[at]\n\ts_sub_u32 s40, s40, 1\n\ts_waitcnt lgkmcnt(0)\n\ts_cbranch_scc1 2f\n\t"
               "v_cmp_eq_u32 vcc, 0, %[k]\n\ts_cbranch_vccnz 1b\n"
               "2:"
               : [k] "=&v"(tilekey), [s] "=&v"(st2) : [cap] "n"(SPIN_CAP * 8), [ak] "v"((u32)(CTL_OFF + 16 + grp * 4)), [at] "v"((u32)(TAB_OFF + (rt * 16 + r16) * 8))
               : "memory", "vcc", "scc", "s40");
  u32 ok[10];
#pragma unroll
  for (int e = 0; e < 10; ++e) ok[e] = cvt2(__builtin_elementwise_fma(hf[e] - (f32x2){st2[0], st2[0]}, (f32x2){st2[1], st2[1]}, hf[e]));
  {
#pragma unroll
    for (int e = 0; e < 5; ++e) asm volatile("ds_write_b64 %0, %1 offset:%2" ::"v"(aow), "v"((u32x2){ok[2 * e], ok[2 * e + 1]}), "n"(e * 8) : "memory");
    u32x4 o3[3];
    asm volatile("ds_read_b128 %0, %3\n\tds_read_b128 %1, %3 offset:1024\n\tds_read_b128 %2, %3 offset:2048\n\ts_waitcnt lgkmcnt(0)"
                 : "=&v"(o3[0]), "=&v"(o3[1]), "=&v"(o3[2]) : "v"(aor) : "memory");
#pragma unroll
    for (int i = 0; i < 3; ++i)
      if (rr3[i] != (tilekey & 2047u) && rr3[i] != 4096u)
        asm volatile("global_store_dwordx4 %0, %1, %2" " sc1" "\n\ts_nop 1" ::"v"(vo3[i]), "v"(o3[i]), "s"(obase) : "memory");
  }
}

extern "C" void kernel_launch(void* const* d_in, const int* in_sizes, int n_in,
                              void* d_out, int out_size, void* d_ws, size_t ws_size,
                              hipStream_t stream) {
  const u16* x = (const u16*)d_in[0];
  const u16* wqkv = (const u16*)d_in[1];
  const u16* wo = (const u16*)d_in[2];
  const u16* w_r = (const u16*)d_in[4];
  const u16* w_gate = (const u16*)d_in[5];
  const u16* w_up = (const u16*)d_in[6];
  const u16* w_down = (const u16*)d_in[7];
  (void)hipFuncSetAttribute((const void*)fused_block, hipFuncAttributeMaxDynamicSharedMemorySize, LDS_TOTAL);
  fused_block<<<256, 768, LDS_TOTAL, stream>>>(x, wqkv, wo, w_r, w_gate, w_up, w_down, (u16*)d_out, (char*)d_ws);
}
```
